```python
import torch

def _softmax(x, axis=-1):
    return torch.softmax(x, dim=axis)
def _gelu(x):
    return torch.nn.functional.gelu(x, approximate="tanh")
def _silu(x):
    return torch.nn.functional.silu(x)
def _sigmoid(x):
    return torch.sigmoid(x)
def _layernorm(x, axis=-1, eps=1e-5):
    m = x.mean(dim=axis, keepdim=True)
    v = x.var(dim=axis, keepdim=True, unbiased=False)
    return (x - m) / torch.sqrt(v + eps)
def _scatter_add(tgt, idx, src, axis=0):
    return tgt.clone().index_add_(axis, idx, src)
def _segment_sum(src, seg, n):
    out = torch.zeros((n,) + src.shape[1:], dtype=src.dtype)
    return out.index_add_(0, seg, src)
def _pad(x, pw):
    flat = tuple(v for pair in reversed(pw) for v in pair)
    return torch.nn.functional.pad(x, flat)

kernel_name = 'concat_heads__linear_kernel_hedgehog__compressed_kv_cla__prompt'

def setup_inputs():

    _w = (lambda a: (lambda t: t.to(torch.bfloat16) if t.is_floating_point() else t)(torch.as_tensor(a)))
    def _A_setup():
        B, S, D, H = 1, 2048, 512, 32
        Dh = D // H
        return {
            "x": _w((lambda *s: torch.randn(*s, dtype=torch.bfloat16) * 0.4)(B, S, D)),
            "wqkv": _w((lambda *s: torch.randn(*s, dtype=torch.bfloat16) * 0.4)(D, 3 * D)),
            "w_phi": _w((lambda *s: torch.randn(*s, dtype=torch.bfloat16) * 0.4)(H, Dh, Dh) * (Dh ** -0.5)),
            "wo": _w((lambda *s: torch.randn(*s, dtype=torch.bfloat16) * 0.05)(D, D)),
        }
    _A_in = _A_setup()

    def _B_setup():
        B, H, Sq, Sk, D, M = 1, 32, 2048, 2048, 128, 512
        Dc = 4 * D
        return {
            "x": _w((lambda *s: torch.randn(*s, dtype=torch.bfloat16) * 0.8)(B, Sq, M)),
            "c_kv": _w((lambda *s: torch.randn(*s, dtype=torch.bfloat16) * 0.4)(B, Sk, Dc)),
            "w_q": _w((lambda *s: torch.randn(*s, dtype=torch.bfloat16) * 6.4)(M, H, D) * (M ** -0.5)),
            "w_uk": _w((lambda *s: torch.randn(*s, dtype=torch.bfloat16) * 6.4)(Dc, H, D) * (Dc ** -0.5)),
            "w_uv": _w((lambda *s: torch.randn(*s, dtype=torch.bfloat16) * 0.4)(Dc, H, D) * (Dc ** -0.5)),
            "w_o": _w((lambda *s: torch.randn(*s, dtype=torch.bfloat16) * 0.4)(H, D, M) * ((H * D) ** -0.5)),
        }
    _B_in = _B_setup()

    return {
        "A_x": _A_in["x"],
        "A_wqkv": _A_in["wqkv"],
        "A_w_phi": _A_in["w_phi"],
        "A_wo": _A_in["wo"],
        "B_x": _B_in["x"],
        "B_c_kv": _B_in["c_kv"],
        "B_w_q": _B_in["w_q"],
        "B_w_uk": _B_in["w_uk"],
        "B_w_uv": _B_in["w_uv"],
        "B_w_o": _B_in["w_o"],
    }

def reference(A_x, A_wqkv, A_w_phi, A_wo, B_x, B_c_kv, B_w_q, B_w_uk, B_w_uv, B_w_o):
    def _A_ref(x, wqkv, w_phi, wo):
        B, S, D = x.shape
        H = 32; Dh = D // H
        qkv = (lambda x, *d: x.permute(*d))((x @ wqkv).reshape(B, S, 3, H, Dh), 2, 0, 3, 1, 4)
        q, k, v = qkv[0], qkv[1], qkv[2]
        pq = torch.einsum("bhsd,hdf->bhsf", q, w_phi)
        pk = torch.einsum("bhsd,hdf->bhsf", k, w_phi)
        phi_q = torch.concatenate([_softmax(pq, axis=-1), _softmax(-pq, axis=-1)], axis=-1)
        phi_k = torch.concatenate([_softmax(pk, axis=-1), _softmax(-pk, axis=-1)], axis=-1)
        kv = torch.einsum("bhsf,bhsd->bhfd", phi_k, v)
        z  = torch.einsum("bhsf->bhf", phi_k)
        num = torch.einsum("bhqf,bhfd->bhqd", phi_q, kv)
        den = torch.einsum("bhqf,bhf->bhq", phi_q, z)[..., None] + 1e-6
        o = num / den
        return (lambda x, *d: x.permute(*d))(o, 0, 2, 1, 3).reshape(B, S, D) @ wo

    def _B_ref(x, c_kv, w_q, w_uk, w_uv, w_o):
        B, Sq, M = x.shape[0], x.shape[1], x.shape[2]
        Sk = c_kv.shape[1]
        H, D = w_q.shape[1], w_q.shape[2]
        q = torch.einsum("bqm,mhd->bhqd", x, w_q)
        k = torch.einsum("bkc,chd->bhkd", c_kv, w_uk)
        v = torch.einsum("bkc,chd->bhkd", c_kv, w_uv)
        scores = torch.einsum("bhqd,bhkd->bhqk", q, k) * (D ** -0.5)
        off = Sk - Sq
        qi = torch.arange(Sq)[:, None]
        kj = torch.arange(Sk)[None, :]
        cmask = kj <= (qi + off)
        scores = torch.where(cmask[None, None], scores, -1e9)
        p = _softmax(scores, axis=-1)
        ctx = torch.einsum("bhqk,bhkd->bhqd", p, v)
        return torch.einsum("bhqd,hdm->bqm", ctx, w_o)

    a = _A_ref(A_x, A_wqkv, A_w_phi, A_wo)
    b = _B_ref(B_x, B_c_kv, B_w_q, B_w_uk, B_w_uv, B_w_o)
    return torch.concatenate([a, b], axis=-1)
```

```cpp
#include <hip/hip_runtime.h>

__global__ __launch_bounds__(256, 1) void fused_fwd(const void* a_x, const void* a_wqkv, const void* a_wphi, const void* a_wo, const void* b_x, const void* b_c, const void* b_wq, const void* b_wk, const void* b_wv, const void* b_wo, void* out, void* ws) {
  __shared__ __attribute__((aligned(16))) char lds[163840];
  const unsigned wave = __builtin_amdgcn_readfirstlane(threadIdx.x >> 6);
  const unsigned bid = blockIdx.x;
  asm volatile(
R"(.macro mm x0, x1, x2, x3:vararg
v_mfma_f32_16x16x32_bf16 \x0, \x1, \x2, \x3
.endm
.macro dr x0, x1:vararg
ds_read_b128 \x0, \x1
.endm
.macro dt x0, x1:vararg
ds_read_b64_tr_b16 \x0, \x1
.endm
.macro cv x0, x1, x2:vararg
v_cvt_pk_bf16_f32 \x0, \x1, \x2
.endm
.macro ar x0, x1:vararg
v_accvgpr_read_b32 \x0, \x1
.endm
.macro ex x0, x1:vararg
)"
R"(v_exp_f32 \x0, \x1
.endm
.macro bl x0, x1, x2:vararg
buffer_load_dwordx4 \x0, \x1, \x2
.endm
.macro sa x0, x1, x2:vararg
s_add_u32 \x0, \x1, \x2
.endm
s_load_dwordx16 s[64:79], %0, 0x0
s_load_dwordx8 s[80:87], %0, 0x40
s_mov_b32 s51, %1
s_mov_b32 s50, %2
v_mbcnt_lo_u32_b32 v213, -1, 0
v_mbcnt_hi_u32_b32 v213, -1, v213
s_lshr_b32 s62, %1, 3
s_lshl_b32 s62, s62, 2
)"
R"(sa s62,s62,%2
s_lshl_b32 s62, s62, 10
v_lshlrev_b32 v215, 7, v213
v_add_u32 v215, s62, v215
v_cmp_gt_u32 vcc, 8, v213
v_mov_b32 v152, 0x7ffffff0
s_nop 0
v_cndmask_b32 v215, v152, v215, vcc
s_getpc_b64 s[88:89]
L_ct_entry:
sa s88,s88,(L_kv_start-L_ct_entry)
s_addc_u32 s89, s89, 0
s_and_b32 s89, s89, 0xffff
s_mov_b32 s90, (L_kv_done-L_kv_start)
s_mov_b32 s91, 0x20000
buffer_load_dword v164, v215, s[88:91], 0 offen
)"
R"(s_waitcnt lgkmcnt(0)
s_and_b32 s48, s51, 7
s_lshl_b32 s48, s48, 2
s_bfe_u32 s56, s51, 0x20003
sa s48,s48,s56
s_lshr_b32 s49, s51, 5
s_mov_b32 s36, s74
s_and_b32 s37, s75, 0xffff
s_mov_b32 s38, 0x200000
s_mov_b32 s39, 0x20000
s_cmp_lt_u32 s50, 2
s_cselect_b32 s40, s78, s80
s_cselect_b32 s41, s79, s81
s_and_b32 s41, s41, 0xffff
s_mov_b32 s42, 0x400000
s_mov_b32 s43, 0x20000
)"
R"(s_lshl_b32 s52, s49, 2
sa s52,s52,s50
s_lshl_b32 s52, s52, 16
s_and_b32 s56, s50, 1
s_lshl_b32 s53, s56, 18
s_lshl_b32 s56, s48, 8
sa s53,s53,s56
s_lshl_b32 s54, s50, 13
sa s55,s54,0x8000
v_lshrrev_b32 v152, 3, v213
v_mul_u32_u24 v148, 1024, v152
v_lshrrev_b32 v152, 4, v213
v_and_b32 v153, 7, v213
v_xor_b32 v153, v153, v152
v_lshl_add_u32 v148, v153, 4, v148
v_xor_b32 v149, 64, v148
)"
R"(v_lshrrev_b32 v152, 4, v213
v_lshlrev_b32 v150, 13, v152
v_lshlrev_b32 v152, 1, v152
v_and_b32 v153, 15, v213
v_xor_b32 v153, v153, v152
v_lshl_add_u32 v150, v153, 4, v150
v_xor_b32 v151, 128, v150
v_lshlrev_b32 v214, 4, v213
sa m0,s54,0x0
sa s56,s52,0x0
bl v148,s[36:39],s56 offen lds
sa s56,s52,0x1c00
bl v149,s[36:39],s56 offen offset:1024 lds
sa s56,s52,0x3800
bl v148,s[36:39],s56 offen offset:2048 lds
sa s56,s52,0x5400
)"
R"(bl v149,s[36:39],s56 offen offset:3072 lds
sa m0,s54,0x1000
sa s56,s52,0x8000
bl v148,s[36:39],s56 offen lds
sa s56,s52,0x9c00
bl v149,s[36:39],s56 offen offset:1024 lds
sa s56,s52,0xb800
bl v148,s[36:39],s56 offen offset:2048 lds
sa s56,s52,0xd400
bl v149,s[36:39],s56 offen offset:3072 lds
sa m0,s55,0x0
sa s56,s53,0x0
bl v150,s[40:43],s56 offen nt lds
sa s56,s53,0x7c00
bl v150,s[40:43],s56 offen offset:1024 nt lds
sa s56,s53,0xf800
)"
R"(bl v151,s[40:43],s56 offen offset:2048 nt lds
sa s56,s53,0x17400
bl v151,s[40:43],s56 offen offset:3072 nt lds
sa m0,s55,0x1000
sa s56,s53,0x20000
bl v150,s[40:43],s56 offen nt lds
sa s56,s53,0x27c00
bl v150,s[40:43],s56 offen offset:1024 nt lds
sa s56,s53,0x2f800
bl v151,s[40:43],s56 offen offset:2048 nt lds
sa s56,s53,0x37400
bl v151,s[40:43],s56 offen offset:3072 nt lds
s_mov_b32 s44, s86
s_and_b32 s45, s87, 0xffff
s_mov_b32 s46, 0x4900000
s_mov_b32 s47, 0x20000
)"
R"(s_lshl_b32 s58, s48, 6
s_lshl_b32 s62, s49, 2
sa s58,s58,s62
sa s58,s58,0x10000
s_lshl_b32 s59, s49, 7
s_lshl_b32 s62, s48, 2
sa s59,s59,s62
sa s59,s59,0x12000
s_lshl_b32 s60, s49, 5
sa s60,s60,s48
s_lshl_b32 s60, s60, 4
sa s60,s60,0x11000
s_lshl_b32 s62, s51, 7
sa s62,s62,0x14000
s_cmp_eq_u32 s50, 1
s_cselect_b32 s62, s60, s62
)"
R"(s_cselect_b32 s63, 4, 32
sa s60,s60,0x2000
s_cmp_eq_u32 s50, 2
s_cselect_b32 s62, s60, s62
s_cselect_b32 s63, 4, s63
s_cmp_eq_u32 s50, 3
s_cselect_b32 s63, 0, s63
v_lshl_add_u32 v152, v213, 2, s62
v_mov_b32 v153, 0x7ffffff0
v_cmp_gt_u32 vcc, s63, v213
s_nop 1
v_cndmask_b32 v152, v153, v152, vcc
v_mov_b32 v153, 0
s_cmp_eq_u32 s50, 3
s_cbranch_scc0 L_clr
v_writelane_b32 v152, s58, 0
)"
R"(v_writelane_b32 v152, s59, 1
sa s58,s58,0x800
v_writelane_b32 v152, s58, 2
L_clr:
buffer_store_dword v153, v152, s[44:47], 0 offen sc1
v_lshrrev_b32 v156, 1, v213
v_lshlrev_b32 v156, 13, v156
v_and_b32 v153, 1, v213
v_lshl_add_u32 v156, v153, 7, v156
v_lshlrev_b32 v155, 7, v213
v_lshlrev_b32 v158, 7, v213
v_cmp_gt_u32 vcc, 16, v213
v_mov_b32 v152, 0x7ffffff0
s_nop 0
v_cndmask_b32 v158, v152, v158, vcc
v_and_b32 v152, 15, v213
)"
R"(v_lshlrev_b32 v128, 7, v152
v_lshrrev_b32 v152, 1, v152
v_lshrrev_b32 v153, 4, v213
v_xor_b32 v152, v152, v153
v_lshl_add_u32 v128, v152, 4, v128
v_xor_b32 v129, 64, v128
s_and_b32 s56, s50, 1
s_lshl_b32 s56, s56, 14
v_add_u32 v128, s56, v128
v_add_u32 v129, s56, v129
v_add_u32 v130, 0x10000, v128
v_add_u32 v131, 0x10000, v129
v_lshrrev_b32 v152, 4, v213
v_bfe_u32 v153, v213, 2, 2
v_lshl_add_u32 v154, v152, 3, v153
v_lshlrev_b32 v154, 8, v154
)"
R"(v_and_b32 v152, 1, v152
v_lshl_or_b32 v152, v152, 2, v153
v_lshlrev_b32 v152, 5, v152
v_and_b32 v153, 3, v213
v_lshl_add_u32 v154, v153, 3, v154
v_add_u32 v154, v154, v152
v_xor_b32 v132, 0, v154
v_xor_b32 v133, 32, v154
v_xor_b32 v134, 64, v154
v_xor_b32 v135, 96, v154
v_xor_b32 v136, 128, v154
v_xor_b32 v137, 160, v154
v_xor_b32 v138, 192, v154
v_xor_b32 v139, 224, v154
s_lshr_b32 s56, s50, 1
s_lshl_b32 s56, s56, 14
)"
R"(sa s56,s56,0x8000
v_add_u32 v132, s56, v132
v_add_u32 v140, 0x10000, v132
v_add_u32 v133, s56, v133
v_add_u32 v141, 0x10000, v133
v_add_u32 v134, s56, v134
v_add_u32 v142, 0x10000, v134
v_add_u32 v135, s56, v135
v_add_u32 v143, 0x10000, v135
v_add_u32 v136, s56, v136
v_add_u32 v144, 0x10000, v136
v_add_u32 v137, s56, v137
v_add_u32 v145, 0x10000, v137
v_add_u32 v138, s56, v138
v_add_u32 v146, 0x10000, v138
v_add_u32 v139, s56, v139
)"
R"(v_add_u32 v147, 0x10000, v139
v_lshlrev_b32 v183, 10, v213
v_cmp_gt_u32 vcc, 16, v213
v_mov_b32 v152, 0x7ffffff0
s_and_b32 s62, s48, 3
s_lshl_b32 s62, s62, 14
v_add_u32 v183, s62, v183
v_cndmask_b32 v183, v152, v183, vcc
v_cmp_gt_u32 vcc, 8, v213
s_lshl_b32 s62, s49, 15
v_add_u32 v184, s62, v156
v_cndmask_b32 v184, v152, v184, vcc
s_lshl_b32 s62, s50, 2
v_add_u32 v152, s62, v213
v_cmp_gt_u32 vcc, 4, v213
v_mov_b32 v153, 0x7ffffff0
)"
R"(s_and_b32 s62, s51, 0xf0
s_lshl_b32 s62, s62, 7
s_and_b32 s63, s51, 15
s_lshl_b32 s63, s63, 2
sa s62,s62,s63
v_lshl_add_u32 v178, v152, 7, s62
v_cndmask_b32 v178, v153, v178, vcc
s_and_b32 s62, s51, 15
s_lshl_b32 s62, s62, 7
s_lshr_b32 s63, s51, 4
s_lshl_b32 s63, s63, 2
sa s62,s62,s63
sa s62,s62,64
v_lshl_add_u32 v179, v152, 11, s62
v_cndmask_b32 v179, v153, v179, vcc
s_lshl_b32 s62, s51, 7
)"
R"(v_lshl_add_u32 v180, v152, 2, s62
v_cndmask_b32 v180, v153, v180, vcc
v_mov_b32 v181, 1
s_mov_b32 s61, 0x14000
sa m0,s54,0x10000
sa s56,s52,0x80
bl v148,s[36:39],s56 offen lds
sa s56,s52,0x1c80
bl v149,s[36:39],s56 offen offset:1024 lds
sa s56,s52,0x3880
bl v148,s[36:39],s56 offen offset:2048 lds
sa s56,s52,0x5480
bl v149,s[36:39],s56 offen offset:3072 lds
sa m0,s54,0x11000
sa s56,s52,0x8080
bl v148,s[36:39],s56 offen lds
)"
R"(sa s56,s52,0x9c80
bl v149,s[36:39],s56 offen offset:1024 lds
sa s56,s52,0xb880
bl v148,s[36:39],s56 offen offset:2048 lds
sa s56,s52,0xd480
bl v149,s[36:39],s56 offen offset:3072 lds
s_waitcnt vmcnt(8)
s_barrier
dt v[64:65],v132 offset:0
dt v[66:67],v132 offset:1024
dt v[68:69],v132 offset:8192
dt v[70:71],v132 offset:9216
dr v[0:3],v128 offset:0
dr v[4:7],v129 offset:0
dr v[8:11],v128 offset:2048
dr v[12:15],v129 offset:2048
)"
R"(dr v[16:19],v128 offset:4096
dr v[20:23],v129 offset:4096
dr v[24:27],v128 offset:6144
dr v[28:31],v129 offset:6144
dt v[72:73],v133 offset:0
dt v[74:75],v133 offset:1024
dt v[76:77],v133 offset:8192
dt v[78:79],v133 offset:9216
s_waitcnt lgkmcnt(4)
L_kv_start:
s_cmp_ge_u32 s50, 2
s_cbranch_scc1 L_kv_v
s_branch L_kv_k
L_tok_20:
s_mov_b32 s57, 0x8000
L_tokl_20:
)"
R"(buffer_load_dword v182, v180, s[44:47], s61 offen offset:0 sc1
s_waitcnt vmcnt(0)
v_cmp_eq_u32 vcc, 1, v182
s_nop 1
s_cmp_eq_u32 vcc_lo, 15
s_cbranch_scc1 L_tokd_20
s_sleep 1
s_sub_u32 s57, s57, 1
s_cmp_lg_u32 s57, 0
s_cbranch_scc1 L_tokl_20
s_branch L_tokd_20
L_tok_50:
s_mov_b32 s57, 0x8000
L_tokl_50:
buffer_load_dword v182, v180, s[44:47], s61 offen offset:64 sc1
s_waitcnt vmcnt(0)
)"
R"(v_cmp_eq_u32 vcc, 1, v182
s_nop 1
s_cmp_eq_u32 vcc_lo, 15
s_cbranch_scc1 L_tokd_50
s_sleep 1
s_sub_u32 s57, s57, 1
s_cmp_lg_u32 s57, 0
s_cbranch_scc1 L_tokl_50
s_branch L_tokd_50
L_wa0:
s_mov_b32 s62, 0x8000
L_pollwa0:
buffer_load_dword v199, v198, s[44:47], s81 offen sc1
s_waitcnt vmcnt(0)
v_cmp_eq_u32 vcc, 1, v199
s_nop 1
)"
R"(s_cmp_eq_u64 vcc, exec
s_cbranch_scc1 L_pollwa0_ok
s_sleep 1
s_sub_u32 s62, s62, 1
s_cmp_lg_u32 s62, 0
s_cbranch_scc1 L_pollwa0
L_pollwa0_ok:
s_branch L_wa0d
L_tok_21:
s_mov_b32 s57, 0x8000
L_tokl_21:
buffer_load_dword v182, v180, s[44:47], s61 offen offset:0 sc1
s_waitcnt vmcnt(0)
v_cmp_eq_u32 vcc, 1, v182
s_nop 1
s_cmp_eq_u32 vcc_lo, 15
)"
R"(s_cbranch_scc1 L_tokd_21
s_sleep 1
s_sub_u32 s57, s57, 1
s_cmp_lg_u32 s57, 0
s_cbranch_scc1 L_tokl_21
s_branch L_tokd_21
L_tok_51:
s_mov_b32 s57, 0x8000
L_tokl_51:
buffer_load_dword v182, v180, s[44:47], s61 offen offset:64 sc1
s_waitcnt vmcnt(0)
v_cmp_eq_u32 vcc, 1, v182
s_nop 1
s_cmp_eq_u32 vcc_lo, 15
s_cbranch_scc1 L_tokd_51
s_sleep 1
)"
R"(s_sub_u32 s57, s57, 1
s_cmp_lg_u32 s57, 0
s_cbranch_scc1 L_tokl_51
s_branch L_tokd_51
L_wa1:
s_mov_b32 s62, 0x8000
L_pollwa1:
buffer_load_dword v199, v198, s[44:47], s81 offen sc1
s_waitcnt vmcnt(0)
v_cmp_eq_u32 vcc, 1, v199
s_nop 1
s_cmp_eq_u64 vcc, exec
s_cbranch_scc1 L_pollwa1_ok
s_sleep 1
s_sub_u32 s62, s62, 1
s_cmp_lg_u32 s62, 0
)"
R"(s_cbranch_scc1 L_pollwa1
L_pollwa1_ok:
s_branch L_wa1d
.p2align 12
L_kv_k:
mm a[0:3],v[64:67],v[0:3],0
sa m0,s55,0x10000
dt v[80:81],v134 offset:0
mm a[0:3],v[68:71],v[4:7],a[0:3]
sa s56,s53,0x80000
bl v150,s[40:43],s56 offen nt lds
mm a[32:35],v[64:67],v[8:11],0
dt v[82:83],v134 offset:1024
mm a[32:35],v[68:71],v[12:15],a[32:35]
dt v[84:85],v134 offset:8192
mm a[64:67],v[64:67],v[16:19],0
)"
R"(sa s56,s53,0x87c00
bl v150,s[40:43],s56 offen offset:1024 nt lds
mm a[64:67],v[68:71],v[20:23],a[64:67]
dt v[86:87],v134 offset:9216
mm a[96:99],v[64:67],v[24:27],0
dt v[88:89],v135 offset:0
mm a[96:99],v[68:71],v[28:31],a[96:99]
s_waitcnt lgkmcnt(5)
sa s56,s53,0x8f800
bl v151,s[40:43],s56 offen offset:2048 nt lds
mm a[4:7],v[72:75],v[0:3],0
dt v[90:91],v135 offset:1024
mm a[4:7],v[76:79],v[4:7],a[4:7]
dt v[92:93],v135 offset:8192
mm a[36:39],v[72:75],v[8:11],0
sa s56,s53,0x97400
)"
R"(bl v151,s[40:43],s56 offen offset:3072 nt lds
mm a[36:39],v[76:79],v[12:15],a[36:39]
dt v[94:95],v135 offset:9216
mm a[68:71],v[72:75],v[16:19],0
sa m0,s55,0x11000
dt v[96:97],v136 offset:0
mm a[68:71],v[76:79],v[20:23],a[68:71]
sa s56,s53,0xa0000
bl v150,s[40:43],s56 offen nt lds
mm a[100:103],v[72:75],v[24:27],0
dt v[98:99],v136 offset:1024
mm a[100:103],v[76:79],v[28:31],a[100:103]
s_waitcnt lgkmcnt(6)
dt v[100:101],v136 offset:8192
mm a[8:11],v[80:83],v[0:3],0
sa s56,s53,0xa7c00
)"
R"(bl v150,s[40:43],s56 offen offset:1024 nt lds
mm a[8:11],v[84:87],v[4:7],a[8:11]
dt v[102:103],v136 offset:9216
mm a[40:43],v[80:83],v[8:11],0
dt v[104:105],v137 offset:0
mm a[40:43],v[84:87],v[12:15],a[40:43]
sa s56,s53,0xaf800
bl v151,s[40:43],s56 offen offset:2048 nt lds
mm a[72:75],v[80:83],v[16:19],0
dt v[106:107],v137 offset:1024
mm a[72:75],v[84:87],v[20:23],a[72:75]
dt v[108:109],v137 offset:8192
mm a[104:107],v[80:83],v[24:27],0
sa s56,s53,0xb7400
bl v151,s[40:43],s56 offen offset:3072 nt lds
mm a[104:107],v[84:87],v[28:31],a[104:107]
)"
R"(s_waitcnt lgkmcnt(7)
dt v[110:111],v137 offset:9216
mm a[12:15],v[88:91],v[0:3],0
dt v[112:113],v138 offset:0
mm a[12:15],v[92:95],v[4:7],a[12:15]
sa m0,s55,0x18000
dt v[114:115],v138 offset:1024
mm a[44:47],v[88:91],v[8:11],0
dt v[116:117],v138 offset:8192
mm a[44:47],v[92:95],v[12:15],a[44:47]
dt v[118:119],v138 offset:9216
mm a[76:79],v[88:91],v[16:19],0
dt v[120:121],v139 offset:0
mm a[76:79],v[92:95],v[20:23],a[76:79]
dt v[122:123],v139 offset:1024
mm a[108:111],v[88:91],v[24:27],0
)"
R"(dt v[124:125],v139 offset:8192
mm a[108:111],v[92:95],v[28:31],a[108:111]
s_waitcnt lgkmcnt(11)
dt v[126:127],v139 offset:9216
mm a[16:19],v[96:99],v[0:3],0
dr v[32:35],v128 offset:8192
mm a[16:19],v[100:103],v[4:7],a[16:19]
dr v[36:39],v129 offset:8192
mm a[48:51],v[96:99],v[8:11],0
sa s56,s53,0x100000
bl v150,s[40:43],s56 offen nt lds
mm a[48:51],v[100:103],v[12:15],a[48:51]
dr v[40:43],v128 offset:10240
mm a[80:83],v[96:99],v[16:19],0
sa s56,s53,0x107c00
bl v150,s[40:43],s56 offen offset:1024 nt lds
)"
R"(mm a[80:83],v[100:103],v[20:23],a[80:83]
dr v[44:47],v129 offset:10240
mm a[112:115],v[96:99],v[24:27],0
sa s56,s53,0x10f800
bl v151,s[40:43],s56 offen offset:2048 nt lds
mm a[112:115],v[100:103],v[28:31],a[112:115]
s_waitcnt lgkmcnt(12)
dr v[48:51],v128 offset:12288
mm a[20:23],v[104:107],v[0:3],0
sa s56,s53,0x117400
bl v151,s[40:43],s56 offen offset:3072 nt lds
mm a[20:23],v[108:111],v[4:7],a[20:23]
sa m0,s55,0x19000
dr v[52:55],v129 offset:12288
mm a[52:55],v[104:107],v[8:11],0
sa s56,s53,0x120000
)"
R"(bl v150,s[40:43],s56 offen nt lds
mm a[52:55],v[108:111],v[12:15],a[52:55]
dr v[56:59],v128 offset:14336
mm a[84:87],v[104:107],v[16:19],0
mm a[84:87],v[108:111],v[20:23],a[84:87]
dr v[60:63],v129 offset:14336
mm a[116:119],v[104:107],v[24:27],0
sa s56,s53,0x127c00
bl v150,s[40:43],s56 offen offset:1024 nt lds
mm a[116:119],v[108:111],v[28:31],a[116:119]
s_waitcnt lgkmcnt(12)
mm a[24:27],v[112:115],v[0:3],0
mm a[24:27],v[116:119],v[4:7],a[24:27]
mm a[56:59],v[112:115],v[8:11],0
sa s56,s53,0x12f800
bl v151,s[40:43],s56 offen offset:2048 nt lds
)"
R"(mm a[56:59],v[116:119],v[12:15],a[56:59]
mm a[88:91],v[112:115],v[16:19],0
mm a[88:91],v[116:119],v[20:23],a[88:91]
mm a[120:123],v[112:115],v[24:27],0
sa s56,s53,0x137400
bl v151,s[40:43],s56 offen offset:3072 nt lds
mm a[120:123],v[116:119],v[28:31],a[120:123]
s_waitcnt lgkmcnt(8)
mm a[28:31],v[120:123],v[0:3],0
mm a[28:31],v[124:127],v[4:7],a[28:31]
mm a[60:63],v[120:123],v[8:11],0
mm a[60:63],v[124:127],v[12:15],a[60:63]
mm a[92:95],v[120:123],v[16:19],0
mm a[92:95],v[124:127],v[20:23],a[92:95]
mm a[124:127],v[120:123],v[24:27],0
mm a[124:127],v[124:127],v[28:31],a[124:127]
)"
R"(s_waitcnt vmcnt(8) lgkmcnt(0)
s_barrier
mm a[128:131],v[64:67],v[32:35],0
dr v[0:3],v130 offset:0
mm a[128:131],v[68:71],v[36:39],a[128:131]
dr v[4:7],v131 offset:0
mm a[160:163],v[64:67],v[40:43],0
dr v[8:11],v130 offset:2048
mm a[160:163],v[68:71],v[44:47],a[160:163]
dr v[12:15],v131 offset:2048
mm a[192:195],v[64:67],v[48:51],0
dr v[16:19],v130 offset:4096
mm a[192:195],v[68:71],v[52:55],a[192:195]
dr v[20:23],v131 offset:4096
mm a[224:227],v[64:67],v[56:59],0
dr v[24:27],v130 offset:6144
)"
R"(mm a[224:227],v[68:71],v[60:63],a[224:227]
dt v[64:65],v140 offset:0
mm a[132:135],v[72:75],v[32:35],0
dt v[66:67],v140 offset:1024
mm a[132:135],v[76:79],v[36:39],a[132:135]
dt v[68:69],v140 offset:8192
mm a[164:167],v[72:75],v[40:43],0
dt v[70:71],v140 offset:9216
mm a[164:167],v[76:79],v[44:47],a[164:167]
dr v[28:31],v131 offset:6144
mm a[196:199],v[72:75],v[48:51],0
buffer_store_dword v181, v178, s[44:47], s61 offen sc1
mm a[196:199],v[76:79],v[52:55],a[196:199]
sa m0,s54,0x0
mm a[228:231],v[72:75],v[56:59],0
sa s56,s52,0x100
)"
R"(bl v148,s[36:39],s56 offen lds
mm a[228:231],v[76:79],v[60:63],a[228:231]
dt v[72:73],v141 offset:0
mm a[136:139],v[80:83],v[32:35],0
dt v[74:75],v141 offset:1024
mm a[136:139],v[84:87],v[36:39],a[136:139]
dt v[76:77],v141 offset:8192
mm a[168:171],v[80:83],v[40:43],0
dt v[78:79],v141 offset:9216
mm a[168:171],v[84:87],v[44:47],a[168:171]
sa s56,s52,0x1d00
bl v149,s[36:39],s56 offen offset:1024 lds
mm a[200:203],v[80:83],v[48:51],0
sa s56,s52,0x3900
bl v148,s[36:39],s56 offen offset:2048 lds
mm a[200:203],v[84:87],v[52:55],a[200:203]
)"
R"(mm a[232:235],v[80:83],v[56:59],0
sa s56,s52,0x5500
bl v149,s[36:39],s56 offen offset:3072 lds
mm a[232:235],v[84:87],v[60:63],a[232:235]
dt v[80:81],v142 offset:0
mm a[140:143],v[88:91],v[32:35],0
dt v[82:83],v142 offset:1024
mm a[140:143],v[92:95],v[36:39],a[140:143]
dt v[84:85],v142 offset:8192
mm a[172:175],v[88:91],v[40:43],0
dt v[86:87],v142 offset:9216
mm a[172:175],v[92:95],v[44:47],a[172:175]
sa m0,s54,0x1000
mm a[204:207],v[88:91],v[48:51],0
sa s56,s52,0x8100
bl v148,s[36:39],s56 offen lds
)"
R"(mm a[204:207],v[92:95],v[52:55],a[204:207]
mm a[236:239],v[88:91],v[56:59],0
sa s56,s52,0x9d00
bl v149,s[36:39],s56 offen offset:1024 lds
mm a[236:239],v[92:95],v[60:63],a[236:239]
dt v[88:89],v143 offset:0
mm a[144:147],v[96:99],v[32:35],0
dt v[90:91],v143 offset:1024
mm a[144:147],v[100:103],v[36:39],a[144:147]
dt v[92:93],v143 offset:8192
mm a[176:179],v[96:99],v[40:43],0
dt v[94:95],v143 offset:9216
mm a[176:179],v[100:103],v[44:47],a[176:179]
mm a[208:211],v[96:99],v[48:51],0
sa s56,s52,0xb900
bl v148,s[36:39],s56 offen offset:2048 lds
)"
R"(mm a[208:211],v[100:103],v[52:55],a[208:211]
mm a[240:243],v[96:99],v[56:59],0
sa s56,s52,0xd500
bl v149,s[36:39],s56 offen offset:3072 lds
mm a[240:243],v[100:103],v[60:63],a[240:243]
dt v[96:97],v144 offset:0
mm a[148:151],v[104:107],v[32:35],0
dt v[98:99],v144 offset:1024
mm a[148:151],v[108:111],v[36:39],a[148:151]
dt v[100:101],v144 offset:8192
mm a[180:183],v[104:107],v[40:43],0
dt v[102:103],v144 offset:9216
mm a[180:183],v[108:111],v[44:47],a[180:183]
mm a[212:215],v[104:107],v[48:51],0
mm a[212:215],v[108:111],v[52:55],a[212:215]
mm a[244:247],v[104:107],v[56:59],0
)"
R"(mm a[244:247],v[108:111],v[60:63],a[244:247]
sa m0,s55,0x0
dt v[104:105],v145 offset:0
mm a[152:155],v[112:115],v[32:35],0
dt v[106:107],v145 offset:1024
mm a[152:155],v[116:119],v[36:39],a[152:155]
dt v[108:109],v145 offset:8192
mm a[184:187],v[112:115],v[40:43],0
dt v[110:111],v145 offset:9216
mm a[184:187],v[116:119],v[44:47],a[184:187]
sa s56,s53,0x180000
bl v150,s[40:43],s56 offen nt lds
mm a[216:219],v[112:115],v[48:51],0
mm a[216:219],v[116:119],v[52:55],a[216:219]
mm a[248:251],v[112:115],v[56:59],0
sa s56,s53,0x187c00
)"
R"(bl v150,s[40:43],s56 offen offset:1024 nt lds
mm a[248:251],v[116:119],v[60:63],a[248:251]
dt v[112:113],v146 offset:0
mm a[156:159],v[120:123],v[32:35],0
dt v[114:115],v146 offset:1024
mm a[156:159],v[124:127],v[36:39],a[156:159]
dt v[116:117],v146 offset:8192
mm a[188:191],v[120:123],v[40:43],0
dt v[118:119],v146 offset:9216
mm a[188:191],v[124:127],v[44:47],a[188:191]
mm a[220:223],v[120:123],v[48:51],0
sa s56,s53,0x18f800
bl v151,s[40:43],s56 offen offset:2048 nt lds
mm a[220:223],v[124:127],v[52:55],a[220:223]
mm a[252:255],v[120:123],v[56:59],0
mm a[252:255],v[124:127],v[60:63],a[252:255]
)"
R"(s_waitcnt lgkmcnt(15)
dt v[120:121],v147 offset:0
mm a[0:3],v[64:67],v[0:3],a[0:3]
dt v[122:123],v147 offset:1024
mm a[0:3],v[68:71],v[4:7],a[0:3]
dt v[124:125],v147 offset:8192
mm a[32:35],v[64:67],v[8:11],a[32:35]
dt v[126:127],v147 offset:9216
mm a[32:35],v[68:71],v[12:15],a[32:35]
sa s56,s53,0x197400
bl v151,s[40:43],s56 offen offset:3072 nt lds
mm a[64:67],v[64:67],v[16:19],a[64:67]
mm a[64:67],v[68:71],v[20:23],a[64:67]
mm a[96:99],v[64:67],v[24:27],a[96:99]
dr v[32:35],v130 offset:8192
mm a[96:99],v[68:71],v[28:31],a[96:99]
)"
R"(s_waitcnt lgkmcnt(15)
sa m0,s55,0x1000
mm a[4:7],v[72:75],v[0:3],a[4:7]
sa s56,s53,0x1a0000
bl v150,s[40:43],s56 offen nt lds
mm a[4:7],v[76:79],v[4:7],a[4:7]
mm a[36:39],v[72:75],v[8:11],a[36:39]
mm a[36:39],v[76:79],v[12:15],a[36:39]
dr v[36:39],v131 offset:8192
mm a[68:71],v[72:75],v[16:19],a[68:71]
mm a[68:71],v[76:79],v[20:23],a[68:71]
mm a[100:103],v[72:75],v[24:27],a[100:103]
sa s56,s53,0x1a7c00
bl v150,s[40:43],s56 offen offset:1024 nt lds
mm a[100:103],v[76:79],v[28:31],a[100:103]
s_waitcnt lgkmcnt(15)
)"
R"(mm a[8:11],v[80:83],v[0:3],a[8:11]
dr v[40:43],v130 offset:10240
mm a[8:11],v[84:87],v[4:7],a[8:11]
mm a[40:43],v[80:83],v[8:11],a[40:43]
mm a[40:43],v[84:87],v[12:15],a[40:43]
mm a[72:75],v[80:83],v[16:19],a[72:75]
sa s56,s53,0x1af800
bl v151,s[40:43],s56 offen offset:2048 nt lds
mm a[72:75],v[84:87],v[20:23],a[72:75]
dr v[44:47],v131 offset:10240
mm a[104:107],v[80:83],v[24:27],a[104:107]
mm a[104:107],v[84:87],v[28:31],a[104:107]
s_waitcnt lgkmcnt(15)
mm a[12:15],v[88:91],v[0:3],a[12:15]
mm a[12:15],v[92:95],v[4:7],a[12:15]
mm a[44:47],v[88:91],v[8:11],a[44:47]
)"
R"(dr v[48:51],v130 offset:12288
mm a[44:47],v[92:95],v[12:15],a[44:47]
sa s56,s53,0x1b7400
bl v151,s[40:43],s56 offen offset:3072 nt lds
mm a[76:79],v[88:91],v[16:19],a[76:79]
mm a[76:79],v[92:95],v[20:23],a[76:79]
mm a[108:111],v[88:91],v[24:27],a[108:111]
mm a[108:111],v[92:95],v[28:31],a[108:111]
s_waitcnt lgkmcnt(15)
dr v[52:55],v131 offset:12288
mm a[16:19],v[96:99],v[0:3],a[16:19]
mm a[16:19],v[100:103],v[4:7],a[16:19]
mm a[48:51],v[96:99],v[8:11],a[48:51]
mm a[48:51],v[100:103],v[12:15],a[48:51]
mm a[80:83],v[96:99],v[16:19],a[80:83]
dr v[56:59],v130 offset:14336
)"
R"(mm a[80:83],v[100:103],v[20:23],a[80:83]
mm a[112:115],v[96:99],v[24:27],a[112:115]
mm a[112:115],v[100:103],v[28:31],a[112:115]
s_waitcnt lgkmcnt(15)
mm a[20:23],v[104:107],v[0:3],a[20:23]
mm a[20:23],v[108:111],v[4:7],a[20:23]
dr v[60:63],v131 offset:14336
mm a[52:55],v[104:107],v[8:11],a[52:55]
mm a[52:55],v[108:111],v[12:15],a[52:55]
mm a[84:87],v[104:107],v[16:19],a[84:87]
mm a[84:87],v[108:111],v[20:23],a[84:87]
mm a[116:119],v[104:107],v[24:27],a[116:119]
mm a[116:119],v[108:111],v[28:31],a[116:119]
s_waitcnt lgkmcnt(12)
mm a[24:27],v[112:115],v[0:3],a[24:27]
mm a[24:27],v[116:119],v[4:7],a[24:27]
)"
R"(mm a[56:59],v[112:115],v[8:11],a[56:59]
mm a[56:59],v[116:119],v[12:15],a[56:59]
mm a[88:91],v[112:115],v[16:19],a[88:91]
mm a[88:91],v[116:119],v[20:23],a[88:91]
mm a[120:123],v[112:115],v[24:27],a[120:123]
mm a[120:123],v[116:119],v[28:31],a[120:123]
s_waitcnt lgkmcnt(8)
mm a[28:31],v[120:123],v[0:3],a[28:31]
mm a[28:31],v[124:127],v[4:7],a[28:31]
mm a[60:63],v[120:123],v[8:11],a[60:63]
mm a[60:63],v[124:127],v[12:15],a[60:63]
mm a[92:95],v[120:123],v[16:19],a[92:95]
mm a[92:95],v[124:127],v[20:23],a[92:95]
mm a[124:127],v[120:123],v[24:27],a[124:127]
mm a[124:127],v[124:127],v[28:31],a[124:127]
s_waitcnt vmcnt(8) lgkmcnt(0)
)"
R"(s_barrier
mm a[128:131],v[64:67],v[32:35],a[128:131]
dr v[0:3],v128 offset:0
mm a[128:131],v[68:71],v[36:39],a[128:131]
dr v[4:7],v129 offset:0
mm a[160:163],v[64:67],v[40:43],a[160:163]
s_mov_b32 s88, s66
dr v[8:11],v128 offset:2048
mm a[160:163],v[68:71],v[44:47],a[160:163]
dr v[12:15],v129 offset:2048
mm a[192:195],v[64:67],v[48:51],a[192:195]
s_and_b32 s89, s67, 0xffff
dr v[16:19],v128 offset:4096
mm a[192:195],v[68:71],v[52:55],a[192:195]
dr v[20:23],v129 offset:4096
mm a[224:227],v[64:67],v[56:59],a[224:227]
)"
R"(s_mov_b32 s90, 0x180000
dr v[24:27],v128 offset:6144
mm a[224:227],v[68:71],v[60:63],a[224:227]
dt v[64:65],v140 offset:32768
mm a[132:135],v[72:75],v[32:35],a[132:135]
dt v[66:67],v140 offset:33792
mm a[132:135],v[76:79],v[36:39],a[132:135]
s_mov_b32 s91, 0x20000
dt v[68:69],v140 offset:40960
mm a[164:167],v[72:75],v[40:43],a[164:167]
dt v[70:71],v140 offset:41984
mm a[164:167],v[76:79],v[44:47],a[164:167]
s_lshl_b32 s80, s49, 2
dr v[28:31],v129 offset:6144
mm a[196:199],v[72:75],v[48:51],a[196:199]
buffer_load_dword v182, v180, s[44:47], s61 offen offset:0 sc1
)"
R"(mm a[196:199],v[76:79],v[52:55],a[196:199]
sa m0,s54,0x10000
mm a[228:231],v[72:75],v[56:59],a[228:231]
sa s56,s52,0x180
bl v148,s[36:39],s56 offen lds
mm a[228:231],v[76:79],v[60:63],a[228:231]
sa s80,s80,s50
dt v[72:73],v141 offset:32768
mm a[136:139],v[80:83],v[32:35],a[136:139]
s_mul_i32 s80, s80, 49152
dt v[74:75],v141 offset:33792
mm a[136:139],v[84:87],v[36:39],a[136:139]
dt v[76:77],v141 offset:40960
mm a[168:171],v[80:83],v[40:43],a[168:171]
dt v[78:79],v141 offset:41984
mm a[168:171],v[84:87],v[44:47],a[168:171]
)"
R"(sa s56,s52,0x1d80
bl v149,s[36:39],s56 offen offset:1024 lds
mm a[200:203],v[80:83],v[48:51],a[200:203]
sa s56,s52,0x3980
bl v148,s[36:39],s56 offen offset:2048 lds
mm a[200:203],v[84:87],v[52:55],a[200:203]
s_lshl_b32 s81, s48, 5
mm a[232:235],v[80:83],v[56:59],a[232:235]
sa s56,s52,0x5580
bl v149,s[36:39],s56 offen offset:3072 lds
mm a[232:235],v[84:87],v[60:63],a[232:235]
sa s80,s80,s81
dt v[80:81],v142 offset:32768
mm a[140:143],v[88:91],v[32:35],a[140:143]
sa s81,s80,24576
dt v[82:83],v142 offset:33792
)"
R"(mm a[140:143],v[92:95],v[36:39],a[140:143]
dt v[84:85],v142 offset:40960
mm a[172:175],v[88:91],v[40:43],a[172:175]
dt v[86:87],v142 offset:41984
v_bfe_u32 v241, v213, 4, 1
mm a[172:175],v[92:95],v[44:47],a[172:175]
sa m0,s54,0x11000
mm a[204:207],v[88:91],v[48:51],a[204:207]
sa s56,s52,0x8180
bl v148,s[36:39],s56 offen lds
mm a[204:207],v[92:95],v[52:55],a[204:207]
v_lshl_or_b32 v241, v241, 1, 0
mm a[236:239],v[88:91],v[56:59],a[236:239]
sa s56,s52,0x9d80
bl v149,s[36:39],s56 offen offset:1024 lds
mm a[236:239],v[92:95],v[60:63],a[236:239]
)"
R"(dt v[88:89],v143 offset:32768
v_and_b32 v242, 7, v213
mm a[144:147],v[96:99],v[32:35],a[144:147]
dt v[90:91],v143 offset:33792
mm a[144:147],v[100:103],v[36:39],a[144:147]
dt v[92:93],v143 offset:40960
mm a[176:179],v[96:99],v[40:43],a[176:179]
dt v[94:95],v143 offset:41984
v_xor_b32 v242, v242, v241
mm a[176:179],v[100:103],v[44:47],a[176:179]
mm a[208:211],v[96:99],v[48:51],a[208:211]
sa s56,s52,0xb980
bl v148,s[36:39],s56 offen offset:2048 lds
mm a[208:211],v[100:103],v[52:55],a[208:211]
v_lshrrev_b32 v241, 1, v242
mm a[240:243],v[96:99],v[56:59],a[240:243]
)"
R"(sa s56,s52,0xd580
bl v149,s[36:39],s56 offen offset:3072 lds
mm a[240:243],v[100:103],v[60:63],a[240:243]
dt v[96:97],v144 offset:32768
v_min_u32 v241, 2, v241
mm a[148:151],v[104:107],v[32:35],a[148:151]
dt v[98:99],v144 offset:33792
mm a[148:151],v[108:111],v[36:39],a[148:151]
dt v[100:101],v144 offset:40960
v_lshlrev_b32 v241, 10, v241
mm a[180:183],v[104:107],v[40:43],a[180:183]
dt v[102:103],v144 offset:41984
mm a[180:183],v[108:111],v[44:47],a[180:183]
mm a[212:215],v[104:107],v[48:51],a[212:215]
v_and_b32 v242, 1, v242
mm a[212:215],v[108:111],v[52:55],a[212:215]
)"
R"(mm a[244:247],v[104:107],v[56:59],a[244:247]
v_lshl_add_u32 v241, v242, 4, v241
mm a[244:247],v[108:111],v[60:63],a[244:247]
sa m0,s55,0x10000
dt v[104:105],v145 offset:32768
mm a[152:155],v[112:115],v[32:35],a[152:155]
dt v[106:107],v145 offset:33792
v_lshrrev_b32 v242, 3, v213
mm a[152:155],v[116:119],v[36:39],a[152:155]
dt v[108:109],v145 offset:40960
mm a[184:187],v[112:115],v[40:43],a[184:187]
dt v[110:111],v145 offset:41984
mm a[184:187],v[116:119],v[44:47],a[184:187]
sa s56,s53,0x200000
bl v150,s[40:43],s56 offen nt lds
mm a[216:219],v[112:115],v[48:51],a[216:219]
)"
R"(v_mul_u32_u24 v242, 3072, v242
mm a[216:219],v[116:119],v[52:55],a[216:219]
v_add_u32 v239, v241, v242
mm a[248:251],v[112:115],v[56:59],a[248:251]
sa s56,s53,0x207c00
bl v150,s[40:43],s56 offen offset:1024 nt lds
mm a[248:251],v[116:119],v[60:63],a[248:251]
dt v[112:113],v146 offset:32768
mm a[156:159],v[120:123],v[32:35],a[156:159]
dt v[114:115],v146 offset:33792
v_bfe_u32 v241, v213, 4, 1
mm a[156:159],v[124:127],v[36:39],a[156:159]
s_lshl_b32 s62, s51, 2
dt v[116:117],v146 offset:40960
mm a[188:191],v[120:123],v[40:43],a[188:191]
dt v[118:119],v146 offset:41984
)"
R"(v_lshl_or_b32 v241, v241, 1, 4
mm a[188:191],v[124:127],v[44:47],a[188:191]
mm a[220:223],v[120:123],v[48:51],a[220:223]
sa s56,s53,0x20f800
bl v151,s[40:43],s56 offen offset:2048 nt lds
mm a[220:223],v[124:127],v[52:55],a[220:223]
sa s62,s62,s50
v_and_b32 v242, 7, v213
mm a[252:255],v[120:123],v[56:59],a[252:255]
mm a[252:255],v[124:127],v[60:63],a[252:255]
s_waitcnt lgkmcnt(15)
dt v[120:121],v147 offset:32768
v_xor_b32 v242, v242, v241
mm a[0:3],v[64:67],v[0:3],a[0:3]
dt v[122:123],v147 offset:33792
mm a[0:3],v[68:71],v[4:7],a[0:3]
)"
R"(s_mul_i32 s62, s62, 2048
dt v[124:125],v147 offset:40960
mm a[32:35],v[64:67],v[8:11],a[32:35]
dt v[126:127],v147 offset:41984
v_lshrrev_b32 v241, 1, v242
mm a[32:35],v[68:71],v[12:15],a[32:35]
sa s56,s53,0x217400
bl v151,s[40:43],s56 offen offset:3072 nt lds
mm a[64:67],v[64:67],v[16:19],a[64:67]
v_min_u32 v241, 2, v241
mm a[64:67],v[68:71],v[20:23],a[64:67]
sa s92,s64,s62
s_addc_u32 s93, s65, 0
mm a[96:99],v[64:67],v[24:27],a[96:99]
dr v[32:35],v128 offset:8192
mm a[96:99],v[68:71],v[28:31],a[96:99]
)"
R"(s_waitcnt lgkmcnt(15)
sa m0,s55,0x11000
v_lshlrev_b32 v241, 10, v241
mm a[4:7],v[72:75],v[0:3],a[4:7]
sa s56,s53,0x220000
bl v150,s[40:43],s56 offen nt lds
mm a[4:7],v[76:79],v[4:7],a[4:7]
v_and_b32 v242, 1, v242
mm a[36:39],v[72:75],v[8:11],a[36:39]
s_and_b32 s93, s93, 0xffff
mm a[36:39],v[76:79],v[12:15],a[36:39]
dr v[36:39],v129 offset:8192
mm a[68:71],v[72:75],v[16:19],a[68:71]
v_lshl_add_u32 v241, v242, 4, v241
mm a[68:71],v[76:79],v[20:23],a[68:71]
mm a[100:103],v[72:75],v[24:27],a[100:103]
)"
R"(sa s56,s53,0x227c00
bl v150,s[40:43],s56 offen offset:1024 nt lds
mm a[100:103],v[76:79],v[28:31],a[100:103]
s_waitcnt lgkmcnt(15)
s_mov_b32 s94, 2048
v_lshrrev_b32 v242, 3, v213
mm a[8:11],v[80:83],v[0:3],a[8:11]
dr v[40:43],v128 offset:10240
mm a[8:11],v[84:87],v[4:7],a[8:11]
v_mul_u32_u24 v242, 3072, v242
mm a[40:43],v[80:83],v[8:11],a[40:43]
s_mov_b32 s95, 0x20000
mm a[40:43],v[84:87],v[12:15],a[40:43]
v_add_u32 v240, v241, v242
mm a[72:75],v[80:83],v[16:19],a[72:75]
sa s56,s53,0x22f800
)"
R"(bl v151,s[40:43],s56 offen offset:2048 nt lds
mm a[72:75],v[84:87],v[20:23],a[72:75]
dr v[44:47],v129 offset:10240
mm a[104:107],v[80:83],v[24:27],a[104:107]
mm a[104:107],v[84:87],v[28:31],a[104:107]
s_waitcnt lgkmcnt(15)
mm a[12:15],v[88:91],v[0:3],a[12:15]
mm a[12:15],v[92:95],v[4:7],a[12:15]
mm a[44:47],v[88:91],v[8:11],a[44:47]
dr v[48:51],v128 offset:12288
mm a[44:47],v[92:95],v[12:15],a[44:47]
sa s56,s53,0x237400
bl v151,s[40:43],s56 offen offset:3072 nt lds
mm a[76:79],v[88:91],v[16:19],a[76:79]
mm a[76:79],v[92:95],v[20:23],a[76:79]
mm a[108:111],v[88:91],v[24:27],a[108:111]
)"
R"(mm a[108:111],v[92:95],v[28:31],a[108:111]
s_waitcnt lgkmcnt(15)
dr v[52:55],v129 offset:12288
mm a[16:19],v[96:99],v[0:3],a[16:19]
mm a[16:19],v[100:103],v[4:7],a[16:19]
bl v[190:193],v239,s[88:91],s80 offen
mm a[48:51],v[96:99],v[8:11],a[48:51]
mm a[48:51],v[100:103],v[12:15],a[48:51]
mm a[80:83],v[96:99],v[16:19],a[80:83]
dr v[56:59],v128 offset:14336
mm a[80:83],v[100:103],v[20:23],a[80:83]
mm a[112:115],v[96:99],v[24:27],a[112:115]
mm a[112:115],v[100:103],v[28:31],a[112:115]
s_waitcnt lgkmcnt(15)
bl v[194:197],v240,s[88:91],s81 offen
mm a[20:23],v[104:107],v[0:3],a[20:23]
)"
R"(s_mov_b32 s63, 0
buffer_load_dword v167, v158, s[92:95], s63 offen
mm a[20:23],v[108:111],v[4:7],a[20:23]
dr v[60:63],v129 offset:14336
mm a[52:55],v[104:107],v[8:11],a[52:55]
mm a[52:55],v[108:111],v[12:15],a[52:55]
mm a[84:87],v[104:107],v[16:19],a[84:87]
mm a[84:87],v[108:111],v[20:23],a[84:87]
mm a[116:119],v[104:107],v[24:27],a[116:119]
mm a[116:119],v[108:111],v[28:31],a[116:119]
s_waitcnt lgkmcnt(12)
mm a[24:27],v[112:115],v[0:3],a[24:27]
mm a[24:27],v[116:119],v[4:7],a[24:27]
mm a[56:59],v[112:115],v[8:11],a[56:59]
mm a[56:59],v[116:119],v[12:15],a[56:59]
mm a[88:91],v[112:115],v[16:19],a[88:91]
)"
R"(mm a[88:91],v[116:119],v[20:23],a[88:91]
mm a[120:123],v[112:115],v[24:27],a[120:123]
mm a[120:123],v[116:119],v[28:31],a[120:123]
s_waitcnt lgkmcnt(8)
mm a[28:31],v[120:123],v[0:3],a[28:31]
mm a[28:31],v[124:127],v[4:7],a[28:31]
mm a[60:63],v[120:123],v[8:11],a[60:63]
mm a[60:63],v[124:127],v[12:15],a[60:63]
mm a[92:95],v[120:123],v[16:19],a[92:95]
mm a[92:95],v[124:127],v[20:23],a[92:95]
mm a[124:127],v[120:123],v[24:27],a[124:127]
mm a[124:127],v[124:127],v[28:31],a[124:127]
s_waitcnt vmcnt(11) lgkmcnt(0)
s_barrier
mm a[128:131],v[64:67],v[32:35],a[128:131]
dr v[0:3],v130 offset:0
)"
R"(mm a[128:131],v[68:71],v[36:39],a[128:131]
v_cmp_eq_u32 vcc, 1, v182
s_nop 1
s_cmp_eq_u32 vcc_lo, 15
s_cbranch_scc0 L_tok_20
L_tokd_20:
dr v[4:7],v131 offset:0
mm a[160:163],v[64:67],v[40:43],a[160:163]
dr v[8:11],v130 offset:2048
mm a[160:163],v[68:71],v[44:47],a[160:163]
dr v[12:15],v131 offset:2048
mm a[192:195],v[64:67],v[48:51],a[192:195]
dr v[16:19],v130 offset:4096
mm a[192:195],v[68:71],v[52:55],a[192:195]
dr v[20:23],v131 offset:4096
mm a[224:227],v[64:67],v[56:59],a[224:227]
)"
R"(dr v[24:27],v130 offset:6144
mm a[224:227],v[68:71],v[60:63],a[224:227]
dt v[64:65],v132 offset:0
mm a[132:135],v[72:75],v[32:35],a[132:135]
dt v[66:67],v132 offset:1024
mm a[132:135],v[76:79],v[36:39],a[132:135]
dt v[68:69],v132 offset:8192
mm a[164:167],v[72:75],v[40:43],a[164:167]
dt v[70:71],v132 offset:9216
mm a[164:167],v[76:79],v[44:47],a[164:167]
dr v[28:31],v131 offset:6144
mm a[196:199],v[72:75],v[48:51],a[196:199]
buffer_store_dword v181, v178, s[44:47], s61 offen sc1
mm a[196:199],v[76:79],v[52:55],a[196:199]
sa m0,s54,0x0
mm a[228:231],v[72:75],v[56:59],a[228:231]
)"
R"(sa s56,s52,0x200
bl v148,s[36:39],s56 offen lds
mm a[228:231],v[76:79],v[60:63],a[228:231]
dt v[72:73],v133 offset:0
mm a[136:139],v[80:83],v[32:35],a[136:139]
dt v[74:75],v133 offset:1024
mm a[136:139],v[84:87],v[36:39],a[136:139]
dt v[76:77],v133 offset:8192
mm a[168:171],v[80:83],v[40:43],a[168:171]
dt v[78:79],v133 offset:9216
mm a[168:171],v[84:87],v[44:47],a[168:171]
sa s56,s52,0x1e00
bl v149,s[36:39],s56 offen offset:1024 lds
mm a[200:203],v[80:83],v[48:51],a[200:203]
sa s56,s52,0x3a00
bl v148,s[36:39],s56 offen offset:2048 lds
)"
R"(mm a[200:203],v[84:87],v[52:55],a[200:203]
mm a[232:235],v[80:83],v[56:59],a[232:235]
sa s56,s52,0x5600
bl v149,s[36:39],s56 offen offset:3072 lds
mm a[232:235],v[84:87],v[60:63],a[232:235]
dt v[80:81],v134 offset:0
mm a[140:143],v[88:91],v[32:35],a[140:143]
dt v[82:83],v134 offset:1024
mm a[140:143],v[92:95],v[36:39],a[140:143]
dt v[84:85],v134 offset:8192
mm a[172:175],v[88:91],v[40:43],a[172:175]
dt v[86:87],v134 offset:9216
mm a[172:175],v[92:95],v[44:47],a[172:175]
sa m0,s54,0x1000
mm a[204:207],v[88:91],v[48:51],a[204:207]
sa s56,s52,0x8200
)"
R"(bl v148,s[36:39],s56 offen lds
mm a[204:207],v[92:95],v[52:55],a[204:207]
mm a[236:239],v[88:91],v[56:59],a[236:239]
sa s56,s52,0x9e00
bl v149,s[36:39],s56 offen offset:1024 lds
mm a[236:239],v[92:95],v[60:63],a[236:239]
dt v[88:89],v135 offset:0
mm a[144:147],v[96:99],v[32:35],a[144:147]
dt v[90:91],v135 offset:1024
mm a[144:147],v[100:103],v[36:39],a[144:147]
dt v[92:93],v135 offset:8192
mm a[176:179],v[96:99],v[40:43],a[176:179]
dt v[94:95],v135 offset:9216
mm a[176:179],v[100:103],v[44:47],a[176:179]
mm a[208:211],v[96:99],v[48:51],a[208:211]
sa s56,s52,0xba00
)"
R"(bl v148,s[36:39],s56 offen offset:2048 lds
mm a[208:211],v[100:103],v[52:55],a[208:211]
mm a[240:243],v[96:99],v[56:59],a[240:243]
sa s56,s52,0xd600
bl v149,s[36:39],s56 offen offset:3072 lds
mm a[240:243],v[100:103],v[60:63],a[240:243]
dt v[96:97],v136 offset:0
mm a[148:151],v[104:107],v[32:35],a[148:151]
dt v[98:99],v136 offset:1024
mm a[148:151],v[108:111],v[36:39],a[148:151]
dt v[100:101],v136 offset:8192
mm a[180:183],v[104:107],v[40:43],a[180:183]
dt v[102:103],v136 offset:9216
mm a[180:183],v[108:111],v[44:47],a[180:183]
mm a[212:215],v[104:107],v[48:51],a[212:215]
mm a[212:215],v[108:111],v[52:55],a[212:215]
)"
R"(mm a[244:247],v[104:107],v[56:59],a[244:247]
mm a[244:247],v[108:111],v[60:63],a[244:247]
sa m0,s55,0x18000
dt v[104:105],v137 offset:0
mm a[152:155],v[112:115],v[32:35],a[152:155]
dt v[106:107],v137 offset:1024
mm a[152:155],v[116:119],v[36:39],a[152:155]
dt v[108:109],v137 offset:8192
mm a[184:187],v[112:115],v[40:43],a[184:187]
dt v[110:111],v137 offset:9216
mm a[184:187],v[116:119],v[44:47],a[184:187]
sa s56,s53,0x280000
bl v150,s[40:43],s56 offen nt lds
mm a[216:219],v[112:115],v[48:51],a[216:219]
mm a[216:219],v[116:119],v[52:55],a[216:219]
mm a[248:251],v[112:115],v[56:59],a[248:251]
)"
R"(sa s56,s53,0x287c00
bl v150,s[40:43],s56 offen offset:1024 nt lds
mm a[248:251],v[116:119],v[60:63],a[248:251]
dt v[112:113],v138 offset:0
mm a[156:159],v[120:123],v[32:35],a[156:159]
dt v[114:115],v138 offset:1024
mm a[156:159],v[124:127],v[36:39],a[156:159]
s_lshl_b32 s62, s51, 2
dt v[116:117],v138 offset:8192
mm a[188:191],v[120:123],v[40:43],a[188:191]
dt v[118:119],v138 offset:9216
mm a[188:191],v[124:127],v[44:47],a[188:191]
mm a[220:223],v[120:123],v[48:51],a[220:223]
sa s56,s53,0x28f800
bl v151,s[40:43],s56 offen offset:2048 nt lds
mm a[220:223],v[124:127],v[52:55],a[220:223]
)"
R"(sa s62,s62,s50
mm a[252:255],v[120:123],v[56:59],a[252:255]
mm a[252:255],v[124:127],v[60:63],a[252:255]
s_waitcnt lgkmcnt(15)
dt v[120:121],v139 offset:0
mm a[0:3],v[64:67],v[0:3],a[0:3]
dt v[122:123],v139 offset:1024
mm a[0:3],v[68:71],v[4:7],a[0:3]
s_mul_i32 s62, s62, 2048
dt v[124:125],v139 offset:8192
mm a[32:35],v[64:67],v[8:11],a[32:35]
dt v[126:127],v139 offset:9216
mm a[32:35],v[68:71],v[12:15],a[32:35]
sa s56,s53,0x297400
bl v151,s[40:43],s56 offen offset:3072 nt lds
mm a[64:67],v[64:67],v[16:19],a[64:67]
)"
R"(mm a[64:67],v[68:71],v[20:23],a[64:67]
sa s92,s72,s62
s_addc_u32 s93, s73, 0
mm a[96:99],v[64:67],v[24:27],a[96:99]
dr v[32:35],v130 offset:8192
mm a[96:99],v[68:71],v[28:31],a[96:99]
s_waitcnt lgkmcnt(15)
sa m0,s55,0x19000
mm a[4:7],v[72:75],v[0:3],a[4:7]
sa s56,s53,0x2a0000
bl v150,s[40:43],s56 offen nt lds
mm a[4:7],v[76:79],v[4:7],a[4:7]
mm a[36:39],v[72:75],v[8:11],a[36:39]
s_and_b32 s93, s93, 0xffff
s_lshl_b32 s59, s48, 16
mm a[36:39],v[76:79],v[12:15],a[36:39]
)"
R"(dr v[36:39],v131 offset:8192
mm a[68:71],v[72:75],v[16:19],a[68:71]
mm a[68:71],v[76:79],v[20:23],a[68:71]
mm a[100:103],v[72:75],v[24:27],a[100:103]
sa s56,s53,0x2a7c00
bl v150,s[40:43],s56 offen offset:1024 nt lds
mm a[100:103],v[76:79],v[28:31],a[100:103]
s_waitcnt lgkmcnt(15)
s_mov_b32 s94, 2048
mm a[8:11],v[80:83],v[0:3],a[8:11]
s_lshl_b32 s80, s50, 11
dr v[40:43],v130 offset:10240
mm a[8:11],v[84:87],v[4:7],a[8:11]
mm a[40:43],v[80:83],v[8:11],a[40:43]
s_mov_b32 s95, 0x20000
mm a[40:43],v[84:87],v[12:15],a[40:43]
)"
R"(mm a[72:75],v[80:83],v[16:19],a[72:75]
sa s56,s53,0x2af800
bl v151,s[40:43],s56 offen offset:2048 nt lds
mm a[72:75],v[84:87],v[20:23],a[72:75]
dr v[44:47],v131 offset:10240
mm a[104:107],v[80:83],v[24:27],a[104:107]
mm a[104:107],v[84:87],v[28:31],a[104:107]
s_waitcnt lgkmcnt(15)
sa s59,s59,s80
mm a[12:15],v[88:91],v[0:3],a[12:15]
mm a[12:15],v[92:95],v[4:7],a[12:15]
mm a[44:47],v[88:91],v[8:11],a[44:47]
dr v[48:51],v130 offset:12288
mm a[44:47],v[92:95],v[12:15],a[44:47]
sa s56,s53,0x2b7400
bl v151,s[40:43],s56 offen offset:3072 nt lds
)"
R"(mm a[76:79],v[88:91],v[16:19],a[76:79]
mm a[76:79],v[92:95],v[20:23],a[76:79]
mm a[108:111],v[88:91],v[24:27],a[108:111]
sa s59,s59,0x3c00000
mm a[108:111],v[92:95],v[28:31],a[108:111]
s_waitcnt lgkmcnt(15)
dr v[52:55],v131 offset:12288
mm a[16:19],v[96:99],v[0:3],a[16:19]
mm a[16:19],v[100:103],v[4:7],a[16:19]
s_mov_b32 s63, 0
buffer_load_dword v175, v158, s[92:95], s63 offen
mm a[48:51],v[96:99],v[8:11],a[48:51]
mm a[48:51],v[100:103],v[12:15],a[48:51]
mm a[80:83],v[96:99],v[16:19],a[80:83]
s_lshl_b32 s80, s49, 13
dr v[56:59],v130 offset:14336
)"
R"(mm a[80:83],v[100:103],v[20:23],a[80:83]
mm a[112:115],v[96:99],v[24:27],a[112:115]
mm a[112:115],v[100:103],v[28:31],a[112:115]
s_waitcnt lgkmcnt(15)
mm a[20:23],v[104:107],v[0:3],a[20:23]
mm a[20:23],v[108:111],v[4:7],a[20:23]
dr v[60:63],v131 offset:14336
mm a[52:55],v[104:107],v[8:11],a[52:55]
mm a[52:55],v[108:111],v[12:15],a[52:55]
sa s80,s80,s59
mm a[84:87],v[104:107],v[16:19],a[84:87]
mm a[84:87],v[108:111],v[20:23],a[84:87]
mm a[116:119],v[104:107],v[24:27],a[116:119]
mm a[116:119],v[108:111],v[28:31],a[116:119]
s_waitcnt lgkmcnt(12)
mm a[24:27],v[112:115],v[0:3],a[24:27]
)"
R"(mm a[24:27],v[116:119],v[4:7],a[24:27]
mm a[56:59],v[112:115],v[8:11],a[56:59]
mm a[56:59],v[116:119],v[12:15],a[56:59]
mm a[88:91],v[112:115],v[16:19],a[88:91]
mm a[88:91],v[116:119],v[20:23],a[88:91]
mm a[120:123],v[112:115],v[24:27],a[120:123]
mm a[120:123],v[116:119],v[28:31],a[120:123]
s_waitcnt lgkmcnt(8)
mm a[28:31],v[120:123],v[0:3],a[28:31]
mm a[28:31],v[124:127],v[4:7],a[28:31]
mm a[60:63],v[120:123],v[8:11],a[60:63]
mm a[60:63],v[124:127],v[12:15],a[60:63]
mm a[92:95],v[120:123],v[16:19],a[92:95]
mm a[92:95],v[124:127],v[20:23],a[92:95]
mm a[124:127],v[120:123],v[24:27],a[124:127]
mm a[124:127],v[124:127],v[28:31],a[124:127]
)"
R"(s_waitcnt vmcnt(9) lgkmcnt(0)
s_barrier
mm a[128:131],v[64:67],v[32:35],a[128:131]
dr v[0:3],v128 offset:0
mm a[128:131],v[68:71],v[36:39],a[128:131]
dr v[4:7],v129 offset:0
mm a[160:163],v[64:67],v[40:43],a[160:163]
dr v[8:11],v128 offset:2048
mm a[160:163],v[68:71],v[44:47],a[160:163]
dr v[12:15],v129 offset:2048
mm a[192:195],v[64:67],v[48:51],a[192:195]
dr v[16:19],v128 offset:4096
mm a[192:195],v[68:71],v[52:55],a[192:195]
dr v[20:23],v129 offset:4096
mm a[224:227],v[64:67],v[56:59],a[224:227]
dr v[24:27],v128 offset:6144
)"
R"(mm a[224:227],v[68:71],v[60:63],a[224:227]
dt v[64:65],v140 offset:0
mm a[132:135],v[72:75],v[32:35],a[132:135]
dt v[66:67],v140 offset:1024
mm a[132:135],v[76:79],v[36:39],a[132:135]
dt v[68:69],v140 offset:8192
mm a[164:167],v[72:75],v[40:43],a[164:167]
dt v[70:71],v140 offset:9216
mm a[164:167],v[76:79],v[44:47],a[164:167]
dr v[28:31],v129 offset:6144
mm a[196:199],v[72:75],v[48:51],a[196:199]
buffer_store_dword v181, v179, s[44:47], s61 offen sc1
mm a[196:199],v[76:79],v[52:55],a[196:199]
buffer_store_dwordx4 v[190:193], v214, s[44:47], s80 offen offset:0 sc1
mm a[228:231],v[72:75],v[56:59],a[228:231]
buffer_store_dwordx4 v[194:197], v214, s[44:47], s80 offen offset:1024 sc1
)"
R"(mm a[228:231],v[76:79],v[60:63],a[228:231]
sa m0,s54,0x10000
dt v[72:73],v141 offset:0
mm a[136:139],v[80:83],v[32:35],a[136:139]
dt v[74:75],v141 offset:1024
mm a[136:139],v[84:87],v[36:39],a[136:139]
dt v[76:77],v141 offset:8192
mm a[168:171],v[80:83],v[40:43],a[168:171]
dt v[78:79],v141 offset:9216
mm a[168:171],v[84:87],v[44:47],a[168:171]
sa s56,s52,0x280
bl v148,s[36:39],s56 offen lds
mm a[200:203],v[80:83],v[48:51],a[200:203]
sa s56,s52,0x1e80
bl v149,s[36:39],s56 offen offset:1024 lds
mm a[200:203],v[84:87],v[52:55],a[200:203]
)"
R"(sa s56,s52,0x3a80
bl v148,s[36:39],s56 offen offset:2048 lds
mm a[232:235],v[80:83],v[56:59],a[232:235]
sa s56,s52,0x5680
bl v149,s[36:39],s56 offen offset:3072 lds
mm a[232:235],v[84:87],v[60:63],a[232:235]
dt v[80:81],v142 offset:0
mm a[140:143],v[88:91],v[32:35],a[140:143]
dt v[82:83],v142 offset:1024
mm a[140:143],v[92:95],v[36:39],a[140:143]
dt v[84:85],v142 offset:8192
mm a[172:175],v[88:91],v[40:43],a[172:175]
s_lshl_b32 s81, s48, 6
dt v[86:87],v142 offset:9216
mm a[172:175],v[92:95],v[44:47],a[172:175]
sa m0,s54,0x11000
)"
R"(mm a[204:207],v[88:91],v[48:51],a[204:207]
sa s56,s52,0x8280
bl v148,s[36:39],s56 offen lds
mm a[204:207],v[92:95],v[52:55],a[204:207]
mm a[236:239],v[88:91],v[56:59],a[236:239]
sa s56,s52,0x9e80
bl v149,s[36:39],s56 offen offset:1024 lds
mm a[236:239],v[92:95],v[60:63],a[236:239]
dt v[88:89],v143 offset:0
mm a[144:147],v[96:99],v[32:35],a[144:147]
dt v[90:91],v143 offset:1024
mm a[144:147],v[100:103],v[36:39],a[144:147]
dt v[92:93],v143 offset:8192
mm a[176:179],v[96:99],v[40:43],a[176:179]
sa s81,s81,0x10800
dt v[94:95],v143 offset:9216
)"
R"(mm a[176:179],v[100:103],v[44:47],a[176:179]
mm a[208:211],v[96:99],v[48:51],a[208:211]
sa s56,s52,0xba80
bl v148,s[36:39],s56 offen offset:2048 lds
mm a[208:211],v[100:103],v[52:55],a[208:211]
mm a[240:243],v[96:99],v[56:59],a[240:243]
sa s56,s52,0xd680
bl v149,s[36:39],s56 offen offset:3072 lds
mm a[240:243],v[100:103],v[60:63],a[240:243]
dt v[96:97],v144 offset:0
mm a[148:151],v[104:107],v[32:35],a[148:151]
dt v[98:99],v144 offset:1024
mm a[148:151],v[108:111],v[36:39],a[148:151]
dt v[100:101],v144 offset:8192
mm a[180:183],v[104:107],v[40:43],a[180:183]
dt v[102:103],v144 offset:9216
)"
R"(mm a[180:183],v[108:111],v[44:47],a[180:183]
s_lshl_b32 s79, s49, 2
mm a[212:215],v[104:107],v[48:51],a[212:215]
mm a[212:215],v[108:111],v[52:55],a[212:215]
mm a[244:247],v[104:107],v[56:59],a[244:247]
mm a[244:247],v[108:111],v[60:63],a[244:247]
sa m0,s55,0x0
dt v[104:105],v145 offset:0
mm a[152:155],v[112:115],v[32:35],a[152:155]
dt v[106:107],v145 offset:1024
mm a[152:155],v[116:119],v[36:39],a[152:155]
dt v[108:109],v145 offset:8192
mm a[184:187],v[112:115],v[40:43],a[184:187]
dt v[110:111],v145 offset:9216
mm a[184:187],v[116:119],v[44:47],a[184:187]
sa s56,s53,0x300000
)"
R"(bl v150,s[40:43],s56 offen nt lds
mm a[216:219],v[112:115],v[48:51],a[216:219]
sa s79,s79,s81
mm a[216:219],v[116:119],v[52:55],a[216:219]
mm a[248:251],v[112:115],v[56:59],a[248:251]
sa s56,s53,0x307c00
bl v150,s[40:43],s56 offen offset:1024 nt lds
mm a[248:251],v[116:119],v[60:63],a[248:251]
dt v[112:113],v146 offset:0
mm a[156:159],v[120:123],v[32:35],a[156:159]
dt v[114:115],v146 offset:1024
mm a[156:159],v[124:127],v[36:39],a[156:159]
s_lshl_b32 s62, s51, 2
dt v[116:117],v146 offset:8192
mm a[188:191],v[120:123],v[40:43],a[188:191]
dt v[118:119],v146 offset:9216
)"
R"(mm a[188:191],v[124:127],v[44:47],a[188:191]
mm a[220:223],v[120:123],v[48:51],a[220:223]
sa s56,s53,0x30f800
bl v151,s[40:43],s56 offen offset:2048 nt lds
mm a[220:223],v[124:127],v[52:55],a[220:223]
s_cmp_eq_u32 s50, 0
s_cselect_b32 s79, s79, 0x4900000
sa s62,s62,s50
mm a[252:255],v[120:123],v[56:59],a[252:255]
mm a[252:255],v[124:127],v[60:63],a[252:255]
s_waitcnt lgkmcnt(15)
dt v[120:121],v147 offset:0
mm a[0:3],v[64:67],v[0:3],a[0:3]
dt v[122:123],v147 offset:1024
mm a[0:3],v[68:71],v[4:7],a[0:3]
s_mul_i32 s62, s62, 4096
)"
R"(dt v[124:125],v147 offset:8192
mm a[32:35],v[64:67],v[8:11],a[32:35]
dt v[126:127],v147 offset:9216
mm a[32:35],v[68:71],v[12:15],a[32:35]
sa s56,s53,0x317400
bl v151,s[40:43],s56 offen offset:3072 nt lds
mm a[64:67],v[64:67],v[16:19],a[64:67]
mm a[64:67],v[68:71],v[20:23],a[64:67]
sa s92,s76,s62
s_addc_u32 s93, s77, 0
mm a[96:99],v[64:67],v[24:27],a[96:99]
dr v[32:35],v128 offset:8192
v_and_b32 v198, 7, v213
mm a[96:99],v[68:71],v[28:31],a[96:99]
s_waitcnt lgkmcnt(15)
sa m0,s55,0x1000
)"
R"(mm a[4:7],v[72:75],v[0:3],a[4:7]
sa s56,s53,0x320000
bl v150,s[40:43],s56 offen nt lds
mm a[4:7],v[76:79],v[4:7],a[4:7]
mm a[36:39],v[72:75],v[8:11],a[36:39]
s_and_b32 s93, s93, 0xffff
mm a[36:39],v[76:79],v[12:15],a[36:39]
dr v[36:39],v129 offset:8192
mm a[68:71],v[72:75],v[16:19],a[68:71]
mm a[68:71],v[76:79],v[20:23],a[68:71]
mm a[100:103],v[72:75],v[24:27],a[100:103]
sa s56,s53,0x327c00
bl v150,s[40:43],s56 offen offset:1024 nt lds
mm a[100:103],v[76:79],v[28:31],a[100:103]
s_waitcnt lgkmcnt(15)
s_mov_b32 s94, 4096
)"
R"(v_lshlrev_b32 v198, 2, v198
mm a[8:11],v[80:83],v[0:3],a[8:11]
dr v[40:43],v128 offset:10240
mm a[8:11],v[84:87],v[4:7],a[8:11]
mm a[40:43],v[80:83],v[8:11],a[40:43]
s_mov_b32 s95, 0x20000
mm a[40:43],v[84:87],v[12:15],a[40:43]
mm a[72:75],v[80:83],v[16:19],a[72:75]
sa s56,s53,0x32f800
bl v151,s[40:43],s56 offen offset:2048 nt lds
mm a[72:75],v[84:87],v[20:23],a[72:75]
dr v[44:47],v129 offset:10240
mm a[104:107],v[80:83],v[24:27],a[104:107]
mm a[104:107],v[84:87],v[28:31],a[104:107]
s_waitcnt lgkmcnt(15)
mm a[12:15],v[88:91],v[0:3],a[12:15]
)"
R"(v_mov_b32 v199, 0
mm a[12:15],v[92:95],v[4:7],a[12:15]
mm a[44:47],v[88:91],v[8:11],a[44:47]
dr v[48:51],v128 offset:12288
mm a[44:47],v[92:95],v[12:15],a[44:47]
sa s56,s53,0x337400
bl v151,s[40:43],s56 offen offset:3072 nt lds
mm a[76:79],v[88:91],v[16:19],a[76:79]
mm a[76:79],v[92:95],v[20:23],a[76:79]
mm a[108:111],v[88:91],v[24:27],a[108:111]
mm a[108:111],v[92:95],v[28:31],a[108:111]
s_waitcnt lgkmcnt(15)
dr v[52:55],v129 offset:12288
mm a[16:19],v[96:99],v[0:3],a[16:19]
mm a[16:19],v[100:103],v[4:7],a[16:19]
s_mov_b32 s63, 0
)"
R"(buffer_load_dword v170, v158, s[92:95], s63 offen
mm a[48:51],v[96:99],v[8:11],a[48:51]
mm a[48:51],v[100:103],v[12:15],a[48:51]
mm a[80:83],v[96:99],v[16:19],a[80:83]
dr v[56:59],v128 offset:14336
mm a[80:83],v[100:103],v[20:23],a[80:83]
mm a[112:115],v[96:99],v[24:27],a[112:115]
mm a[112:115],v[100:103],v[28:31],a[112:115]
s_waitcnt lgkmcnt(15)
mm a[20:23],v[104:107],v[0:3],a[20:23]
mm a[20:23],v[108:111],v[4:7],a[20:23]
dr v[60:63],v129 offset:14336
mm a[52:55],v[104:107],v[8:11],a[52:55]
mm a[52:55],v[108:111],v[12:15],a[52:55]
mm a[84:87],v[104:107],v[16:19],a[84:87]
mm a[84:87],v[108:111],v[20:23],a[84:87]
)"
R"(mm a[116:119],v[104:107],v[24:27],a[116:119]
mm a[116:119],v[108:111],v[28:31],a[116:119]
s_waitcnt lgkmcnt(12)
mm a[24:27],v[112:115],v[0:3],a[24:27]
mm a[24:27],v[116:119],v[4:7],a[24:27]
mm a[56:59],v[112:115],v[8:11],a[56:59]
mm a[56:59],v[116:119],v[12:15],a[56:59]
mm a[88:91],v[112:115],v[16:19],a[88:91]
mm a[88:91],v[116:119],v[20:23],a[88:91]
mm a[120:123],v[112:115],v[24:27],a[120:123]
mm a[120:123],v[116:119],v[28:31],a[120:123]
s_waitcnt lgkmcnt(8)
mm a[28:31],v[120:123],v[0:3],a[28:31]
mm a[28:31],v[124:127],v[4:7],a[28:31]
mm a[60:63],v[120:123],v[8:11],a[60:63]
mm a[60:63],v[124:127],v[12:15],a[60:63]
)"
R"(mm a[92:95],v[120:123],v[16:19],a[92:95]
mm a[92:95],v[124:127],v[20:23],a[92:95]
mm a[124:127],v[120:123],v[24:27],a[124:127]
mm a[124:127],v[124:127],v[28:31],a[124:127]
s_waitcnt vmcnt(9) lgkmcnt(0)
s_barrier
mm a[128:131],v[64:67],v[32:35],a[128:131]
dr v[0:3],v130 offset:0
mm a[128:131],v[68:71],v[36:39],a[128:131]
dr v[4:7],v131 offset:0
mm a[160:163],v[64:67],v[40:43],a[160:163]
dr v[8:11],v130 offset:2048
mm a[160:163],v[68:71],v[44:47],a[160:163]
dr v[12:15],v131 offset:2048
mm a[192:195],v[64:67],v[48:51],a[192:195]
dr v[16:19],v130 offset:4096
)"
R"(mm a[192:195],v[68:71],v[52:55],a[192:195]
dr v[20:23],v131 offset:4096
mm a[224:227],v[64:67],v[56:59],a[224:227]
dr v[24:27],v130 offset:6144
mm a[224:227],v[68:71],v[60:63],a[224:227]
dt v[64:65],v140 offset:32768
mm a[132:135],v[72:75],v[32:35],a[132:135]
dt v[66:67],v140 offset:33792
mm a[132:135],v[76:79],v[36:39],a[132:135]
dt v[68:69],v140 offset:40960
mm a[164:167],v[72:75],v[40:43],a[164:167]
dt v[70:71],v140 offset:41984
mm a[164:167],v[76:79],v[44:47],a[164:167]
dr v[28:31],v131 offset:6144
mm a[196:199],v[72:75],v[48:51],a[196:199]
buffer_load_dword v182, v180, s[44:47], s61 offen offset:64 sc1
)"
R"(mm a[196:199],v[76:79],v[52:55],a[196:199]
buffer_store_dword v181, v199, s[44:47], s79 offen sc1
mm a[228:231],v[72:75],v[56:59],a[228:231]
sa m0,s54,0x0
mm a[228:231],v[76:79],v[60:63],a[228:231]
dt v[72:73],v141 offset:32768
mm a[136:139],v[80:83],v[32:35],a[136:139]
dt v[74:75],v141 offset:33792
mm a[136:139],v[84:87],v[36:39],a[136:139]
dt v[76:77],v141 offset:40960
mm a[168:171],v[80:83],v[40:43],a[168:171]
dt v[78:79],v141 offset:41984
mm a[168:171],v[84:87],v[44:47],a[168:171]
sa s56,s52,0x300
bl v148,s[36:39],s56 offen lds
mm a[200:203],v[80:83],v[48:51],a[200:203]
)"
R"(sa s56,s52,0x1f00
bl v149,s[36:39],s56 offen offset:1024 lds
mm a[200:203],v[84:87],v[52:55],a[200:203]
sa s56,s52,0x3b00
bl v148,s[36:39],s56 offen offset:2048 lds
mm a[232:235],v[80:83],v[56:59],a[232:235]
sa s56,s52,0x5700
bl v149,s[36:39],s56 offen offset:3072 lds
mm a[232:235],v[84:87],v[60:63],a[232:235]
dt v[80:81],v142 offset:32768
mm a[140:143],v[88:91],v[32:35],a[140:143]
dt v[82:83],v142 offset:33792
mm a[140:143],v[92:95],v[36:39],a[140:143]
dt v[84:85],v142 offset:40960
mm a[172:175],v[88:91],v[40:43],a[172:175]
dt v[86:87],v142 offset:41984
)"
R"(mm a[172:175],v[92:95],v[44:47],a[172:175]
sa m0,s54,0x1000
mm a[204:207],v[88:91],v[48:51],a[204:207]
sa s56,s52,0x8300
bl v148,s[36:39],s56 offen lds
mm a[204:207],v[92:95],v[52:55],a[204:207]
mm a[236:239],v[88:91],v[56:59],a[236:239]
sa s56,s52,0x9f00
bl v149,s[36:39],s56 offen offset:1024 lds
mm a[236:239],v[92:95],v[60:63],a[236:239]
dt v[88:89],v143 offset:32768
mm a[144:147],v[96:99],v[32:35],a[144:147]
dt v[90:91],v143 offset:33792
mm a[144:147],v[100:103],v[36:39],a[144:147]
dt v[92:93],v143 offset:40960
mm a[176:179],v[96:99],v[40:43],a[176:179]
)"
R"(dt v[94:95],v143 offset:41984
mm a[176:179],v[100:103],v[44:47],a[176:179]
mm a[208:211],v[96:99],v[48:51],a[208:211]
sa s56,s52,0xbb00
bl v148,s[36:39],s56 offen offset:2048 lds
mm a[208:211],v[100:103],v[52:55],a[208:211]
mm a[240:243],v[96:99],v[56:59],a[240:243]
sa s56,s52,0xd700
bl v149,s[36:39],s56 offen offset:3072 lds
mm a[240:243],v[100:103],v[60:63],a[240:243]
dt v[96:97],v144 offset:32768
mm a[148:151],v[104:107],v[32:35],a[148:151]
dt v[98:99],v144 offset:33792
mm a[148:151],v[108:111],v[36:39],a[148:151]
dt v[100:101],v144 offset:40960
mm a[180:183],v[104:107],v[40:43],a[180:183]
)"
R"(dt v[102:103],v144 offset:41984
mm a[180:183],v[108:111],v[44:47],a[180:183]
mm a[212:215],v[104:107],v[48:51],a[212:215]
mm a[212:215],v[108:111],v[52:55],a[212:215]
mm a[244:247],v[104:107],v[56:59],a[244:247]
mm a[244:247],v[108:111],v[60:63],a[244:247]
sa m0,s55,0x10000
dt v[104:105],v145 offset:32768
mm a[152:155],v[112:115],v[32:35],a[152:155]
dt v[106:107],v145 offset:33792
mm a[152:155],v[116:119],v[36:39],a[152:155]
dt v[108:109],v145 offset:40960
mm a[184:187],v[112:115],v[40:43],a[184:187]
dt v[110:111],v145 offset:41984
mm a[184:187],v[116:119],v[44:47],a[184:187]
sa s56,s53,0x380000
)"
R"(bl v150,s[40:43],s56 offen nt lds
mm a[216:219],v[112:115],v[48:51],a[216:219]
mm a[216:219],v[116:119],v[52:55],a[216:219]
mm a[248:251],v[112:115],v[56:59],a[248:251]
sa s56,s53,0x387c00
bl v150,s[40:43],s56 offen offset:1024 nt lds
mm a[248:251],v[116:119],v[60:63],a[248:251]
dt v[112:113],v146 offset:32768
mm a[156:159],v[120:123],v[32:35],a[156:159]
dt v[114:115],v146 offset:33792
mm a[156:159],v[124:127],v[36:39],a[156:159]
dt v[116:117],v146 offset:40960
mm a[188:191],v[120:123],v[40:43],a[188:191]
dt v[118:119],v146 offset:41984
mm a[188:191],v[124:127],v[44:47],a[188:191]
mm a[220:223],v[120:123],v[48:51],a[220:223]
)"
R"(sa s56,s53,0x38f800
bl v151,s[40:43],s56 offen offset:2048 nt lds
mm a[220:223],v[124:127],v[52:55],a[220:223]
mm a[252:255],v[120:123],v[56:59],a[252:255]
mm a[252:255],v[124:127],v[60:63],a[252:255]
s_waitcnt lgkmcnt(15)
dt v[120:121],v147 offset:32768
mm a[0:3],v[64:67],v[0:3],a[0:3]
dt v[122:123],v147 offset:33792
mm a[0:3],v[68:71],v[4:7],a[0:3]
dt v[124:125],v147 offset:40960
mm a[32:35],v[64:67],v[8:11],a[32:35]
dt v[126:127],v147 offset:41984
mm a[32:35],v[68:71],v[12:15],a[32:35]
sa s56,s53,0x397400
bl v151,s[40:43],s56 offen offset:3072 nt lds
)"
R"(mm a[64:67],v[64:67],v[16:19],a[64:67]
s_getpc_b64 s[88:89]
L_ct_kv0:
sa s88,s88,(L_kv_done-L_ct_kv0)
s_addc_u32 s89, s89, 0
mm a[64:67],v[68:71],v[20:23],a[64:67]
mm a[96:99],v[64:67],v[24:27],a[96:99]
dr v[32:35],v130 offset:8192
mm a[96:99],v[68:71],v[28:31],a[96:99]
s_waitcnt lgkmcnt(15)
sa m0,s55,0x11000
mm a[4:7],v[72:75],v[0:3],a[4:7]
sa s56,s53,0x3a0000
bl v150,s[40:43],s56 offen nt lds
mm a[4:7],v[76:79],v[4:7],a[4:7]
s_and_b32 s89, s89, 0xffff
)"
R"(mm a[36:39],v[72:75],v[8:11],a[36:39]
mm a[36:39],v[76:79],v[12:15],a[36:39]
dr v[36:39],v131 offset:8192
mm a[68:71],v[72:75],v[16:19],a[68:71]
mm a[68:71],v[76:79],v[20:23],a[68:71]
mm a[100:103],v[72:75],v[24:27],a[100:103]
sa s56,s53,0x3a7c00
bl v150,s[40:43],s56 offen offset:1024 nt lds
mm a[100:103],v[76:79],v[28:31],a[100:103]
s_waitcnt lgkmcnt(15)
mm a[8:11],v[80:83],v[0:3],a[8:11]
s_mov_b32 s90, (L_att_base-L_kv_done)
dr v[40:43],v130 offset:10240
mm a[8:11],v[84:87],v[4:7],a[8:11]
mm a[40:43],v[80:83],v[8:11],a[40:43]
mm a[40:43],v[84:87],v[12:15],a[40:43]
)"
R"(mm a[72:75],v[80:83],v[16:19],a[72:75]
sa s56,s53,0x3af800
bl v151,s[40:43],s56 offen offset:2048 nt lds
mm a[72:75],v[84:87],v[20:23],a[72:75]
dr v[44:47],v131 offset:10240
mm a[104:107],v[80:83],v[24:27],a[104:107]
s_mov_b32 s91, 0x20000
mm a[104:107],v[84:87],v[28:31],a[104:107]
s_waitcnt lgkmcnt(15)
mm a[12:15],v[88:91],v[0:3],a[12:15]
mm a[12:15],v[92:95],v[4:7],a[12:15]
mm a[44:47],v[88:91],v[8:11],a[44:47]
dr v[48:51],v130 offset:12288
mm a[44:47],v[92:95],v[12:15],a[44:47]
sa s56,s53,0x3b7400
bl v151,s[40:43],s56 offen offset:3072 nt lds
)"
R"(mm a[76:79],v[88:91],v[16:19],a[76:79]
mm a[76:79],v[92:95],v[20:23],a[76:79]
mm a[108:111],v[88:91],v[24:27],a[108:111]
mm a[108:111],v[92:95],v[28:31],a[108:111]
s_waitcnt lgkmcnt(15)
dr v[52:55],v131 offset:12288
mm a[16:19],v[96:99],v[0:3],a[16:19]
mm a[16:19],v[100:103],v[4:7],a[16:19]
buffer_load_dword v157, v215, s[88:91], 0 offen
mm a[48:51],v[96:99],v[8:11],a[48:51]
mm a[48:51],v[100:103],v[12:15],a[48:51]
mm a[80:83],v[96:99],v[16:19],a[80:83]
dr v[56:59],v130 offset:14336
mm a[80:83],v[100:103],v[20:23],a[80:83]
mm a[112:115],v[96:99],v[24:27],a[112:115]
mm a[112:115],v[100:103],v[28:31],a[112:115]
)"
R"(s_waitcnt lgkmcnt(15)
mm a[20:23],v[104:107],v[0:3],a[20:23]
s_mov_b32 s63, 2048
buffer_load_dword v171, v158, s[92:95], s63 offen
mm a[20:23],v[108:111],v[4:7],a[20:23]
dr v[60:63],v131 offset:14336
mm a[52:55],v[104:107],v[8:11],a[52:55]
mm a[52:55],v[108:111],v[12:15],a[52:55]
mm a[84:87],v[104:107],v[16:19],a[84:87]
mm a[84:87],v[108:111],v[20:23],a[84:87]
mm a[116:119],v[104:107],v[24:27],a[116:119]
mm a[116:119],v[108:111],v[28:31],a[116:119]
s_waitcnt lgkmcnt(12)
mm a[24:27],v[112:115],v[0:3],a[24:27]
mm a[24:27],v[116:119],v[4:7],a[24:27]
mm a[56:59],v[112:115],v[8:11],a[56:59]
)"
R"(mm a[56:59],v[116:119],v[12:15],a[56:59]
mm a[88:91],v[112:115],v[16:19],a[88:91]
mm a[88:91],v[116:119],v[20:23],a[88:91]
mm a[120:123],v[112:115],v[24:27],a[120:123]
mm a[120:123],v[116:119],v[28:31],a[120:123]
s_waitcnt lgkmcnt(8)
mm a[28:31],v[120:123],v[0:3],a[28:31]
mm a[28:31],v[124:127],v[4:7],a[28:31]
mm a[60:63],v[120:123],v[8:11],a[60:63]
mm a[60:63],v[124:127],v[12:15],a[60:63]
mm a[92:95],v[120:123],v[16:19],a[92:95]
mm a[92:95],v[124:127],v[20:23],a[92:95]
mm a[124:127],v[120:123],v[24:27],a[124:127]
mm a[124:127],v[124:127],v[28:31],a[124:127]
s_waitcnt vmcnt(10) lgkmcnt(0)
s_barrier
)"
R"(mm a[128:131],v[64:67],v[32:35],a[128:131]
dr v[0:3],v128 offset:0
mm a[128:131],v[68:71],v[36:39],a[128:131]
v_cmp_eq_u32 vcc, 1, v182
s_nop 1
s_cmp_eq_u32 vcc_lo, 15
s_cbranch_scc0 L_tok_50
L_tokd_50:
dr v[4:7],v129 offset:0
mm a[160:163],v[64:67],v[40:43],a[160:163]
dr v[8:11],v128 offset:2048
mm a[160:163],v[68:71],v[44:47],a[160:163]
dr v[12:15],v129 offset:2048
mm a[192:195],v[64:67],v[48:51],a[192:195]
dr v[16:19],v128 offset:4096
mm a[192:195],v[68:71],v[52:55],a[192:195]
)"
R"(dr v[20:23],v129 offset:4096
mm a[224:227],v[64:67],v[56:59],a[224:227]
dr v[24:27],v128 offset:6144
mm a[224:227],v[68:71],v[60:63],a[224:227]
dt v[64:65],v132 offset:0
mm a[132:135],v[72:75],v[32:35],a[132:135]
dt v[66:67],v132 offset:1024
mm a[132:135],v[76:79],v[36:39],a[132:135]
dt v[68:69],v132 offset:8192
mm a[164:167],v[72:75],v[40:43],a[164:167]
dt v[70:71],v132 offset:9216
mm a[164:167],v[76:79],v[44:47],a[164:167]
dr v[28:31],v129 offset:6144
mm a[196:199],v[72:75],v[48:51],a[196:199]
buffer_store_dword v181, v179, s[44:47], s61 offen sc1
mm a[196:199],v[76:79],v[52:55],a[196:199]
)"
R"(sa m0,s54,0x10000
mm a[228:231],v[72:75],v[56:59],a[228:231]
sa s56,s52,0x380
bl v148,s[36:39],s56 offen lds
mm a[228:231],v[76:79],v[60:63],a[228:231]
dt v[72:73],v133 offset:0
mm a[136:139],v[80:83],v[32:35],a[136:139]
dt v[74:75],v133 offset:1024
mm a[136:139],v[84:87],v[36:39],a[136:139]
dt v[76:77],v133 offset:8192
mm a[168:171],v[80:83],v[40:43],a[168:171]
dt v[78:79],v133 offset:9216
mm a[168:171],v[84:87],v[44:47],a[168:171]
sa s56,s52,0x1f80
bl v149,s[36:39],s56 offen offset:1024 lds
mm a[200:203],v[80:83],v[48:51],a[200:203]
)"
R"(sa s56,s52,0x3b80
bl v148,s[36:39],s56 offen offset:2048 lds
mm a[200:203],v[84:87],v[52:55],a[200:203]
mm a[232:235],v[80:83],v[56:59],a[232:235]
sa s56,s52,0x5780
bl v149,s[36:39],s56 offen offset:3072 lds
mm a[232:235],v[84:87],v[60:63],a[232:235]
dt v[80:81],v134 offset:0
mm a[140:143],v[88:91],v[32:35],a[140:143]
dt v[82:83],v134 offset:1024
mm a[140:143],v[92:95],v[36:39],a[140:143]
dt v[84:85],v134 offset:8192
mm a[172:175],v[88:91],v[40:43],a[172:175]
dt v[86:87],v134 offset:9216
mm a[172:175],v[92:95],v[44:47],a[172:175]
sa m0,s54,0x11000
)"
R"(mm a[204:207],v[88:91],v[48:51],a[204:207]
sa s56,s52,0x8380
bl v148,s[36:39],s56 offen lds
mm a[204:207],v[92:95],v[52:55],a[204:207]
mm a[236:239],v[88:91],v[56:59],a[236:239]
sa s56,s52,0x9f80
bl v149,s[36:39],s56 offen offset:1024 lds
mm a[236:239],v[92:95],v[60:63],a[236:239]
dt v[88:89],v135 offset:0
mm a[144:147],v[96:99],v[32:35],a[144:147]
dt v[90:91],v135 offset:1024
mm a[144:147],v[100:103],v[36:39],a[144:147]
dt v[92:93],v135 offset:8192
mm a[176:179],v[96:99],v[40:43],a[176:179]
dt v[94:95],v135 offset:9216
mm a[176:179],v[100:103],v[44:47],a[176:179]
)"
R"(mm a[208:211],v[96:99],v[48:51],a[208:211]
sa s56,s52,0xbb80
bl v148,s[36:39],s56 offen offset:2048 lds
mm a[208:211],v[100:103],v[52:55],a[208:211]
mm a[240:243],v[96:99],v[56:59],a[240:243]
sa s56,s52,0xd780
bl v149,s[36:39],s56 offen offset:3072 lds
mm a[240:243],v[100:103],v[60:63],a[240:243]
dt v[96:97],v136 offset:0
mm a[148:151],v[104:107],v[32:35],a[148:151]
dt v[98:99],v136 offset:1024
mm a[148:151],v[108:111],v[36:39],a[148:151]
dt v[100:101],v136 offset:8192
mm a[180:183],v[104:107],v[40:43],a[180:183]
dt v[102:103],v136 offset:9216
mm a[180:183],v[108:111],v[44:47],a[180:183]
)"
R"(mm a[212:215],v[104:107],v[48:51],a[212:215]
mm a[212:215],v[108:111],v[52:55],a[212:215]
mm a[244:247],v[104:107],v[56:59],a[244:247]
mm a[244:247],v[108:111],v[60:63],a[244:247]
dt v[104:105],v137 offset:0
mm a[152:155],v[112:115],v[32:35],a[152:155]
dt v[106:107],v137 offset:1024
mm a[152:155],v[116:119],v[36:39],a[152:155]
dt v[108:109],v137 offset:8192
mm a[184:187],v[112:115],v[40:43],a[184:187]
dt v[110:111],v137 offset:9216
mm a[184:187],v[116:119],v[44:47],a[184:187]
mm a[216:219],v[112:115],v[48:51],a[216:219]
mm a[216:219],v[116:119],v[52:55],a[216:219]
mm a[248:251],v[112:115],v[56:59],a[248:251]
mm a[248:251],v[116:119],v[60:63],a[248:251]
)"
R"(dt v[112:113],v138 offset:0
mm a[156:159],v[120:123],v[32:35],a[156:159]
dt v[114:115],v138 offset:1024
mm a[156:159],v[124:127],v[36:39],a[156:159]
dt v[116:117],v138 offset:8192
mm a[188:191],v[120:123],v[40:43],a[188:191]
dt v[118:119],v138 offset:9216
mm a[188:191],v[124:127],v[44:47],a[188:191]
mm a[220:223],v[120:123],v[48:51],a[220:223]
mm a[220:223],v[124:127],v[52:55],a[220:223]
mm a[252:255],v[120:123],v[56:59],a[252:255]
mm a[252:255],v[124:127],v[60:63],a[252:255]
s_waitcnt lgkmcnt(15)
dt v[120:121],v139 offset:0
mm a[0:3],v[64:67],v[0:3],a[0:3]
dt v[122:123],v139 offset:1024
)"
R"(mm a[0:3],v[68:71],v[4:7],a[0:3]
dt v[124:125],v139 offset:8192
mm a[32:35],v[64:67],v[8:11],a[32:35]
dt v[126:127],v139 offset:9216
mm a[32:35],v[68:71],v[12:15],a[32:35]
mm a[64:67],v[64:67],v[16:19],a[64:67]
mm a[64:67],v[68:71],v[20:23],a[64:67]
mm a[96:99],v[64:67],v[24:27],a[96:99]
dr v[32:35],v128 offset:8192
mm a[96:99],v[68:71],v[28:31],a[96:99]
s_waitcnt lgkmcnt(15)
mm a[4:7],v[72:75],v[0:3],a[4:7]
mm a[4:7],v[76:79],v[4:7],a[4:7]
mm a[36:39],v[72:75],v[8:11],a[36:39]
mm a[36:39],v[76:79],v[12:15],a[36:39]
dr v[36:39],v129 offset:8192
)"
R"(mm a[68:71],v[72:75],v[16:19],a[68:71]
mm a[68:71],v[76:79],v[20:23],a[68:71]
mm a[100:103],v[72:75],v[24:27],a[100:103]
mm a[100:103],v[76:79],v[28:31],a[100:103]
s_waitcnt lgkmcnt(15)
mm a[8:11],v[80:83],v[0:3],a[8:11]
dr v[40:43],v128 offset:10240
mm a[8:11],v[84:87],v[4:7],a[8:11]
mm a[40:43],v[80:83],v[8:11],a[40:43]
mm a[40:43],v[84:87],v[12:15],a[40:43]
mm a[72:75],v[80:83],v[16:19],a[72:75]
mm a[72:75],v[84:87],v[20:23],a[72:75]
dr v[44:47],v129 offset:10240
mm a[104:107],v[80:83],v[24:27],a[104:107]
mm a[104:107],v[84:87],v[28:31],a[104:107]
s_waitcnt lgkmcnt(15)
)"
R"(mm a[12:15],v[88:91],v[0:3],a[12:15]
mm a[12:15],v[92:95],v[4:7],a[12:15]
mm a[44:47],v[88:91],v[8:11],a[44:47]
dr v[48:51],v128 offset:12288
mm a[44:47],v[92:95],v[12:15],a[44:47]
mm a[76:79],v[88:91],v[16:19],a[76:79]
mm a[76:79],v[92:95],v[20:23],a[76:79]
mm a[108:111],v[88:91],v[24:27],a[108:111]
mm a[108:111],v[92:95],v[28:31],a[108:111]
s_waitcnt lgkmcnt(15)
dr v[52:55],v129 offset:12288
mm a[16:19],v[96:99],v[0:3],a[16:19]
mm a[16:19],v[100:103],v[4:7],a[16:19]
mm a[48:51],v[96:99],v[8:11],a[48:51]
mm a[48:51],v[100:103],v[12:15],a[48:51]
mm a[80:83],v[96:99],v[16:19],a[80:83]
)"
R"(dr v[56:59],v128 offset:14336
mm a[80:83],v[100:103],v[20:23],a[80:83]
mm a[112:115],v[96:99],v[24:27],a[112:115]
mm a[112:115],v[100:103],v[28:31],a[112:115]
s_waitcnt lgkmcnt(15)
mm a[20:23],v[104:107],v[0:3],a[20:23]
mm a[20:23],v[108:111],v[4:7],a[20:23]
dr v[60:63],v129 offset:14336
mm a[52:55],v[104:107],v[8:11],a[52:55]
mm a[52:55],v[108:111],v[12:15],a[52:55]
mm a[84:87],v[104:107],v[16:19],a[84:87]
mm a[84:87],v[108:111],v[20:23],a[84:87]
mm a[116:119],v[104:107],v[24:27],a[116:119]
mm a[116:119],v[108:111],v[28:31],a[116:119]
s_waitcnt lgkmcnt(12)
mm a[24:27],v[112:115],v[0:3],a[24:27]
)"
R"(s_mov_b32 s88, s64
mm a[24:27],v[116:119],v[4:7],a[24:27]
s_and_b32 s89, s65, 0xffff
mm a[56:59],v[112:115],v[8:11],a[56:59]
s_mov_b32 s90, 0x200000
mm a[56:59],v[116:119],v[12:15],a[56:59]
s_mov_b32 s91, 0x20000
mm a[88:91],v[112:115],v[16:19],a[88:91]
s_mov_b32 s92, s68
mm a[88:91],v[116:119],v[20:23],a[88:91]
s_and_b32 s93, s69, 0xffff
mm a[120:123],v[112:115],v[24:27],a[120:123]
s_mov_b32 s94, 0x4000
mm a[120:123],v[116:119],v[28:31],a[120:123]
s_mov_b32 s95, 0x20000
s_waitcnt lgkmcnt(8)
)"
R"(mm a[28:31],v[120:123],v[0:3],a[28:31]
v_lshrrev_b32 v241, 4, v213
v_lshlrev_b32 v241, 7, v241
mm a[28:31],v[124:127],v[4:7],a[28:31]
v_and_b32 v242, 15, v213
v_lshl_add_u32 v243, v242, 1, v241
mm a[60:63],v[120:123],v[8:11],a[60:63]
s_lshl_b32 s62, s48, 9
mm a[60:63],v[124:127],v[12:15],a[60:63]
buffer_load_ushort v220, v243, s[92:95], s62 offen offset:0
mm a[92:95],v[120:123],v[16:19],a[92:95]
buffer_load_ushort v221, v243, s[92:95], s62 offen offset:32
mm a[92:95],v[124:127],v[20:23],a[92:95]
buffer_load_ushort v222, v243, s[92:95], s62 offen offset:64
mm a[124:127],v[120:123],v[24:27],a[124:127]
buffer_load_ushort v223, v243, s[92:95], s62 offen offset:96
)"
R"(mm a[124:127],v[124:127],v[28:31],a[124:127]
s_waitcnt vmcnt(4) lgkmcnt(0)
s_barrier
s_lshl_b32 s58, s49, 2
mm a[128:131],v[64:67],v[32:35],a[128:131]
sa s58,s58,s50
dr v[0:3],v130 offset:0
mm a[128:131],v[68:71],v[36:39],a[128:131]
s_lshl_b32 s58, s58, 16
dr v[4:7],v131 offset:0
mm a[160:163],v[64:67],v[40:43],a[160:163]
s_lshl_b32 s60, s50, 11
dr v[8:11],v130 offset:2048
mm a[160:163],v[68:71],v[44:47],a[160:163]
sa s60,s60,0x20000
dr v[12:15],v131 offset:2048
)"
R"(mm a[192:195],v[64:67],v[48:51],a[192:195]
dr v[16:19],v130 offset:4096
v_and_b32 v241, 15, v213
mm a[192:195],v[68:71],v[52:55],a[192:195]
dr v[20:23],v131 offset:4096
v_lshlrev_b32 v232, 7, v241
mm a[224:227],v[64:67],v[56:59],a[224:227]
dr v[24:27],v130 offset:6144
v_lshrrev_b32 v241, 1, v241
mm a[224:227],v[68:71],v[60:63],a[224:227]
dt v[64:65],v140 offset:0
v_lshrrev_b32 v242, 4, v213
mm a[132:135],v[72:75],v[32:35],a[132:135]
dt v[66:67],v140 offset:1024
v_xor_b32 v241, v241, v242
mm a[132:135],v[76:79],v[36:39],a[132:135]
)"
R"(dt v[68:69],v140 offset:8192
v_lshl_add_u32 v232, v241, 4, v232
mm a[164:167],v[72:75],v[40:43],a[164:167]
dt v[70:71],v140 offset:9216
v_xor_b32 v233, 64, v232
mm a[164:167],v[76:79],v[44:47],a[164:167]
dr v[28:31],v131 offset:6144
v_add_u32 v232, s54, v232
mm a[196:199],v[72:75],v[48:51],a[196:199]
v_add_u32 v233, s54, v233
v_add_u32 v234, 0x10000, v232
mm a[196:199],v[76:79],v[52:55],a[196:199]
v_add_u32 v235, 0x10000, v233
v_lshrrev_b32 v241, 4, v213
mm a[228:231],v[72:75],v[56:59],a[228:231]
v_bfe_u32 v242, v213, 2, 2
)"
R"(v_lshl_add_u32 v243, v241, 3, v242
mm a[228:231],v[76:79],v[60:63],a[228:231]
dt v[72:73],v141 offset:0
v_lshlrev_b32 v243, 7, v243
mm a[136:139],v[80:83],v[32:35],a[136:139]
dt v[74:75],v141 offset:1024
v_and_b32 v241, 1, v241
mm a[136:139],v[84:87],v[36:39],a[136:139]
dt v[76:77],v141 offset:8192
v_lshrrev_b32 v242, 1, v242
mm a[168:171],v[80:83],v[40:43],a[168:171]
dt v[78:79],v141 offset:9216
v_lshl_or_b32 v241, v241, 1, v242
mm a[168:171],v[84:87],v[44:47],a[168:171]
v_lshl_add_u32 v243, v241, 5, v243
v_and_b32 v241, 3, v213
)"
R"(mm a[200:203],v[80:83],v[48:51],a[200:203]
v_lshl_add_u32 v243, v241, 3, v243
v_add_u32 v243, 0x20000, v243
mm a[200:203],v[84:87],v[52:55],a[200:203]
v_xor_b32 v236, 0, v243
v_xor_b32 v237, 32, v243
mm a[232:235],v[80:83],v[56:59],a[232:235]
v_xor_b32 v238, 64, v243
mm a[232:235],v[84:87],v[60:63],a[232:235]
dt v[80:81],v142 offset:0
mm a[140:143],v[88:91],v[32:35],a[140:143]
dt v[82:83],v142 offset:1024
mm a[140:143],v[92:95],v[36:39],a[140:143]
dt v[84:85],v142 offset:8192
mm a[172:175],v[88:91],v[40:43],a[172:175]
dt v[86:87],v142 offset:9216
)"
R"(mm a[172:175],v[92:95],v[44:47],a[172:175]
buffer_load_dword v199, v198, s[44:47], s81 offen sc1
mm a[204:207],v[88:91],v[48:51],a[204:207]
sa m0,s54,0x0
mm a[204:207],v[92:95],v[52:55],a[204:207]
sa s56,s58,0x0
bl v148,s[88:91],s56 offen lds
mm a[236:239],v[88:91],v[56:59],a[236:239]
sa s56,s58,0x1c00
bl v149,s[88:91],s56 offen offset:1024 lds
mm a[236:239],v[92:95],v[60:63],a[236:239]
dt v[88:89],v143 offset:0
mm a[144:147],v[96:99],v[32:35],a[144:147]
dt v[90:91],v143 offset:1024
mm a[144:147],v[100:103],v[36:39],a[144:147]
dt v[92:93],v143 offset:8192
)"
R"(mm a[176:179],v[96:99],v[40:43],a[176:179]
dt v[94:95],v143 offset:9216
mm a[176:179],v[100:103],v[44:47],a[176:179]
sa s56,s58,0x3800
bl v148,s[88:91],s56 offen offset:2048 lds
mm a[208:211],v[96:99],v[48:51],a[208:211]
sa s56,s58,0x5400
bl v149,s[88:91],s56 offen offset:3072 lds
mm a[208:211],v[100:103],v[52:55],a[208:211]
sa m0,s54,0x1000
mm a[240:243],v[96:99],v[56:59],a[240:243]
sa s56,s58,0x8000
bl v148,s[88:91],s56 offen lds
mm a[240:243],v[100:103],v[60:63],a[240:243]
dt v[96:97],v144 offset:0
mm a[148:151],v[104:107],v[32:35],a[148:151]
)"
R"(dt v[98:99],v144 offset:1024
mm a[148:151],v[108:111],v[36:39],a[148:151]
dt v[100:101],v144 offset:8192
mm a[180:183],v[104:107],v[40:43],a[180:183]
dt v[102:103],v144 offset:9216
mm a[180:183],v[108:111],v[44:47],a[180:183]
sa s56,s58,0x9c00
bl v149,s[88:91],s56 offen offset:1024 lds
mm a[212:215],v[104:107],v[48:51],a[212:215]
sa s56,s58,0xb800
bl v148,s[88:91],s56 offen offset:2048 lds
mm a[212:215],v[108:111],v[52:55],a[212:215]
sa s56,s58,0xd400
bl v149,s[88:91],s56 offen offset:3072 lds
mm a[244:247],v[104:107],v[56:59],a[244:247]
mm a[244:247],v[108:111],v[60:63],a[244:247]
)"
R"(dt v[104:105],v145 offset:0
mm a[152:155],v[112:115],v[32:35],a[152:155]
dt v[106:107],v145 offset:1024
mm a[152:155],v[116:119],v[36:39],a[152:155]
dt v[108:109],v145 offset:8192
mm a[184:187],v[112:115],v[40:43],a[184:187]
sa m0,s54,0x8000
dt v[110:111],v145 offset:9216
mm a[184:187],v[116:119],v[44:47],a[184:187]
sa s56,s58,0x80
bl v148,s[88:91],s56 offen lds
mm a[216:219],v[112:115],v[48:51],a[216:219]
mm a[216:219],v[116:119],v[52:55],a[216:219]
mm a[248:251],v[112:115],v[56:59],a[248:251]
mm a[248:251],v[116:119],v[60:63],a[248:251]
dt v[112:113],v146 offset:0
)"
R"(mm a[156:159],v[120:123],v[32:35],a[156:159]
dt v[114:115],v146 offset:1024
mm a[156:159],v[124:127],v[36:39],a[156:159]
dt v[116:117],v146 offset:8192
mm a[188:191],v[120:123],v[40:43],a[188:191]
dt v[118:119],v146 offset:9216
mm a[188:191],v[124:127],v[44:47],a[188:191]
sa s56,s58,0x1c80
bl v149,s[88:91],s56 offen offset:1024 lds
mm a[220:223],v[120:123],v[48:51],a[220:223]
mm a[220:223],v[124:127],v[52:55],a[220:223]
mm a[252:255],v[120:123],v[56:59],a[252:255]
mm a[252:255],v[124:127],v[60:63],a[252:255]
s_waitcnt lgkmcnt(15)
dt v[120:121],v147 offset:0
mm a[0:3],v[64:67],v[0:3],a[0:3]
)"
R"(dt v[122:123],v147 offset:1024
mm a[0:3],v[68:71],v[4:7],a[0:3]
dt v[124:125],v147 offset:8192
mm a[32:35],v[64:67],v[8:11],a[32:35]
dt v[126:127],v147 offset:9216
mm a[32:35],v[68:71],v[12:15],a[32:35]
sa s56,s58,0x3880
bl v148,s[88:91],s56 offen offset:2048 lds
mm a[64:67],v[64:67],v[16:19],a[64:67]
mm a[64:67],v[68:71],v[20:23],a[64:67]
sa s56,s58,0x5480
bl v149,s[88:91],s56 offen offset:3072 lds
mm a[96:99],v[64:67],v[24:27],a[96:99]
dr v[32:35],v130 offset:8192
mm a[96:99],v[68:71],v[28:31],a[96:99]
s_waitcnt lgkmcnt(15)
)"
R"(mm a[4:7],v[72:75],v[0:3],a[4:7]
mm a[4:7],v[76:79],v[4:7],a[4:7]
mm a[36:39],v[72:75],v[8:11],a[36:39]
sa m0,s54,0x9000
mm a[36:39],v[76:79],v[12:15],a[36:39]
dr v[36:39],v131 offset:8192
mm a[68:71],v[72:75],v[16:19],a[68:71]
sa s56,s58,0x8080
bl v148,s[88:91],s56 offen lds
mm a[68:71],v[76:79],v[20:23],a[68:71]
mm a[100:103],v[72:75],v[24:27],a[100:103]
mm a[100:103],v[76:79],v[28:31],a[100:103]
s_waitcnt lgkmcnt(15)
mm a[8:11],v[80:83],v[0:3],a[8:11]
dr v[40:43],v130 offset:10240
mm a[8:11],v[84:87],v[4:7],a[8:11]
)"
R"(sa s56,s58,0x9c80
bl v149,s[88:91],s56 offen offset:1024 lds
mm a[40:43],v[80:83],v[8:11],a[40:43]
mm a[40:43],v[84:87],v[12:15],a[40:43]
mm a[72:75],v[80:83],v[16:19],a[72:75]
mm a[72:75],v[84:87],v[20:23],a[72:75]
dr v[44:47],v131 offset:10240
mm a[104:107],v[80:83],v[24:27],a[104:107]
mm a[104:107],v[84:87],v[28:31],a[104:107]
s_waitcnt lgkmcnt(15)
mm a[12:15],v[88:91],v[0:3],a[12:15]
sa s56,s58,0xb880
bl v148,s[88:91],s56 offen offset:2048 lds
mm a[12:15],v[92:95],v[4:7],a[12:15]
mm a[44:47],v[88:91],v[8:11],a[44:47]
dr v[48:51],v130 offset:12288
)"
R"(mm a[44:47],v[92:95],v[12:15],a[44:47]
mm a[76:79],v[88:91],v[16:19],a[76:79]
mm a[76:79],v[92:95],v[20:23],a[76:79]
sa s56,s58,0xd480
bl v149,s[88:91],s56 offen offset:3072 lds
mm a[108:111],v[88:91],v[24:27],a[108:111]
mm a[108:111],v[92:95],v[28:31],a[108:111]
s_waitcnt lgkmcnt(15)
dr v[52:55],v131 offset:12288
mm a[16:19],v[96:99],v[0:3],a[16:19]
s_waitcnt vmcnt(16)
v_cmp_eq_u32 vcc, 1, v199
s_nop 1
s_cmp_eq_u64 vcc, exec
s_cbranch_scc0 L_wa0
L_wa0d:
)"
R"(mm a[16:19],v[100:103],v[4:7],a[16:19]
mm a[48:51],v[96:99],v[8:11],a[48:51]
mm a[48:51],v[100:103],v[12:15],a[48:51]
mm a[80:83],v[96:99],v[16:19],a[80:83]
dr v[56:59],v130 offset:14336
mm a[80:83],v[100:103],v[20:23],a[80:83]
sa m0,s60,0x0
mm a[112:115],v[96:99],v[24:27],a[112:115]
sa s56,s59,0x0
bl v214,s[44:47],s56 offen sc1 lds
mm a[112:115],v[100:103],v[28:31],a[112:115]
s_waitcnt lgkmcnt(15)
mm a[20:23],v[104:107],v[0:3],a[20:23]
mm a[20:23],v[108:111],v[4:7],a[20:23]
dr v[60:63],v131 offset:14336
mm a[52:55],v[104:107],v[8:11],a[52:55]
)"
R"(mm a[52:55],v[108:111],v[12:15],a[52:55]
sa s56,s59,0x0
bl v214,s[44:47],s56 offen offset:1024 sc1 lds
mm a[84:87],v[104:107],v[16:19],a[84:87]
mm a[84:87],v[108:111],v[20:23],a[84:87]
mm a[116:119],v[104:107],v[24:27],a[116:119]
mm a[116:119],v[108:111],v[28:31],a[116:119]
s_waitcnt lgkmcnt(12)
sa m0,s60,0x2000
mm a[24:27],v[112:115],v[0:3],a[24:27]
sa s56,s59,0x2000
bl v214,s[44:47],s56 offen sc1 lds
mm a[24:27],v[116:119],v[4:7],a[24:27]
mm a[56:59],v[112:115],v[8:11],a[56:59]
mm a[56:59],v[116:119],v[12:15],a[56:59]
mm a[88:91],v[112:115],v[16:19],a[88:91]
)"
R"(mm a[88:91],v[116:119],v[20:23],a[88:91]
sa s56,s59,0x2000
bl v214,s[44:47],s56 offen offset:1024 sc1 lds
mm a[120:123],v[112:115],v[24:27],a[120:123]
mm a[120:123],v[116:119],v[28:31],a[120:123]
s_waitcnt lgkmcnt(8)
mm a[28:31],v[120:123],v[0:3],a[28:31]
mm a[28:31],v[124:127],v[4:7],a[28:31]
mm a[60:63],v[120:123],v[8:11],a[60:63]
mm a[60:63],v[124:127],v[12:15],a[60:63]
mm a[92:95],v[120:123],v[16:19],a[92:95]
mm a[92:95],v[124:127],v[20:23],a[92:95]
mm a[124:127],v[120:123],v[24:27],a[124:127]
mm a[124:127],v[124:127],v[28:31],a[124:127]
s_waitcnt lgkmcnt(0)
s_barrier
)"
R"(mm a[128:131],v[64:67],v[32:35],a[128:131]
sa m0,s54,0x10000
mm a[128:131],v[68:71],v[36:39],a[128:131]
sa s56,s58,0x100
bl v148,s[88:91],s56 offen lds
mm a[160:163],v[64:67],v[40:43],a[160:163]
mm a[160:163],v[68:71],v[44:47],a[160:163]
mm a[192:195],v[64:67],v[48:51],a[192:195]
sa s56,s58,0x1d00
bl v149,s[88:91],s56 offen offset:1024 lds
mm a[192:195],v[68:71],v[52:55],a[192:195]
mm a[224:227],v[64:67],v[56:59],a[224:227]
mm a[224:227],v[68:71],v[60:63],a[224:227]
sa s56,s58,0x3900
bl v148,s[88:91],s56 offen offset:2048 lds
mm a[132:135],v[72:75],v[32:35],a[132:135]
)"
R"(mm a[132:135],v[76:79],v[36:39],a[132:135]
mm a[164:167],v[72:75],v[40:43],a[164:167]
sa s56,s58,0x5500
bl v149,s[88:91],s56 offen offset:3072 lds
mm a[164:167],v[76:79],v[44:47],a[164:167]
mm a[196:199],v[72:75],v[48:51],a[196:199]
sa m0,s54,0x11000
mm a[196:199],v[76:79],v[52:55],a[196:199]
sa s56,s58,0x8100
bl v148,s[88:91],s56 offen lds
mm a[228:231],v[72:75],v[56:59],a[228:231]
mm a[228:231],v[76:79],v[60:63],a[228:231]
mm a[136:139],v[80:83],v[32:35],a[136:139]
sa s56,s58,0x9d00
bl v149,s[88:91],s56 offen offset:1024 lds
mm a[136:139],v[84:87],v[36:39],a[136:139]
)"
R"(mm a[168:171],v[80:83],v[40:43],a[168:171]
mm a[168:171],v[84:87],v[44:47],a[168:171]
sa s56,s58,0xb900
bl v148,s[88:91],s56 offen offset:2048 lds
mm a[200:203],v[80:83],v[48:51],a[200:203]
mm a[200:203],v[84:87],v[52:55],a[200:203]
mm a[232:235],v[80:83],v[56:59],a[232:235]
sa s56,s58,0xd500
bl v149,s[88:91],s56 offen offset:3072 lds
mm a[232:235],v[84:87],v[60:63],a[232:235]
mm a[140:143],v[88:91],v[32:35],a[140:143]
sa m0,s60,0x4000
mm a[140:143],v[92:95],v[36:39],a[140:143]
sa s56,s59,0x4000
bl v214,s[44:47],s56 offen sc1 lds
mm a[172:175],v[88:91],v[40:43],a[172:175]
)"
R"(mm a[172:175],v[92:95],v[44:47],a[172:175]
mm a[204:207],v[88:91],v[48:51],a[204:207]
sa s56,s59,0x4000
bl v214,s[44:47],s56 offen offset:1024 sc1 lds
mm a[204:207],v[92:95],v[52:55],a[204:207]
mm a[236:239],v[88:91],v[56:59],a[236:239]
sa m0,s54,0x18000
mm a[236:239],v[92:95],v[60:63],a[236:239]
sa s56,s58,0x180
bl v148,s[88:91],s56 offen lds
mm a[144:147],v[96:99],v[32:35],a[144:147]
mm a[144:147],v[100:103],v[36:39],a[144:147]
mm a[176:179],v[96:99],v[40:43],a[176:179]
sa s56,s58,0x1d80
bl v149,s[88:91],s56 offen offset:1024 lds
mm a[176:179],v[100:103],v[44:47],a[176:179]
)"
R"(mm a[208:211],v[96:99],v[48:51],a[208:211]
mm a[208:211],v[100:103],v[52:55],a[208:211]
sa s56,s58,0x3980
bl v148,s[88:91],s56 offen offset:2048 lds
mm a[240:243],v[96:99],v[56:59],a[240:243]
mm a[240:243],v[100:103],v[60:63],a[240:243]
mm a[148:151],v[104:107],v[32:35],a[148:151]
sa s56,s58,0x5580
bl v149,s[88:91],s56 offen offset:3072 lds
mm a[148:151],v[108:111],v[36:39],a[148:151]
mm a[180:183],v[104:107],v[40:43],a[180:183]
sa m0,s54,0x19000
mm a[180:183],v[108:111],v[44:47],a[180:183]
sa s56,s58,0x8180
bl v148,s[88:91],s56 offen lds
mm a[212:215],v[104:107],v[48:51],a[212:215]
)"
R"(mm a[212:215],v[108:111],v[52:55],a[212:215]
mm a[244:247],v[104:107],v[56:59],a[244:247]
sa s56,s58,0x9d80
bl v149,s[88:91],s56 offen offset:1024 lds
mm a[244:247],v[108:111],v[60:63],a[244:247]
mm a[152:155],v[112:115],v[32:35],a[152:155]
mm a[152:155],v[116:119],v[36:39],a[152:155]
sa s56,s58,0xb980
bl v148,s[88:91],s56 offen offset:2048 lds
mm a[184:187],v[112:115],v[40:43],a[184:187]
mm a[184:187],v[116:119],v[44:47],a[184:187]
mm a[216:219],v[112:115],v[48:51],a[216:219]
sa s56,s58,0xd580
bl v149,s[88:91],s56 offen offset:3072 lds
mm a[216:219],v[116:119],v[52:55],a[216:219]
mm a[248:251],v[112:115],v[56:59],a[248:251]
)"
R"(sa m0,s60,0x6000
mm a[248:251],v[116:119],v[60:63],a[248:251]
sa s56,s59,0x6000
bl v214,s[44:47],s56 offen sc1 lds
mm a[156:159],v[120:123],v[32:35],a[156:159]
mm a[156:159],v[124:127],v[36:39],a[156:159]
mm a[188:191],v[120:123],v[40:43],a[188:191]
sa s56,s59,0x6000
bl v214,s[44:47],s56 offen offset:1024 sc1 lds
mm a[188:191],v[124:127],v[44:47],a[188:191]
mm a[220:223],v[120:123],v[48:51],a[220:223]
mm a[220:223],v[124:127],v[52:55],a[220:223]
mm a[252:255],v[120:123],v[56:59],a[252:255]
mm a[252:255],v[124:127],v[60:63],a[252:255]
s_branch L_kv_done
.p2align 12
)"
R"(L_kv_v:
mm a[0:3],v[0:3],v[64:67],0
sa m0,s55,0x10000
dt v[80:81],v134 offset:0
mm a[0:3],v[4:7],v[68:71],a[0:3]
sa s56,s53,0x80000
bl v150,s[40:43],s56 offen nt lds
mm a[4:7],v[8:11],v[64:67],0
dt v[82:83],v134 offset:1024
mm a[4:7],v[12:15],v[68:71],a[4:7]
dt v[84:85],v134 offset:8192
mm a[64:67],v[16:19],v[64:67],0
sa s56,s53,0x87c00
bl v150,s[40:43],s56 offen offset:1024 nt lds
mm a[64:67],v[20:23],v[68:71],a[64:67]
dt v[86:87],v134 offset:9216
)"
R"(mm a[68:71],v[24:27],v[64:67],0
dt v[88:89],v135 offset:0
mm a[68:71],v[28:31],v[68:71],a[68:71]
s_waitcnt lgkmcnt(5)
sa s56,s53,0x8f800
bl v151,s[40:43],s56 offen offset:2048 nt lds
mm a[8:11],v[0:3],v[72:75],0
dt v[90:91],v135 offset:1024
mm a[8:11],v[4:7],v[76:79],a[8:11]
dt v[92:93],v135 offset:8192
mm a[12:15],v[8:11],v[72:75],0
sa s56,s53,0x97400
bl v151,s[40:43],s56 offen offset:3072 nt lds
mm a[12:15],v[12:15],v[76:79],a[12:15]
dt v[94:95],v135 offset:9216
mm a[72:75],v[16:19],v[72:75],0
)"
R"(sa m0,s55,0x11000
dt v[96:97],v136 offset:0
mm a[72:75],v[20:23],v[76:79],a[72:75]
sa s56,s53,0xa0000
bl v150,s[40:43],s56 offen nt lds
mm a[76:79],v[24:27],v[72:75],0
dt v[98:99],v136 offset:1024
mm a[76:79],v[28:31],v[76:79],a[76:79]
s_waitcnt lgkmcnt(6)
dt v[100:101],v136 offset:8192
mm a[16:19],v[0:3],v[80:83],0
sa s56,s53,0xa7c00
bl v150,s[40:43],s56 offen offset:1024 nt lds
mm a[16:19],v[4:7],v[84:87],a[16:19]
dt v[102:103],v136 offset:9216
mm a[20:23],v[8:11],v[80:83],0
)"
R"(dt v[104:105],v137 offset:0
mm a[20:23],v[12:15],v[84:87],a[20:23]
sa s56,s53,0xaf800
bl v151,s[40:43],s56 offen offset:2048 nt lds
mm a[80:83],v[16:19],v[80:83],0
dt v[106:107],v137 offset:1024
mm a[80:83],v[20:23],v[84:87],a[80:83]
dt v[108:109],v137 offset:8192
mm a[84:87],v[24:27],v[80:83],0
sa s56,s53,0xb7400
bl v151,s[40:43],s56 offen offset:3072 nt lds
mm a[84:87],v[28:31],v[84:87],a[84:87]
s_waitcnt lgkmcnt(7)
dt v[110:111],v137 offset:9216
mm a[24:27],v[0:3],v[88:91],0
dt v[112:113],v138 offset:0
)"
R"(mm a[24:27],v[4:7],v[92:95],a[24:27]
sa m0,s55,0x18000
dt v[114:115],v138 offset:1024
mm a[28:31],v[8:11],v[88:91],0
dt v[116:117],v138 offset:8192
mm a[28:31],v[12:15],v[92:95],a[28:31]
dt v[118:119],v138 offset:9216
mm a[88:91],v[16:19],v[88:91],0
dt v[120:121],v139 offset:0
mm a[88:91],v[20:23],v[92:95],a[88:91]
dt v[122:123],v139 offset:1024
mm a[92:95],v[24:27],v[88:91],0
dt v[124:125],v139 offset:8192
mm a[92:95],v[28:31],v[92:95],a[92:95]
s_waitcnt lgkmcnt(11)
dt v[126:127],v139 offset:9216
)"
R"(mm a[32:35],v[0:3],v[96:99],0
dr v[32:35],v128 offset:8192
mm a[32:35],v[4:7],v[100:103],a[32:35]
dr v[36:39],v129 offset:8192
mm a[36:39],v[8:11],v[96:99],0
sa s56,s53,0x100000
bl v150,s[40:43],s56 offen nt lds
mm a[36:39],v[12:15],v[100:103],a[36:39]
dr v[40:43],v128 offset:10240
mm a[96:99],v[16:19],v[96:99],0
sa s56,s53,0x107c00
bl v150,s[40:43],s56 offen offset:1024 nt lds
mm a[96:99],v[20:23],v[100:103],a[96:99]
dr v[44:47],v129 offset:10240
mm a[100:103],v[24:27],v[96:99],0
sa s56,s53,0x10f800
)"
R"(bl v151,s[40:43],s56 offen offset:2048 nt lds
mm a[100:103],v[28:31],v[100:103],a[100:103]
s_waitcnt lgkmcnt(12)
dr v[48:51],v128 offset:12288
mm a[40:43],v[0:3],v[104:107],0
sa s56,s53,0x117400
bl v151,s[40:43],s56 offen offset:3072 nt lds
mm a[40:43],v[4:7],v[108:111],a[40:43]
sa m0,s55,0x19000
dr v[52:55],v129 offset:12288
mm a[44:47],v[8:11],v[104:107],0
sa s56,s53,0x120000
bl v150,s[40:43],s56 offen nt lds
mm a[44:47],v[12:15],v[108:111],a[44:47]
dr v[56:59],v128 offset:14336
mm a[104:107],v[16:19],v[104:107],0
)"
R"(mm a[104:107],v[20:23],v[108:111],a[104:107]
dr v[60:63],v129 offset:14336
mm a[108:111],v[24:27],v[104:107],0
sa s56,s53,0x127c00
bl v150,s[40:43],s56 offen offset:1024 nt lds
mm a[108:111],v[28:31],v[108:111],a[108:111]
s_waitcnt lgkmcnt(12)
mm a[48:51],v[0:3],v[112:115],0
mm a[48:51],v[4:7],v[116:119],a[48:51]
mm a[52:55],v[8:11],v[112:115],0
sa s56,s53,0x12f800
bl v151,s[40:43],s56 offen offset:2048 nt lds
mm a[52:55],v[12:15],v[116:119],a[52:55]
mm a[112:115],v[16:19],v[112:115],0
mm a[112:115],v[20:23],v[116:119],a[112:115]
mm a[116:119],v[24:27],v[112:115],0
)"
R"(sa s56,s53,0x137400
bl v151,s[40:43],s56 offen offset:3072 nt lds
mm a[116:119],v[28:31],v[116:119],a[116:119]
s_waitcnt lgkmcnt(8)
mm a[56:59],v[0:3],v[120:123],0
mm a[56:59],v[4:7],v[124:127],a[56:59]
mm a[60:63],v[8:11],v[120:123],0
mm a[60:63],v[12:15],v[124:127],a[60:63]
mm a[120:123],v[16:19],v[120:123],0
mm a[120:123],v[20:23],v[124:127],a[120:123]
mm a[124:127],v[24:27],v[120:123],0
mm a[124:127],v[28:31],v[124:127],a[124:127]
s_waitcnt vmcnt(8) lgkmcnt(0)
s_barrier
mm a[128:131],v[32:35],v[64:67],0
dr v[0:3],v130 offset:0
)"
R"(mm a[128:131],v[36:39],v[68:71],a[128:131]
dr v[4:7],v131 offset:0
mm a[132:135],v[40:43],v[64:67],0
dr v[8:11],v130 offset:2048
mm a[132:135],v[44:47],v[68:71],a[132:135]
dr v[12:15],v131 offset:2048
mm a[192:195],v[48:51],v[64:67],0
dr v[16:19],v130 offset:4096
mm a[192:195],v[52:55],v[68:71],a[192:195]
dr v[20:23],v131 offset:4096
mm a[196:199],v[56:59],v[64:67],0
dr v[24:27],v130 offset:6144
mm a[196:199],v[60:63],v[68:71],a[196:199]
dt v[64:65],v140 offset:0
mm a[136:139],v[32:35],v[72:75],0
dt v[66:67],v140 offset:1024
)"
R"(mm a[136:139],v[36:39],v[76:79],a[136:139]
dt v[68:69],v140 offset:8192
mm a[140:143],v[40:43],v[72:75],0
dt v[70:71],v140 offset:9216
mm a[140:143],v[44:47],v[76:79],a[140:143]
dr v[28:31],v131 offset:6144
mm a[200:203],v[48:51],v[72:75],0
buffer_store_dword v181, v178, s[44:47], s61 offen sc1
mm a[200:203],v[52:55],v[76:79],a[200:203]
sa m0,s54,0x0
mm a[204:207],v[56:59],v[72:75],0
sa s56,s52,0x100
bl v148,s[36:39],s56 offen lds
mm a[204:207],v[60:63],v[76:79],a[204:207]
dt v[72:73],v141 offset:0
mm a[144:147],v[32:35],v[80:83],0
)"
R"(dt v[74:75],v141 offset:1024
mm a[144:147],v[36:39],v[84:87],a[144:147]
dt v[76:77],v141 offset:8192
mm a[148:151],v[40:43],v[80:83],0
dt v[78:79],v141 offset:9216
mm a[148:151],v[44:47],v[84:87],a[148:151]
sa s56,s52,0x1d00
bl v149,s[36:39],s56 offen offset:1024 lds
mm a[208:211],v[48:51],v[80:83],0
sa s56,s52,0x3900
bl v148,s[36:39],s56 offen offset:2048 lds
mm a[208:211],v[52:55],v[84:87],a[208:211]
mm a[212:215],v[56:59],v[80:83],0
sa s56,s52,0x5500
bl v149,s[36:39],s56 offen offset:3072 lds
mm a[212:215],v[60:63],v[84:87],a[212:215]
)"
R"(dt v[80:81],v142 offset:0
mm a[152:155],v[32:35],v[88:91],0
dt v[82:83],v142 offset:1024
mm a[152:155],v[36:39],v[92:95],a[152:155]
dt v[84:85],v142 offset:8192
mm a[156:159],v[40:43],v[88:91],0
dt v[86:87],v142 offset:9216
mm a[156:159],v[44:47],v[92:95],a[156:159]
sa m0,s54,0x1000
mm a[216:219],v[48:51],v[88:91],0
sa s56,s52,0x8100
bl v148,s[36:39],s56 offen lds
mm a[216:219],v[52:55],v[92:95],a[216:219]
mm a[220:223],v[56:59],v[88:91],0
sa s56,s52,0x9d00
bl v149,s[36:39],s56 offen offset:1024 lds
)"
R"(mm a[220:223],v[60:63],v[92:95],a[220:223]
dt v[88:89],v143 offset:0
mm a[160:163],v[32:35],v[96:99],0
dt v[90:91],v143 offset:1024
mm a[160:163],v[36:39],v[100:103],a[160:163]
dt v[92:93],v143 offset:8192
mm a[164:167],v[40:43],v[96:99],0
dt v[94:95],v143 offset:9216
mm a[164:167],v[44:47],v[100:103],a[164:167]
mm a[224:227],v[48:51],v[96:99],0
sa s56,s52,0xb900
bl v148,s[36:39],s56 offen offset:2048 lds
mm a[224:227],v[52:55],v[100:103],a[224:227]
mm a[228:231],v[56:59],v[96:99],0
sa s56,s52,0xd500
bl v149,s[36:39],s56 offen offset:3072 lds
)"
R"(mm a[228:231],v[60:63],v[100:103],a[228:231]
dt v[96:97],v144 offset:0
mm a[168:171],v[32:35],v[104:107],0
dt v[98:99],v144 offset:1024
mm a[168:171],v[36:39],v[108:111],a[168:171]
dt v[100:101],v144 offset:8192
mm a[172:175],v[40:43],v[104:107],0
dt v[102:103],v144 offset:9216
mm a[172:175],v[44:47],v[108:111],a[172:175]
mm a[232:235],v[48:51],v[104:107],0
mm a[232:235],v[52:55],v[108:111],a[232:235]
mm a[236:239],v[56:59],v[104:107],0
mm a[236:239],v[60:63],v[108:111],a[236:239]
sa m0,s55,0x0
dt v[104:105],v145 offset:0
mm a[176:179],v[32:35],v[112:115],0
)"
R"(dt v[106:107],v145 offset:1024
mm a[176:179],v[36:39],v[116:119],a[176:179]
dt v[108:109],v145 offset:8192
mm a[180:183],v[40:43],v[112:115],0
dt v[110:111],v145 offset:9216
mm a[180:183],v[44:47],v[116:119],a[180:183]
sa s56,s53,0x180000
bl v150,s[40:43],s56 offen nt lds
mm a[240:243],v[48:51],v[112:115],0
mm a[240:243],v[52:55],v[116:119],a[240:243]
mm a[244:247],v[56:59],v[112:115],0
sa s56,s53,0x187c00
bl v150,s[40:43],s56 offen offset:1024 nt lds
mm a[244:247],v[60:63],v[116:119],a[244:247]
dt v[112:113],v146 offset:0
mm a[184:187],v[32:35],v[120:123],0
)"
R"(dt v[114:115],v146 offset:1024
mm a[184:187],v[36:39],v[124:127],a[184:187]
dt v[116:117],v146 offset:8192
mm a[188:191],v[40:43],v[120:123],0
dt v[118:119],v146 offset:9216
mm a[188:191],v[44:47],v[124:127],a[188:191]
mm a[248:251],v[48:51],v[120:123],0
sa s56,s53,0x18f800
bl v151,s[40:43],s56 offen offset:2048 nt lds
mm a[248:251],v[52:55],v[124:127],a[248:251]
mm a[252:255],v[56:59],v[120:123],0
mm a[252:255],v[60:63],v[124:127],a[252:255]
s_waitcnt lgkmcnt(15)
dt v[120:121],v147 offset:0
mm a[0:3],v[0:3],v[64:67],a[0:3]
dt v[122:123],v147 offset:1024
)"
R"(mm a[0:3],v[4:7],v[68:71],a[0:3]
dt v[124:125],v147 offset:8192
mm a[4:7],v[8:11],v[64:67],a[4:7]
dt v[126:127],v147 offset:9216
mm a[4:7],v[12:15],v[68:71],a[4:7]
sa s56,s53,0x197400
bl v151,s[40:43],s56 offen offset:3072 nt lds
mm a[64:67],v[16:19],v[64:67],a[64:67]
mm a[64:67],v[20:23],v[68:71],a[64:67]
mm a[68:71],v[24:27],v[64:67],a[68:71]
dr v[32:35],v130 offset:8192
mm a[68:71],v[28:31],v[68:71],a[68:71]
s_waitcnt lgkmcnt(15)
sa m0,s55,0x1000
mm a[8:11],v[0:3],v[72:75],a[8:11]
sa s56,s53,0x1a0000
)"
R"(bl v150,s[40:43],s56 offen nt lds
mm a[8:11],v[4:7],v[76:79],a[8:11]
mm a[12:15],v[8:11],v[72:75],a[12:15]
mm a[12:15],v[12:15],v[76:79],a[12:15]
dr v[36:39],v131 offset:8192
mm a[72:75],v[16:19],v[72:75],a[72:75]
mm a[72:75],v[20:23],v[76:79],a[72:75]
mm a[76:79],v[24:27],v[72:75],a[76:79]
sa s56,s53,0x1a7c00
bl v150,s[40:43],s56 offen offset:1024 nt lds
mm a[76:79],v[28:31],v[76:79],a[76:79]
s_waitcnt lgkmcnt(15)
mm a[16:19],v[0:3],v[80:83],a[16:19]
dr v[40:43],v130 offset:10240
mm a[16:19],v[4:7],v[84:87],a[16:19]
mm a[20:23],v[8:11],v[80:83],a[20:23]
)"
R"(mm a[20:23],v[12:15],v[84:87],a[20:23]
mm a[80:83],v[16:19],v[80:83],a[80:83]
sa s56,s53,0x1af800
bl v151,s[40:43],s56 offen offset:2048 nt lds
mm a[80:83],v[20:23],v[84:87],a[80:83]
dr v[44:47],v131 offset:10240
mm a[84:87],v[24:27],v[80:83],a[84:87]
mm a[84:87],v[28:31],v[84:87],a[84:87]
s_waitcnt lgkmcnt(15)
mm a[24:27],v[0:3],v[88:91],a[24:27]
mm a[24:27],v[4:7],v[92:95],a[24:27]
mm a[28:31],v[8:11],v[88:91],a[28:31]
dr v[48:51],v130 offset:12288
mm a[28:31],v[12:15],v[92:95],a[28:31]
sa s56,s53,0x1b7400
bl v151,s[40:43],s56 offen offset:3072 nt lds
)"
R"(mm a[88:91],v[16:19],v[88:91],a[88:91]
mm a[88:91],v[20:23],v[92:95],a[88:91]
mm a[92:95],v[24:27],v[88:91],a[92:95]
mm a[92:95],v[28:31],v[92:95],a[92:95]
s_waitcnt lgkmcnt(15)
dr v[52:55],v131 offset:12288
mm a[32:35],v[0:3],v[96:99],a[32:35]
mm a[32:35],v[4:7],v[100:103],a[32:35]
mm a[36:39],v[8:11],v[96:99],a[36:39]
mm a[36:39],v[12:15],v[100:103],a[36:39]
mm a[96:99],v[16:19],v[96:99],a[96:99]
dr v[56:59],v130 offset:14336
mm a[96:99],v[20:23],v[100:103],a[96:99]
mm a[100:103],v[24:27],v[96:99],a[100:103]
mm a[100:103],v[28:31],v[100:103],a[100:103]
s_waitcnt lgkmcnt(15)
)"
R"(mm a[40:43],v[0:3],v[104:107],a[40:43]
mm a[40:43],v[4:7],v[108:111],a[40:43]
dr v[60:63],v131 offset:14336
mm a[44:47],v[8:11],v[104:107],a[44:47]
mm a[44:47],v[12:15],v[108:111],a[44:47]
mm a[104:107],v[16:19],v[104:107],a[104:107]
mm a[104:107],v[20:23],v[108:111],a[104:107]
mm a[108:111],v[24:27],v[104:107],a[108:111]
mm a[108:111],v[28:31],v[108:111],a[108:111]
s_waitcnt lgkmcnt(12)
mm a[48:51],v[0:3],v[112:115],a[48:51]
mm a[48:51],v[4:7],v[116:119],a[48:51]
mm a[52:55],v[8:11],v[112:115],a[52:55]
mm a[52:55],v[12:15],v[116:119],a[52:55]
mm a[112:115],v[16:19],v[112:115],a[112:115]
mm a[112:115],v[20:23],v[116:119],a[112:115]
)"
R"(mm a[116:119],v[24:27],v[112:115],a[116:119]
mm a[116:119],v[28:31],v[116:119],a[116:119]
s_waitcnt lgkmcnt(8)
mm a[56:59],v[0:3],v[120:123],a[56:59]
mm a[56:59],v[4:7],v[124:127],a[56:59]
mm a[60:63],v[8:11],v[120:123],a[60:63]
mm a[60:63],v[12:15],v[124:127],a[60:63]
mm a[120:123],v[16:19],v[120:123],a[120:123]
mm a[120:123],v[20:23],v[124:127],a[120:123]
mm a[124:127],v[24:27],v[120:123],a[124:127]
mm a[124:127],v[28:31],v[124:127],a[124:127]
s_waitcnt vmcnt(8) lgkmcnt(0)
s_barrier
mm a[128:131],v[32:35],v[64:67],a[128:131]
dr v[0:3],v128 offset:0
mm a[128:131],v[36:39],v[68:71],a[128:131]
)"
R"(dr v[4:7],v129 offset:0
mm a[132:135],v[40:43],v[64:67],a[132:135]
s_mov_b32 s88, s66
dr v[8:11],v128 offset:2048
mm a[132:135],v[44:47],v[68:71],a[132:135]
dr v[12:15],v129 offset:2048
mm a[192:195],v[48:51],v[64:67],a[192:195]
s_and_b32 s89, s67, 0xffff
dr v[16:19],v128 offset:4096
mm a[192:195],v[52:55],v[68:71],a[192:195]
dr v[20:23],v129 offset:4096
mm a[196:199],v[56:59],v[64:67],a[196:199]
s_mov_b32 s90, 0x180000
dr v[24:27],v128 offset:6144
mm a[196:199],v[60:63],v[68:71],a[196:199]
dt v[64:65],v140 offset:32768
)"
R"(mm a[136:139],v[32:35],v[72:75],a[136:139]
dt v[66:67],v140 offset:33792
mm a[136:139],v[36:39],v[76:79],a[136:139]
s_mov_b32 s91, 0x20000
dt v[68:69],v140 offset:40960
mm a[140:143],v[40:43],v[72:75],a[140:143]
dt v[70:71],v140 offset:41984
mm a[140:143],v[44:47],v[76:79],a[140:143]
s_lshl_b32 s80, s49, 2
dr v[28:31],v129 offset:6144
mm a[200:203],v[48:51],v[72:75],a[200:203]
buffer_load_dword v182, v180, s[44:47], s61 offen offset:0 sc1
mm a[200:203],v[52:55],v[76:79],a[200:203]
sa m0,s54,0x10000
mm a[204:207],v[56:59],v[72:75],a[204:207]
sa s56,s52,0x180
)"
R"(bl v148,s[36:39],s56 offen lds
mm a[204:207],v[60:63],v[76:79],a[204:207]
sa s80,s80,s50
dt v[72:73],v141 offset:32768
mm a[144:147],v[32:35],v[80:83],a[144:147]
s_mul_i32 s80, s80, 49152
dt v[74:75],v141 offset:33792
mm a[144:147],v[36:39],v[84:87],a[144:147]
dt v[76:77],v141 offset:40960
mm a[148:151],v[40:43],v[80:83],a[148:151]
dt v[78:79],v141 offset:41984
mm a[148:151],v[44:47],v[84:87],a[148:151]
sa s56,s52,0x1d80
bl v149,s[36:39],s56 offen offset:1024 lds
mm a[208:211],v[48:51],v[80:83],a[208:211]
sa s56,s52,0x3980
)"
R"(bl v148,s[36:39],s56 offen offset:2048 lds
mm a[208:211],v[52:55],v[84:87],a[208:211]
s_lshl_b32 s81, s48, 5
mm a[212:215],v[56:59],v[80:83],a[212:215]
sa s56,s52,0x5580
bl v149,s[36:39],s56 offen offset:3072 lds
mm a[212:215],v[60:63],v[84:87],a[212:215]
sa s80,s80,s81
dt v[80:81],v142 offset:32768
mm a[152:155],v[32:35],v[88:91],a[152:155]
sa s81,s80,24576
dt v[82:83],v142 offset:33792
mm a[152:155],v[36:39],v[92:95],a[152:155]
dt v[84:85],v142 offset:40960
mm a[156:159],v[40:43],v[88:91],a[156:159]
dt v[86:87],v142 offset:41984
)"
R"(v_bfe_u32 v241, v213, 4, 1
mm a[156:159],v[44:47],v[92:95],a[156:159]
sa m0,s54,0x11000
mm a[216:219],v[48:51],v[88:91],a[216:219]
sa s56,s52,0x8180
bl v148,s[36:39],s56 offen lds
mm a[216:219],v[52:55],v[92:95],a[216:219]
v_lshl_or_b32 v241, v241, 1, 0
mm a[220:223],v[56:59],v[88:91],a[220:223]
sa s56,s52,0x9d80
bl v149,s[36:39],s56 offen offset:1024 lds
mm a[220:223],v[60:63],v[92:95],a[220:223]
dt v[88:89],v143 offset:32768
v_and_b32 v242, 7, v213
mm a[160:163],v[32:35],v[96:99],a[160:163]
dt v[90:91],v143 offset:33792
)"
R"(mm a[160:163],v[36:39],v[100:103],a[160:163]
dt v[92:93],v143 offset:40960
mm a[164:167],v[40:43],v[96:99],a[164:167]
dt v[94:95],v143 offset:41984
v_xor_b32 v242, v242, v241
mm a[164:167],v[44:47],v[100:103],a[164:167]
mm a[224:227],v[48:51],v[96:99],a[224:227]
sa s56,s52,0xb980
bl v148,s[36:39],s56 offen offset:2048 lds
mm a[224:227],v[52:55],v[100:103],a[224:227]
v_lshrrev_b32 v241, 1, v242
mm a[228:231],v[56:59],v[96:99],a[228:231]
sa s56,s52,0xd580
bl v149,s[36:39],s56 offen offset:3072 lds
mm a[228:231],v[60:63],v[100:103],a[228:231]
dt v[96:97],v144 offset:32768
)"
R"(v_min_u32 v241, 2, v241
mm a[168:171],v[32:35],v[104:107],a[168:171]
dt v[98:99],v144 offset:33792
mm a[168:171],v[36:39],v[108:111],a[168:171]
dt v[100:101],v144 offset:40960
v_lshlrev_b32 v241, 10, v241
mm a[172:175],v[40:43],v[104:107],a[172:175]
dt v[102:103],v144 offset:41984
mm a[172:175],v[44:47],v[108:111],a[172:175]
mm a[232:235],v[48:51],v[104:107],a[232:235]
v_and_b32 v242, 1, v242
mm a[232:235],v[52:55],v[108:111],a[232:235]
mm a[236:239],v[56:59],v[104:107],a[236:239]
v_lshl_add_u32 v241, v242, 4, v241
mm a[236:239],v[60:63],v[108:111],a[236:239]
sa m0,s55,0x10000
)"
R"(dt v[104:105],v145 offset:32768
mm a[176:179],v[32:35],v[112:115],a[176:179]
dt v[106:107],v145 offset:33792
v_lshrrev_b32 v242, 3, v213
mm a[176:179],v[36:39],v[116:119],a[176:179]
dt v[108:109],v145 offset:40960
mm a[180:183],v[40:43],v[112:115],a[180:183]
dt v[110:111],v145 offset:41984
mm a[180:183],v[44:47],v[116:119],a[180:183]
sa s56,s53,0x200000
bl v150,s[40:43],s56 offen nt lds
mm a[240:243],v[48:51],v[112:115],a[240:243]
v_mul_u32_u24 v242, 3072, v242
mm a[240:243],v[52:55],v[116:119],a[240:243]
v_add_u32 v239, v241, v242
mm a[244:247],v[56:59],v[112:115],a[244:247]
)"
R"(sa s56,s53,0x207c00
bl v150,s[40:43],s56 offen offset:1024 nt lds
mm a[244:247],v[60:63],v[116:119],a[244:247]
dt v[112:113],v146 offset:32768
mm a[184:187],v[32:35],v[120:123],a[184:187]
dt v[114:115],v146 offset:33792
v_bfe_u32 v241, v213, 4, 1
mm a[184:187],v[36:39],v[124:127],a[184:187]
s_lshl_b32 s62, s51, 2
dt v[116:117],v146 offset:40960
mm a[188:191],v[40:43],v[120:123],a[188:191]
dt v[118:119],v146 offset:41984
v_lshl_or_b32 v241, v241, 1, 4
mm a[188:191],v[44:47],v[124:127],a[188:191]
mm a[248:251],v[48:51],v[120:123],a[248:251]
sa s56,s53,0x20f800
)"
R"(bl v151,s[40:43],s56 offen offset:2048 nt lds
mm a[248:251],v[52:55],v[124:127],a[248:251]
sa s62,s62,s50
v_and_b32 v242, 7, v213
mm a[252:255],v[56:59],v[120:123],a[252:255]
mm a[252:255],v[60:63],v[124:127],a[252:255]
s_waitcnt lgkmcnt(15)
dt v[120:121],v147 offset:32768
v_xor_b32 v242, v242, v241
mm a[0:3],v[0:3],v[64:67],a[0:3]
dt v[122:123],v147 offset:33792
mm a[0:3],v[4:7],v[68:71],a[0:3]
s_mul_i32 s62, s62, 2048
dt v[124:125],v147 offset:40960
mm a[4:7],v[8:11],v[64:67],a[4:7]
dt v[126:127],v147 offset:41984
)"
R"(v_lshrrev_b32 v241, 1, v242
mm a[4:7],v[12:15],v[68:71],a[4:7]
sa s56,s53,0x217400
bl v151,s[40:43],s56 offen offset:3072 nt lds
mm a[64:67],v[16:19],v[64:67],a[64:67]
v_min_u32 v241, 2, v241
mm a[64:67],v[20:23],v[68:71],a[64:67]
sa s92,s64,s62
s_addc_u32 s93, s65, 0
mm a[68:71],v[24:27],v[64:67],a[68:71]
dr v[32:35],v128 offset:8192
mm a[68:71],v[28:31],v[68:71],a[68:71]
s_waitcnt lgkmcnt(15)
sa m0,s55,0x11000
v_lshlrev_b32 v241, 10, v241
mm a[8:11],v[0:3],v[72:75],a[8:11]
)"
R"(sa s56,s53,0x220000
bl v150,s[40:43],s56 offen nt lds
mm a[8:11],v[4:7],v[76:79],a[8:11]
v_and_b32 v242, 1, v242
mm a[12:15],v[8:11],v[72:75],a[12:15]
s_and_b32 s93, s93, 0xffff
mm a[12:15],v[12:15],v[76:79],a[12:15]
dr v[36:39],v129 offset:8192
mm a[72:75],v[16:19],v[72:75],a[72:75]
v_lshl_add_u32 v241, v242, 4, v241
mm a[72:75],v[20:23],v[76:79],a[72:75]
mm a[76:79],v[24:27],v[72:75],a[76:79]
sa s56,s53,0x227c00
bl v150,s[40:43],s56 offen offset:1024 nt lds
mm a[76:79],v[28:31],v[76:79],a[76:79]
s_waitcnt lgkmcnt(15)
)"
R"(s_mov_b32 s94, 2048
v_lshrrev_b32 v242, 3, v213
mm a[16:19],v[0:3],v[80:83],a[16:19]
dr v[40:43],v128 offset:10240
mm a[16:19],v[4:7],v[84:87],a[16:19]
v_mul_u32_u24 v242, 3072, v242
mm a[20:23],v[8:11],v[80:83],a[20:23]
s_mov_b32 s95, 0x20000
mm a[20:23],v[12:15],v[84:87],a[20:23]
v_add_u32 v240, v241, v242
mm a[80:83],v[16:19],v[80:83],a[80:83]
sa s56,s53,0x22f800
bl v151,s[40:43],s56 offen offset:2048 nt lds
mm a[80:83],v[20:23],v[84:87],a[80:83]
dr v[44:47],v129 offset:10240
mm a[84:87],v[24:27],v[80:83],a[84:87]
)"
R"(mm a[84:87],v[28:31],v[84:87],a[84:87]
s_waitcnt lgkmcnt(15)
mm a[24:27],v[0:3],v[88:91],a[24:27]
mm a[24:27],v[4:7],v[92:95],a[24:27]
mm a[28:31],v[8:11],v[88:91],a[28:31]
dr v[48:51],v128 offset:12288
mm a[28:31],v[12:15],v[92:95],a[28:31]
sa s56,s53,0x237400
bl v151,s[40:43],s56 offen offset:3072 nt lds
mm a[88:91],v[16:19],v[88:91],a[88:91]
mm a[88:91],v[20:23],v[92:95],a[88:91]
mm a[92:95],v[24:27],v[88:91],a[92:95]
mm a[92:95],v[28:31],v[92:95],a[92:95]
s_waitcnt lgkmcnt(15)
dr v[52:55],v129 offset:12288
mm a[32:35],v[0:3],v[96:99],a[32:35]
)"
R"(mm a[32:35],v[4:7],v[100:103],a[32:35]
bl v[190:193],v239,s[88:91],s80 offen
mm a[36:39],v[8:11],v[96:99],a[36:39]
mm a[36:39],v[12:15],v[100:103],a[36:39]
mm a[96:99],v[16:19],v[96:99],a[96:99]
dr v[56:59],v128 offset:14336
mm a[96:99],v[20:23],v[100:103],a[96:99]
mm a[100:103],v[24:27],v[96:99],a[100:103]
mm a[100:103],v[28:31],v[100:103],a[100:103]
s_waitcnt lgkmcnt(15)
bl v[194:197],v240,s[88:91],s81 offen
mm a[40:43],v[0:3],v[104:107],a[40:43]
s_mov_b32 s63, 0
buffer_load_dword v167, v158, s[92:95], s63 offen
mm a[40:43],v[4:7],v[108:111],a[40:43]
dr v[60:63],v129 offset:14336
)"
R"(mm a[44:47],v[8:11],v[104:107],a[44:47]
mm a[44:47],v[12:15],v[108:111],a[44:47]
mm a[104:107],v[16:19],v[104:107],a[104:107]
mm a[104:107],v[20:23],v[108:111],a[104:107]
mm a[108:111],v[24:27],v[104:107],a[108:111]
mm a[108:111],v[28:31],v[108:111],a[108:111]
s_waitcnt lgkmcnt(12)
mm a[48:51],v[0:3],v[112:115],a[48:51]
mm a[48:51],v[4:7],v[116:119],a[48:51]
mm a[52:55],v[8:11],v[112:115],a[52:55]
mm a[52:55],v[12:15],v[116:119],a[52:55]
mm a[112:115],v[16:19],v[112:115],a[112:115]
mm a[112:115],v[20:23],v[116:119],a[112:115]
mm a[116:119],v[24:27],v[112:115],a[116:119]
mm a[116:119],v[28:31],v[116:119],a[116:119]
s_waitcnt lgkmcnt(8)
)"
R"(mm a[56:59],v[0:3],v[120:123],a[56:59]
mm a[56:59],v[4:7],v[124:127],a[56:59]
mm a[60:63],v[8:11],v[120:123],a[60:63]
mm a[60:63],v[12:15],v[124:127],a[60:63]
mm a[120:123],v[16:19],v[120:123],a[120:123]
mm a[120:123],v[20:23],v[124:127],a[120:123]
mm a[124:127],v[24:27],v[120:123],a[124:127]
mm a[124:127],v[28:31],v[124:127],a[124:127]
s_waitcnt vmcnt(11) lgkmcnt(0)
s_barrier
mm a[128:131],v[32:35],v[64:67],a[128:131]
dr v[0:3],v130 offset:0
mm a[128:131],v[36:39],v[68:71],a[128:131]
v_cmp_eq_u32 vcc, 1, v182
s_nop 1
s_cmp_eq_u32 vcc_lo, 15
)"
R"(s_cbranch_scc0 L_tok_21
L_tokd_21:
dr v[4:7],v131 offset:0
mm a[132:135],v[40:43],v[64:67],a[132:135]
dr v[8:11],v130 offset:2048
mm a[132:135],v[44:47],v[68:71],a[132:135]
dr v[12:15],v131 offset:2048
mm a[192:195],v[48:51],v[64:67],a[192:195]
dr v[16:19],v130 offset:4096
mm a[192:195],v[52:55],v[68:71],a[192:195]
dr v[20:23],v131 offset:4096
mm a[196:199],v[56:59],v[64:67],a[196:199]
dr v[24:27],v130 offset:6144
mm a[196:199],v[60:63],v[68:71],a[196:199]
dt v[64:65],v132 offset:0
mm a[136:139],v[32:35],v[72:75],a[136:139]
)"
R"(dt v[66:67],v132 offset:1024
mm a[136:139],v[36:39],v[76:79],a[136:139]
dt v[68:69],v132 offset:8192
mm a[140:143],v[40:43],v[72:75],a[140:143]
dt v[70:71],v132 offset:9216
mm a[140:143],v[44:47],v[76:79],a[140:143]
dr v[28:31],v131 offset:6144
mm a[200:203],v[48:51],v[72:75],a[200:203]
buffer_store_dword v181, v178, s[44:47], s61 offen sc1
mm a[200:203],v[52:55],v[76:79],a[200:203]
sa m0,s54,0x0
mm a[204:207],v[56:59],v[72:75],a[204:207]
sa s56,s52,0x200
bl v148,s[36:39],s56 offen lds
mm a[204:207],v[60:63],v[76:79],a[204:207]
dt v[72:73],v133 offset:0
)"
R"(mm a[144:147],v[32:35],v[80:83],a[144:147]
dt v[74:75],v133 offset:1024
mm a[144:147],v[36:39],v[84:87],a[144:147]
dt v[76:77],v133 offset:8192
mm a[148:151],v[40:43],v[80:83],a[148:151]
dt v[78:79],v133 offset:9216
mm a[148:151],v[44:47],v[84:87],a[148:151]
sa s56,s52,0x1e00
bl v149,s[36:39],s56 offen offset:1024 lds
mm a[208:211],v[48:51],v[80:83],a[208:211]
sa s56,s52,0x3a00
bl v148,s[36:39],s56 offen offset:2048 lds
mm a[208:211],v[52:55],v[84:87],a[208:211]
mm a[212:215],v[56:59],v[80:83],a[212:215]
sa s56,s52,0x5600
bl v149,s[36:39],s56 offen offset:3072 lds
)"
R"(mm a[212:215],v[60:63],v[84:87],a[212:215]
dt v[80:81],v134 offset:0
mm a[152:155],v[32:35],v[88:91],a[152:155]
dt v[82:83],v134 offset:1024
mm a[152:155],v[36:39],v[92:95],a[152:155]
dt v[84:85],v134 offset:8192
mm a[156:159],v[40:43],v[88:91],a[156:159]
dt v[86:87],v134 offset:9216
mm a[156:159],v[44:47],v[92:95],a[156:159]
sa m0,s54,0x1000
mm a[216:219],v[48:51],v[88:91],a[216:219]
sa s56,s52,0x8200
bl v148,s[36:39],s56 offen lds
mm a[216:219],v[52:55],v[92:95],a[216:219]
mm a[220:223],v[56:59],v[88:91],a[220:223]
sa s56,s52,0x9e00
)"
R"(bl v149,s[36:39],s56 offen offset:1024 lds
mm a[220:223],v[60:63],v[92:95],a[220:223]
dt v[88:89],v135 offset:0
mm a[160:163],v[32:35],v[96:99],a[160:163]
dt v[90:91],v135 offset:1024
mm a[160:163],v[36:39],v[100:103],a[160:163]
dt v[92:93],v135 offset:8192
mm a[164:167],v[40:43],v[96:99],a[164:167]
dt v[94:95],v135 offset:9216
mm a[164:167],v[44:47],v[100:103],a[164:167]
mm a[224:227],v[48:51],v[96:99],a[224:227]
sa s56,s52,0xba00
bl v148,s[36:39],s56 offen offset:2048 lds
mm a[224:227],v[52:55],v[100:103],a[224:227]
mm a[228:231],v[56:59],v[96:99],a[228:231]
sa s56,s52,0xd600
)"
R"(bl v149,s[36:39],s56 offen offset:3072 lds
mm a[228:231],v[60:63],v[100:103],a[228:231]
dt v[96:97],v136 offset:0
mm a[168:171],v[32:35],v[104:107],a[168:171]
dt v[98:99],v136 offset:1024
mm a[168:171],v[36:39],v[108:111],a[168:171]
dt v[100:101],v136 offset:8192
mm a[172:175],v[40:43],v[104:107],a[172:175]
dt v[102:103],v136 offset:9216
mm a[172:175],v[44:47],v[108:111],a[172:175]
mm a[232:235],v[48:51],v[104:107],a[232:235]
mm a[232:235],v[52:55],v[108:111],a[232:235]
mm a[236:239],v[56:59],v[104:107],a[236:239]
mm a[236:239],v[60:63],v[108:111],a[236:239]
sa m0,s55,0x18000
dt v[104:105],v137 offset:0
)"
R"(mm a[176:179],v[32:35],v[112:115],a[176:179]
dt v[106:107],v137 offset:1024
mm a[176:179],v[36:39],v[116:119],a[176:179]
dt v[108:109],v137 offset:8192
mm a[180:183],v[40:43],v[112:115],a[180:183]
dt v[110:111],v137 offset:9216
mm a[180:183],v[44:47],v[116:119],a[180:183]
sa s56,s53,0x280000
bl v150,s[40:43],s56 offen nt lds
mm a[240:243],v[48:51],v[112:115],a[240:243]
mm a[240:243],v[52:55],v[116:119],a[240:243]
mm a[244:247],v[56:59],v[112:115],a[244:247]
sa s56,s53,0x287c00
bl v150,s[40:43],s56 offen offset:1024 nt lds
mm a[244:247],v[60:63],v[116:119],a[244:247]
dt v[112:113],v138 offset:0
)"
R"(mm a[184:187],v[32:35],v[120:123],a[184:187]
dt v[114:115],v138 offset:1024
mm a[184:187],v[36:39],v[124:127],a[184:187]
s_lshl_b32 s62, s51, 2
dt v[116:117],v138 offset:8192
mm a[188:191],v[40:43],v[120:123],a[188:191]
dt v[118:119],v138 offset:9216
mm a[188:191],v[44:47],v[124:127],a[188:191]
mm a[248:251],v[48:51],v[120:123],a[248:251]
sa s56,s53,0x28f800
bl v151,s[40:43],s56 offen offset:2048 nt lds
mm a[248:251],v[52:55],v[124:127],a[248:251]
sa s62,s62,s50
mm a[252:255],v[56:59],v[120:123],a[252:255]
mm a[252:255],v[60:63],v[124:127],a[252:255]
s_waitcnt lgkmcnt(15)
)"
R"(dt v[120:121],v139 offset:0
mm a[0:3],v[0:3],v[64:67],a[0:3]
dt v[122:123],v139 offset:1024
mm a[0:3],v[4:7],v[68:71],a[0:3]
s_mul_i32 s62, s62, 2048
dt v[124:125],v139 offset:8192
mm a[4:7],v[8:11],v[64:67],a[4:7]
dt v[126:127],v139 offset:9216
mm a[4:7],v[12:15],v[68:71],a[4:7]
sa s56,s53,0x297400
bl v151,s[40:43],s56 offen offset:3072 nt lds
mm a[64:67],v[16:19],v[64:67],a[64:67]
mm a[64:67],v[20:23],v[68:71],a[64:67]
sa s92,s72,s62
s_addc_u32 s93, s73, 0
mm a[68:71],v[24:27],v[64:67],a[68:71]
)"
R"(dr v[32:35],v130 offset:8192
mm a[68:71],v[28:31],v[68:71],a[68:71]
s_waitcnt lgkmcnt(15)
sa m0,s55,0x19000
mm a[8:11],v[0:3],v[72:75],a[8:11]
sa s56,s53,0x2a0000
bl v150,s[40:43],s56 offen nt lds
mm a[8:11],v[4:7],v[76:79],a[8:11]
mm a[12:15],v[8:11],v[72:75],a[12:15]
s_and_b32 s93, s93, 0xffff
s_lshl_b32 s59, s48, 16
mm a[12:15],v[12:15],v[76:79],a[12:15]
dr v[36:39],v131 offset:8192
mm a[72:75],v[16:19],v[72:75],a[72:75]
mm a[72:75],v[20:23],v[76:79],a[72:75]
mm a[76:79],v[24:27],v[72:75],a[76:79]
)"
R"(sa s56,s53,0x2a7c00
bl v150,s[40:43],s56 offen offset:1024 nt lds
mm a[76:79],v[28:31],v[76:79],a[76:79]
s_waitcnt lgkmcnt(15)
s_mov_b32 s94, 2048
mm a[16:19],v[0:3],v[80:83],a[16:19]
s_lshl_b32 s80, s50, 11
dr v[40:43],v130 offset:10240
mm a[16:19],v[4:7],v[84:87],a[16:19]
mm a[20:23],v[8:11],v[80:83],a[20:23]
s_mov_b32 s95, 0x20000
mm a[20:23],v[12:15],v[84:87],a[20:23]
mm a[80:83],v[16:19],v[80:83],a[80:83]
sa s56,s53,0x2af800
bl v151,s[40:43],s56 offen offset:2048 nt lds
mm a[80:83],v[20:23],v[84:87],a[80:83]
)"
R"(dr v[44:47],v131 offset:10240
mm a[84:87],v[24:27],v[80:83],a[84:87]
mm a[84:87],v[28:31],v[84:87],a[84:87]
s_waitcnt lgkmcnt(15)
sa s59,s59,s80
mm a[24:27],v[0:3],v[88:91],a[24:27]
mm a[24:27],v[4:7],v[92:95],a[24:27]
mm a[28:31],v[8:11],v[88:91],a[28:31]
dr v[48:51],v130 offset:12288
mm a[28:31],v[12:15],v[92:95],a[28:31]
sa s56,s53,0x2b7400
bl v151,s[40:43],s56 offen offset:3072 nt lds
mm a[88:91],v[16:19],v[88:91],a[88:91]
mm a[88:91],v[20:23],v[92:95],a[88:91]
mm a[92:95],v[24:27],v[88:91],a[92:95]
sa s59,s59,0x3c00000
)"
R"(mm a[92:95],v[28:31],v[92:95],a[92:95]
s_waitcnt lgkmcnt(15)
dr v[52:55],v131 offset:12288
mm a[32:35],v[0:3],v[96:99],a[32:35]
mm a[32:35],v[4:7],v[100:103],a[32:35]
s_mov_b32 s63, 0
buffer_load_dword v175, v158, s[92:95], s63 offen
mm a[36:39],v[8:11],v[96:99],a[36:39]
mm a[36:39],v[12:15],v[100:103],a[36:39]
mm a[96:99],v[16:19],v[96:99],a[96:99]
s_lshl_b32 s80, s49, 13
dr v[56:59],v130 offset:14336
mm a[96:99],v[20:23],v[100:103],a[96:99]
mm a[100:103],v[24:27],v[96:99],a[100:103]
mm a[100:103],v[28:31],v[100:103],a[100:103]
s_waitcnt lgkmcnt(15)
)"
R"(mm a[40:43],v[0:3],v[104:107],a[40:43]
mm a[40:43],v[4:7],v[108:111],a[40:43]
dr v[60:63],v131 offset:14336
mm a[44:47],v[8:11],v[104:107],a[44:47]
mm a[44:47],v[12:15],v[108:111],a[44:47]
sa s80,s80,s59
mm a[104:107],v[16:19],v[104:107],a[104:107]
mm a[104:107],v[20:23],v[108:111],a[104:107]
mm a[108:111],v[24:27],v[104:107],a[108:111]
mm a[108:111],v[28:31],v[108:111],a[108:111]
s_waitcnt lgkmcnt(12)
mm a[48:51],v[0:3],v[112:115],a[48:51]
mm a[48:51],v[4:7],v[116:119],a[48:51]
mm a[52:55],v[8:11],v[112:115],a[52:55]
mm a[52:55],v[12:15],v[116:119],a[52:55]
mm a[112:115],v[16:19],v[112:115],a[112:115]
)"
R"(mm a[112:115],v[20:23],v[116:119],a[112:115]
mm a[116:119],v[24:27],v[112:115],a[116:119]
mm a[116:119],v[28:31],v[116:119],a[116:119]
s_waitcnt lgkmcnt(8)
mm a[56:59],v[0:3],v[120:123],a[56:59]
mm a[56:59],v[4:7],v[124:127],a[56:59]
mm a[60:63],v[8:11],v[120:123],a[60:63]
mm a[60:63],v[12:15],v[124:127],a[60:63]
mm a[120:123],v[16:19],v[120:123],a[120:123]
mm a[120:123],v[20:23],v[124:127],a[120:123]
mm a[124:127],v[24:27],v[120:123],a[124:127]
mm a[124:127],v[28:31],v[124:127],a[124:127]
s_waitcnt vmcnt(9) lgkmcnt(0)
s_barrier
mm a[128:131],v[32:35],v[64:67],a[128:131]
dr v[0:3],v128 offset:0
)"
R"(mm a[128:131],v[36:39],v[68:71],a[128:131]
dr v[4:7],v129 offset:0
mm a[132:135],v[40:43],v[64:67],a[132:135]
dr v[8:11],v128 offset:2048
mm a[132:135],v[44:47],v[68:71],a[132:135]
dr v[12:15],v129 offset:2048
mm a[192:195],v[48:51],v[64:67],a[192:195]
dr v[16:19],v128 offset:4096
mm a[192:195],v[52:55],v[68:71],a[192:195]
dr v[20:23],v129 offset:4096
mm a[196:199],v[56:59],v[64:67],a[196:199]
dr v[24:27],v128 offset:6144
mm a[196:199],v[60:63],v[68:71],a[196:199]
dt v[64:65],v140 offset:0
mm a[136:139],v[32:35],v[72:75],a[136:139]
dt v[66:67],v140 offset:1024
)"
R"(mm a[136:139],v[36:39],v[76:79],a[136:139]
dt v[68:69],v140 offset:8192
mm a[140:143],v[40:43],v[72:75],a[140:143]
dt v[70:71],v140 offset:9216
mm a[140:143],v[44:47],v[76:79],a[140:143]
dr v[28:31],v129 offset:6144
mm a[200:203],v[48:51],v[72:75],a[200:203]
buffer_store_dword v181, v179, s[44:47], s61 offen sc1
mm a[200:203],v[52:55],v[76:79],a[200:203]
buffer_store_dwordx4 v[190:193], v214, s[44:47], s80 offen offset:0 sc1
mm a[204:207],v[56:59],v[72:75],a[204:207]
buffer_store_dwordx4 v[194:197], v214, s[44:47], s80 offen offset:1024 sc1
mm a[204:207],v[60:63],v[76:79],a[204:207]
sa m0,s54,0x10000
dt v[72:73],v141 offset:0
mm a[144:147],v[32:35],v[80:83],a[144:147]
)"
R"(dt v[74:75],v141 offset:1024
mm a[144:147],v[36:39],v[84:87],a[144:147]
dt v[76:77],v141 offset:8192
mm a[148:151],v[40:43],v[80:83],a[148:151]
dt v[78:79],v141 offset:9216
mm a[148:151],v[44:47],v[84:87],a[148:151]
sa s56,s52,0x280
bl v148,s[36:39],s56 offen lds
mm a[208:211],v[48:51],v[80:83],a[208:211]
sa s56,s52,0x1e80
bl v149,s[36:39],s56 offen offset:1024 lds
mm a[208:211],v[52:55],v[84:87],a[208:211]
sa s56,s52,0x3a80
bl v148,s[36:39],s56 offen offset:2048 lds
mm a[212:215],v[56:59],v[80:83],a[212:215]
sa s56,s52,0x5680
)"
R"(bl v149,s[36:39],s56 offen offset:3072 lds
mm a[212:215],v[60:63],v[84:87],a[212:215]
dt v[80:81],v142 offset:0
mm a[152:155],v[32:35],v[88:91],a[152:155]
dt v[82:83],v142 offset:1024
mm a[152:155],v[36:39],v[92:95],a[152:155]
dt v[84:85],v142 offset:8192
mm a[156:159],v[40:43],v[88:91],a[156:159]
s_lshl_b32 s81, s48, 6
dt v[86:87],v142 offset:9216
mm a[156:159],v[44:47],v[92:95],a[156:159]
sa m0,s54,0x11000
mm a[216:219],v[48:51],v[88:91],a[216:219]
sa s56,s52,0x8280
bl v148,s[36:39],s56 offen lds
mm a[216:219],v[52:55],v[92:95],a[216:219]
)"
R"(mm a[220:223],v[56:59],v[88:91],a[220:223]
sa s56,s52,0x9e80
bl v149,s[36:39],s56 offen offset:1024 lds
mm a[220:223],v[60:63],v[92:95],a[220:223]
dt v[88:89],v143 offset:0
mm a[160:163],v[32:35],v[96:99],a[160:163]
dt v[90:91],v143 offset:1024
mm a[160:163],v[36:39],v[100:103],a[160:163]
dt v[92:93],v143 offset:8192
mm a[164:167],v[40:43],v[96:99],a[164:167]
sa s81,s81,0x10800
dt v[94:95],v143 offset:9216
mm a[164:167],v[44:47],v[100:103],a[164:167]
mm a[224:227],v[48:51],v[96:99],a[224:227]
sa s56,s52,0xba80
bl v148,s[36:39],s56 offen offset:2048 lds
)"
R"(mm a[224:227],v[52:55],v[100:103],a[224:227]
mm a[228:231],v[56:59],v[96:99],a[228:231]
sa s56,s52,0xd680
bl v149,s[36:39],s56 offen offset:3072 lds
mm a[228:231],v[60:63],v[100:103],a[228:231]
dt v[96:97],v144 offset:0
mm a[168:171],v[32:35],v[104:107],a[168:171]
dt v[98:99],v144 offset:1024
mm a[168:171],v[36:39],v[108:111],a[168:171]
dt v[100:101],v144 offset:8192
mm a[172:175],v[40:43],v[104:107],a[172:175]
dt v[102:103],v144 offset:9216
mm a[172:175],v[44:47],v[108:111],a[172:175]
s_lshl_b32 s79, s49, 2
mm a[232:235],v[48:51],v[104:107],a[232:235]
mm a[232:235],v[52:55],v[108:111],a[232:235]
)"
R"(mm a[236:239],v[56:59],v[104:107],a[236:239]
mm a[236:239],v[60:63],v[108:111],a[236:239]
sa m0,s55,0x0
dt v[104:105],v145 offset:0
mm a[176:179],v[32:35],v[112:115],a[176:179]
dt v[106:107],v145 offset:1024
mm a[176:179],v[36:39],v[116:119],a[176:179]
dt v[108:109],v145 offset:8192
mm a[180:183],v[40:43],v[112:115],a[180:183]
dt v[110:111],v145 offset:9216
mm a[180:183],v[44:47],v[116:119],a[180:183]
sa s56,s53,0x300000
bl v150,s[40:43],s56 offen nt lds
mm a[240:243],v[48:51],v[112:115],a[240:243]
sa s79,s79,s81
mm a[240:243],v[52:55],v[116:119],a[240:243]
)"
R"(mm a[244:247],v[56:59],v[112:115],a[244:247]
sa s56,s53,0x307c00
bl v150,s[40:43],s56 offen offset:1024 nt lds
mm a[244:247],v[60:63],v[116:119],a[244:247]
dt v[112:113],v146 offset:0
mm a[184:187],v[32:35],v[120:123],a[184:187]
dt v[114:115],v146 offset:1024
mm a[184:187],v[36:39],v[124:127],a[184:187]
s_lshl_b32 s62, s51, 2
dt v[116:117],v146 offset:8192
mm a[188:191],v[40:43],v[120:123],a[188:191]
dt v[118:119],v146 offset:9216
mm a[188:191],v[44:47],v[124:127],a[188:191]
mm a[248:251],v[48:51],v[120:123],a[248:251]
sa s56,s53,0x30f800
bl v151,s[40:43],s56 offen offset:2048 nt lds
)"
R"(mm a[248:251],v[52:55],v[124:127],a[248:251]
s_cmp_eq_u32 s50, 0
s_cselect_b32 s79, s79, 0x4900000
sa s62,s62,s50
mm a[252:255],v[56:59],v[120:123],a[252:255]
mm a[252:255],v[60:63],v[124:127],a[252:255]
s_waitcnt lgkmcnt(15)
dt v[120:121],v147 offset:0
mm a[0:3],v[0:3],v[64:67],a[0:3]
dt v[122:123],v147 offset:1024
mm a[0:3],v[4:7],v[68:71],a[0:3]
s_mul_i32 s62, s62, 4096
dt v[124:125],v147 offset:8192
mm a[4:7],v[8:11],v[64:67],a[4:7]
dt v[126:127],v147 offset:9216
mm a[4:7],v[12:15],v[68:71],a[4:7]
)"
R"(sa s56,s53,0x317400
bl v151,s[40:43],s56 offen offset:3072 nt lds
mm a[64:67],v[16:19],v[64:67],a[64:67]
mm a[64:67],v[20:23],v[68:71],a[64:67]
sa s92,s76,s62
s_addc_u32 s93, s77, 0
mm a[68:71],v[24:27],v[64:67],a[68:71]
dr v[32:35],v128 offset:8192
v_and_b32 v198, 7, v213
mm a[68:71],v[28:31],v[68:71],a[68:71]
s_waitcnt lgkmcnt(15)
sa m0,s55,0x1000
mm a[8:11],v[0:3],v[72:75],a[8:11]
sa s56,s53,0x320000
bl v150,s[40:43],s56 offen nt lds
mm a[8:11],v[4:7],v[76:79],a[8:11]
)"
R"(mm a[12:15],v[8:11],v[72:75],a[12:15]
s_and_b32 s93, s93, 0xffff
mm a[12:15],v[12:15],v[76:79],a[12:15]
dr v[36:39],v129 offset:8192
mm a[72:75],v[16:19],v[72:75],a[72:75]
mm a[72:75],v[20:23],v[76:79],a[72:75]
mm a[76:79],v[24:27],v[72:75],a[76:79]
sa s56,s53,0x327c00
bl v150,s[40:43],s56 offen offset:1024 nt lds
mm a[76:79],v[28:31],v[76:79],a[76:79]
s_waitcnt lgkmcnt(15)
s_mov_b32 s94, 4096
v_lshlrev_b32 v198, 2, v198
mm a[16:19],v[0:3],v[80:83],a[16:19]
dr v[40:43],v128 offset:10240
mm a[16:19],v[4:7],v[84:87],a[16:19]
)"
R"(mm a[20:23],v[8:11],v[80:83],a[20:23]
s_mov_b32 s95, 0x20000
mm a[20:23],v[12:15],v[84:87],a[20:23]
mm a[80:83],v[16:19],v[80:83],a[80:83]
sa s56,s53,0x32f800
bl v151,s[40:43],s56 offen offset:2048 nt lds
mm a[80:83],v[20:23],v[84:87],a[80:83]
dr v[44:47],v129 offset:10240
mm a[84:87],v[24:27],v[80:83],a[84:87]
mm a[84:87],v[28:31],v[84:87],a[84:87]
s_waitcnt lgkmcnt(15)
mm a[24:27],v[0:3],v[88:91],a[24:27]
v_mov_b32 v199, 0
mm a[24:27],v[4:7],v[92:95],a[24:27]
mm a[28:31],v[8:11],v[88:91],a[28:31]
dr v[48:51],v128 offset:12288
)"
R"(mm a[28:31],v[12:15],v[92:95],a[28:31]
sa s56,s53,0x337400
bl v151,s[40:43],s56 offen offset:3072 nt lds
mm a[88:91],v[16:19],v[88:91],a[88:91]
mm a[88:91],v[20:23],v[92:95],a[88:91]
mm a[92:95],v[24:27],v[88:91],a[92:95]
mm a[92:95],v[28:31],v[92:95],a[92:95]
s_waitcnt lgkmcnt(15)
dr v[52:55],v129 offset:12288
mm a[32:35],v[0:3],v[96:99],a[32:35]
mm a[32:35],v[4:7],v[100:103],a[32:35]
s_mov_b32 s63, 0
buffer_load_dword v170, v158, s[92:95], s63 offen
mm a[36:39],v[8:11],v[96:99],a[36:39]
mm a[36:39],v[12:15],v[100:103],a[36:39]
mm a[96:99],v[16:19],v[96:99],a[96:99]
)"
R"(dr v[56:59],v128 offset:14336
mm a[96:99],v[20:23],v[100:103],a[96:99]
mm a[100:103],v[24:27],v[96:99],a[100:103]
mm a[100:103],v[28:31],v[100:103],a[100:103]
s_waitcnt lgkmcnt(15)
mm a[40:43],v[0:3],v[104:107],a[40:43]
mm a[40:43],v[4:7],v[108:111],a[40:43]
dr v[60:63],v129 offset:14336
mm a[44:47],v[8:11],v[104:107],a[44:47]
mm a[44:47],v[12:15],v[108:111],a[44:47]
mm a[104:107],v[16:19],v[104:107],a[104:107]
mm a[104:107],v[20:23],v[108:111],a[104:107]
mm a[108:111],v[24:27],v[104:107],a[108:111]
mm a[108:111],v[28:31],v[108:111],a[108:111]
s_waitcnt lgkmcnt(12)
mm a[48:51],v[0:3],v[112:115],a[48:51]
)"
R"(mm a[48:51],v[4:7],v[116:119],a[48:51]
mm a[52:55],v[8:11],v[112:115],a[52:55]
mm a[52:55],v[12:15],v[116:119],a[52:55]
mm a[112:115],v[16:19],v[112:115],a[112:115]
mm a[112:115],v[20:23],v[116:119],a[112:115]
mm a[116:119],v[24:27],v[112:115],a[116:119]
mm a[116:119],v[28:31],v[116:119],a[116:119]
s_waitcnt lgkmcnt(8)
mm a[56:59],v[0:3],v[120:123],a[56:59]
mm a[56:59],v[4:7],v[124:127],a[56:59]
mm a[60:63],v[8:11],v[120:123],a[60:63]
mm a[60:63],v[12:15],v[124:127],a[60:63]
mm a[120:123],v[16:19],v[120:123],a[120:123]
mm a[120:123],v[20:23],v[124:127],a[120:123]
mm a[124:127],v[24:27],v[120:123],a[124:127]
mm a[124:127],v[28:31],v[124:127],a[124:127]
)"
R"(s_waitcnt vmcnt(9) lgkmcnt(0)
s_barrier
mm a[128:131],v[32:35],v[64:67],a[128:131]
dr v[0:3],v130 offset:0
mm a[128:131],v[36:39],v[68:71],a[128:131]
dr v[4:7],v131 offset:0
mm a[132:135],v[40:43],v[64:67],a[132:135]
dr v[8:11],v130 offset:2048
mm a[132:135],v[44:47],v[68:71],a[132:135]
dr v[12:15],v131 offset:2048
mm a[192:195],v[48:51],v[64:67],a[192:195]
dr v[16:19],v130 offset:4096
mm a[192:195],v[52:55],v[68:71],a[192:195]
dr v[20:23],v131 offset:4096
mm a[196:199],v[56:59],v[64:67],a[196:199]
dr v[24:27],v130 offset:6144
)"
R"(mm a[196:199],v[60:63],v[68:71],a[196:199]
dt v[64:65],v140 offset:32768
mm a[136:139],v[32:35],v[72:75],a[136:139]
dt v[66:67],v140 offset:33792
mm a[136:139],v[36:39],v[76:79],a[136:139]
dt v[68:69],v140 offset:40960
mm a[140:143],v[40:43],v[72:75],a[140:143]
dt v[70:71],v140 offset:41984
mm a[140:143],v[44:47],v[76:79],a[140:143]
dr v[28:31],v131 offset:6144
mm a[200:203],v[48:51],v[72:75],a[200:203]
buffer_load_dword v182, v180, s[44:47], s61 offen offset:64 sc1
mm a[200:203],v[52:55],v[76:79],a[200:203]
buffer_store_dword v181, v199, s[44:47], s79 offen sc1
mm a[204:207],v[56:59],v[72:75],a[204:207]
sa m0,s54,0x0
)"
R"(mm a[204:207],v[60:63],v[76:79],a[204:207]
dt v[72:73],v141 offset:32768
mm a[144:147],v[32:35],v[80:83],a[144:147]
dt v[74:75],v141 offset:33792
mm a[144:147],v[36:39],v[84:87],a[144:147]
dt v[76:77],v141 offset:40960
mm a[148:151],v[40:43],v[80:83],a[148:151]
dt v[78:79],v141 offset:41984
mm a[148:151],v[44:47],v[84:87],a[148:151]
sa s56,s52,0x300
bl v148,s[36:39],s56 offen lds
mm a[208:211],v[48:51],v[80:83],a[208:211]
sa s56,s52,0x1f00
bl v149,s[36:39],s56 offen offset:1024 lds
mm a[208:211],v[52:55],v[84:87],a[208:211]
sa s56,s52,0x3b00
)"
R"(bl v148,s[36:39],s56 offen offset:2048 lds
mm a[212:215],v[56:59],v[80:83],a[212:215]
sa s56,s52,0x5700
bl v149,s[36:39],s56 offen offset:3072 lds
mm a[212:215],v[60:63],v[84:87],a[212:215]
dt v[80:81],v142 offset:32768
mm a[152:155],v[32:35],v[88:91],a[152:155]
dt v[82:83],v142 offset:33792
mm a[152:155],v[36:39],v[92:95],a[152:155]
dt v[84:85],v142 offset:40960
mm a[156:159],v[40:43],v[88:91],a[156:159]
dt v[86:87],v142 offset:41984
mm a[156:159],v[44:47],v[92:95],a[156:159]
sa m0,s54,0x1000
mm a[216:219],v[48:51],v[88:91],a[216:219]
sa s56,s52,0x8300
)"
R"(bl v148,s[36:39],s56 offen lds
mm a[216:219],v[52:55],v[92:95],a[216:219]
mm a[220:223],v[56:59],v[88:91],a[220:223]
sa s56,s52,0x9f00
bl v149,s[36:39],s56 offen offset:1024 lds
mm a[220:223],v[60:63],v[92:95],a[220:223]
dt v[88:89],v143 offset:32768
mm a[160:163],v[32:35],v[96:99],a[160:163]
dt v[90:91],v143 offset:33792
mm a[160:163],v[36:39],v[100:103],a[160:163]
dt v[92:93],v143 offset:40960
mm a[164:167],v[40:43],v[96:99],a[164:167]
dt v[94:95],v143 offset:41984
mm a[164:167],v[44:47],v[100:103],a[164:167]
mm a[224:227],v[48:51],v[96:99],a[224:227]
sa s56,s52,0xbb00
)"
R"(bl v148,s[36:39],s56 offen offset:2048 lds
mm a[224:227],v[52:55],v[100:103],a[224:227]
mm a[228:231],v[56:59],v[96:99],a[228:231]
sa s56,s52,0xd700
bl v149,s[36:39],s56 offen offset:3072 lds
mm a[228:231],v[60:63],v[100:103],a[228:231]
dt v[96:97],v144 offset:32768
mm a[168:171],v[32:35],v[104:107],a[168:171]
dt v[98:99],v144 offset:33792
mm a[168:171],v[36:39],v[108:111],a[168:171]
dt v[100:101],v144 offset:40960
mm a[172:175],v[40:43],v[104:107],a[172:175]
dt v[102:103],v144 offset:41984
mm a[172:175],v[44:47],v[108:111],a[172:175]
mm a[232:235],v[48:51],v[104:107],a[232:235]
mm a[232:235],v[52:55],v[108:111],a[232:235]
)"
R"(mm a[236:239],v[56:59],v[104:107],a[236:239]
mm a[236:239],v[60:63],v[108:111],a[236:239]
sa m0,s55,0x10000
dt v[104:105],v145 offset:32768
mm a[176:179],v[32:35],v[112:115],a[176:179]
dt v[106:107],v145 offset:33792
mm a[176:179],v[36:39],v[116:119],a[176:179]
dt v[108:109],v145 offset:40960
mm a[180:183],v[40:43],v[112:115],a[180:183]
dt v[110:111],v145 offset:41984
mm a[180:183],v[44:47],v[116:119],a[180:183]
sa s56,s53,0x380000
bl v150,s[40:43],s56 offen nt lds
mm a[240:243],v[48:51],v[112:115],a[240:243]
mm a[240:243],v[52:55],v[116:119],a[240:243]
mm a[244:247],v[56:59],v[112:115],a[244:247]
)"
R"(sa s56,s53,0x387c00
bl v150,s[40:43],s56 offen offset:1024 nt lds
mm a[244:247],v[60:63],v[116:119],a[244:247]
dt v[112:113],v146 offset:32768
mm a[184:187],v[32:35],v[120:123],a[184:187]
dt v[114:115],v146 offset:33792
mm a[184:187],v[36:39],v[124:127],a[184:187]
dt v[116:117],v146 offset:40960
mm a[188:191],v[40:43],v[120:123],a[188:191]
dt v[118:119],v146 offset:41984
mm a[188:191],v[44:47],v[124:127],a[188:191]
mm a[248:251],v[48:51],v[120:123],a[248:251]
sa s56,s53,0x38f800
bl v151,s[40:43],s56 offen offset:2048 nt lds
mm a[248:251],v[52:55],v[124:127],a[248:251]
mm a[252:255],v[56:59],v[120:123],a[252:255]
)"
R"(mm a[252:255],v[60:63],v[124:127],a[252:255]
s_waitcnt lgkmcnt(15)
dt v[120:121],v147 offset:32768
mm a[0:3],v[0:3],v[64:67],a[0:3]
dt v[122:123],v147 offset:33792
mm a[0:3],v[4:7],v[68:71],a[0:3]
dt v[124:125],v147 offset:40960
mm a[4:7],v[8:11],v[64:67],a[4:7]
dt v[126:127],v147 offset:41984
mm a[4:7],v[12:15],v[68:71],a[4:7]
sa s56,s53,0x397400
bl v151,s[40:43],s56 offen offset:3072 nt lds
mm a[64:67],v[16:19],v[64:67],a[64:67]
s_getpc_b64 s[88:89]
L_ct_kv1:
sa s88,s88,(L_kv_done-L_ct_kv1)
)"
R"(s_addc_u32 s89, s89, 0
mm a[64:67],v[20:23],v[68:71],a[64:67]
mm a[68:71],v[24:27],v[64:67],a[68:71]
dr v[32:35],v130 offset:8192
mm a[68:71],v[28:31],v[68:71],a[68:71]
s_waitcnt lgkmcnt(15)
sa m0,s55,0x11000
mm a[8:11],v[0:3],v[72:75],a[8:11]
sa s56,s53,0x3a0000
bl v150,s[40:43],s56 offen nt lds
mm a[8:11],v[4:7],v[76:79],a[8:11]
s_and_b32 s89, s89, 0xffff
mm a[12:15],v[8:11],v[72:75],a[12:15]
mm a[12:15],v[12:15],v[76:79],a[12:15]
dr v[36:39],v131 offset:8192
mm a[72:75],v[16:19],v[72:75],a[72:75]
)"
R"(mm a[72:75],v[20:23],v[76:79],a[72:75]
mm a[76:79],v[24:27],v[72:75],a[76:79]
sa s56,s53,0x3a7c00
bl v150,s[40:43],s56 offen offset:1024 nt lds
mm a[76:79],v[28:31],v[76:79],a[76:79]
s_waitcnt lgkmcnt(15)
mm a[16:19],v[0:3],v[80:83],a[16:19]
s_mov_b32 s90, (L_att_base-L_kv_done)
dr v[40:43],v130 offset:10240
mm a[16:19],v[4:7],v[84:87],a[16:19]
mm a[20:23],v[8:11],v[80:83],a[20:23]
mm a[20:23],v[12:15],v[84:87],a[20:23]
mm a[80:83],v[16:19],v[80:83],a[80:83]
sa s56,s53,0x3af800
bl v151,s[40:43],s56 offen offset:2048 nt lds
mm a[80:83],v[20:23],v[84:87],a[80:83]
)"
R"(dr v[44:47],v131 offset:10240
mm a[84:87],v[24:27],v[80:83],a[84:87]
s_mov_b32 s91, 0x20000
mm a[84:87],v[28:31],v[84:87],a[84:87]
s_waitcnt lgkmcnt(15)
mm a[24:27],v[0:3],v[88:91],a[24:27]
mm a[24:27],v[4:7],v[92:95],a[24:27]
mm a[28:31],v[8:11],v[88:91],a[28:31]
dr v[48:51],v130 offset:12288
mm a[28:31],v[12:15],v[92:95],a[28:31]
sa s56,s53,0x3b7400
bl v151,s[40:43],s56 offen offset:3072 nt lds
mm a[88:91],v[16:19],v[88:91],a[88:91]
mm a[88:91],v[20:23],v[92:95],a[88:91]
mm a[92:95],v[24:27],v[88:91],a[92:95]
mm a[92:95],v[28:31],v[92:95],a[92:95]
)"
R"(s_waitcnt lgkmcnt(15)
dr v[52:55],v131 offset:12288
mm a[32:35],v[0:3],v[96:99],a[32:35]
mm a[32:35],v[4:7],v[100:103],a[32:35]
buffer_load_dword v157, v215, s[88:91], 0 offen
mm a[36:39],v[8:11],v[96:99],a[36:39]
mm a[36:39],v[12:15],v[100:103],a[36:39]
mm a[96:99],v[16:19],v[96:99],a[96:99]
dr v[56:59],v130 offset:14336
mm a[96:99],v[20:23],v[100:103],a[96:99]
mm a[100:103],v[24:27],v[96:99],a[100:103]
mm a[100:103],v[28:31],v[100:103],a[100:103]
s_waitcnt lgkmcnt(15)
mm a[40:43],v[0:3],v[104:107],a[40:43]
s_mov_b32 s63, 2048
buffer_load_dword v171, v158, s[92:95], s63 offen
)"
R"(mm a[40:43],v[4:7],v[108:111],a[40:43]
dr v[60:63],v131 offset:14336
mm a[44:47],v[8:11],v[104:107],a[44:47]
mm a[44:47],v[12:15],v[108:111],a[44:47]
mm a[104:107],v[16:19],v[104:107],a[104:107]
mm a[104:107],v[20:23],v[108:111],a[104:107]
mm a[108:111],v[24:27],v[104:107],a[108:111]
mm a[108:111],v[28:31],v[108:111],a[108:111]
s_waitcnt lgkmcnt(12)
mm a[48:51],v[0:3],v[112:115],a[48:51]
mm a[48:51],v[4:7],v[116:119],a[48:51]
mm a[52:55],v[8:11],v[112:115],a[52:55]
mm a[52:55],v[12:15],v[116:119],a[52:55]
mm a[112:115],v[16:19],v[112:115],a[112:115]
mm a[112:115],v[20:23],v[116:119],a[112:115]
mm a[116:119],v[24:27],v[112:115],a[116:119]
)"
R"(mm a[116:119],v[28:31],v[116:119],a[116:119]
s_waitcnt lgkmcnt(8)
mm a[56:59],v[0:3],v[120:123],a[56:59]
mm a[56:59],v[4:7],v[124:127],a[56:59]
mm a[60:63],v[8:11],v[120:123],a[60:63]
mm a[60:63],v[12:15],v[124:127],a[60:63]
mm a[120:123],v[16:19],v[120:123],a[120:123]
mm a[120:123],v[20:23],v[124:127],a[120:123]
mm a[124:127],v[24:27],v[120:123],a[124:127]
mm a[124:127],v[28:31],v[124:127],a[124:127]
s_waitcnt vmcnt(10) lgkmcnt(0)
s_barrier
mm a[128:131],v[32:35],v[64:67],a[128:131]
dr v[0:3],v128 offset:0
mm a[128:131],v[36:39],v[68:71],a[128:131]
v_cmp_eq_u32 vcc, 1, v182
)"
R"(s_nop 1
s_cmp_eq_u32 vcc_lo, 15
s_cbranch_scc0 L_tok_51
L_tokd_51:
dr v[4:7],v129 offset:0
mm a[132:135],v[40:43],v[64:67],a[132:135]
dr v[8:11],v128 offset:2048
mm a[132:135],v[44:47],v[68:71],a[132:135]
dr v[12:15],v129 offset:2048
mm a[192:195],v[48:51],v[64:67],a[192:195]
dr v[16:19],v128 offset:4096
mm a[192:195],v[52:55],v[68:71],a[192:195]
dr v[20:23],v129 offset:4096
mm a[196:199],v[56:59],v[64:67],a[196:199]
dr v[24:27],v128 offset:6144
mm a[196:199],v[60:63],v[68:71],a[196:199]
)"
R"(dt v[64:65],v132 offset:0
mm a[136:139],v[32:35],v[72:75],a[136:139]
dt v[66:67],v132 offset:1024
mm a[136:139],v[36:39],v[76:79],a[136:139]
dt v[68:69],v132 offset:8192
mm a[140:143],v[40:43],v[72:75],a[140:143]
dt v[70:71],v132 offset:9216
mm a[140:143],v[44:47],v[76:79],a[140:143]
dr v[28:31],v129 offset:6144
mm a[200:203],v[48:51],v[72:75],a[200:203]
buffer_store_dword v181, v179, s[44:47], s61 offen sc1
mm a[200:203],v[52:55],v[76:79],a[200:203]
sa m0,s54,0x10000
mm a[204:207],v[56:59],v[72:75],a[204:207]
sa s56,s52,0x380
bl v148,s[36:39],s56 offen lds
)"
R"(mm a[204:207],v[60:63],v[76:79],a[204:207]
dt v[72:73],v133 offset:0
mm a[144:147],v[32:35],v[80:83],a[144:147]
dt v[74:75],v133 offset:1024
mm a[144:147],v[36:39],v[84:87],a[144:147]
dt v[76:77],v133 offset:8192
mm a[148:151],v[40:43],v[80:83],a[148:151]
dt v[78:79],v133 offset:9216
mm a[148:151],v[44:47],v[84:87],a[148:151]
sa s56,s52,0x1f80
bl v149,s[36:39],s56 offen offset:1024 lds
mm a[208:211],v[48:51],v[80:83],a[208:211]
sa s56,s52,0x3b80
bl v148,s[36:39],s56 offen offset:2048 lds
mm a[208:211],v[52:55],v[84:87],a[208:211]
mm a[212:215],v[56:59],v[80:83],a[212:215]
)"
R"(sa s56,s52,0x5780
bl v149,s[36:39],s56 offen offset:3072 lds
mm a[212:215],v[60:63],v[84:87],a[212:215]
dt v[80:81],v134 offset:0
mm a[152:155],v[32:35],v[88:91],a[152:155]
dt v[82:83],v134 offset:1024
mm a[152:155],v[36:39],v[92:95],a[152:155]
dt v[84:85],v134 offset:8192
mm a[156:159],v[40:43],v[88:91],a[156:159]
dt v[86:87],v134 offset:9216
mm a[156:159],v[44:47],v[92:95],a[156:159]
sa m0,s54,0x11000
mm a[216:219],v[48:51],v[88:91],a[216:219]
sa s56,s52,0x8380
bl v148,s[36:39],s56 offen lds
mm a[216:219],v[52:55],v[92:95],a[216:219]
)"
R"(mm a[220:223],v[56:59],v[88:91],a[220:223]
sa s56,s52,0x9f80
bl v149,s[36:39],s56 offen offset:1024 lds
mm a[220:223],v[60:63],v[92:95],a[220:223]
dt v[88:89],v135 offset:0
mm a[160:163],v[32:35],v[96:99],a[160:163]
dt v[90:91],v135 offset:1024
mm a[160:163],v[36:39],v[100:103],a[160:163]
dt v[92:93],v135 offset:8192
mm a[164:167],v[40:43],v[96:99],a[164:167]
dt v[94:95],v135 offset:9216
mm a[164:167],v[44:47],v[100:103],a[164:167]
mm a[224:227],v[48:51],v[96:99],a[224:227]
sa s56,s52,0xbb80
bl v148,s[36:39],s56 offen offset:2048 lds
mm a[224:227],v[52:55],v[100:103],a[224:227]
)"
R"(mm a[228:231],v[56:59],v[96:99],a[228:231]
sa s56,s52,0xd780
bl v149,s[36:39],s56 offen offset:3072 lds
mm a[228:231],v[60:63],v[100:103],a[228:231]
dt v[96:97],v136 offset:0
mm a[168:171],v[32:35],v[104:107],a[168:171]
dt v[98:99],v136 offset:1024
mm a[168:171],v[36:39],v[108:111],a[168:171]
dt v[100:101],v136 offset:8192
mm a[172:175],v[40:43],v[104:107],a[172:175]
dt v[102:103],v136 offset:9216
mm a[172:175],v[44:47],v[108:111],a[172:175]
mm a[232:235],v[48:51],v[104:107],a[232:235]
mm a[232:235],v[52:55],v[108:111],a[232:235]
mm a[236:239],v[56:59],v[104:107],a[236:239]
mm a[236:239],v[60:63],v[108:111],a[236:239]
)"
R"(dt v[104:105],v137 offset:0
mm a[176:179],v[32:35],v[112:115],a[176:179]
dt v[106:107],v137 offset:1024
mm a[176:179],v[36:39],v[116:119],a[176:179]
dt v[108:109],v137 offset:8192
mm a[180:183],v[40:43],v[112:115],a[180:183]
dt v[110:111],v137 offset:9216
mm a[180:183],v[44:47],v[116:119],a[180:183]
mm a[240:243],v[48:51],v[112:115],a[240:243]
mm a[240:243],v[52:55],v[116:119],a[240:243]
mm a[244:247],v[56:59],v[112:115],a[244:247]
mm a[244:247],v[60:63],v[116:119],a[244:247]
dt v[112:113],v138 offset:0
mm a[184:187],v[32:35],v[120:123],a[184:187]
dt v[114:115],v138 offset:1024
mm a[184:187],v[36:39],v[124:127],a[184:187]
)"
R"(dt v[116:117],v138 offset:8192
mm a[188:191],v[40:43],v[120:123],a[188:191]
dt v[118:119],v138 offset:9216
mm a[188:191],v[44:47],v[124:127],a[188:191]
mm a[248:251],v[48:51],v[120:123],a[248:251]
mm a[248:251],v[52:55],v[124:127],a[248:251]
mm a[252:255],v[56:59],v[120:123],a[252:255]
mm a[252:255],v[60:63],v[124:127],a[252:255]
s_waitcnt lgkmcnt(15)
dt v[120:121],v139 offset:0
mm a[0:3],v[0:3],v[64:67],a[0:3]
dt v[122:123],v139 offset:1024
mm a[0:3],v[4:7],v[68:71],a[0:3]
dt v[124:125],v139 offset:8192
mm a[4:7],v[8:11],v[64:67],a[4:7]
dt v[126:127],v139 offset:9216
)"
R"(mm a[4:7],v[12:15],v[68:71],a[4:7]
mm a[64:67],v[16:19],v[64:67],a[64:67]
mm a[64:67],v[20:23],v[68:71],a[64:67]
mm a[68:71],v[24:27],v[64:67],a[68:71]
dr v[32:35],v128 offset:8192
mm a[68:71],v[28:31],v[68:71],a[68:71]
s_waitcnt lgkmcnt(15)
mm a[8:11],v[0:3],v[72:75],a[8:11]
mm a[8:11],v[4:7],v[76:79],a[8:11]
mm a[12:15],v[8:11],v[72:75],a[12:15]
mm a[12:15],v[12:15],v[76:79],a[12:15]
dr v[36:39],v129 offset:8192
mm a[72:75],v[16:19],v[72:75],a[72:75]
mm a[72:75],v[20:23],v[76:79],a[72:75]
mm a[76:79],v[24:27],v[72:75],a[76:79]
mm a[76:79],v[28:31],v[76:79],a[76:79]
)"
R"(s_waitcnt lgkmcnt(15)
mm a[16:19],v[0:3],v[80:83],a[16:19]
dr v[40:43],v128 offset:10240
mm a[16:19],v[4:7],v[84:87],a[16:19]
mm a[20:23],v[8:11],v[80:83],a[20:23]
mm a[20:23],v[12:15],v[84:87],a[20:23]
mm a[80:83],v[16:19],v[80:83],a[80:83]
mm a[80:83],v[20:23],v[84:87],a[80:83]
dr v[44:47],v129 offset:10240
mm a[84:87],v[24:27],v[80:83],a[84:87]
mm a[84:87],v[28:31],v[84:87],a[84:87]
s_waitcnt lgkmcnt(15)
mm a[24:27],v[0:3],v[88:91],a[24:27]
mm a[24:27],v[4:7],v[92:95],a[24:27]
mm a[28:31],v[8:11],v[88:91],a[28:31]
dr v[48:51],v128 offset:12288
)"
R"(mm a[28:31],v[12:15],v[92:95],a[28:31]
mm a[88:91],v[16:19],v[88:91],a[88:91]
mm a[88:91],v[20:23],v[92:95],a[88:91]
mm a[92:95],v[24:27],v[88:91],a[92:95]
mm a[92:95],v[28:31],v[92:95],a[92:95]
s_waitcnt lgkmcnt(15)
dr v[52:55],v129 offset:12288
mm a[32:35],v[0:3],v[96:99],a[32:35]
mm a[32:35],v[4:7],v[100:103],a[32:35]
mm a[36:39],v[8:11],v[96:99],a[36:39]
mm a[36:39],v[12:15],v[100:103],a[36:39]
mm a[96:99],v[16:19],v[96:99],a[96:99]
dr v[56:59],v128 offset:14336
mm a[96:99],v[20:23],v[100:103],a[96:99]
mm a[100:103],v[24:27],v[96:99],a[100:103]
mm a[100:103],v[28:31],v[100:103],a[100:103]
)"
R"(s_waitcnt lgkmcnt(15)
mm a[40:43],v[0:3],v[104:107],a[40:43]
mm a[40:43],v[4:7],v[108:111],a[40:43]
dr v[60:63],v129 offset:14336
mm a[44:47],v[8:11],v[104:107],a[44:47]
mm a[44:47],v[12:15],v[108:111],a[44:47]
mm a[104:107],v[16:19],v[104:107],a[104:107]
mm a[104:107],v[20:23],v[108:111],a[104:107]
mm a[108:111],v[24:27],v[104:107],a[108:111]
mm a[108:111],v[28:31],v[108:111],a[108:111]
s_waitcnt lgkmcnt(12)
mm a[48:51],v[0:3],v[112:115],a[48:51]
s_mov_b32 s88, s64
mm a[48:51],v[4:7],v[116:119],a[48:51]
s_and_b32 s89, s65, 0xffff
mm a[52:55],v[8:11],v[112:115],a[52:55]
)"
R"(s_mov_b32 s90, 0x200000
mm a[52:55],v[12:15],v[116:119],a[52:55]
s_mov_b32 s91, 0x20000
mm a[112:115],v[16:19],v[112:115],a[112:115]
s_mov_b32 s92, s68
mm a[112:115],v[20:23],v[116:119],a[112:115]
s_and_b32 s93, s69, 0xffff
mm a[116:119],v[24:27],v[112:115],a[116:119]
s_mov_b32 s94, 0x4000
mm a[116:119],v[28:31],v[116:119],a[116:119]
s_mov_b32 s95, 0x20000
s_waitcnt lgkmcnt(8)
mm a[56:59],v[0:3],v[120:123],a[56:59]
v_lshrrev_b32 v241, 4, v213
v_lshlrev_b32 v241, 7, v241
mm a[56:59],v[4:7],v[124:127],a[56:59]
)"
R"(v_and_b32 v242, 15, v213
v_lshl_add_u32 v243, v242, 1, v241
mm a[60:63],v[8:11],v[120:123],a[60:63]
s_lshl_b32 s62, s48, 9
mm a[60:63],v[12:15],v[124:127],a[60:63]
buffer_load_ushort v220, v243, s[92:95], s62 offen offset:0
mm a[120:123],v[16:19],v[120:123],a[120:123]
buffer_load_ushort v221, v243, s[92:95], s62 offen offset:32
mm a[120:123],v[20:23],v[124:127],a[120:123]
buffer_load_ushort v222, v243, s[92:95], s62 offen offset:64
mm a[124:127],v[24:27],v[120:123],a[124:127]
buffer_load_ushort v223, v243, s[92:95], s62 offen offset:96
mm a[124:127],v[28:31],v[124:127],a[124:127]
s_waitcnt vmcnt(4) lgkmcnt(0)
s_barrier
s_lshl_b32 s58, s49, 2
)"
R"(mm a[128:131],v[32:35],v[64:67],a[128:131]
sa s58,s58,s50
dr v[0:3],v130 offset:0
mm a[128:131],v[36:39],v[68:71],a[128:131]
s_lshl_b32 s58, s58, 16
dr v[4:7],v131 offset:0
mm a[132:135],v[40:43],v[64:67],a[132:135]
s_lshl_b32 s60, s50, 11
dr v[8:11],v130 offset:2048
mm a[132:135],v[44:47],v[68:71],a[132:135]
sa s60,s60,0x20000
dr v[12:15],v131 offset:2048
mm a[192:195],v[48:51],v[64:67],a[192:195]
dr v[16:19],v130 offset:4096
v_and_b32 v241, 15, v213
mm a[192:195],v[52:55],v[68:71],a[192:195]
)"
R"(dr v[20:23],v131 offset:4096
v_lshlrev_b32 v232, 7, v241
mm a[196:199],v[56:59],v[64:67],a[196:199]
dr v[24:27],v130 offset:6144
v_lshrrev_b32 v241, 1, v241
mm a[196:199],v[60:63],v[68:71],a[196:199]
dt v[64:65],v140 offset:0
v_lshrrev_b32 v242, 4, v213
mm a[136:139],v[32:35],v[72:75],a[136:139]
dt v[66:67],v140 offset:1024
v_xor_b32 v241, v241, v242
mm a[136:139],v[36:39],v[76:79],a[136:139]
dt v[68:69],v140 offset:8192
v_lshl_add_u32 v232, v241, 4, v232
mm a[140:143],v[40:43],v[72:75],a[140:143]
dt v[70:71],v140 offset:9216
)"
R"(v_xor_b32 v233, 64, v232
mm a[140:143],v[44:47],v[76:79],a[140:143]
dr v[28:31],v131 offset:6144
v_add_u32 v232, s54, v232
mm a[200:203],v[48:51],v[72:75],a[200:203]
v_add_u32 v233, s54, v233
v_add_u32 v234, 0x10000, v232
mm a[200:203],v[52:55],v[76:79],a[200:203]
v_add_u32 v235, 0x10000, v233
v_lshrrev_b32 v241, 4, v213
mm a[204:207],v[56:59],v[72:75],a[204:207]
v_bfe_u32 v242, v213, 2, 2
v_lshl_add_u32 v243, v241, 3, v242
mm a[204:207],v[60:63],v[76:79],a[204:207]
dt v[72:73],v141 offset:0
v_lshlrev_b32 v243, 7, v243
)"
R"(mm a[144:147],v[32:35],v[80:83],a[144:147]
dt v[74:75],v141 offset:1024
v_and_b32 v241, 1, v241
mm a[144:147],v[36:39],v[84:87],a[144:147]
dt v[76:77],v141 offset:8192
v_lshrrev_b32 v242, 1, v242
mm a[148:151],v[40:43],v[80:83],a[148:151]
dt v[78:79],v141 offset:9216
v_lshl_or_b32 v241, v241, 1, v242
mm a[148:151],v[44:47],v[84:87],a[148:151]
v_lshl_add_u32 v243, v241, 5, v243
v_and_b32 v241, 3, v213
mm a[208:211],v[48:51],v[80:83],a[208:211]
v_lshl_add_u32 v243, v241, 3, v243
v_add_u32 v243, 0x20000, v243
mm a[208:211],v[52:55],v[84:87],a[208:211]
)"
R"(v_xor_b32 v236, 0, v243
v_xor_b32 v237, 32, v243
mm a[212:215],v[56:59],v[80:83],a[212:215]
v_xor_b32 v238, 64, v243
mm a[212:215],v[60:63],v[84:87],a[212:215]
dt v[80:81],v142 offset:0
mm a[152:155],v[32:35],v[88:91],a[152:155]
dt v[82:83],v142 offset:1024
mm a[152:155],v[36:39],v[92:95],a[152:155]
dt v[84:85],v142 offset:8192
mm a[156:159],v[40:43],v[88:91],a[156:159]
dt v[86:87],v142 offset:9216
mm a[156:159],v[44:47],v[92:95],a[156:159]
buffer_load_dword v199, v198, s[44:47], s81 offen sc1
mm a[216:219],v[48:51],v[88:91],a[216:219]
sa m0,s54,0x0
)"
R"(mm a[216:219],v[52:55],v[92:95],a[216:219]
sa s56,s58,0x0
bl v148,s[88:91],s56 offen lds
mm a[220:223],v[56:59],v[88:91],a[220:223]
sa s56,s58,0x1c00
bl v149,s[88:91],s56 offen offset:1024 lds
mm a[220:223],v[60:63],v[92:95],a[220:223]
dt v[88:89],v143 offset:0
mm a[160:163],v[32:35],v[96:99],a[160:163]
dt v[90:91],v143 offset:1024
mm a[160:163],v[36:39],v[100:103],a[160:163]
dt v[92:93],v143 offset:8192
mm a[164:167],v[40:43],v[96:99],a[164:167]
dt v[94:95],v143 offset:9216
mm a[164:167],v[44:47],v[100:103],a[164:167]
sa s56,s58,0x3800
)"
R"(bl v148,s[88:91],s56 offen offset:2048 lds
mm a[224:227],v[48:51],v[96:99],a[224:227]
sa s56,s58,0x5400
bl v149,s[88:91],s56 offen offset:3072 lds
mm a[224:227],v[52:55],v[100:103],a[224:227]
sa m0,s54,0x1000
mm a[228:231],v[56:59],v[96:99],a[228:231]
sa s56,s58,0x8000
bl v148,s[88:91],s56 offen lds
mm a[228:231],v[60:63],v[100:103],a[228:231]
dt v[96:97],v144 offset:0
mm a[168:171],v[32:35],v[104:107],a[168:171]
dt v[98:99],v144 offset:1024
mm a[168:171],v[36:39],v[108:111],a[168:171]
dt v[100:101],v144 offset:8192
mm a[172:175],v[40:43],v[104:107],a[172:175]
)"
R"(dt v[102:103],v144 offset:9216
mm a[172:175],v[44:47],v[108:111],a[172:175]
sa s56,s58,0x9c00
bl v149,s[88:91],s56 offen offset:1024 lds
mm a[232:235],v[48:51],v[104:107],a[232:235]
sa s56,s58,0xb800
bl v148,s[88:91],s56 offen offset:2048 lds
mm a[232:235],v[52:55],v[108:111],a[232:235]
sa s56,s58,0xd400
bl v149,s[88:91],s56 offen offset:3072 lds
mm a[236:239],v[56:59],v[104:107],a[236:239]
mm a[236:239],v[60:63],v[108:111],a[236:239]
dt v[104:105],v145 offset:0
mm a[176:179],v[32:35],v[112:115],a[176:179]
dt v[106:107],v145 offset:1024
mm a[176:179],v[36:39],v[116:119],a[176:179]
)"
R"(dt v[108:109],v145 offset:8192
mm a[180:183],v[40:43],v[112:115],a[180:183]
sa m0,s54,0x8000
dt v[110:111],v145 offset:9216
mm a[180:183],v[44:47],v[116:119],a[180:183]
sa s56,s58,0x80
bl v148,s[88:91],s56 offen lds
mm a[240:243],v[48:51],v[112:115],a[240:243]
mm a[240:243],v[52:55],v[116:119],a[240:243]
mm a[244:247],v[56:59],v[112:115],a[244:247]
mm a[244:247],v[60:63],v[116:119],a[244:247]
dt v[112:113],v146 offset:0
mm a[184:187],v[32:35],v[120:123],a[184:187]
dt v[114:115],v146 offset:1024
mm a[184:187],v[36:39],v[124:127],a[184:187]
dt v[116:117],v146 offset:8192
)"
R"(mm a[188:191],v[40:43],v[120:123],a[188:191]
dt v[118:119],v146 offset:9216
mm a[188:191],v[44:47],v[124:127],a[188:191]
sa s56,s58,0x1c80
bl v149,s[88:91],s56 offen offset:1024 lds
mm a[248:251],v[48:51],v[120:123],a[248:251]
mm a[248:251],v[52:55],v[124:127],a[248:251]
mm a[252:255],v[56:59],v[120:123],a[252:255]
mm a[252:255],v[60:63],v[124:127],a[252:255]
s_waitcnt lgkmcnt(15)
dt v[120:121],v147 offset:0
mm a[0:3],v[0:3],v[64:67],a[0:3]
dt v[122:123],v147 offset:1024
mm a[0:3],v[4:7],v[68:71],a[0:3]
dt v[124:125],v147 offset:8192
mm a[4:7],v[8:11],v[64:67],a[4:7]
)"
R"(dt v[126:127],v147 offset:9216
mm a[4:7],v[12:15],v[68:71],a[4:7]
sa s56,s58,0x3880
bl v148,s[88:91],s56 offen offset:2048 lds
mm a[64:67],v[16:19],v[64:67],a[64:67]
mm a[64:67],v[20:23],v[68:71],a[64:67]
sa s56,s58,0x5480
bl v149,s[88:91],s56 offen offset:3072 lds
mm a[68:71],v[24:27],v[64:67],a[68:71]
dr v[32:35],v130 offset:8192
mm a[68:71],v[28:31],v[68:71],a[68:71]
s_waitcnt lgkmcnt(15)
mm a[8:11],v[0:3],v[72:75],a[8:11]
mm a[8:11],v[4:7],v[76:79],a[8:11]
mm a[12:15],v[8:11],v[72:75],a[12:15]
sa m0,s54,0x9000
)"
R"(mm a[12:15],v[12:15],v[76:79],a[12:15]
dr v[36:39],v131 offset:8192
mm a[72:75],v[16:19],v[72:75],a[72:75]
sa s56,s58,0x8080
bl v148,s[88:91],s56 offen lds
mm a[72:75],v[20:23],v[76:79],a[72:75]
mm a[76:79],v[24:27],v[72:75],a[76:79]
mm a[76:79],v[28:31],v[76:79],a[76:79]
s_waitcnt lgkmcnt(15)
mm a[16:19],v[0:3],v[80:83],a[16:19]
dr v[40:43],v130 offset:10240
mm a[16:19],v[4:7],v[84:87],a[16:19]
sa s56,s58,0x9c80
bl v149,s[88:91],s56 offen offset:1024 lds
mm a[20:23],v[8:11],v[80:83],a[20:23]
mm a[20:23],v[12:15],v[84:87],a[20:23]
)"
R"(mm a[80:83],v[16:19],v[80:83],a[80:83]
mm a[80:83],v[20:23],v[84:87],a[80:83]
dr v[44:47],v131 offset:10240
mm a[84:87],v[24:27],v[80:83],a[84:87]
mm a[84:87],v[28:31],v[84:87],a[84:87]
s_waitcnt lgkmcnt(15)
mm a[24:27],v[0:3],v[88:91],a[24:27]
sa s56,s58,0xb880
bl v148,s[88:91],s56 offen offset:2048 lds
mm a[24:27],v[4:7],v[92:95],a[24:27]
mm a[28:31],v[8:11],v[88:91],a[28:31]
dr v[48:51],v130 offset:12288
mm a[28:31],v[12:15],v[92:95],a[28:31]
mm a[88:91],v[16:19],v[88:91],a[88:91]
mm a[88:91],v[20:23],v[92:95],a[88:91]
sa s56,s58,0xd480
)"
R"(bl v149,s[88:91],s56 offen offset:3072 lds
mm a[92:95],v[24:27],v[88:91],a[92:95]
mm a[92:95],v[28:31],v[92:95],a[92:95]
s_waitcnt lgkmcnt(15)
dr v[52:55],v131 offset:12288
mm a[32:35],v[0:3],v[96:99],a[32:35]
s_waitcnt vmcnt(16)
v_cmp_eq_u32 vcc, 1, v199
s_nop 1
s_cmp_eq_u64 vcc, exec
s_cbranch_scc0 L_wa1
L_wa1d:
mm a[32:35],v[4:7],v[100:103],a[32:35]
mm a[36:39],v[8:11],v[96:99],a[36:39]
mm a[36:39],v[12:15],v[100:103],a[36:39]
mm a[96:99],v[16:19],v[96:99],a[96:99]
)"
R"(dr v[56:59],v130 offset:14336
mm a[96:99],v[20:23],v[100:103],a[96:99]
sa m0,s60,0x0
mm a[100:103],v[24:27],v[96:99],a[100:103]
sa s56,s59,0x0
bl v214,s[44:47],s56 offen sc1 lds
mm a[100:103],v[28:31],v[100:103],a[100:103]
s_waitcnt lgkmcnt(15)
mm a[40:43],v[0:3],v[104:107],a[40:43]
mm a[40:43],v[4:7],v[108:111],a[40:43]
dr v[60:63],v131 offset:14336
mm a[44:47],v[8:11],v[104:107],a[44:47]
mm a[44:47],v[12:15],v[108:111],a[44:47]
sa s56,s59,0x0
bl v214,s[44:47],s56 offen offset:1024 sc1 lds
mm a[104:107],v[16:19],v[104:107],a[104:107]
)"
R"(mm a[104:107],v[20:23],v[108:111],a[104:107]
mm a[108:111],v[24:27],v[104:107],a[108:111]
mm a[108:111],v[28:31],v[108:111],a[108:111]
s_waitcnt lgkmcnt(12)
sa m0,s60,0x2000
mm a[48:51],v[0:3],v[112:115],a[48:51]
sa s56,s59,0x2000
bl v214,s[44:47],s56 offen sc1 lds
mm a[48:51],v[4:7],v[116:119],a[48:51]
mm a[52:55],v[8:11],v[112:115],a[52:55]
mm a[52:55],v[12:15],v[116:119],a[52:55]
mm a[112:115],v[16:19],v[112:115],a[112:115]
mm a[112:115],v[20:23],v[116:119],a[112:115]
sa s56,s59,0x2000
bl v214,s[44:47],s56 offen offset:1024 sc1 lds
mm a[116:119],v[24:27],v[112:115],a[116:119]
)"
R"(mm a[116:119],v[28:31],v[116:119],a[116:119]
s_waitcnt lgkmcnt(8)
mm a[56:59],v[0:3],v[120:123],a[56:59]
mm a[56:59],v[4:7],v[124:127],a[56:59]
mm a[60:63],v[8:11],v[120:123],a[60:63]
mm a[60:63],v[12:15],v[124:127],a[60:63]
mm a[120:123],v[16:19],v[120:123],a[120:123]
mm a[120:123],v[20:23],v[124:127],a[120:123]
mm a[124:127],v[24:27],v[120:123],a[124:127]
mm a[124:127],v[28:31],v[124:127],a[124:127]
s_waitcnt lgkmcnt(0)
s_barrier
mm a[128:131],v[32:35],v[64:67],a[128:131]
sa m0,s54,0x10000
mm a[128:131],v[36:39],v[68:71],a[128:131]
sa s56,s58,0x100
)"
R"(bl v148,s[88:91],s56 offen lds
mm a[132:135],v[40:43],v[64:67],a[132:135]
mm a[132:135],v[44:47],v[68:71],a[132:135]
mm a[192:195],v[48:51],v[64:67],a[192:195]
sa s56,s58,0x1d00
bl v149,s[88:91],s56 offen offset:1024 lds
mm a[192:195],v[52:55],v[68:71],a[192:195]
mm a[196:199],v[56:59],v[64:67],a[196:199]
mm a[196:199],v[60:63],v[68:71],a[196:199]
sa s56,s58,0x3900
bl v148,s[88:91],s56 offen offset:2048 lds
mm a[136:139],v[32:35],v[72:75],a[136:139]
mm a[136:139],v[36:39],v[76:79],a[136:139]
mm a[140:143],v[40:43],v[72:75],a[140:143]
sa s56,s58,0x5500
bl v149,s[88:91],s56 offen offset:3072 lds
)"
R"(mm a[140:143],v[44:47],v[76:79],a[140:143]
mm a[200:203],v[48:51],v[72:75],a[200:203]
sa m0,s54,0x11000
mm a[200:203],v[52:55],v[76:79],a[200:203]
sa s56,s58,0x8100
bl v148,s[88:91],s56 offen lds
mm a[204:207],v[56:59],v[72:75],a[204:207]
mm a[204:207],v[60:63],v[76:79],a[204:207]
mm a[144:147],v[32:35],v[80:83],a[144:147]
sa s56,s58,0x9d00
bl v149,s[88:91],s56 offen offset:1024 lds
mm a[144:147],v[36:39],v[84:87],a[144:147]
mm a[148:151],v[40:43],v[80:83],a[148:151]
mm a[148:151],v[44:47],v[84:87],a[148:151]
sa s56,s58,0xb900
bl v148,s[88:91],s56 offen offset:2048 lds
)"
R"(mm a[208:211],v[48:51],v[80:83],a[208:211]
mm a[208:211],v[52:55],v[84:87],a[208:211]
mm a[212:215],v[56:59],v[80:83],a[212:215]
sa s56,s58,0xd500
bl v149,s[88:91],s56 offen offset:3072 lds
mm a[212:215],v[60:63],v[84:87],a[212:215]
mm a[152:155],v[32:35],v[88:91],a[152:155]
sa m0,s60,0x4000
mm a[152:155],v[36:39],v[92:95],a[152:155]
sa s56,s59,0x4000
bl v214,s[44:47],s56 offen sc1 lds
mm a[156:159],v[40:43],v[88:91],a[156:159]
mm a[156:159],v[44:47],v[92:95],a[156:159]
mm a[216:219],v[48:51],v[88:91],a[216:219]
sa s56,s59,0x4000
bl v214,s[44:47],s56 offen offset:1024 sc1 lds
)"
R"(mm a[216:219],v[52:55],v[92:95],a[216:219]
mm a[220:223],v[56:59],v[88:91],a[220:223]
sa m0,s54,0x18000
mm a[220:223],v[60:63],v[92:95],a[220:223]
sa s56,s58,0x180
bl v148,s[88:91],s56 offen lds
mm a[160:163],v[32:35],v[96:99],a[160:163]
mm a[160:163],v[36:39],v[100:103],a[160:163]
mm a[164:167],v[40:43],v[96:99],a[164:167]
sa s56,s58,0x1d80
bl v149,s[88:91],s56 offen offset:1024 lds
mm a[164:167],v[44:47],v[100:103],a[164:167]
mm a[224:227],v[48:51],v[96:99],a[224:227]
mm a[224:227],v[52:55],v[100:103],a[224:227]
sa s56,s58,0x3980
bl v148,s[88:91],s56 offen offset:2048 lds
)"
R"(mm a[228:231],v[56:59],v[96:99],a[228:231]
mm a[228:231],v[60:63],v[100:103],a[228:231]
mm a[168:171],v[32:35],v[104:107],a[168:171]
sa s56,s58,0x5580
bl v149,s[88:91],s56 offen offset:3072 lds
mm a[168:171],v[36:39],v[108:111],a[168:171]
mm a[172:175],v[40:43],v[104:107],a[172:175]
sa m0,s54,0x19000
mm a[172:175],v[44:47],v[108:111],a[172:175]
sa s56,s58,0x8180
bl v148,s[88:91],s56 offen lds
mm a[232:235],v[48:51],v[104:107],a[232:235]
mm a[232:235],v[52:55],v[108:111],a[232:235]
mm a[236:239],v[56:59],v[104:107],a[236:239]
sa s56,s58,0x9d80
bl v149,s[88:91],s56 offen offset:1024 lds
)"
R"(mm a[236:239],v[60:63],v[108:111],a[236:239]
mm a[176:179],v[32:35],v[112:115],a[176:179]
mm a[176:179],v[36:39],v[116:119],a[176:179]
sa s56,s58,0xb980
bl v148,s[88:91],s56 offen offset:2048 lds
mm a[180:183],v[40:43],v[112:115],a[180:183]
mm a[180:183],v[44:47],v[116:119],a[180:183]
mm a[240:243],v[48:51],v[112:115],a[240:243]
sa s56,s58,0xd580
bl v149,s[88:91],s56 offen offset:3072 lds
mm a[240:243],v[52:55],v[116:119],a[240:243]
mm a[244:247],v[56:59],v[112:115],a[244:247]
sa m0,s60,0x6000
mm a[244:247],v[60:63],v[116:119],a[244:247]
sa s56,s59,0x6000
bl v214,s[44:47],s56 offen sc1 lds
)"
R"(mm a[184:187],v[32:35],v[120:123],a[184:187]
mm a[184:187],v[36:39],v[124:127],a[184:187]
mm a[188:191],v[40:43],v[120:123],a[188:191]
sa s56,s59,0x6000
bl v214,s[44:47],s56 offen offset:1024 sc1 lds
mm a[188:191],v[44:47],v[124:127],a[188:191]
mm a[248:251],v[48:51],v[120:123],a[248:251]
mm a[248:251],v[52:55],v[124:127],a[248:251]
mm a[252:255],v[56:59],v[120:123],a[252:255]
mm a[252:255],v[60:63],v[124:127],a[252:255]
L_kv_done:
s_waitcnt vmcnt(22)
s_barrier
dt v[32:33],v236 offset:0
dt v[34:35],v236 offset:512
dt v[36:37],v237 offset:0
)"
R"(dt v[38:39],v237 offset:512
dt v[40:41],v238 offset:0
dt v[42:43],v238 offset:512
dr v[0:3],v232 offset:0
dr v[4:7],v232 offset:2048
dr v[8:11],v232 offset:4096
dr v[12:15],v232 offset:6144
s_lshl_b32 s61, s48, 3
sa s61,s61,s49
s_lshl_b32 s61, s61, 1
s_and_b32 s62, s50, 1
sa s61,s61,s62
s_lshl_b32 s61, s61, 16
s_lshr_b32 s62, s50, 1
s_lshl_b32 s62, s62, 14
sa s61,s61,s62
)"
R"(sa s61,s61,0x100000
v_lshl_or_b32 v220, v221, 16, v220
v_lshl_or_b32 v221, v223, 16, v222
s_waitcnt lgkmcnt(0)
mm v[164:167],v[32:35],v[0:3],0
s_mov_b32 s36, s72
dt v[44:45],v236 offset:4096
ar v64,a160
mm v[180:183],v[36:39],v[0:3],0
s_and_b32 s37, s73, 0xffff
dt v[46:47],v236 offset:4608
ar v65,a161
mm v[196:199],v[0:3],v[40:43],0
s_mov_b32 s38, 0x200000
dt v[48:49],v237 offset:4096
cv v66,v64,v65
)"
R"(mm v[168:171],v[32:35],v[4:7],0
s_mov_b32 s39, 0x20000
dt v[50:51],v237 offset:4608
ar v67,a162
mm v[184:187],v[36:39],v[4:7],0
s_mov_b32 s40, s76
dt v[52:53],v238 offset:4096
ar v68,a163
mm v[200:203],v[4:7],v[40:43],0
s_and_b32 s41, s77, 0xffff
dt v[54:55],v238 offset:4608
v_accvgpr_write_b32 a160, v66
mm v[172:175],v[32:35],v[8:11],0
s_mov_b32 s42, 0x400000
dr v[16:19],v233 offset:0
cv v69,v67,v68
)"
R"(mm v[188:191],v[36:39],v[8:11],0
s_mov_b32 s43, 0x20000
dr v[20:23],v233 offset:2048
v_accvgpr_write_b32 a161, v69
mm v[204:207],v[8:11],v[40:43],0
s_lshl_b32 s53, s51, 2
dr v[24:27],v233 offset:4096
ar v64,a164
mm v[176:179],v[32:35],v[12:15],0
sa s53,s53,s50
dr v[28:31],v233 offset:6144
ar v65,a165
mm v[192:195],v[36:39],v[12:15],0
s_lshl_b32 s53, s53, 11
cv v66,v64,v65
ar v67,a166
)"
R"(mm v[208:211],v[12:15],v[40:43],0
s_waitcnt vmcnt(20) lgkmcnt(0)
s_barrier
ar v68,a167
mm v[164:167],v[44:47],v[16:19],v[164:167]
sa m0,s54,0x0
dt v[32:33],v236 offset:8192
v_accvgpr_write_b32 a162, v66
mm v[180:183],v[48:51],v[16:19],v[180:183]
dt v[34:35],v236 offset:8704
cv v69,v67,v68
mm v[196:199],v[16:19],v[52:55],v[196:199]
dt v[36:37],v237 offset:8192
sa s56,s58,0x200
bl v148,s[88:91],s56 offen lds
v_accvgpr_write_b32 a163, v69
)"
R"(mm v[168:171],v[44:47],v[20:23],v[168:171]
dt v[38:39],v237 offset:8704
ar v64,a168
ar v65,a169
mm v[184:187],v[48:51],v[20:23],v[184:187]
dt v[40:41],v238 offset:8192
sa s56,s58,0x1e00
bl v149,s[88:91],s56 offen offset:1024 lds
cv v66,v64,v65
ar v67,a170
mm v[200:203],v[20:23],v[52:55],v[200:203]
dt v[42:43],v238 offset:8704
ar v68,a171
v_accvgpr_write_b32 a168, v66
mm v[172:175],v[44:47],v[24:27],v[172:175]
dr v[0:3],v232 offset:32768
)"
R"(sa s56,s58,0x3a00
bl v148,s[88:91],s56 offen offset:2048 lds
cv v69,v67,v68
v_accvgpr_write_b32 a169, v69
mm v[188:191],v[48:51],v[24:27],v[188:191]
dr v[4:7],v232 offset:34816
ar v64,a172
ar v65,a173
mm v[204:207],v[24:27],v[52:55],v[204:207]
s_lshl_b32 s96, s49, 1
dr v[8:11],v232 offset:36864
sa s56,s58,0x5600
bl v149,s[88:91],s56 offen offset:3072 lds
cv v66,v64,v65
v_accvgpr_write_b32 a170, v66
mm v[176:179],v[44:47],v[28:31],v[176:179]
)"
R"(sa m0,s54,0x1000
dr v[12:15],v232 offset:38912
ar v67,a174
ar v68,a175
mm v[192:195],v[48:51],v[28:31],v[192:195]
sa s56,s58,0x8200
bl v148,s[88:91],s56 offen lds
cv v69,v67,v68
v_accvgpr_write_b32 a171, v69
mm v[208:211],v[28:31],v[52:55],v[208:211]
s_waitcnt lgkmcnt(0)
ar v64,a176
ar v65,a177
mm v[164:167],v[32:35],v[0:3],v[164:167]
sa s96,s96,2
dt v[44:45],v236 offset:12288
)"
R"(sa s56,s58,0x9e00
bl v149,s[88:91],s56 offen offset:1024 lds
cv v66,v64,v65
ar v67,a178
mm v[180:183],v[36:39],v[0:3],v[180:183]
dt v[46:47],v236 offset:12800
ar v68,a179
v_accvgpr_write_b32 a176, v66
mm v[196:199],v[0:3],v[40:43],v[196:199]
dt v[48:49],v237 offset:12288
cv v69,v67,v68
ar v56,a0
mm v[168:171],v[32:35],v[4:7],v[168:171]
dt v[50:51],v237 offset:12800
sa s56,s58,0xba00
bl v148,s[88:91],s56 offen offset:2048 lds
)"
R"(ar v57,a1
v_accvgpr_write_b32 a177, v69
mm v[184:187],v[36:39],v[4:7],v[184:187]
dt v[52:53],v238 offset:12288
ar v64,a180
ar v65,a181
mm v[200:203],v[4:7],v[40:43],v[200:203]
dt v[54:55],v238 offset:12800
sa s56,s58,0xd600
bl v149,s[88:91],s56 offen offset:3072 lds
cv v66,v64,v65
cv v72,v56,v57
mm v[172:175],v[32:35],v[8:11],v[172:175]
sa m0,s60,0x0
dr v[16:19],v233 offset:32768
v_accvgpr_write_b32 a178, v66
)"
R"(ar v67,a182
mm v[188:191],v[36:39],v[8:11],v[188:191]
dr v[20:23],v233 offset:34816
sa s56,s59,0x8000
bl v214,s[44:47],s56 offen sc1 lds
ar v68,a183
ar v58,a2
mm v[204:207],v[8:11],v[40:43],v[204:207]
dr v[24:27],v233 offset:36864
ar v59,a3
v_add_u32 v244, 2, v213
mm v[176:179],v[32:35],v[12:15],v[176:179]
dr v[28:31],v233 offset:38912
sa s56,s59,0x8000
bl v214,s[44:47],s56 offen offset:1024 sc1 lds
cv v69,v67,v68
)"
R"(v_lshlrev_b32 v245, 1, v244
mm v[192:195],v[36:39],v[12:15],v[192:195]
v_accvgpr_write_b32 a179, v69
cv v73,v58,v59
mm v[208:211],v[12:15],v[40:43],v[208:211]
s_waitcnt vmcnt(20) lgkmcnt(0)
s_barrier
ar v64,a184
ar v65,a185
ar v56,a4
ar v57,a5
cv v74,v56,v57
ar v58,a6
ar v59,a7
cv v75,v58,v59
sa s56,s61,0x0
)"
R"(buffer_store_dwordx4 v[72:75], v214, s[44:47], s56 offen offset:0 sc1
mm v[164:167],v[44:47],v[16:19],v[164:167]
sa m0,s54,0x8000
dt v[32:33],v236 offset:16384
v_subrev_u32 v245, s96, v245
cv v66,v64,v65
mm v[180:183],v[48:51],v[16:19],v[180:183]
dt v[34:35],v236 offset:16896
ar v67,a186
ar v68,a187
mm v[196:199],v[16:19],v[52:55],v[196:199]
dt v[36:37],v237 offset:16384
sa s56,s58,0x280
bl v148,s[88:91],s56 offen lds
v_cmp_gt_u32 vcc, s96, v244
v_accvgpr_write_b32 a184, v66
)"
R"(mm v[168:171],v[44:47],v[20:23],v[168:171]
dt v[38:39],v237 offset:16896
cv v69,v67,v68
v_accvgpr_write_b32 a185, v69
mm v[184:187],v[48:51],v[20:23],v[184:187]
dt v[40:41],v238 offset:16384
sa s56,s58,0x1e80
bl v149,s[88:91],s56 offen offset:1024 lds
ar v64,a188
ar v65,a189
mm v[200:203],v[20:23],v[52:55],v[200:203]
dt v[42:43],v238 offset:16896
v_cndmask_b32 v245, v245, v244, vcc
cv v66,v64,v65
mm v[172:175],v[44:47],v[24:27],v[172:175]
dr v[0:3],v234 offset:0
)"
R"(sa s56,s58,0x3a80
bl v148,s[88:91],s56 offen offset:2048 lds
ar v67,a190
ar v68,a191
mm v[188:191],v[48:51],v[24:27],v[188:191]
dr v[4:7],v234 offset:2048
v_lshlrev_b32 v217, 15, v245
v_accvgpr_write_b32 a186, v66
mm v[204:207],v[24:27],v[52:55],v[204:207]
s_lshl_b32 s97, s50, 13
dr v[8:11],v234 offset:4096
sa s56,s58,0x5680
bl v149,s[88:91],s56 offen offset:3072 lds
cv v69,v67,v68
v_accvgpr_write_b32 a187, v69
mm v[176:179],v[44:47],v[28:31],v[176:179]
)"
R"(sa m0,s54,0x9000
dr v[12:15],v234 offset:6144
ar v64,a192
ar v65,a193
mm v[192:195],v[48:51],v[28:31],v[192:195]
sa s56,s58,0x8280
bl v148,s[88:91],s56 offen lds
v_mov_b32 v245, s97
cv v66,v64,v65
mm v[208:211],v[28:31],v[52:55],v[208:211]
s_waitcnt lgkmcnt(0)
ar v67,a194
ar v68,a195
mm v[164:167],v[32:35],v[0:3],v[164:167]
dt v[44:45],v236 offset:20480
sa s56,s58,0x9e80
)"
R"(bl v149,s[88:91],s56 offen offset:1024 lds
v_accvgpr_write_b32 a192, v66
cv v69,v67,v68
mm v[180:183],v[36:39],v[0:3],v[180:183]
dt v[46:47],v236 offset:20992
v_lshlrev_b32 v246, 1, v245
v_accvgpr_write_b32 a193, v69
mm v[196:199],v[0:3],v[40:43],v[196:199]
dt v[48:49],v237 offset:20480
ar v64,a196
ar v65,a197
mm v[168:171],v[32:35],v[4:7],v[168:171]
dt v[50:51],v237 offset:20992
sa s56,s58,0xba80
bl v148,s[88:91],s56 offen offset:2048 lds
v_cndmask_b32 v245, v246, v245, vcc
)"
R"(cv v66,v64,v65
mm v[184:187],v[36:39],v[4:7],v[184:187]
dt v[52:53],v238 offset:20480
v_accvgpr_write_b32 a194, v66
ar v67,a198
mm v[200:203],v[4:7],v[40:43],v[200:203]
dt v[54:55],v238 offset:20992
sa s56,s58,0xd680
bl v149,s[88:91],s56 offen offset:3072 lds
ar v68,a199
v_add_u32 v217, v217, v245
mm v[172:175],v[32:35],v[8:11],v[172:175]
s_lshl_b32 s97, s48, 20
dr v[16:19],v235 offset:0
cv v69,v67,v68
v_accvgpr_write_b32 a195, v69
)"
R"(mm v[188:191],v[36:39],v[8:11],v[188:191]
sa s97,s97,0x100000
dr v[20:23],v235 offset:2048
ar v64,a200
ar v65,a201
sa m0,s60,0x2000
sa s56,s59,0xa000
bl v214,s[44:47],s56 offen sc1 lds
mm v[204:207],v[8:11],v[40:43],v[204:207]
dr v[24:27],v235 offset:4096
cv v66,v64,v65
ar v67,a202
mm v[176:179],v[32:35],v[12:15],v[176:179]
dr v[28:31],v235 offset:6144
sa s56,s59,0xa000
bl v214,s[44:47],s56 offen offset:1024 sc1 lds
)"
R"(ar v68,a203
v_add_u32 v217, s97, v217
mm v[192:195],v[36:39],v[12:15],v[192:195]
v_accvgpr_write_b32 a200, v66
cv v69,v67,v68
mm v[208:211],v[12:15],v[40:43],v[208:211]
s_waitcnt vmcnt(21) lgkmcnt(0)
s_barrier
v_accvgpr_write_b32 a201, v69
ar v64,a204
ar v56,a8
ar v57,a9
cv v76,v56,v57
ar v58,a10
ar v59,a11
cv v77,v58,v59
)"
R"(ar v56,a12
ar v57,a13
cv v78,v56,v57
ar v58,a14
ar v59,a15
cv v79,v58,v59
sa s56,s61,0x0
buffer_store_dwordx4 v[76:79], v214, s[44:47], s56 offen offset:1024 sc1
mm v[164:167],v[44:47],v[16:19],v[164:167]
s_sub_u32 s62, 15, s49
dt v[32:33],v236 offset:24576
ar v65,a205
v_and_b32 v246, 1, v213
mm v[180:183],v[48:51],v[16:19],v[180:183]
sa m0,s54,0x10000
dt v[34:35],v236 offset:25088
)"
R"(cv v66,v64,v65
v_lshl_add_u32 v218, v246, 16, v245
mm v[196:199],v[16:19],v[52:55],v[196:199]
dt v[36:37],v237 offset:24576
sa s56,s58,0x300
bl v148,s[88:91],s56 offen lds
v_accvgpr_write_b32 a202, v66
ar v67,a206
mm v[168:171],v[44:47],v[20:23],v[168:171]
dt v[38:39],v237 offset:25088
ar v68,a207
cv v69,v67,v68
mm v[184:187],v[48:51],v[20:23],v[184:187]
dt v[40:41],v238 offset:24576
sa s56,s58,0x1f00
bl v149,s[88:91],s56 offen offset:1024 lds
)"
R"(v_mov_b32 v216, (L_att_full16-L_att_base)
v_accvgpr_write_b32 a203, v69
mm v[200:203],v[20:23],v[52:55],v[200:203]
dt v[42:43],v238 offset:25088
v_mov_b32 v245, (L_att_full8-L_att_base)
ar v64,a208
mm v[172:175],v[44:47],v[24:27],v[172:175]
dr v[0:3],v234 offset:32768
sa s56,s58,0x3b00
bl v148,s[88:91],s56 offen offset:2048 lds
ar v65,a209
cv v66,v64,v65
mm v[188:191],v[48:51],v[24:27],v[188:191]
dr v[4:7],v234 offset:34816
ar v67,a210
ar v68,a211
)"
R"(mm v[204:207],v[24:27],v[52:55],v[204:207]
dr v[8:11],v234 offset:36864
sa s56,s58,0x5700
bl v149,s[88:91],s56 offen offset:3072 lds
v_cndmask_b32 v216, v216, v245, vcc
v_accvgpr_write_b32 a208, v66
mm v[176:179],v[44:47],v[28:31],v[176:179]
sa m0,s54,0x11000
dr v[12:15],v234 offset:38912
cv v69,v67,v68
v_cmp_lt_u32 vcc, 16, v244
mm v[192:195],v[48:51],v[28:31],v[192:195]
sa s56,s58,0x8300
bl v148,s[88:91],s56 offen lds
v_accvgpr_write_b32 a209, v69
ar v64,a212
)"
R"(mm v[208:211],v[28:31],v[52:55],v[208:211]
s_waitcnt lgkmcnt(0)
ar v65,a213
cv v66,v64,v65
mm v[164:167],v[32:35],v[0:3],v[164:167]
dt v[44:45],v236 offset:28672
sa s56,s58,0x9f00
bl v149,s[88:91],s56 offen offset:1024 lds
v_mov_b32 v245, (L_att_full0-L_att_base)
v_accvgpr_write_b32 a210, v66
mm v[180:183],v[36:39],v[0:3],v[180:183]
dt v[46:47],v236 offset:29184
ar v67,a214
ar v68,a215
mm v[196:199],v[0:3],v[40:43],v[196:199]
s_cmp_lt_u32 s50, 2
)"
R"(s_cselect_b32 s57, s62, s49
dt v[48:49],v237 offset:28672
v_cndmask_b32 v216, v216, v245, vcc
mm v[168:171],v[32:35],v[4:7],v[168:171]
s_lshl_b32 s52, s57, 17
dt v[50:51],v237 offset:29184
sa s56,s58,0xbb00
bl v148,s[88:91],s56 offen offset:2048 lds
cv v69,v67,v68
v_accvgpr_write_b32 a211, v69
mm v[184:187],v[36:39],v[4:7],v[184:187]
s_and_b32 s62, s50, 1
dt v[52:53],v238 offset:28672
v_add_u32 v244, 1, v213
ar v64,a216
mm v[200:203],v[4:7],v[40:43],v[200:203]
)"
R"(s_lshl_b32 s62, s62, 14
dt v[54:55],v238 offset:29184
sa s56,s58,0xd700
bl v149,s[88:91],s56 offen offset:3072 lds
ar v65,a217
v_and_b32 v245, 1, v244
mm v[172:175],v[32:35],v[8:11],v[172:175]
sa s52,s52,s62
dr v[16:19],v235 offset:32768
cv v66,v64,v65
ar v67,a218
mm v[188:191],v[36:39],v[8:11],v[188:191]
s_lshl_b32 s53, s50, 17
dr v[20:23],v235 offset:34816
ar v68,a219
v_accvgpr_write_b32 a216, v66
)"
R"(sa m0,s60,0x4000
sa s56,s59,0xc000
bl v214,s[44:47],s56 offen sc1 lds
mm v[204:207],v[8:11],v[40:43],v[204:207]
s_lshl_b32 s62, s48, 8
dr v[24:27],v235 offset:36864
cv v69,v67,v68
v_lshlrev_b32 v219, 16, v245
mm v[176:179],v[32:35],v[12:15],v[176:179]
s_lshr_b32 s97, s50, 1
dr v[28:31],v235 offset:38912
sa s56,s59,0xc000
bl v214,s[44:47],s56 offen offset:1024 sc1 lds
v_accvgpr_write_b32 a217, v69
ar v64,a220
mm v[192:195],v[36:39],v[12:15],v[192:195]
)"
R"(sa s53,s53,s62
ar v65,a221
cv v66,v64,v65
mm v[208:211],v[12:15],v[40:43],v[208:211]
s_lshl_b32 s55, s50, 12
s_waitcnt vmcnt(22) lgkmcnt(0)
s_barrier
ar v67,a222
ar v68,a223
ar v56,a16
ar v57,a17
cv v72,v56,v57
ar v58,a18
ar v59,a19
cv v73,v58,v59
ar v56,a20
)"
R"(ar v57,a21
cv v74,v56,v57
ar v58,a22
ar v59,a23
cv v75,v58,v59
sa s56,s61,0x0
buffer_store_dwordx4 v[72:75], v214, s[44:47], s56 offen offset:2048 sc1
mm v[164:167],v[44:47],v[16:19],v[164:167]
s_lshl_b32 s97, s97, 15
dt v[32:33],v236 offset:0
v_accvgpr_write_b32 a218, v66
cv v69,v67,v68
mm v[180:183],v[48:51],v[16:19],v[180:183]
sa s55,s55,0x8000
dt v[34:35],v236 offset:512
v_accvgpr_write_b32 a219, v69
)"
R"(v_add_u32 v245, s97, v219
mm v[196:199],v[16:19],v[52:55],v[196:199]
sa m0,s54,0x18000
dt v[36:37],v237 offset:0
ar v64,a224
ar v65,a225
sa s56,s58,0x380
bl v148,s[88:91],s56 offen lds
mm v[168:171],v[44:47],v[20:23],v[168:171]
dt v[38:39],v237 offset:512
v_and_b32 v152, 15, v213
v_cmp_gt_u32 vcc, s96, v244
mm v[184:187],v[48:51],v[20:23],v[184:187]
dt v[40:41],v238 offset:0
sa s56,s58,0x1f80
bl v149,s[88:91],s56 offen offset:1024 lds
)"
R"(cv v66,v64,v65
ar v67,a226
mm v[200:203],v[20:23],v[52:55],v[200:203]
dt v[42:43],v238 offset:512
ar v68,a227
v_lshlrev_b32 v128, 7, v152
mm v[172:175],v[44:47],v[24:27],v[172:175]
dr v[0:3],v232 offset:0
sa s56,s58,0x3b80
bl v148,s[88:91],s56 offen offset:2048 lds
v_accvgpr_write_b32 a224, v66
cv v69,v67,v68
mm v[188:191],v[48:51],v[24:27],v[188:191]
dr v[4:7],v232 offset:2048
v_lshrrev_b32 v152, 1, v152
v_cndmask_b32 v219, v245, v219, vcc
)"
R"(mm v[204:207],v[24:27],v[52:55],v[204:207]
s_lshl_b32 s63, s50, 3
dr v[8:11],v232 offset:4096
v_accvgpr_write_b32 a225, v69
ar v64,a228
mm v[176:179],v[44:47],v[28:31],v[176:179]
sa s63,s63,s49
dr v[12:15],v232 offset:6144
sa s56,s58,0x5780
bl v149,s[88:91],s56 offen offset:3072 lds
ar v65,a229
v_lshrrev_b32 v153, 4, v213
mm v[192:195],v[48:51],v[28:31],v[192:195]
s_mov_b32 s99, (L_att_a0_0-L_att_base)
cv v66,v64,v65
v_xor_b32 v152, v152, v153
)"
R"(mm v[208:211],v[28:31],v[52:55],v[208:211]
s_waitcnt lgkmcnt(0)
v_accvgpr_write_b32 a226, v66
ar v67,a230
sa m0,s54,0x19000
sa s56,s58,0x8380
bl v148,s[88:91],s56 offen lds
mm v[164:167],v[32:35],v[0:3],v[164:167]
dt v[44:45],v236 offset:4096
ar v68,a231
v_lshl_add_u32 v128, v152, 4, v128
mm v[180:183],v[36:39],v[0:3],v[180:183]
dt v[46:47],v236 offset:4608
cv v69,v67,v68
v_xor_b32 v129, 64, v128
mm v[196:199],v[0:3],v[40:43],v[196:199]
)"
R"(dt v[48:49],v237 offset:4096
sa s56,s58,0x9f80
bl v149,s[88:91],s56 offen offset:1024 lds
v_accvgpr_write_b32 a227, v69
v_add_u32 v128, s54, v128
mm v[168:171],v[32:35],v[4:7],v[168:171]
dt v[50:51],v237 offset:4608
ar v64,a232
ar v65,a233
mm v[184:187],v[36:39],v[4:7],v[184:187]
s_cmp_eq_u32 s50, 1
s_cmov_b32 s99, (L_att_a1_0-L_att_base)
dt v[52:53],v238 offset:4096
sa s56,s58,0xbb80
bl v148,s[88:91],s56 offen offset:2048 lds
v_add_u32 v129, s54, v129
)"
R"(mm v[200:203],v[4:7],v[40:43],v[200:203]
dt v[54:55],v238 offset:4608
cv v66,v64,v65
ar v67,a234
mm v[172:175],v[32:35],v[8:11],v[172:175]
dr v[16:19],v233 offset:0
sa s56,s58,0xd780
bl v149,s[88:91],s56 offen offset:3072 lds
ar v68,a235
v_add_u32 v130, 0x18000, v128
mm v[188:191],v[36:39],v[8:11],v[188:191]
sa m0,s60,0x6000
dr v[20:23],v233 offset:2048
v_accvgpr_write_b32 a232, v66
cv v69,v67,v68
mm v[204:207],v[8:11],v[40:43],v[204:207]
)"
R"(s_cmp_eq_u32 s50, 2
s_cmov_b32 s99, (L_att_b0_0-L_att_base)
dr v[24:27],v233 offset:4096
v_add_u32 v131, 0x18000, v129
mm v[176:179],v[32:35],v[12:15],v[176:179]
dr v[28:31],v233 offset:6144
sa s56,s59,0xe000
bl v214,s[44:47],s56 offen sc1 lds
v_accvgpr_write_b32 a233, v69
ar v64,a236
mm v[192:195],v[36:39],v[12:15],v[192:195]
ar v65,a237
v_lshrrev_b32 v152, 4, v213
mm v[208:211],v[12:15],v[40:43],v[208:211]
s_cmp_eq_u32 s50, 3
s_cmov_b32 s99, (L_att_b1_0-L_att_base)
)"
R"(s_waitcnt vmcnt(21) lgkmcnt(0)
s_barrier
sa s56,s59,0xe000
bl v214,s[44:47],s56 offen offset:1024 sc1 lds
cv v66,v64,v65
mm v[164:167],v[44:47],v[16:19],v[164:167]
dt v[32:33],v236 offset:8192
v_bfe_u32 v153, v213, 2, 2
v_accvgpr_write_b32 a234, v66
mm v[180:183],v[48:51],v[16:19],v[180:183]
dt v[34:35],v236 offset:8704
ar v67,a238
ar v68,a239
mm v[196:199],v[16:19],v[52:55],v[196:199]
dt v[36:37],v237 offset:8192
v_lshl_add_u32 v154, v152, 3, v153
)"
R"(v_cmp_eq_u32 vcc, 16, v213
ar v56,a24
ar v57,a25
cv v76,v56,v57
ar v58,a26
ar v59,a27
cv v77,v58,v59
ar v56,a28
ar v57,a29
cv v78,v56,v57
ar v58,a30
ar v59,a31
cv v79,v58,v59
sa s56,s61,0x0
buffer_store_dwordx4 v[76:79], v214, s[44:47], s56 offen offset:3072 sc1
mm v[168:171],v[44:47],v[20:23],v[168:171]
)"
R"(dt v[38:39],v237 offset:8704
cv v69,v67,v68
v_lshlrev_b32 v154, 8, v154
mm v[184:187],v[48:51],v[20:23],v[184:187]
dt v[40:41],v238 offset:8192
v_mov_b32 v245, s99
v_accvgpr_write_b32 a235, v69
mm v[200:203],v[20:23],v[52:55],v[200:203]
dt v[42:43],v238 offset:8704
v_and_b32 v152, 1, v152
ar v64,a240
mm v[172:175],v[44:47],v[24:27],v[172:175]
dr v[0:3],v232 offset:32768
ar v65,a241
v_lshl_or_b32 v152, v152, 2, v153
mm v[188:191],v[48:51],v[24:27],v[188:191]
)"
R"(dr v[4:7],v232 offset:34816
v_cndmask_b32 v216, v216, v245, vcc
cv v66,v64,v65
mm v[204:207],v[24:27],v[52:55],v[204:207]
s_mov_b32 s99, (L_att_finlo-L_att_base)
dr v[8:11],v232 offset:36864
ar v67,a242
ar v68,a243
ar v56,a32
ar v57,a33
cv v72,v56,v57
ar v58,a34
ar v59,a35
cv v73,v58,v59
ar v56,a36
ar v57,a37
)"
R"(cv v74,v56,v57
ar v58,a38
ar v59,a39
cv v75,v58,v59
sa s56,s61,0x1000
buffer_store_dwordx4 v[72:75], v214, s[44:47], s56 offen offset:0 sc1
mm v[176:179],v[44:47],v[28:31],v[176:179]
dr v[12:15],v232 offset:38912
v_lshlrev_b32 v152, 5, v152
v_accvgpr_write_b32 a240, v66
mm v[192:195],v[48:51],v[28:31],v[192:195]
cv v69,v67,v68
v_and_b32 v153, 3, v213
mm v[208:211],v[28:31],v[52:55],v[208:211]
s_waitcnt lgkmcnt(0)
v_accvgpr_write_b32 a241, v69
)"
R"(ar v64,a244
mm v[164:167],v[32:35],v[0:3],v[164:167]
dt v[44:45],v236 offset:12288
ar v65,a245
v_lshl_add_u32 v154, v153, 3, v154
mm v[180:183],v[36:39],v[0:3],v[180:183]
s_cmp_eq_u32 s50, 2
s_cmov_b32 s99, (L_att_finhi0-L_att_base)
dt v[46:47],v236 offset:12800
cv v66,v64,v65
mm v[196:199],v[0:3],v[40:43],v[196:199]
s_cmp_eq_u32 s50, 3
s_cmov_b32 s99, (L_att_finhi1-L_att_base)
dt v[48:49],v237 offset:12288
v_add_u32 v154, v154, v152
ar v56,a40
)"
R"(ar v57,a41
cv v76,v56,v57
ar v58,a42
ar v59,a43
cv v77,v58,v59
ar v56,a44
ar v57,a45
cv v78,v56,v57
ar v58,a46
ar v59,a47
cv v79,v58,v59
sa s56,s61,0x1000
buffer_store_dwordx4 v[76:79], v214, s[44:47], s56 offen offset:1024 sc1
mm v[168:171],v[32:35],v[4:7],v[168:171]
dt v[50:51],v237 offset:12800
v_accvgpr_write_b32 a242, v66
)"
R"(ar v67,a246
mm v[184:187],v[36:39],v[4:7],v[184:187]
dt v[52:53],v238 offset:12288
ar v68,a247
v_xor_b32 v132, 0, v154
mm v[200:203],v[4:7],v[40:43],v[200:203]
dt v[54:55],v238 offset:12800
cv v69,v67,v68
v_xor_b32 v133, 32, v154
mm v[172:175],v[32:35],v[8:11],v[172:175]
dr v[16:19],v233 offset:32768
v_cmp_eq_u32 vcc, 17, v213
v_accvgpr_write_b32 a243, v69
mm v[188:191],v[36:39],v[8:11],v[188:191]
dr v[20:23],v233 offset:34816
v_xor_b32 v134, 64, v154
)"
R"(v_mov_b32 v245, s99
mm v[204:207],v[8:11],v[40:43],v[204:207]
dr v[24:27],v233 offset:36864
ar v64,a248
ar v65,a249
ar v56,a48
ar v57,a49
cv v72,v56,v57
ar v58,a50
ar v59,a51
cv v73,v58,v59
ar v56,a52
ar v57,a53
cv v74,v56,v57
ar v58,a54
ar v59,a55
)"
R"(cv v75,v58,v59
sa s56,s61,0x1000
buffer_store_dwordx4 v[72:75], v214, s[44:47], s56 offen offset:2048 sc1
mm v[176:179],v[32:35],v[12:15],v[176:179]
dr v[28:31],v233 offset:38912
v_xor_b32 v135, 96, v154
cv v66,v64,v65
mm v[192:195],v[36:39],v[12:15],v[192:195]
ar v67,a250
ar v68,a251
mm v[208:211],v[12:15],v[40:43],v[208:211]
s_waitcnt vmcnt(15) lgkmcnt(0)
s_barrier
v_xor_b32 v136, 128, v154
v_cndmask_b32 v216, v216, v245, vcc
mm v[164:167],v[44:47],v[16:19],v[164:167]
)"
R"(s_sub_u32 s97, s96, 2
dt v[32:33],v236 offset:16384
v_accvgpr_write_b32 a248, v66
cv v69,v67,v68
mm v[180:183],v[48:51],v[16:19],v[180:183]
dt v[34:35],v236 offset:16896
v_xor_b32 v137, 160, v154
v_accvgpr_write_b32 a249, v69
mm v[196:199],v[16:19],v[52:55],v[196:199]
dt v[36:37],v237 offset:16384
ar v64,a252
ar v65,a253
v_xor_b32 v138, 192, v154
v_xor_b32 v139, 224, v154
v_add_u32 v132, 0x8000, v132
v_add_u32 v140, 0x18000, v132
)"
R"(v_add_u32 v133, 0x8000, v133
v_add_u32 v141, 0x18000, v133
v_add_u32 v134, 0x8000, v134
v_add_u32 v142, 0x18000, v134
v_add_u32 v135, 0x8000, v135
v_add_u32 v143, 0x18000, v135
v_add_u32 v136, 0x8000, v136
v_add_u32 v144, 0x18000, v136
v_add_u32 v137, 0x8000, v137
v_add_u32 v145, 0x18000, v137
v_add_u32 v138, 0x8000, v138
v_add_u32 v146, 0x18000, v138
v_add_u32 v139, 0x8000, v139
v_add_u32 v147, 0x18000, v139
s_and_b32 s60, s50, 1
s_lshl_b32 s60, s60, 15
)"
R"(s_sub_u32 s60, s52, s60
sa m0,s54,0x0
sa s56,s52,0x0
bl v148,s[36:39],s56 offen lds
mm v[168:171],v[44:47],v[20:23],v[168:171]
dt v[38:39],v237 offset:16896
cv v66,v64,v65
ar v67,a254
mm v[184:187],v[48:51],v[20:23],v[184:187]
s_cmp_ge_u32 s50, 2
s_cselect_b32 s97, s97, 99
dt v[40:41],v238 offset:16384
sa s56,s52,0x1c00
bl v149,s[36:39],s56 offen offset:1024 lds
ar v68,a255
mm v[200:203],v[20:23],v[52:55],v[200:203]
)"
R"(dt v[42:43],v238 offset:16896
v_accvgpr_write_b32 a250, v66
cv v69,v67,v68
mm v[172:175],v[44:47],v[24:27],v[172:175]
s_cmp_le_u32 s50, 3
s_cselect_b32 s97, s97, 99
dr v[0:3],v234 offset:0
sa s56,s60,0xb800
bl v148,s[36:39],s56 offen offset:2048 lds
v_accvgpr_write_b32 a251, v69
mm v[188:191],v[48:51],v[24:27],v[188:191]
s_mov_b32 s99, (L_att_a0_16-L_att_base)
dr v[4:7],v234 offset:2048
ar v64,a80
ar v65,a81
mm v[204:207],v[24:27],v[52:55],v[204:207]
)"
R"(dr v[8:11],v234 offset:4096
sa s56,s60,0xd400
bl v149,s[36:39],s56 offen offset:3072 lds
cv v66,v64,v65
ar v67,a82
mm v[176:179],v[44:47],v[28:31],v[176:179]
s_cmp_eq_u32 s50, 3
s_cmov_b32 s99, (L_att_a1_16-L_att_base)
dr v[12:15],v234 offset:6144
ar v68,a83
mm v[192:195],v[48:51],v[28:31],v[192:195]
sa m0,s54,0x1000
v_accvgpr_write_b32 a180, v66
cv v69,v67,v68
sa s56,s52,0x10000
bl v148,s[36:39],s56 offen lds
)"
R"(mm v[208:211],v[28:31],v[52:55],v[208:211]
s_waitcnt lgkmcnt(0)
v_accvgpr_write_b32 a181, v69
ar v64,a84
mm v[164:167],v[32:35],v[0:3],v[164:167]
dt v[44:45],v236 offset:20480
sa s56,s52,0x11c00
bl v149,s[36:39],s56 offen offset:1024 lds
ar v65,a85
v_cmp_eq_u32 vcc, s97, v213
mm v[180:183],v[36:39],v[0:3],v[180:183]
dt v[46:47],v236 offset:20992
cv v66,v64,v65
v_mov_b32 v245, s99
mm v[196:199],v[0:3],v[40:43],v[196:199]
dt v[48:49],v237 offset:20480
)"
R"(sa s56,s60,0x1b800
bl v148,s[36:39],s56 offen offset:2048 lds
v_accvgpr_write_b32 a182, v66
ar v67,a86
mm v[168:171],v[32:35],v[4:7],v[168:171]
dt v[50:51],v237 offset:20992
ar v68,a87
cv v69,v67,v68
mm v[184:187],v[36:39],v[4:7],v[184:187]
dt v[52:53],v238 offset:20480
sa s56,s60,0x1d400
bl v149,s[36:39],s56 offen offset:3072 lds
v_cndmask_b32 v216, v216, v245, vcc
v_accvgpr_write_b32 a183, v69
mm v[200:203],v[4:7],v[40:43],v[200:203]
s_sub_u32 s97, s96, 1
)"
R"(dt v[54:55],v238 offset:20992
ar v64,a88
ar v65,a89
mm v[172:175],v[32:35],v[8:11],v[172:175]
sa m0,s55,0x0
dr v[16:19],v235 offset:0
cv v66,v64,v65
ar v67,a90
sa s56,s53,0x0
bl v150,s[40:43],s56 offen lds
mm v[188:191],v[36:39],v[8:11],v[188:191]
s_cmp_ge_u32 s50, 2
s_cselect_b32 s97, s97, 99
dr v[20:23],v235 offset:2048
ar v68,a91
mm v[204:207],v[8:11],v[40:43],v[204:207]
)"
R"(dr v[24:27],v235 offset:4096
sa s56,s53,0x7c00
bl v150,s[40:43],s56 offen offset:1024 lds
v_accvgpr_write_b32 a188, v66
cv v69,v67,v68
mm v[176:179],v[32:35],v[12:15],v[176:179]
s_cmp_le_u32 s50, 3
s_cselect_b32 s97, s97, 99
dr v[28:31],v235 offset:6144
v_accvgpr_write_b32 a189, v69
mm v[192:195],v[36:39],v[12:15],v[192:195]
s_mov_b32 s99, (L_att_seg0_16-L_att_base)
sa s56,s53,0xf800
bl v151,s[40:43],s56 offen offset:2048 lds
ar v64,a92
ar v65,a93
)"
R"(mm v[208:211],v[12:15],v[40:43],v[208:211]
s_waitcnt vmcnt(15) lgkmcnt(0)
s_barrier
cv v66,v64,v65
v_accvgpr_write_b32 a190, v66
mm v[164:167],v[44:47],v[16:19],v[164:167]
dt v[32:33],v236 offset:24576
sa s56,s53,0x17400
bl v151,s[40:43],s56 offen offset:3072 lds
ar v67,a94
ar v68,a95
mm v[180:183],v[48:51],v[16:19],v[180:183]
s_cmp_eq_u32 s50, 3
s_cmov_b32 s99, (L_att_seg1_16-L_att_base)
dt v[34:35],v236 offset:25088
cv v69,v67,v68
)"
R"(mm v[196:199],v[16:19],v[52:55],v[196:199]
s_cmp_eq_u32 s63, 23
s_cmov_b32 s99, (L_att_seg0_0-L_att_base)
dt v[36:37],v237 offset:24576
v_accvgpr_write_b32 a191, v69
sa m0,s54,0xc000
sa s56,s52,0x80
bl v148,s[36:39],s56 offen lds
mm v[168:171],v[44:47],v[20:23],v[168:171]
dt v[38:39],v237 offset:25088
ar v64,a96
ar v65,a97
mm v[184:187],v[48:51],v[20:23],v[184:187]
s_cmp_eq_u32 s63, 31
s_cmov_b32 s99, (L_att_seg1_0-L_att_base)
dt v[40:41],v238 offset:24576
)"
R"(sa s56,s52,0x1c80
bl v149,s[36:39],s56 offen offset:1024 lds
cv v66,v64,v65
mm v[200:203],v[20:23],v[52:55],v[200:203]
dt v[42:43],v238 offset:25088
ar v67,a98
ar v68,a99
mm v[172:175],v[44:47],v[24:27],v[172:175]
dr v[0:3],v234 offset:32768
sa s56,s60,0xb880
bl v148,s[36:39],s56 offen offset:2048 lds
v_accvgpr_write_b32 a196, v66
cv v69,v67,v68
mm v[188:191],v[48:51],v[24:27],v[188:191]
dr v[4:7],v234 offset:34816
v_cmp_eq_u32 vcc, s97, v213
)"
R"(v_accvgpr_write_b32 a197, v69
mm v[204:207],v[24:27],v[52:55],v[204:207]
dr v[8:11],v234 offset:36864
sa s56,s60,0xd480
bl v149,s[36:39],s56 offen offset:3072 lds
ar v64,a100
ar v65,a101
mm v[176:179],v[44:47],v[28:31],v[176:179]
sa m0,s54,0xd000
dr v[12:15],v234 offset:38912
v_mov_b32 v245, s99
cv v66,v64,v65
mm v[192:195],v[48:51],v[28:31],v[192:195]
sa s56,s52,0x10080
bl v148,s[36:39],s56 offen lds
ar v67,a102
)"
R"(ar v68,a103
mm v[208:211],v[28:31],v[52:55],v[208:211]
s_waitcnt lgkmcnt(0)
v_accvgpr_write_b32 a198, v66
cv v69,v67,v68
mm v[164:167],v[32:35],v[0:3],v[164:167]
dt v[44:45],v236 offset:28672
sa s56,s52,0x11c80
bl v149,s[36:39],s56 offen offset:1024 lds
v_cndmask_b32 v216, v216, v245, vcc
v_accvgpr_write_b32 a199, v69
mm v[180:183],v[36:39],v[0:3],v[180:183]
s_mov_b32 s97, s96
dt v[46:47],v236 offset:29184
ar v64,a104
ar v65,a105
)"
R"(mm v[196:199],v[0:3],v[40:43],v[196:199]
dt v[48:49],v237 offset:28672
sa s56,s60,0x1b880
bl v148,s[36:39],s56 offen offset:2048 lds
cv v66,v64,v65
ar v67,a106
mm v[168:171],v[32:35],v[4:7],v[168:171]
s_cmp_ge_u32 s50, 2
s_cselect_b32 s97, s97, 99
dt v[50:51],v237 offset:29184
ar v68,a107
mm v[184:187],v[36:39],v[4:7],v[184:187]
dt v[52:53],v238 offset:28672
sa s56,s60,0x1d480
bl v149,s[36:39],s56 offen offset:3072 lds
v_accvgpr_write_b32 a204, v66
)"
R"(cv v69,v67,v68
mm v[200:203],v[4:7],v[40:43],v[200:203]
s_cmp_le_u32 s50, 3
s_cselect_b32 s97, s97, 99
dt v[54:55],v238 offset:29184
v_accvgpr_write_b32 a205, v69
mm v[172:175],v[32:35],v[8:11],v[172:175]
s_mov_b32 s99, (L_att_firstb16-L_att_base)
dr v[16:19],v235 offset:32768
ar v64,a108
ar v65,a109
sa m0,s55,0xc000
sa s56,s53,0x80000
bl v150,s[40:43],s56 offen lds
mm v[188:191],v[36:39],v[8:11],v[188:191]
dr v[20:23],v235 offset:34816
)"
R"(cv v66,v64,v65
v_accvgpr_write_b32 a206, v66
mm v[204:207],v[8:11],v[40:43],v[204:207]
sa s56,s53,0x87c00
bl v150,s[40:43],s56 offen offset:1024 lds
dr v[24:27],v235 offset:36864
ar v67,a110
ar v68,a111
mm v[176:179],v[32:35],v[12:15],v[176:179]
s_cmp_eq_u32 s63, 23
s_cmov_b32 s99, (L_att_fb0_0-L_att_base)
dr v[28:31],v235 offset:38912
cv v69,v67,v68
mm v[192:195],v[36:39],v[12:15],v[192:195]
s_cmp_eq_u32 s63, 31
s_cmov_b32 s99, (L_att_fb1_0-L_att_base)
)"
R"(sa s56,s53,0x8f800
bl v151,s[40:43],s56 offen offset:2048 lds
v_accvgpr_write_b32 a207, v69
mm v[208:211],v[12:15],v[40:43],v[208:211]
s_waitcnt vmcnt(11) lgkmcnt(0)
s_barrier
ar v64,a112
ar v65,a113
mm v[164:167],v[44:47],v[16:19],v[164:167]
sa s56,s53,0x97400
bl v151,s[40:43],s56 offen offset:3072 lds
v_cmp_eq_u32 vcc, s97, v213
cv v66,v64,v65
mm v[180:183],v[48:51],v[16:19],v[180:183]
ar v67,a114
ar v68,a115
)"
R"(mm v[196:199],v[16:19],v[52:55],v[196:199]
v_accvgpr_write_b32 a212, v66
cv v69,v67,v68
ar v56,a56
ar v57,a57
cv v76,v56,v57
ar v58,a58
ar v59,a59
cv v77,v58,v59
ar v56,a60
ar v57,a61
cv v78,v56,v57
ar v58,a62
ar v59,a63
cv v79,v58,v59
sa s56,s61,0x1000
)"
R"(buffer_store_dwordx4 v[76:79], v214, s[44:47], s56 offen offset:3072 sc1
mm v[168:171],v[44:47],v[20:23],v[168:171]
v_mov_b32 v245, s99
v_accvgpr_write_b32 a213, v69
mm v[184:187],v[48:51],v[20:23],v[184:187]
ar v64,a116
ar v65,a117
mm v[200:203],v[20:23],v[52:55],v[200:203]
v_cndmask_b32 v216, v216, v245, vcc
cv v66,v64,v65
ar v56,a64
ar v57,a65
cv v72,v56,v57
ar v58,a66
ar v59,a67
cv v73,v58,v59
)"
R"(ar v56,a68
ar v57,a69
cv v74,v56,v57
ar v58,a70
ar v59,a71
cv v75,v58,v59
sa s56,s61,0x2000
buffer_store_dwordx4 v[72:75], v214, s[44:47], s56 offen offset:0 sc1
mm v[172:175],v[44:47],v[24:27],v[172:175]
s_mov_b32 s99, (L_att_first8-L_att_base)
v_accvgpr_write_b32 a214, v66
ar v67,a118
mm v[188:191],v[48:51],v[24:27],v[188:191]
ar v68,a119
cv v69,v67,v68
mm v[204:207],v[24:27],v[52:55],v[204:207]
)"
R"(s_cmp_eq_u32 s49, 0
s_cmov_b32 s99, (L_att_first16-L_att_base)
v_accvgpr_write_b32 a215, v69
mm v[176:179],v[44:47],v[28:31],v[176:179]
ar v64,a120
ar v65,a121
ar v56,a72
ar v57,a73
cv v76,v56,v57
ar v58,a74
ar v59,a75
cv v77,v58,v59
ar v56,a76
ar v57,a77
cv v78,v56,v57
ar v58,a78
)"
R"(ar v59,a79
cv v79,v58,v59
sa s56,s61,0x2000
buffer_store_dwordx4 v[76:79], v214, s[44:47], s56 offen offset:1024 sc1
mm v[192:195],v[48:51],v[28:31],v[192:195]
s_cmp_eq_u32 s63, 16
s_cmov_b32 s99, (L_att_fa0_16-L_att_base)
cv v66,v64,v65
mm v[208:211],v[28:31],v[52:55],v[208:211]
ar v67,a122
ar v68,a123
s_cmp_eq_u32 s63, 24
s_cmov_b32 s99, (L_att_fa1_16-L_att_base)
v_accvgpr_write_b32 a220, v66
cv v69,v67,v68
v_accvgpr_write_b32 a221, v69
)"
R"(ar v64,a124
ar v65,a125
v_cmp_eq_u32 vcc, 0, v213
cv v66,v64,v65
v_mov_b32 v245, s99
v_accvgpr_write_b32 a222, v66
ar v67,a126
ar v68,a127
cv v69,v67,v68
v_cndmask_b32 v216, v216, v245, vcc
v_accvgpr_write_b32 a223, v69
ar v64,a128
ar v65,a129
cv v66,v64,v65
ar v67,a130
ar v68,a131
)"
R"(v_accvgpr_write_b32 a228, v66
cv v69,v67,v68
v_accvgpr_write_b32 a229, v69
ar v64,a132
ar v65,a133
cv v66,v64,v65
ar v67,a134
ar v68,a135
v_accvgpr_write_b32 a230, v66
cv v69,v67,v68
v_accvgpr_write_b32 a231, v69
ar v64,a136
ar v65,a137
cv v66,v64,v65
ar v67,a138
ar v68,a139
)"
R"(v_accvgpr_write_b32 a236, v66
cv v69,v67,v68
v_accvgpr_write_b32 a237, v69
ar v64,a140
ar v65,a141
cv v66,v64,v65
v_accvgpr_write_b32 a238, v66
ar v67,a142
ar v68,a143
cv v69,v67,v68
v_accvgpr_write_b32 a239, v69
ar v64,a144
ar v65,a145
cv v66,v64,v65
ar v67,a146
ar v68,a147
)"
R"(v_accvgpr_write_b32 a244, v66
cv v69,v67,v68
v_accvgpr_write_b32 a245, v69
ar v64,a148
ar v65,a149
cv v66,v64,v65
v_accvgpr_write_b32 a246, v66
ar v67,a150
ar v68,a151
cv v69,v67,v68
v_accvgpr_write_b32 a247, v69
ar v64,a152
ar v65,a153
cv v66,v64,v65
ar v67,a154
ar v68,a155
)"
R"(v_accvgpr_write_b32 a252, v66
cv v69,v67,v68
v_accvgpr_write_b32 a253, v69
ar v64,a156
ar v65,a157
cv v66,v64,v65
v_accvgpr_write_b32 a254, v66
ar v67,a158
ar v68,a159
cv v69,v67,v68
v_accvgpr_write_b32 a255, v69
dt a[96:97],v132 offset:0
dt a[98:99],v132 offset:1024
dt a[100:101],v132 offset:8192
dt a[102:103],v132 offset:9216
dr a[16:19],v128 offset:0
)"
R"(dr a[20:23],v129 offset:0
dr a[24:27],v128 offset:2048
dr a[28:31],v129 offset:2048
dr a[32:35],v128 offset:4096
dr a[36:39],v129 offset:4096
dr a[40:43],v128 offset:6144
dr a[44:47],v129 offset:6144
dt a[104:105],v133 offset:0
dt a[106:107],v133 offset:1024
dt a[108:109],v133 offset:8192
dt a[110:111],v133 offset:9216
dt a[112:113],v134 offset:0
dt a[114:115],v134 offset:1024
dt a[116:117],v134 offset:8192
dt a[118:119],v134 offset:9216
dt a[120:121],v135 offset:0
)"
R"(dt a[122:123],v135 offset:1024
dt a[124:125],v135 offset:8192
dt a[126:127],v135 offset:9216
dt a[128:129],v136 offset:0
dt a[130:131],v136 offset:1024
dt a[132:133],v136 offset:8192
dt a[134:135],v136 offset:9216
dt a[136:137],v137 offset:0
dt a[138:139],v137 offset:1024
dt a[140:141],v137 offset:8192
dt a[142:143],v137 offset:9216
dt a[144:145],v138 offset:0
dt a[146:147],v138 offset:1024
dt a[148:149],v138 offset:8192
dt a[150:151],v138 offset:9216
dt a[152:153],v139 offset:0
)"
R"(dt a[154:155],v139 offset:1024
dt a[156:157],v139 offset:8192
dt a[158:159],v139 offset:9216
s_mov_b32 s63, 0x3fb8aa3b
v_and_b32 v228, 15, v213
v_lshrrev_b32 v229, 4, v213
v_lshlrev_b32 v229, 2, v229
v_sub_u32 v228, v228, v229
v_mov_b32 v229, 0x3f80
v_cmp_eq_u32 vcc, 0, v228
s_nop 1
v_cndmask_b32 v230, 0, v229, vcc
v_mov_b32 v229, 0x3f800000
v_cmp_eq_u32 vcc, 1, v228
s_nop 1
v_cndmask_b32 v230, v230, v229, vcc
)"
R"(v_accvgpr_write_b32 a0, v230
v_accvgpr_write_b32 a6, v230
v_accvgpr_write_b32 a2, 0
v_accvgpr_write_b32 a4, 0
v_mov_b32 v229, 0x3f80
v_cmp_eq_u32 vcc, 2, v228
s_nop 1
v_cndmask_b32 v230, 0, v229, vcc
v_mov_b32 v229, 0x3f800000
v_cmp_eq_u32 vcc, 3, v228
s_nop 1
v_cndmask_b32 v230, v230, v229, vcc
v_accvgpr_write_b32 a1, v230
v_accvgpr_write_b32 a7, v230
v_accvgpr_write_b32 a3, 0
v_accvgpr_write_b32 a5, 0
)"
R"(v_mov_b32 v229, 0x3f803f80
v_accvgpr_write_b32 a8, v229
v_accvgpr_write_b32 a9, v229
v_accvgpr_write_b32 a10, v229
v_accvgpr_write_b32 a11, v229
s_waitcnt lgkmcnt(15)
mm v[0:3],a[96:99],a[16:19],0
sa m0,s54,0x18000
cv v236,v180,v181
cv v237,v182,v183
mm v[0:3],a[100:103],a[20:23],v[0:3]
sa s56,s52,0x100
bl v148,s[36:39],s56 offen lds
mm v[32:35],a[96:99],a[24:27],0
cv v196,v196,v197
cv v204,v204,v205
)"
R"(mm v[32:35],a[100:103],a[28:31],v[32:35]
cv v197,v198,v199
cv v205,v206,v207
mm v[64:67],a[96:99],a[32:35],0
sa s56,s52,0x1d00
bl v149,s[36:39],s56 offen offset:1024 lds
mm v[64:67],a[100:103],a[36:39],v[64:67]
cv v198,v200,v201
cv v206,v208,v209
v_mfma_f32_16x16x16_bf16 v[180:183], v[220:221], v[236:237], 0
cv v199,v202,v203
cv v207,v210,v211
mm v[96:99],a[96:99],a[40:43],0
mm v[96:99],a[100:103],a[44:47],v[96:99]
s_waitcnt lgkmcnt(15)
mm v[4:7],a[104:107],a[16:19],0
)"
R"(sa s56,s60,0xb900
bl v148,s[36:39],s56 offen offset:2048 lds
mm v[4:7],a[108:111],a[20:23],v[4:7]
sa s56,s61,0xa000
buffer_store_dwordx4 a[192:195], v214, s[44:47], s56 offen offset:0 sc1
mm v[36:39],a[104:107],a[24:27],0
s_waitcnt vmcnt(7) lgkmcnt(0)
s_barrier
v_max3_f32 v238, v180, v181, v182
mm v[36:39],a[108:111],a[28:31],v[36:39]
dt a[96:97],v132 offset:49152
v_max3_f32 v239, -v180, -v181, -v182
mm v[68:71],a[104:107],a[32:35],0
dt a[98:99],v132 offset:50176
v_max_f32 v238, v238, v183
mm v[68:71],a[108:111],a[36:39],v[68:71]
)"
R"(dt a[100:101],v132 offset:57344
v_max_f32 v239, v239, -v183
mm v[100:103],a[104:107],a[40:43],0
dt a[102:103],v132 offset:58368
mm v[100:103],a[108:111],a[44:47],v[100:103]
s_waitcnt lgkmcnt(15)
dt a[104:105],v133 offset:49152
mm v[8:11],a[112:115],a[16:19],0
dt a[106:107],v133 offset:50176
mm v[8:11],a[116:119],a[20:23],v[8:11]
dt a[108:109],v133 offset:57344
mm v[40:43],a[112:115],a[24:27],0
dt a[110:111],v133 offset:58368
mm v[40:43],a[116:119],a[28:31],v[40:43]
dr a[64:67],v128 offset:49152
mm v[72:75],a[112:115],a[32:35],0
)"
R"(dr a[68:71],v129 offset:49152
mm v[72:75],a[116:119],a[36:39],v[72:75]
sa s56,s60,0xd500
bl v149,s[36:39],s56 offen offset:3072 lds
mm v[104:107],a[112:115],a[40:43],0
sa m0,s54,0x19000
v_permlane32_swap_b32 v238, v239
mm v[104:107],a[116:119],a[44:47],v[104:107]
s_waitcnt lgkmcnt(15)
dt a[112:113],v134 offset:49152
v_max_f32 v238, v238, v239
mm v[12:15],a[120:123],a[16:19],0
dt a[114:115],v134 offset:50176
v_mov_b32 v239, v238
mm v[12:15],a[124:127],a[20:23],v[12:15]
dt a[116:117],v134 offset:57344
)"
R"(mm v[44:47],a[120:123],a[24:27],0
dt a[118:119],v134 offset:58368
mm v[44:47],a[124:127],a[28:31],v[44:47]
dr a[72:75],v128 offset:51200
mm v[76:79],a[120:123],a[32:35],0
dr a[76:79],v129 offset:51200
mm v[76:79],a[124:127],a[36:39],v[76:79]
sa s56,s52,0x10100
bl v148,s[36:39],s56 offen lds
mm v[108:111],a[120:123],a[40:43],0
sa s56,s52,0x11d00
bl v149,s[36:39],s56 offen offset:1024 lds
sa s56,s61,0x3000
buffer_store_dwordx4 a[196:199], v214, s[44:47], s56 offen offset:0 sc1
mm v[108:111],a[124:127],a[44:47],v[108:111]
s_waitcnt lgkmcnt(15)
)"
R"(dt a[120:121],v135 offset:49152
mm v[16:19],a[128:131],a[16:19],0
dt a[122:123],v135 offset:50176
mm v[16:19],a[132:135],a[20:23],v[16:19]
dt a[124:125],v135 offset:57344
mm v[48:51],a[128:131],a[24:27],0
dt a[126:127],v135 offset:58368
mm v[48:51],a[132:135],a[28:31],v[48:51]
dr a[80:83],v128 offset:53248
mm v[80:83],a[128:131],a[32:35],0
dr a[84:87],v129 offset:53248
mm v[80:83],a[132:135],a[36:39],v[80:83]
sa s56,s60,0x1b900
bl v148,s[36:39],s56 offen offset:2048 lds
mm v[112:115],a[128:131],a[40:43],0
sa s56,s60,0x1d500
)"
R"(bl v149,s[36:39],s56 offen offset:3072 lds
mm v[112:115],a[132:135],a[44:47],v[112:115]
sa m0,s55,0x18000
s_waitcnt lgkmcnt(15)
dt a[128:129],v136 offset:49152
mm v[20:23],a[136:139],a[16:19],0
dt a[130:131],v136 offset:50176
mm v[20:23],a[140:143],a[20:23],v[20:23]
dt a[132:133],v136 offset:57344
mm v[52:55],a[136:139],a[24:27],0
dt a[134:135],v136 offset:58368
mm v[52:55],a[140:143],a[28:31],v[52:55]
dr a[88:91],v128 offset:55296
mm v[84:87],a[136:139],a[32:35],0
dr a[92:95],v129 offset:55296
mm v[84:87],a[140:143],a[36:39],v[84:87]
)"
R"(sa s56,s53,0x100000
bl v150,s[40:43],s56 offen lds
mm v[116:119],a[136:139],a[40:43],0
sa s56,s53,0x107c00
bl v150,s[40:43],s56 offen offset:1024 lds
mm v[116:119],a[140:143],a[44:47],v[116:119]
s_waitcnt lgkmcnt(15)
dt a[136:137],v137 offset:49152
v_permlane16_swap_b32 v238, v239
mm v[24:27],a[144:147],a[16:19],0
dt a[138:139],v137 offset:50176
v_max_f32 v238, v238, v239
mm v[24:27],a[148:151],a[20:23],v[24:27]
dt a[140:141],v137 offset:57344
v_mov_b32 v239, v238
mm v[56:59],a[144:147],a[24:27],0
)"
R"(dt a[142:143],v137 offset:58368
sa s56,s61,0xa000
buffer_store_dwordx4 a[200:203], v214, s[44:47], s56 offen offset:1024 sc1
mm v[56:59],a[148:151],a[28:31],v[56:59]
sa s56,s53,0x10f800
bl v151,s[40:43],s56 offen offset:2048 lds
mm v[88:91],a[144:147],a[32:35],0
sa s56,s53,0x117400
bl v151,s[40:43],s56 offen offset:3072 lds
mm v[88:91],a[148:151],a[36:39],v[88:91]
sa m0,s54,0x0
v_permlane32_swap_b32 v238, v239
mm v[120:123],a[144:147],a[40:43],0
sa s56,s52,0x180
bl v148,s[36:39],s56 offen lds
v_mul_f32 v239, s63, v239
)"
R"(mm v[120:123],a[148:151],a[44:47],v[120:123]
s_waitcnt lgkmcnt(15)
dt a[144:145],v138 offset:49152
v_mul_f32 v238, s63, v238
v_fma_f32 v240, -v180, s63, -v239
cv v244,v184,v185
cv v245,v186,v187
v_fma_f32 v241, -v181, s63, -v239
v_fma_f32 v242, -v182, s63, -v239
v_mfma_f32_16x16x16_bf16 v[184:187], v[220:221], v[244:245], 0
sa s56,s52,0x1d80
bl v149,s[36:39],s56 offen offset:1024 lds
v_fma_f32 v243, -v183, s63, -v239
mm v[28:31],a[152:155],a[16:19],0
dt a[146:147],v138 offset:50176
v_fma_f32 v180, v180, s63, -v238
)"
R"(v_fma_f32 v181, v181, s63, -v238
mm v[28:31],a[156:159],a[20:23],v[28:31]
dt a[148:149],v138 offset:57344
v_fma_f32 v182, v182, s63, -v238
v_fma_f32 v183, v183, s63, -v238
mm v[60:63],a[152:155],a[24:27],0
dt a[150:151],v138 offset:58368
ex v240,v240
mm v[60:63],a[156:159],a[28:31],v[60:63]
ex v180,v180
mm v[92:95],a[152:155],a[32:35],0
sa s56,s60,0xb980
bl v148,s[36:39],s56 offen offset:2048 lds
mm v[92:95],a[156:159],a[36:39],v[92:95]
ex v241,v241
mm v[124:127],a[152:155],a[40:43],0
)"
R"(ex v181,v181
mm v[124:127],a[156:159],a[44:47],v[124:127]
s_waitcnt lgkmcnt(8)
dt a[152:153],v139 offset:49152
ex v242,v242
mm v[0:3],a[96:99],a[64:67],v[0:3]
dt a[154:155],v139 offset:50176
ex v182,v182
mm v[0:3],a[100:103],a[68:71],v[0:3]
dt a[156:157],v139 offset:57344
ex v243,v243
mm v[32:35],a[96:99],a[72:75],v[32:35]
dt a[158:159],v139 offset:58368
ex v183,v183
mm v[32:35],a[100:103],a[76:79],v[32:35]
sa s56,s61,0x3000
)"
R"(buffer_store_dwordx4 a[204:207], v214, s[44:47], s56 offen offset:1024 sc1
mm v[64:67],a[96:99],a[80:83],v[64:67]
sa s56,s60,0xd580
bl v149,s[36:39],s56 offen offset:3072 lds
v_add_f32 v238, v180, v181
mm v[64:67],a[100:103],a[84:87],v[64:67]
sa m0,s54,0x1000
v_add_f32 v239, v240, v241
v_add_f32 v238, v238, v182
v_add_f32 v239, v239, v242
mm v[96:99],a[96:99],a[88:91],v[96:99]
sa s56,s52,0x10180
bl v148,s[36:39],s56 offen lds
v_add_f32 v238, v238, v183
mm v[96:99],a[100:103],a[92:95],v[96:99]
s_waitcnt lgkmcnt(15)
)"
R"(v_add_f32 v239, v239, v243
v_max3_f32 v246, v184, v185, v186
mm v[4:7],a[104:107],a[64:67],v[4:7]
v_permlane32_swap_b32 v238, v239
mm v[4:7],a[108:111],a[68:71],v[4:7]
v_add_f32 v238, v238, v239
v_mov_b32 v239, v238
mm v[36:39],a[104:107],a[72:75],v[36:39]
s_waitcnt vmcnt(6) lgkmcnt(0)
s_barrier
v_max3_f32 v247, -v184, -v185, -v186
mm v[36:39],a[108:111],a[76:79],v[36:39]
dt a[96:97],v140 offset:0
v_permlane16_swap_b32 v238, v239
mm v[68:71],a[104:107],a[80:83],v[68:71]
dt a[98:99],v140 offset:1024
)"
R"(v_add_f32 v238, v238, v239
v_mov_b32 v239, v238
mm v[68:71],a[108:111],a[84:87],v[68:71]
dt a[100:101],v140 offset:8192
v_max_f32 v246, v246, v187
mm v[100:103],a[104:107],a[88:91],v[100:103]
dt a[102:103],v140 offset:9216
v_permlane32_swap_b32 v238, v239
mm v[100:103],a[108:111],a[92:95],v[100:103]
s_waitcnt lgkmcnt(15)
dt a[104:105],v141 offset:0
v_rcp_f32 v238, v238
mm v[8:11],a[112:115],a[64:67],v[8:11]
dt a[106:107],v141 offset:1024
v_mul_f32 v180, v180, v238
v_mul_f32 v181, v181, v238
)"
R"(mm v[8:11],a[116:119],a[68:71],v[8:11]
dt a[108:109],v141 offset:8192
v_rcp_f32 v239, v239
mm v[40:43],a[112:115],a[72:75],v[40:43]
dt a[110:111],v141 offset:9216
v_mul_f32 v182, v182, v238
v_mul_f32 v183, v183, v238
mm v[40:43],a[116:119],a[76:79],v[40:43]
dr a[16:19],v130 offset:0
cv v180,v180,v181
v_mul_f32 v240, v240, v239
mm v[72:75],a[112:115],a[80:83],v[72:75]
dr a[20:23],v131 offset:0
v_mul_f32 v241, v241, v239
cv v181,v182,v183
mm v[72:75],a[116:119],a[84:87],v[72:75]
)"
R"(sa s56,s52,0x11d80
bl v149,s[36:39],s56 offen offset:1024 lds
v_mul_f32 v242, v242, v239
mm v[104:107],a[112:115],a[88:91],v[104:107]
sa s56,s61,0x2000
buffer_store_dwordx4 a[180:183], v214, s[44:47], s56 offen offset:2048 sc1
mm v[104:107],a[116:119],a[92:95],v[104:107]
s_waitcnt lgkmcnt(15)
dt a[112:113],v142 offset:0
v_mul_f32 v243, v243, v239
cv v182,v240,v241
mm v[12:15],a[120:123],a[64:67],v[12:15]
dt a[114:115],v142 offset:1024
cv v183,v242,v243
mm v[12:15],a[124:127],a[68:71],v[12:15]
dt a[116:117],v142 offset:8192
)"
R"(v_max_f32 v247, v247, -v187
mm v[44:47],a[120:123],a[72:75],v[44:47]
dt a[118:119],v142 offset:9216
mm v[44:47],a[124:127],a[76:79],v[44:47]
dr a[24:27],v130 offset:2048
mm v[76:79],a[120:123],a[80:83],v[76:79]
dr a[28:31],v131 offset:2048
mm v[76:79],a[124:127],a[84:87],v[76:79]
sa s56,s60,0x1b980
bl v148,s[36:39],s56 offen offset:2048 lds
mm v[108:111],a[120:123],a[88:91],v[108:111]
sa s56,s60,0x1d580
bl v149,s[36:39],s56 offen offset:3072 lds
mm v[108:111],a[124:127],a[92:95],v[108:111]
sa m0,s55,0x0
s_waitcnt lgkmcnt(15)
)"
R"(dt a[120:121],v143 offset:0
mm v[16:19],a[128:131],a[64:67],v[16:19]
dt a[122:123],v143 offset:1024
mm v[16:19],a[132:135],a[68:71],v[16:19]
dt a[124:125],v143 offset:8192
mm v[48:51],a[128:131],a[72:75],v[48:51]
dt a[126:127],v143 offset:9216
v_permlane32_swap_b32 v246, v247
mm v[48:51],a[132:135],a[76:79],v[48:51]
dr a[32:35],v130 offset:4096
v_max_f32 v246, v246, v247
mm v[80:83],a[128:131],a[80:83],v[80:83]
dr a[36:39],v131 offset:4096
v_mov_b32 v247, v246
mm v[80:83],a[132:135],a[84:87],v[80:83]
sa s56,s53,0x180000
)"
R"(bl v150,s[40:43],s56 offen lds
mm v[112:115],a[128:131],a[88:91],v[112:115]
sa s56,s53,0x187c00
bl v150,s[40:43],s56 offen offset:1024 lds
mm v[112:115],a[132:135],a[92:95],v[112:115]
s_waitcnt lgkmcnt(15)
dt a[128:129],v144 offset:0
v_permlane16_swap_b32 v246, v247
mm v[20:23],a[136:139],a[64:67],v[20:23]
dt a[130:131],v144 offset:1024
v_max_f32 v246, v246, v247
v_mov_b32 v247, v246
mm v[20:23],a[140:143],a[68:71],v[20:23]
dt a[132:133],v144 offset:8192
cv v228,v188,v189
cv v229,v190,v191
)"
R"(v_permlane32_swap_b32 v246, v247
s_nop 0
v_mfma_f32_16x16x16_bf16 v[188:191], v[220:221], v[228:229], 0
sa s56,s61,0x2000
buffer_store_dwordx4 a[188:191], v214, s[44:47], s56 offen offset:3072 sc1
mm v[52:55],a[136:139],a[72:75],v[52:55]
dt a[134:135],v144 offset:9216
v_mul_f32 v247, s63, v247
mm v[52:55],a[140:143],a[76:79],v[52:55]
dr a[40:43],v130 offset:6144
v_mul_f32 v246, s63, v246
v_fma_f32 v248, -v184, s63, -v247
mm v[84:87],a[136:139],a[80:83],v[84:87]
dr a[44:47],v131 offset:6144
v_fma_f32 v249, -v185, s63, -v247
v_fma_f32 v250, -v186, s63, -v247
)"
R"(mm v[84:87],a[140:143],a[84:87],v[84:87]
sa s56,s53,0x18f800
bl v151,s[40:43],s56 offen offset:2048 lds
v_fma_f32 v251, -v187, s63, -v247
mm v[116:119],a[136:139],a[88:91],v[116:119]
sa s56,s53,0x197400
bl v151,s[40:43],s56 offen offset:3072 lds
v_fma_f32 v184, v184, s63, -v246
mm v[116:119],a[140:143],a[92:95],v[116:119]
sa m0,s54,0xc000
s_waitcnt lgkmcnt(15)
dt a[136:137],v145 offset:0
v_fma_f32 v185, v185, s63, -v246
v_fma_f32 v186, v186, s63, -v246
mm v[24:27],a[144:147],a[64:67],v[24:27]
dt a[138:139],v145 offset:1024
)"
R"(v_fma_f32 v187, v187, s63, -v246
mm v[24:27],a[148:151],a[68:71],v[24:27]
dt a[140:141],v145 offset:8192
ex v248,v248
mm v[56:59],a[144:147],a[72:75],v[56:59]
dt a[142:143],v145 offset:9216
ex v184,v184
mm v[56:59],a[148:151],a[76:79],v[56:59]
sa s56,s52,0x200
bl v148,s[36:39],s56 offen lds
mm v[88:91],a[144:147],a[80:83],v[88:91]
sa s56,s52,0x1e00
bl v149,s[36:39],s56 offen offset:1024 lds
mm v[88:91],a[148:151],a[84:87],v[88:91]
sa s56,s60,0xba00
bl v148,s[36:39],s56 offen offset:2048 lds
)"
R"(mm v[120:123],a[144:147],a[88:91],v[120:123]
ex v249,v249
mm v[120:123],a[148:151],a[92:95],v[120:123]
s_waitcnt lgkmcnt(15)
dt a[144:145],v146 offset:0
ex v185,v185
mm v[28:31],a[152:155],a[64:67],v[28:31]
dt a[146:147],v146 offset:1024
ex v250,v250
mm v[28:31],a[156:159],a[68:71],v[28:31]
dt a[148:149],v146 offset:8192
ex v186,v186
mm v[60:63],a[152:155],a[72:75],v[60:63]
dt a[150:151],v146 offset:9216
ex v251,v251
mm v[60:63],a[156:159],a[76:79],v[60:63]
)"
R"(sa s56,s60,0xd600
bl v149,s[36:39],s56 offen offset:3072 lds
mm v[92:95],a[152:155],a[80:83],v[92:95]
sa m0,s54,0xd000
ex v187,v187
mm v[92:95],a[156:159],a[84:87],v[92:95]
sa s56,s61,0x3000
buffer_store_dwordx4 a[212:215], v214, s[44:47], s56 offen offset:2048 sc1
mm v[124:127],a[152:155],a[88:91],v[124:127]
sa s56,s52,0x10200
bl v148,s[36:39],s56 offen lds
v_add_f32 v246, v184, v185
mm v[124:127],a[156:159],a[92:95],v[124:127]
s_waitcnt lgkmcnt(8)
dt a[152:153],v147 offset:0
v_add_f32 v247, v248, v249
)"
R"(v_add_f32 v246, v246, v186
mm v[0:3],a[96:99],a[16:19],v[0:3]
dt a[154:155],v147 offset:1024
v_add_f32 v247, v247, v250
v_add_f32 v246, v246, v187
mm v[0:3],a[100:103],a[20:23],v[0:3]
dt a[156:157],v147 offset:8192
v_add_f32 v247, v247, v251
mm v[32:35],a[96:99],a[24:27],v[32:35]
dt a[158:159],v147 offset:9216
v_max3_f32 v230, v188, v189, v190
mm v[32:35],a[100:103],a[28:31],v[32:35]
sa s56,s52,0x11e00
bl v149,s[36:39],s56 offen offset:1024 lds
mm v[64:67],a[96:99],a[32:35],v[64:67]
v_permlane32_swap_b32 v246, v247
)"
R"(mm v[64:67],a[100:103],a[36:39],v[64:67]
v_add_f32 v246, v246, v247
v_mov_b32 v247, v246
mm v[96:99],a[96:99],a[40:43],v[96:99]
sa s56,s60,0x1ba00
bl v148,s[36:39],s56 offen offset:2048 lds
mm v[96:99],a[100:103],a[44:47],v[96:99]
s_waitcnt lgkmcnt(15)
v_permlane16_swap_b32 v246, v247
mm v[4:7],a[104:107],a[16:19],v[4:7]
v_add_f32 v246, v246, v247
v_mov_b32 v247, v246
mm v[4:7],a[108:111],a[20:23],v[4:7]
v_max3_f32 v231, -v188, -v189, -v190
v_max_f32 v230, v230, v191
mm v[36:39],a[104:107],a[24:27],v[36:39]
)"
R"(s_waitcnt vmcnt(8) lgkmcnt(0)
s_barrier
v_permlane32_swap_b32 v246, v247
mm v[36:39],a[108:111],a[28:31],v[36:39]
dt a[96:97],v132 offset:0
v_rcp_f32 v246, v246
mm v[68:71],a[104:107],a[32:35],v[68:71]
dt a[98:99],v132 offset:1024
v_mul_f32 v184, v184, v246
v_mul_f32 v185, v185, v246
mm v[68:71],a[108:111],a[36:39],v[68:71]
dt a[100:101],v132 offset:8192
v_rcp_f32 v247, v247
mm v[100:103],a[104:107],a[40:43],v[100:103]
dt a[102:103],v132 offset:9216
v_mul_f32 v186, v186, v246
)"
R"(v_mul_f32 v187, v187, v246
mm v[100:103],a[108:111],a[44:47],v[100:103]
s_waitcnt lgkmcnt(15)
dt a[104:105],v133 offset:0
cv v184,v184,v185
v_mul_f32 v248, v248, v247
mm v[8:11],a[112:115],a[16:19],v[8:11]
dt a[106:107],v133 offset:1024
v_mul_f32 v249, v249, v247
cv v185,v186,v187
mm v[8:11],a[116:119],a[20:23],v[8:11]
dt a[108:109],v133 offset:8192
v_mul_f32 v250, v250, v247
v_mul_f32 v251, v251, v247
mm v[40:43],a[112:115],a[24:27],v[40:43]
dt a[110:111],v133 offset:9216
)"
R"(cv v186,v248,v249
cv v187,v250,v251
mm v[40:43],a[116:119],a[28:31],v[40:43]
dr a[64:67],v128 offset:0
sa s56,s61,0x3000
buffer_store_dwordx4 a[220:223], v214, s[44:47], s56 offen offset:3072 sc1
mm v[72:75],a[112:115],a[32:35],v[72:75]
dr a[68:71],v129 offset:0
v_max_f32 v231, v231, -v191
mm v[72:75],a[116:119],a[36:39],v[72:75]
sa s56,s60,0x1d600
bl v149,s[36:39],s56 offen offset:3072 lds
mm v[104:107],a[112:115],a[40:43],v[104:107]
sa m0,s55,0xc000
v_permlane32_swap_b32 v230, v231
mm v[104:107],a[116:119],a[44:47],v[104:107]
)"
R"(s_waitcnt lgkmcnt(15)
dt a[112:113],v134 offset:0
v_max_f32 v230, v230, v231
mm v[12:15],a[120:123],a[16:19],v[12:15]
dt a[114:115],v134 offset:1024
cv v236,v192,v193
mm v[12:15],a[124:127],a[20:23],v[12:15]
dt a[116:117],v134 offset:8192
cv v237,v194,v195
mm v[44:47],a[120:123],a[24:27],v[44:47]
dt a[118:119],v134 offset:9216
v_mov_b32 v231, v230
mm v[44:47],a[124:127],a[28:31],v[44:47]
dr a[72:75],v128 offset:2048
v_mfma_f32_16x16x16_bf16 v[192:195], v[220:221], v[236:237], 0
sa s56,s53,0x200000
)"
R"(bl v150,s[40:43],s56 offen lds
mm v[76:79],a[120:123],a[32:35],v[76:79]
dr a[76:79],v129 offset:2048
v_permlane16_swap_b32 v230, v231
mm v[76:79],a[124:127],a[36:39],v[76:79]
sa s56,s53,0x207c00
bl v150,s[40:43],s56 offen offset:1024 lds
v_max_f32 v230, v230, v231
mm v[108:111],a[120:123],a[40:43],v[108:111]
sa s56,s53,0x20f800
bl v151,s[40:43],s56 offen offset:2048 lds
v_mov_b32 v231, v230
mm v[108:111],a[124:127],a[44:47],v[108:111]
s_waitcnt lgkmcnt(15)
dt a[120:121],v135 offset:0
v_max3_f32 v238, v192, v193, v194
)"
R"(mm v[16:19],a[128:131],a[16:19],v[16:19]
dt a[122:123],v135 offset:1024
v_permlane32_swap_b32 v230, v231
mm v[16:19],a[132:135],a[20:23],v[16:19]
dt a[124:125],v135 offset:8192
v_mul_f32 v231, s63, v231
mm v[48:51],a[128:131],a[24:27],v[48:51]
dt a[126:127],v135 offset:9216
v_mul_f32 v230, s63, v230
v_fma_f32 v232, -v188, s63, -v231
mm v[48:51],a[132:135],a[28:31],v[48:51]
dr a[80:83],v128 offset:4096
v_fma_f32 v233, -v189, s63, -v231
v_fma_f32 v234, -v190, s63, -v231
mm v[80:83],a[128:131],a[32:35],v[80:83]
dr a[84:87],v129 offset:4096
)"
R"(v_fma_f32 v235, -v191, s63, -v231
v_fma_f32 v188, v188, s63, -v230
mm v[80:83],a[132:135],a[36:39],v[80:83]
sa s56,s61,0x8000
buffer_store_dwordx4 a[228:231], v214, s[44:47], s56 offen offset:0 sc1
mm v[112:115],a[128:131],a[40:43],v[112:115]
sa s56,s53,0x217400
bl v151,s[40:43],s56 offen offset:3072 lds
v_fma_f32 v189, v189, s63, -v230
mm v[112:115],a[132:135],a[44:47],v[112:115]
sa m0,s54,0x18000
s_waitcnt lgkmcnt(15)
dt a[128:129],v136 offset:0
v_fma_f32 v190, v190, s63, -v230
v_fma_f32 v191, v191, s63, -v230
mm v[20:23],a[136:139],a[16:19],v[20:23]
)"
R"(dt a[130:131],v136 offset:1024
ex v232,v232
mm v[20:23],a[140:143],a[20:23],v[20:23]
dt a[132:133],v136 offset:8192
ex v188,v188
mm v[52:55],a[136:139],a[24:27],v[52:55]
dt a[134:135],v136 offset:9216
ex v233,v233
mm v[52:55],a[140:143],a[28:31],v[52:55]
dr a[88:91],v128 offset:6144
ex v189,v189
mm v[84:87],a[136:139],a[32:35],v[84:87]
dr a[92:95],v129 offset:6144
ex v234,v234
mm v[84:87],a[140:143],a[36:39],v[84:87]
sa s56,s52,0x280
)"
R"(bl v148,s[36:39],s56 offen lds
mm v[116:119],a[136:139],a[40:43],v[116:119]
sa s56,s52,0x1e80
bl v149,s[36:39],s56 offen offset:1024 lds
mm v[116:119],a[140:143],a[44:47],v[116:119]
s_waitcnt lgkmcnt(15)
dt a[136:137],v137 offset:0
ex v190,v190
mm v[24:27],a[144:147],a[16:19],v[24:27]
dt a[138:139],v137 offset:1024
ex v235,v235
mm v[24:27],a[148:151],a[20:23],v[24:27]
dt a[140:141],v137 offset:8192
ex v191,v191
mm v[56:59],a[144:147],a[24:27],v[56:59]
dt a[142:143],v137 offset:9216
)"
R"(v_add_f32 v230, v188, v189
v_add_f32 v231, v232, v233
mm v[56:59],a[148:151],a[28:31],v[56:59]
sa s56,s60,0xba80
bl v148,s[36:39],s56 offen offset:2048 lds
v_add_f32 v230, v230, v190
mm v[88:91],a[144:147],a[32:35],v[88:91]
sa s56,s60,0xd680
bl v149,s[36:39],s56 offen offset:3072 lds
v_add_f32 v231, v231, v234
mm v[88:91],a[148:151],a[36:39],v[88:91]
sa m0,s54,0x19000
v_add_f32 v230, v230, v191
v_add_f32 v231, v231, v235
mm v[120:123],a[144:147],a[40:43],v[120:123]
sa s56,s52,0x10280
)"
R"(bl v148,s[36:39],s56 offen lds
mm v[120:123],a[148:151],a[44:47],v[120:123]
s_waitcnt lgkmcnt(15)
dt a[144:145],v138 offset:0
v_permlane32_swap_b32 v230, v231
mm v[28:31],a[152:155],a[16:19],v[28:31]
dt a[146:147],v138 offset:1024
v_add_f32 v230, v230, v231
v_mov_b32 v231, v230
mm v[28:31],a[156:159],a[20:23],v[28:31]
dt a[148:149],v138 offset:8192
v_max3_f32 v239, -v192, -v193, -v194
mm v[60:63],a[152:155],a[24:27],v[60:63]
dt a[150:151],v138 offset:9216
sa s56,s61,0x8000
buffer_store_dwordx4 a[236:239], v214, s[44:47], s56 offen offset:1024 sc1
)"
R"(mm v[60:63],a[156:159],a[28:31],v[60:63]
sa s56,s52,0x11e80
bl v149,s[36:39],s56 offen offset:1024 lds
mm v[92:95],a[152:155],a[32:35],v[92:95]
s_getpc_b64 s[88:89]
L_ct_q:
sa s88,s88,(L_att_base-L_ct_q)
s_addc_u32 s89, s89, 0
mm v[92:95],a[156:159],a[36:39],v[92:95]
sa s56,s60,0x1ba80
bl v148,s[36:39],s56 offen offset:2048 lds
mm v[124:127],a[152:155],a[40:43],v[124:127]
s_and_b32 s89, s89, 0xffff
v_permlane16_swap_b32 v230, v231
mm v[124:127],a[156:159],a[44:47],v[124:127]
s_mov_b32 s90, (L_att_end-L_att_base)
)"
R"(s_waitcnt lgkmcnt(8)
dt a[152:153],v139 offset:0
v_add_f32 v230, v230, v231
v_mov_b32 v231, v230
mm v[0:3],a[96:99],a[64:67],v[0:3]
s_mov_b32 s91, 0x20000
dt a[154:155],v139 offset:1024
v_max_f32 v238, v238, v195
mm v[0:3],a[100:103],a[68:71],v[0:3]
dt a[156:157],v139 offset:8192
v_permlane32_swap_b32 v230, v231
mm v[32:35],a[96:99],a[72:75],v[32:35]
dt a[158:159],v139 offset:9216
v_rcp_f32 v230, v230
mm v[32:35],a[100:103],a[76:79],v[32:35]
sa s56,s60,0x1d680
)"
R"(bl v149,s[36:39],s56 offen offset:3072 lds
v_mul_f32 v188, v188, v230
mm v[64:67],a[96:99],a[80:83],v[64:67]
v_mul_f32 v189, v189, v230
v_rcp_f32 v231, v231
mm v[64:67],a[100:103],a[84:87],v[64:67]
sa m0,s55,0x18000
v_mul_f32 v190, v190, v230
v_mul_f32 v191, v191, v230
cv v188,v188,v189
mm v[96:99],a[96:99],a[88:91],v[96:99]
sa s56,s53,0x280000
bl v150,s[40:43],s56 offen lds
v_mul_f32 v232, v232, v231
mm v[96:99],a[100:103],a[92:95],v[96:99]
s_waitcnt lgkmcnt(15)
)"
R"(v_mul_f32 v233, v233, v231
cv v189,v190,v191
v_mul_f32 v234, v234, v231
mm v[4:7],a[104:107],a[64:67],v[4:7]
v_mul_f32 v235, v235, v231
cv v190,v232,v233
cv v191,v234,v235
mm v[4:7],a[108:111],a[68:71],v[4:7]
v_max_f32 v239, v239, -v195
mm v[36:39],a[104:107],a[72:75],v[36:39]
s_waitcnt vmcnt(10) lgkmcnt(0)
s_barrier
mm v[36:39],a[108:111],a[76:79],v[36:39]
dt a[96:97],v132 offset:49152
mm v[68:71],a[104:107],a[80:83],v[68:71]
dt a[98:99],v132 offset:50176
)"
R"(mm v[68:71],a[108:111],a[84:87],v[68:71]
dt a[100:101],v132 offset:57344
mm v[100:103],a[104:107],a[88:91],v[100:103]
dt a[102:103],v132 offset:58368
sa s56,s61,0x8000
buffer_store_dwordx4 a[244:247], v214, s[44:47], s56 offen offset:2048 sc1
mm v[100:103],a[108:111],a[92:95],v[100:103]
s_waitcnt lgkmcnt(15)
dt a[104:105],v133 offset:49152
mm v[8:11],a[112:115],a[64:67],v[8:11]
dt a[106:107],v133 offset:50176
v_permlane32_swap_b32 v238, v239
mm v[8:11],a[116:119],a[68:71],v[8:11]
dt a[108:109],v133 offset:57344
v_max_f32 v238, v238, v239
mm v[40:43],a[112:115],a[72:75],v[40:43]
)"
R"(dt a[110:111],v133 offset:58368
v_mov_b32 v239, v238
mm v[40:43],a[116:119],a[76:79],v[40:43]
dr a[16:19],v128 offset:49152
mm v[72:75],a[112:115],a[80:83],v[72:75]
dr a[20:23],v129 offset:49152
v_permlane16_swap_b32 v238, v239
mm v[72:75],a[116:119],a[84:87],v[72:75]
sa s56,s53,0x287c00
bl v150,s[40:43],s56 offen offset:1024 lds
v_max_f32 v238, v238, v239
mm v[104:107],a[112:115],a[88:91],v[104:107]
sa s56,s53,0x28f800
bl v151,s[40:43],s56 offen offset:2048 lds
v_mov_b32 v239, v238
mm v[104:107],a[116:119],a[92:95],v[104:107]
)"
R"(s_waitcnt lgkmcnt(15)
dt a[112:113],v134 offset:49152
mm v[12:15],a[120:123],a[64:67],v[12:15]
dt a[114:115],v134 offset:50176
v_permlane32_swap_b32 v238, v239
mm v[12:15],a[124:127],a[68:71],v[12:15]
dt a[116:117],v134 offset:57344
v_mul_f32 v239, s63, v239
mm v[44:47],a[120:123],a[72:75],v[44:47]
dt a[118:119],v134 offset:58368
v_mul_f32 v238, s63, v238
v_fma_f32 v240, -v192, s63, -v239
mm v[44:47],a[124:127],a[76:79],v[44:47]
dr a[24:27],v128 offset:51200
v_fma_f32 v241, -v193, s63, -v239
v_fma_f32 v242, -v194, s63, -v239
)"
R"(mm v[76:79],a[120:123],a[80:83],v[76:79]
dr a[28:31],v129 offset:51200
v_fma_f32 v243, -v195, s63, -v239
v_fma_f32 v192, v192, s63, -v238
mm v[76:79],a[124:127],a[84:87],v[76:79]
sa s56,s53,0x297400
bl v151,s[40:43],s56 offen offset:3072 lds
v_fma_f32 v193, v193, s63, -v238
mm v[108:111],a[120:123],a[88:91],v[108:111]
sa s56,s61,0x8000
buffer_store_dwordx4 a[252:255], v214, s[44:47], s56 offen offset:3072 sc1
mm v[108:111],a[124:127],a[92:95],v[108:111]
s_waitcnt lgkmcnt(15)
dt a[120:121],v135 offset:49152
v_fma_f32 v194, v194, s63, -v238
v_fma_f32 v195, v195, s63, -v238
)"
R"(mm v[16:19],a[128:131],a[64:67],v[16:19]
dt a[122:123],v135 offset:50176
ex v240,v240
mm v[16:19],a[132:135],a[68:71],v[16:19]
dt a[124:125],v135 offset:57344
ex v192,v192
mm v[48:51],a[128:131],a[72:75],v[48:51]
dt a[126:127],v135 offset:58368
ex v241,v241
mm v[48:51],a[132:135],a[76:79],v[48:51]
dr a[32:35],v128 offset:53248
ex v193,v193
mm v[80:83],a[128:131],a[80:83],v[80:83]
dr a[36:39],v129 offset:53248
ex v242,v242
mm v[80:83],a[132:135],a[84:87],v[80:83]
)"
R"(buffer_load_dword v154, v215, s[88:91], 0 offen
mm v[112:115],a[128:131],a[88:91],v[112:115]
sa m0,s54,0x0
ex v194,v194
mm v[112:115],a[132:135],a[92:95],v[112:115]
s_waitcnt lgkmcnt(15)
dt a[128:129],v136 offset:49152
ex v243,v243
mm v[20:23],a[136:139],a[64:67],v[20:23]
dt a[130:131],v136 offset:50176
ex v195,v195
mm v[20:23],a[140:143],a[68:71],v[20:23]
dt a[132:133],v136 offset:57344
v_add_f32 v238, v192, v193
v_add_f32 v239, v240, v241
mm v[52:55],a[136:139],a[72:75],v[52:55]
)"
R"(dt a[134:135],v136 offset:58368
v_add_f32 v238, v238, v194
v_add_f32 v239, v239, v242
mm v[52:55],a[140:143],a[76:79],v[52:55]
dr a[40:43],v128 offset:55296
v_add_f32 v238, v238, v195
v_add_f32 v239, v239, v243
mm v[84:87],a[136:139],a[80:83],v[84:87]
dr a[44:47],v129 offset:55296
mm v[84:87],a[140:143],a[84:87],v[84:87]
sa s56,s52,0x300
bl v148,s[36:39],s56 offen lds
mm v[116:119],a[136:139],a[88:91],v[116:119]
sa s56,s52,0x1f00
bl v149,s[36:39],s56 offen offset:1024 lds
mm v[116:119],a[140:143],a[92:95],v[116:119]
)"
R"(s_waitcnt lgkmcnt(15)
dt a[136:137],v137 offset:49152
v_permlane32_swap_b32 v238, v239
mm v[24:27],a[144:147],a[64:67],v[24:27]
dt a[138:139],v137 offset:50176
v_add_f32 v238, v238, v239
v_mov_b32 v239, v238
mm v[24:27],a[148:151],a[68:71],v[24:27]
dt a[140:141],v137 offset:57344
mm v[56:59],a[144:147],a[72:75],v[56:59]
dt a[142:143],v137 offset:58368
v_permlane16_swap_b32 v238, v239
mm v[56:59],a[148:151],a[76:79],v[56:59]
sa s56,s61,0x9000
buffer_store_dwordx4 a[160:163], v214, s[44:47], s56 offen offset:0 sc1
mm v[88:91],a[144:147],a[80:83],v[88:91]
)"
R"(sa s56,s60,0xbb00
bl v148,s[36:39],s56 offen offset:2048 lds
v_add_f32 v238, v238, v239
mm v[88:91],a[148:151],a[84:87],v[88:91]
sa s56,s60,0xd700
bl v149,s[36:39],s56 offen offset:3072 lds
v_mov_b32 v239, v238
mm v[120:123],a[144:147],a[88:91],v[120:123]
sa m0,s54,0x1000
mm v[120:123],a[148:151],a[92:95],v[120:123]
s_waitcnt lgkmcnt(15)
dt a[144:145],v138 offset:49152
v_permlane32_swap_b32 v238, v239
s_nop 0
v_rcp_f32 v239, v239
v_rcp_f32 v238, v238
)"
R"(v_mul_f32 v242, v242, v239
v_mul_f32 v243, v243, v239
v_mul_f32 v192, v192, v238
v_mul_f32 v193, v193, v238
cv v192,v192,v193
v_mul_f32 v194, v194, v238
v_mul_f32 v195, v195, v238
cv v193,v194,v195
v_mul_f32 v240, v240, v239
v_mul_f32 v241, v241, v239
cv v194,v240,v241
cv v195,v242,v243
mm v[228:231],v[180:183],a[0:3],0
sa s56,s52,0x10300
bl v148,s[36:39],s56 offen lds
mm v[232:235],v[184:187],a[0:3],0
)"
R"(sa s56,s52,0x11f00
bl v149,s[36:39],s56 offen offset:1024 lds
mm v[236:239],v[180:183],a[4:7],0
sa s56,s60,0x1bb00
bl v148,s[36:39],s56 offen offset:2048 lds
mm v[240:243],v[184:187],a[4:7],0
mm v[28:31],a[152:155],a[64:67],v[28:31]
dt a[146:147],v138 offset:50176
mm v[28:31],a[156:159],a[68:71],v[28:31]
dt a[148:149],v138 offset:57344
cv v228,v228,v229
mm v[60:63],a[152:155],a[72:75],v[60:63]
dt a[150:151],v138 offset:58368
cv v229,v230,v231
mm v[60:63],a[156:159],a[76:79],v[60:63]
sa s56,s60,0x1d700
)"
R"(bl v149,s[36:39],s56 offen offset:3072 lds
mm v[92:95],a[152:155],a[80:83],v[92:95]
sa m0,s55,0x0
cv v230,v232,v233
cv v236,v236,v237
mm v[92:95],a[156:159],a[84:87],v[92:95]
sa s56,s53,0x300000
bl v150,s[40:43],s56 offen lds
mm v[124:127],a[152:155],a[88:91],v[124:127]
cv v231,v234,v235
cv v237,v238,v239
mm v[124:127],a[156:159],a[92:95],v[124:127]
s_waitcnt lgkmcnt(8)
dt a[152:153],v139 offset:49152
cv v238,v240,v241
mm v[0:3],a[96:99],a[16:19],v[0:3]
)"
R"(dt a[154:155],v139 offset:50176
cv v239,v242,v243
mm v[0:3],a[100:103],a[20:23],v[0:3]
dt a[156:157],v139 offset:57344
mm v[32:35],a[96:99],a[24:27],v[32:35]
dt a[158:159],v139 offset:58368
mm v[32:35],a[100:103],a[28:31],v[32:35]
sa s56,s53,0x307c00
bl v150,s[40:43],s56 offen offset:1024 lds
mm v[64:67],a[96:99],a[32:35],v[64:67]
mm v[64:67],a[100:103],a[36:39],v[64:67]
sa s56,s61,0x9000
buffer_store_dwordx4 a[168:171], v214, s[44:47], s56 offen offset:1024 sc1
mm v[96:99],a[96:99],a[40:43],v[96:99]
sa s56,s53,0x30f800
bl v151,s[40:43],s56 offen offset:2048 lds
)"
R"(mm v[96:99],a[100:103],a[44:47],v[96:99]
s_waitcnt lgkmcnt(15)
mm v[180:183],v[228:231],v[196:199],0
mm v[200:203],v[228:231],a[8:11],0
mm v[184:187],v[236:239],v[196:199],0
mm v[208:211],v[236:239],a[8:11],0
mm v[4:7],a[104:107],a[16:19],v[4:7]
mm v[4:7],a[108:111],a[20:23],v[4:7]
mm v[36:39],a[104:107],a[24:27],v[36:39]
s_waitcnt vmcnt(15) lgkmcnt(0)
s_barrier
mm v[36:39],a[108:111],a[28:31],v[36:39]
dt a[96:97],v140 offset:0
mm v[228:231],v[188:191],a[0:3],0
sa s56,s53,0x317400
bl v151,s[40:43],s56 offen offset:3072 lds
)"
R"(mm v[232:235],v[192:195],a[0:3],0
mm v[236:239],v[188:191],a[4:7],0
mm v[240:243],v[192:195],a[4:7],0
mm v[68:71],a[104:107],a[32:35],v[68:71]
dt a[98:99],v140 offset:1024
mm v[68:71],a[108:111],a[36:39],v[68:71]
dt a[100:101],v140 offset:8192
mm v[100:103],a[104:107],a[40:43],v[100:103]
dt a[102:103],v140 offset:9216
mm v[100:103],a[108:111],a[44:47],v[100:103]
sa m0,s54,0xc000
s_waitcnt lgkmcnt(15)
dt a[104:105],v141 offset:0
mm v[8:11],a[112:115],a[16:19],v[8:11]
dt a[106:107],v141 offset:1024
cv v228,v228,v229
)"
R"(mm v[8:11],a[116:119],a[20:23],v[8:11]
dt a[108:109],v141 offset:8192
cv v229,v230,v231
mm v[40:43],a[112:115],a[24:27],v[40:43]
dt a[110:111],v141 offset:9216
cv v230,v232,v233
mm v[40:43],a[116:119],a[28:31],v[40:43]
dr a[64:67],v130 offset:0
cv v236,v236,v237
mm v[72:75],a[112:115],a[32:35],v[72:75]
dr a[68:71],v131 offset:0
cv v231,v234,v235
mm v[72:75],a[116:119],a[36:39],v[72:75]
sa s56,s52,0x380
bl v148,s[36:39],s56 offen lds
mm v[104:107],a[112:115],a[40:43],v[104:107]
)"
R"(sa s56,s52,0x1f80
bl v149,s[36:39],s56 offen offset:1024 lds
mm v[104:107],a[116:119],a[44:47],v[104:107]
s_waitcnt lgkmcnt(15)
dt a[112:113],v142 offset:0
cv v237,v238,v239
mm v[12:15],a[120:123],a[16:19],v[12:15]
s_lshl_b32 s62, s50, 12
sa s62,s62,0x24000
dt a[114:115],v142 offset:1024
mm v[12:15],a[124:127],a[20:23],v[12:15]
dt a[116:117],v142 offset:8192
v_add_u32 v252, s62, v214
mm v[44:47],a[120:123],a[24:27],v[44:47]
dt a[118:119],v142 offset:9216
cv v238,v240,v241
)"
R"(mm v[44:47],a[124:127],a[28:31],v[44:47]
dr a[72:75],v130 offset:2048
cv v239,v242,v243
mm v[180:183],v[228:231],v[204:207],v[180:183]
sa s56,s61,0x9000
buffer_store_dwordx4 a[176:179], v214, s[44:47], s56 offen offset:2048 sc1
mm v[200:203],v[228:231],a[8:11],v[200:203]
sa s56,s60,0xbb80
bl v148,s[36:39],s56 offen offset:2048 lds
mm v[184:187],v[236:239],v[204:207],v[184:187]
mm v[208:211],v[236:239],a[8:11],v[208:211]
mm v[76:79],a[120:123],a[32:35],v[76:79]
dr a[76:79],v131 offset:2048
mm v[76:79],a[124:127],a[36:39],v[76:79]
sa s56,s60,0xd780
bl v149,s[36:39],s56 offen offset:3072 lds
)"
R"(mm v[108:111],a[120:123],a[40:43],v[108:111]
cv v228,v164,v165
cv v229,v166,v167
mm v[108:111],a[124:127],a[44:47],v[108:111]
s_waitcnt lgkmcnt(15)
dt a[120:121],v143 offset:0
mm v[16:19],a[128:131],a[16:19],v[16:19]
dt a[122:123],v143 offset:1024
mm v[16:19],a[132:135],a[20:23],v[16:19]
sa m0,s54,0xd000
dt a[124:125],v143 offset:8192
mm v[48:51],a[128:131],a[24:27],v[48:51]
dt a[126:127],v143 offset:9216
mm v[48:51],a[132:135],a[28:31],v[48:51]
dr a[80:83],v130 offset:4096
v_mfma_f32_16x16x16_bf16 v[164:167], v[220:221], v[228:229], 0
)"
R"(sa s56,s52,0x10380
bl v148,s[36:39],s56 offen lds
mm v[80:83],a[128:131],a[32:35],v[80:83]
dr a[84:87],v131 offset:4096
mm v[80:83],a[132:135],a[36:39],v[80:83]
mm v[112:115],a[128:131],a[40:43],v[112:115]
mm v[112:115],a[132:135],a[44:47],v[112:115]
s_waitcnt lgkmcnt(15)
dt a[128:129],v144 offset:0
mm v[20:23],a[136:139],a[16:19],v[20:23]
dt a[130:131],v144 offset:1024
v_max3_f32 v230, v164, v165, v166
v_max3_f32 v231, -v164, -v165, -v166
mm v[20:23],a[140:143],a[20:23],v[20:23]
dt a[132:133],v144 offset:8192
v_max_f32 v230, v230, v167
)"
R"(v_max_f32 v231, v231, -v167
mm v[52:55],a[136:139],a[24:27],v[52:55]
dt a[134:135],v144 offset:9216
mm v[52:55],a[140:143],a[28:31],v[52:55]
dr a[88:91],v130 offset:6144
v_permlane32_swap_b32 v230, v231
mm v[84:87],a[136:139],a[32:35],v[84:87]
dr a[92:95],v131 offset:6144
v_max_f32 v230, v230, v231
v_mov_b32 v231, v230
mm v[84:87],a[140:143],a[36:39],v[84:87]
sa s56,s61,0x9000
buffer_store_dwordx4 a[184:187], v214, s[44:47], s56 offen offset:3072 sc1
mm v[116:119],a[136:139],a[40:43],v[116:119]
sa s56,s52,0x11f80
bl v149,s[36:39],s56 offen offset:1024 lds
)"
R"(mm v[116:119],a[140:143],a[44:47],v[116:119]
s_waitcnt lgkmcnt(15)
dt a[136:137],v145 offset:0
v_permlane16_swap_b32 v230, v231
mm v[24:27],a[144:147],a[16:19],v[24:27]
dt a[138:139],v145 offset:1024
v_max_f32 v230, v230, v231
v_mov_b32 v231, v230
mm v[24:27],a[148:151],a[20:23],v[24:27]
dt a[140:141],v145 offset:8192
mm v[56:59],a[144:147],a[24:27],v[56:59]
dt a[142:143],v145 offset:9216
v_permlane32_swap_b32 v230, v231
mm v[56:59],a[148:151],a[28:31],v[56:59]
sa s56,s60,0x1bb80
bl v148,s[36:39],s56 offen offset:2048 lds
)"
R"(v_mul_f32 v231, s63, v231
mm v[88:91],a[144:147],a[32:35],v[88:91]
sa s56,s60,0x1d780
bl v149,s[36:39],s56 offen offset:3072 lds
v_mul_f32 v230, s63, v230
mm v[88:91],a[148:151],a[36:39],v[88:91]
sa m0,s55,0xc000
v_fma_f32 v232, -v164, s63, -v231
v_fma_f32 v233, -v165, s63, -v231
v_fma_f32 v234, -v166, s63, -v231
mm v[120:123],a[144:147],a[40:43],v[120:123]
sa s56,s53,0x380000
bl v150,s[40:43],s56 offen lds
v_fma_f32 v235, -v167, s63, -v231
mm v[120:123],a[148:151],a[44:47],v[120:123]
s_waitcnt lgkmcnt(15)
)"
R"(dt a[144:145],v146 offset:0
v_fma_f32 v164, v164, s63, -v230
v_fma_f32 v165, v165, s63, -v230
mm v[28:31],a[152:155],a[16:19],v[28:31]
dt a[146:147],v146 offset:1024
v_fma_f32 v166, v166, s63, -v230
v_fma_f32 v167, v167, s63, -v230
mm v[28:31],a[156:159],a[20:23],v[28:31]
dt a[148:149],v146 offset:8192
ex v232,v232
mm v[60:63],a[152:155],a[24:27],v[60:63]
dt a[150:151],v146 offset:9216
ex v164,v164
mm v[60:63],a[156:159],a[28:31],v[60:63]
sa s56,s53,0x387c00
bl v150,s[40:43],s56 offen offset:1024 lds
)"
R"(mm v[92:95],a[152:155],a[32:35],v[92:95]
ex v233,v233
mm v[92:95],a[156:159],a[36:39],v[92:95]
ex v165,v165
mm v[124:127],a[152:155],a[40:43],v[124:127]
sa s56,s53,0x38f800
bl v151,s[40:43],s56 offen offset:2048 lds
mm v[124:127],a[156:159],a[44:47],v[124:127]
s_waitcnt lgkmcnt(8)
dt a[152:153],v147 offset:0
ex v234,v234
mm v[0:3],a[96:99],a[64:67],v[0:3]
dt a[154:155],v147 offset:1024
ex v166,v166
mm v[0:3],a[100:103],a[68:71],v[0:3]
dt a[156:157],v147 offset:8192
)"
R"(ex v235,v235
mm v[32:35],a[96:99],a[72:75],v[32:35]
dt a[158:159],v147 offset:9216
ex v167,v167
mm v[32:35],a[100:103],a[76:79],v[32:35]
ds_write_b128 v252, v[180:183] offset:0
sa s56,s61,0xa000
buffer_store_dwordx4 a[208:211], v214, s[44:47], s56 offen offset:2048 sc1
mm v[64:67],a[96:99],a[80:83],v[64:67]
ds_write_b128 v252, v[184:187] offset:1024
mm v[64:67],a[100:103],a[84:87],v[64:67]
ds_write_b128 v252, v[200:203] offset:2048
mm v[96:99],a[96:99],a[88:91],v[96:99]
ds_write_b128 v252, v[208:211] offset:3072
mm v[96:99],a[100:103],a[92:95],v[96:99]
s_waitcnt lgkmcnt(15)
)"
R"(sa s56,s53,0x397400
bl v151,s[40:43],s56 offen offset:3072 lds
v_add_f32 v230, v164, v165
cv v236,v168,v169
cv v237,v170,v171
v_add_f32 v231, v232, v233
v_add_f32 v230, v230, v166
v_mfma_f32_16x16x16_bf16 v[168:171], v[220:221], v[236:237], 0
v_add_f32 v231, v231, v234
v_add_f32 v230, v230, v167
v_add_f32 v231, v231, v235
mm v[4:7],a[104:107],a[64:67],v[4:7]
mm v[4:7],a[108:111],a[68:71],v[4:7]
v_permlane32_swap_b32 v230, v231
mm v[36:39],a[104:107],a[72:75],v[36:39]
s_waitcnt vmcnt(15) lgkmcnt(0)
)"
R"(s_barrier
v_add_f32 v230, v230, v231
v_mov_b32 v231, v230
mm v[36:39],a[108:111],a[76:79],v[36:39]
s_lshl_b32 s62, s50, 10
sa s62,s62,0x24000
v_max3_f32 v238, v168, v169, v170
v_max3_f32 v239, -v168, -v169, -v170
mm v[68:71],a[104:107],a[80:83],v[68:71]
v_add_u32 v252, s62, v214
v_permlane16_swap_b32 v230, v231
mm v[68:71],a[108:111],a[84:87],v[68:71]
dr v[180:183],v252 offset:0
v_add_f32 v230, v230, v231
v_mov_b32 v231, v230
mm v[100:103],a[104:107],a[88:91],v[100:103]
)"
R"(dr v[184:187],v252 offset:4096
v_max_f32 v238, v238, v171
v_max_f32 v239, v239, -v171
mm v[100:103],a[108:111],a[92:95],v[100:103]
s_waitcnt lgkmcnt(15)
dr v[200:203],v252 offset:8192
v_permlane32_swap_b32 v230, v231
mm v[8:11],a[112:115],a[64:67],v[8:11]
dr v[208:211],v252 offset:12288
v_rcp_f32 v230, v230
mm v[8:11],a[116:119],a[68:71],v[8:11]
dt a[96:97],v132 offset:0
v_mul_f32 v164, v164, v230
v_mul_f32 v165, v165, v230
mm v[40:43],a[112:115],a[72:75],v[40:43]
dt a[98:99],v132 offset:1024
)"
R"(v_rcp_f32 v231, v231
mm v[40:43],a[116:119],a[76:79],v[40:43]
dt a[100:101],v132 offset:8192
v_mul_f32 v166, v166, v230
v_mul_f32 v167, v167, v230
mm v[72:75],a[112:115],a[80:83],v[72:75]
dt a[102:103],v132 offset:9216
cv v164,v164,v165
v_mul_f32 v232, v232, v231
mm v[72:75],a[116:119],a[84:87],v[72:75]
dt a[104:105],v133 offset:0
v_mul_f32 v233, v233, v231
cv v165,v166,v167
mm v[104:107],a[112:115],a[88:91],v[104:107]
dt a[106:107],v133 offset:1024
sa s56,s61,0xa000
)"
R"(buffer_store_dwordx4 a[216:219], v214, s[44:47], s56 offen offset:3072 sc1
mm v[104:107],a[116:119],a[92:95],v[104:107]
s_waitcnt lgkmcnt(15)
dt a[108:109],v133 offset:8192
v_mul_f32 v234, v234, v231
v_mul_f32 v235, v235, v231
mm v[12:15],a[120:123],a[64:67],v[12:15]
dt a[110:111],v133 offset:9216
cv v166,v232,v233
cv v167,v234,v235
mm v[12:15],a[124:127],a[68:71],v[12:15]
dr a[16:19],v128 offset:0
v_accvgpr_write_b32 a192, v164
v_accvgpr_write_b32 a193, v165
dr a[20:23],v129 offset:0
dt a[112:113],v134 offset:0
)"
R"(mm v[44:47],a[120:123],a[72:75],v[44:47]
dt a[114:115],v134 offset:1024
v_accvgpr_write_b32 a194, v166
v_accvgpr_write_b32 a195, v167
mm v[44:47],a[124:127],a[76:79],v[44:47]
dt a[116:117],v134 offset:8192
v_permlane32_swap_b32 v238, v239
mm v[76:79],a[120:123],a[80:83],v[76:79]
dt a[118:119],v134 offset:9216
v_max_f32 v238, v238, v239
v_mov_b32 v239, v238
mm v[76:79],a[124:127],a[84:87],v[76:79]
dr a[24:27],v128 offset:2048
mm v[108:111],a[120:123],a[88:91],v[108:111]
s_waitcnt lgkmcnt(15)
dr a[28:31],v129 offset:2048
)"
R"(v_permlane16_swap_b32 v238, v239
mm v[108:111],a[124:127],a[92:95],v[108:111]
s_lshl_b32 s62, s48, 3
s_waitcnt lgkmcnt(15)
dt a[120:121],v135 offset:0
v_add_f32 v180, v180, v184
v_add_f32 v181, v181, v185
mm v[16:19],a[128:131],a[64:67],v[16:19]
dt a[122:123],v135 offset:1024
v_add_f32 v182, v182, v186
v_add_f32 v183, v183, v187
mm v[16:19],a[132:135],a[68:71],v[16:19]
sa s62,s62,s49
dt a[124:125],v135 offset:8192
v_add_f32 v180, v180, v200
v_add_f32 v181, v181, v201
)"
R"(mm v[48:51],a[128:131],a[72:75],v[48:51]
dt a[126:127],v135 offset:9216
v_add_f32 v182, v182, v202
v_add_f32 v183, v183, v203
mm v[48:51],a[132:135],a[76:79],v[48:51]
dr a[32:35],v128 offset:4096
v_add_f32 v180, v180, v208
v_add_f32 v181, v181, v209
mm v[80:83],a[128:131],a[80:83],v[80:83]
s_lshl_b32 s62, s62, 2
dr a[36:39],v129 offset:4096
v_add_f32 v182, v182, v210
v_add_f32 v183, v183, v211
mm v[80:83],a[132:135],a[84:87],v[80:83]
v_max_f32 v238, v238, v239
v_mov_b32 v239, v238
)"
R"(mm v[112:115],a[128:131],a[88:91],v[112:115]
sa s62,s62,s50
cv v244,v172,v173
cv v245,v174,v175
mm v[112:115],a[132:135],a[92:95],v[112:115]
s_waitcnt lgkmcnt(15)
dt a[128:129],v136 offset:0
v_permlane32_swap_b32 v238, v239
mm v[20:23],a[136:139],a[64:67],v[20:23]
dt a[130:131],v136 offset:1024
v_mul_f32 v239, s63, v239
v_mul_f32 v238, s63, v238
mm v[20:23],a[140:143],a[68:71],v[20:23]
s_lshl_b32 s62, s62, 10
dt a[132:133],v136 offset:8192
v_fma_f32 v240, -v168, s63, -v239
)"
R"(v_fma_f32 v241, -v169, s63, -v239
mm v[52:55],a[136:139],a[72:75],v[52:55]
dt a[134:135],v136 offset:9216
sa s56,s61,0xb000
buffer_store_dwordx4 a[224:227], v214, s[44:47], s56 offen offset:0 sc1
mm v[52:55],a[140:143],a[76:79],v[52:55]
sa s62,s62,0x3300000
dr a[40:43],v128 offset:6144
v_fma_f32 v242, -v170, s63, -v239
v_fma_f32 v243, -v171, s63, -v239
v_mfma_f32_16x16x16_bf16 v[172:175], v[220:221], v[244:245], 0
v_fma_f32 v168, v168, s63, -v238
v_fma_f32 v169, v169, s63, -v238
v_fma_f32 v170, v170, s63, -v238
mm v[84:87],a[136:139],a[80:83],v[84:87]
dr a[44:47],v129 offset:6144
)"
R"(v_fma_f32 v171, v171, s63, -v238
mm v[84:87],a[140:143],a[84:87],v[84:87]
ex v240,v240
mm v[116:119],a[136:139],a[88:91],v[116:119]
ex v168,v168
v_max3_f32 v246, v172, v173, v174
mm v[116:119],a[140:143],a[92:95],v[116:119]
s_waitcnt lgkmcnt(15)
dt a[136:137],v137 offset:0
ex v241,v241
mm v[24:27],a[144:147],a[64:67],v[24:27]
dt a[138:139],v137 offset:1024
ex v169,v169
mm v[24:27],a[148:151],a[68:71],v[24:27]
dt a[140:141],v137 offset:8192
ex v242,v242
)"
R"(mm v[56:59],a[144:147],a[72:75],v[56:59]
dt a[142:143],v137 offset:9216
ex v170,v170
mm v[56:59],a[148:151],a[76:79],v[56:59]
ex v243,v243
v_max3_f32 v247, -v172, -v173, -v174
mm v[88:91],a[144:147],a[80:83],v[88:91]
ex v171,v171
v_max_f32 v246, v246, v175
mm v[88:91],a[148:151],a[84:87],v[88:91]
v_add_f32 v238, v168, v169
v_add_f32 v239, v240, v241
v_add_f32 v238, v238, v170
mm v[120:123],a[144:147],a[88:91],v[120:123]
v_add_f32 v239, v239, v242
v_add_f32 v238, v238, v171
)"
R"(v_add_f32 v239, v239, v243
mm v[120:123],a[148:151],a[92:95],v[120:123]
s_waitcnt lgkmcnt(15)
dt a[144:145],v138 offset:0
v_max_f32 v247, v247, -v175
mm v[28:31],a[152:155],a[64:67],v[28:31]
dt a[146:147],v138 offset:1024
v_permlane32_swap_b32 v238, v239
mm v[28:31],a[156:159],a[68:71],v[28:31]
dt a[148:149],v138 offset:8192
v_add_f32 v238, v238, v239
v_mov_b32 v239, v238
mm v[60:63],a[152:155],a[72:75],v[60:63]
dt a[150:151],v138 offset:9216
v_permlane32_swap_b32 v246, v247
mm v[60:63],a[156:159],a[76:79],v[60:63]
)"
R"(sa s56,s61,0xb000
buffer_store_dwordx4 a[232:235], v214, s[44:47], s56 offen offset:1024 sc1
mm v[92:95],a[152:155],a[80:83],v[92:95]
v_permlane16_swap_b32 v238, v239
v_max_f32 v246, v246, v247
mm v[92:95],a[156:159],a[84:87],v[92:95]
v_add_f32 v238, v238, v239
v_mov_b32 v239, v238
v_mov_b32 v247, v246
mm v[124:127],a[152:155],a[88:91],v[124:127]
v_permlane32_swap_b32 v238, v239
mm v[124:127],a[156:159],a[92:95],v[124:127]
s_waitcnt lgkmcnt(8)
dt a[152:153],v139 offset:0
v_rcp_f32 v238, v238
mm v[0:3],a[96:99],a[16:19],v[0:3]
)"
R"(dt a[154:155],v139 offset:1024
v_mul_f32 v168, v168, v238
v_mul_f32 v169, v169, v238
mm v[0:3],a[100:103],a[20:23],v[0:3]
dt a[156:157],v139 offset:8192
v_rcp_f32 v239, v239
mm v[32:35],a[96:99],a[24:27],v[32:35]
dt a[158:159],v139 offset:9216
v_mul_f32 v170, v170, v238
v_mul_f32 v171, v171, v238
mm v[32:35],a[100:103],a[28:31],v[32:35]
cv v168,v168,v169
v_mul_f32 v240, v240, v239
v_mul_f32 v241, v241, v239
mm v[64:67],a[96:99],a[32:35],v[64:67]
cv v169,v170,v171
)"
R"(v_mul_f32 v242, v242, v239
v_mul_f32 v243, v243, v239
mm v[64:67],a[100:103],a[36:39],v[64:67]
cv v170,v240,v241
cv v171,v242,v243
v_accvgpr_write_b32 a196, v168
mm v[96:99],a[96:99],a[40:43],v[96:99]
v_accvgpr_write_b32 a197, v169
v_accvgpr_write_b32 a198, v170
v_accvgpr_write_b32 a199, v171
mm v[96:99],a[100:103],a[44:47],v[96:99]
s_waitcnt lgkmcnt(15)
v_permlane16_swap_b32 v246, v247
mm v[4:7],a[104:107],a[16:19],v[4:7]
v_max_f32 v246, v246, v247
v_mov_b32 v247, v246
)"
R"(mm v[4:7],a[108:111],a[20:23],v[4:7]
mm v[36:39],a[104:107],a[24:27],v[36:39]
s_waitcnt vmcnt(3) lgkmcnt(0)
s_barrier
v_permlane32_swap_b32 v246, v247
mm v[36:39],a[108:111],a[28:31],v[36:39]
dt a[96:97],v132 offset:49152
v_mul_f32 v247, s63, v247
v_mul_f32 v246, s63, v246
mm v[68:71],a[104:107],a[32:35],v[68:71]
dt a[98:99],v132 offset:50176
v_fma_f32 v248, -v172, s63, -v247
v_fma_f32 v249, -v173, s63, -v247
mm v[68:71],a[108:111],a[36:39],v[68:71]
dt a[100:101],v132 offset:57344
v_fma_f32 v250, -v174, s63, -v247
)"
R"(v_fma_f32 v251, -v175, s63, -v247
mm v[100:103],a[104:107],a[40:43],v[100:103]
dt a[102:103],v132 offset:58368
v_fma_f32 v172, v172, s63, -v246
v_fma_f32 v173, v173, s63, -v246
mm v[100:103],a[108:111],a[44:47],v[100:103]
s_waitcnt lgkmcnt(15)
dt a[104:105],v133 offset:49152
v_fma_f32 v174, v174, s63, -v246
v_fma_f32 v175, v175, s63, -v246
cv v228,v176,v177
cv v229,v178,v179
ex v248,v248
ex v172,v172
v_mfma_f32_16x16x16_bf16 v[176:179], v[220:221], v[228:229], 0
sa s56,s61,0xb000
)"
R"(buffer_store_dwordx4 a[240:243], v214, s[44:47], s56 offen offset:2048 sc1
mm v[8:11],a[112:115],a[16:19],v[8:11]
dt a[106:107],v133 offset:50176
ex v249,v249
mm v[8:11],a[116:119],a[20:23],v[8:11]
dt a[108:109],v133 offset:57344
ex v173,v173
mm v[40:43],a[112:115],a[24:27],v[40:43]
dt a[110:111],v133 offset:58368
ex v250,v250
mm v[40:43],a[116:119],a[28:31],v[40:43]
dr a[64:67],v128 offset:49152
ex v174,v174
mm v[72:75],a[112:115],a[32:35],v[72:75]
dr a[68:71],v129 offset:49152
ex v251,v251
)"
R"(mm v[72:75],a[116:119],a[36:39],v[72:75]
ex v175,v175
v_max3_f32 v230, v176, v177, v178
mm v[104:107],a[112:115],a[40:43],v[104:107]
v_add_f32 v246, v172, v173
v_add_f32 v247, v248, v249
v_add_f32 v246, v246, v174
mm v[104:107],a[116:119],a[44:47],v[104:107]
s_waitcnt lgkmcnt(15)
dt a[112:113],v134 offset:49152
v_add_f32 v247, v247, v250
v_add_f32 v246, v246, v175
mm v[12:15],a[120:123],a[16:19],v[12:15]
dt a[114:115],v134 offset:50176
v_add_f32 v247, v247, v251
v_max3_f32 v231, -v176, -v177, -v178
)"
R"(mm v[12:15],a[124:127],a[20:23],v[12:15]
dt a[116:117],v134 offset:57344
v_permlane32_swap_b32 v246, v247
mm v[44:47],a[120:123],a[24:27],v[44:47]
dt a[118:119],v134 offset:58368
v_add_f32 v246, v246, v247
v_mov_b32 v247, v246
mm v[44:47],a[124:127],a[28:31],v[44:47]
dr a[72:75],v128 offset:51200
v_max_f32 v230, v230, v179
v_max_f32 v231, v231, -v179
mm v[76:79],a[120:123],a[32:35],v[76:79]
dr a[76:79],v129 offset:51200
v_permlane16_swap_b32 v246, v247
mm v[76:79],a[124:127],a[36:39],v[76:79]
v_add_f32 v246, v246, v247
)"
R"(v_mov_b32 v247, v246
mm v[108:111],a[120:123],a[40:43],v[108:111]
v_permlane32_swap_b32 v230, v231
mm v[108:111],a[124:127],a[44:47],v[108:111]
s_waitcnt lgkmcnt(15)
dt a[120:121],v135 offset:49152
v_permlane32_swap_b32 v246, v247
mm v[16:19],a[128:131],a[16:19],v[16:19]
dt a[122:123],v135 offset:50176
v_rcp_f32 v246, v246
mm v[16:19],a[132:135],a[20:23],v[16:19]
dt a[124:125],v135 offset:57344
v_mul_f32 v172, v172, v246
v_mul_f32 v173, v173, v246
mm v[48:51],a[128:131],a[24:27],v[48:51]
dt a[126:127],v135 offset:58368
)"
R"(v_rcp_f32 v247, v247
mm v[48:51],a[132:135],a[28:31],v[48:51]
dr a[80:83],v128 offset:53248
v_mul_f32 v174, v174, v246
v_mul_f32 v175, v175, v246
mm v[80:83],a[128:131],a[32:35],v[80:83]
dr a[84:87],v129 offset:53248
sa s56,s61,0xb000
buffer_store_dwordx4 a[248:251], v214, s[44:47], s56 offen offset:3072 sc1
mm v[80:83],a[132:135],a[36:39],v[80:83]
buffer_store_dwordx4 v[180:183], v214, s[44:47], s62 offen sc1
mm v[112:115],a[128:131],a[40:43],v[112:115]
s_lshl_b32 s60, s48, 6
cv v172,v172,v173
v_mul_f32 v248, v248, v247
v_mul_f32 v249, v249, v247
)"
R"(mm v[112:115],a[132:135],a[44:47],v[112:115]
sa s60,s60,0x10000
s_waitcnt lgkmcnt(15)
dt a[128:129],v136 offset:49152
cv v173,v174,v175
v_mul_f32 v250, v250, v247
mm v[20:23],a[136:139],a[16:19],v[20:23]
dt a[130:131],v136 offset:50176
v_mul_f32 v251, v251, v247
cv v174,v248,v249
mm v[20:23],a[140:143],a[20:23],v[20:23]
s_lshl_b32 s62, s49, 2
dt a[132:133],v136 offset:57344
cv v175,v250,v251
v_accvgpr_write_b32 a200, v172
mm v[52:55],a[136:139],a[24:27],v[52:55]
)"
R"(dt a[134:135],v136 offset:58368
v_accvgpr_write_b32 a201, v173
v_accvgpr_write_b32 a202, v174
mm v[52:55],a[140:143],a[28:31],v[52:55]
dr a[88:91],v128 offset:55296
v_accvgpr_write_b32 a203, v175
v_max_f32 v230, v230, v231
mm v[84:87],a[136:139],a[32:35],v[84:87]
sa s62,s62,s60
dr a[92:95],v129 offset:55296
v_mov_b32 v231, v230
mm v[84:87],a[140:143],a[36:39],v[84:87]
mm v[116:119],a[136:139],a[40:43],v[116:119]
v_permlane16_swap_b32 v230, v231
mm v[116:119],a[140:143],a[44:47],v[116:119]
s_waitcnt lgkmcnt(15)
)"
R"(dt a[136:137],v137 offset:49152
v_max_f32 v230, v230, v231
v_mov_b32 v231, v230
mm v[24:27],a[144:147],a[16:19],v[24:27]
s_cmp_eq_u32 s50, 0
s_cselect_b32 s62, s62, 0x4900000
dt a[138:139],v137 offset:50176
mm v[24:27],a[148:151],a[20:23],v[24:27]
dt a[140:141],v137 offset:57344
v_permlane32_swap_b32 v230, v231
mm v[56:59],a[144:147],a[24:27],v[56:59]
dt a[142:143],v137 offset:58368
v_mul_f32 v231, s63, v231
v_mul_f32 v230, s63, v230
mm v[56:59],a[148:151],a[28:31],v[56:59]
v_fma_f32 v232, -v176, s63, -v231
)"
R"(v_fma_f32 v233, -v177, s63, -v231
v_fma_f32 v234, -v178, s63, -v231
mm v[88:91],a[144:147],a[32:35],v[88:91]
v_mov_b32 v152, 1
v_fma_f32 v235, -v179, s63, -v231
v_fma_f32 v176, v176, s63, -v230
mm v[88:91],a[148:151],a[36:39],v[88:91]
v_fma_f32 v177, v177, s63, -v230
v_fma_f32 v178, v178, s63, -v230
v_fma_f32 v179, v179, s63, -v230
mm v[120:123],a[144:147],a[40:43],v[120:123]
ex v232,v232
mm v[120:123],a[148:151],a[44:47],v[120:123]
s_waitcnt lgkmcnt(15)
dt a[144:145],v138 offset:49152
v_mov_b32 v153, 0
)"
R"(mm v[28:31],a[152:155],a[16:19],v[28:31]
s_mov_b32 s58, 0x3e0293ee
dt a[146:147],v138 offset:50176
ex v176,v176
mm v[28:31],a[156:159],a[20:23],v[28:31]
dt a[148:149],v138 offset:57344
ex v233,v233
mm v[60:63],a[152:155],a[24:27],v[60:63]
dt a[150:151],v138 offset:58368
ex v177,v177
mm v[60:63],a[156:159],a[28:31],v[60:63]
v_and_b32 v155, 7, v213
ex v234,v234
mm v[92:95],a[152:155],a[32:35],v[92:95]
ex v178,v178
mm v[92:95],a[156:159],a[36:39],v[92:95]
)"
R"(ex v235,v235
mm v[124:127],a[152:155],a[40:43],v[124:127]
v_lshlrev_b32 v155, 2, v155
ex v179,v179
mm v[124:127],a[156:159],a[44:47],v[124:127]
s_waitcnt lgkmcnt(8)
dt a[152:153],v139 offset:49152
v_add_f32 v230, v176, v177
v_add_f32 v231, v232, v233
mm v[0:3],a[96:99],a[64:67],v[0:3]
dt a[154:155],v139 offset:50176
v_add_f32 v230, v230, v178
v_add_f32 v231, v231, v234
mm v[0:3],a[100:103],a[68:71],v[0:3]
dt a[156:157],v139 offset:57344
v_add_f32 v230, v230, v179
)"
R"(v_add_f32 v231, v231, v235
mm v[32:35],a[96:99],a[72:75],v[32:35]
dt a[158:159],v139 offset:58368
mm v[32:35],a[100:103],a[76:79],v[32:35]
v_permlane32_swap_b32 v230, v231
mm v[64:67],a[96:99],a[80:83],v[64:67]
v_add_f32 v230, v230, v231
v_mov_b32 v231, v230
mm v[64:67],a[100:103],a[84:87],v[64:67]
mm v[96:99],a[96:99],a[88:91],v[96:99]
v_permlane16_swap_b32 v230, v231
mm v[96:99],a[100:103],a[92:95],v[96:99]
s_waitcnt lgkmcnt(15)
v_add_f32 v230, v230, v231
v_mov_b32 v231, v230
mm v[4:7],a[104:107],a[64:67],v[4:7]
)"
R"(mm v[4:7],a[108:111],a[68:71],v[4:7]
v_permlane32_swap_b32 v230, v231
mm v[36:39],a[104:107],a[72:75],v[36:39]
s_waitcnt vmcnt(0) lgkmcnt(0)
s_barrier
v_rcp_f32 v230, v230
mm v[36:39],a[108:111],a[76:79],v[36:39]
buffer_store_dword v152, v153, s[44:47], s62 offen sc1
mm v[68:71],a[104:107],a[80:83],v[68:71]
v_mul_f32 v176, v176, v230
v_mul_f32 v177, v177, v230
v_mul_f32 v178, v178, v230
mm v[68:71],a[108:111],a[84:87],v[68:71]
v_rcp_f32 v231, v231
v_mul_f32 v179, v179, v230
mm v[100:103],a[104:107],a[88:91],v[100:103]
)"
R"(cv v176,v176,v177
v_mul_f32 v232, v232, v231
v_mul_f32 v233, v233, v231
mm v[100:103],a[108:111],a[92:95],v[100:103]
s_waitcnt lgkmcnt(15)
cv v177,v178,v179
v_mul_f32 v234, v234, v231
v_mul_f32 v235, v235, v231
mm v[8:11],a[112:115],a[64:67],v[8:11]
cv v178,v232,v233
cv v179,v234,v235
v_accvgpr_write_b32 a204, v176
mm v[8:11],a[116:119],a[68:71],v[8:11]
v_accvgpr_write_b32 a205, v177
v_accvgpr_write_b32 a206, v178
v_accvgpr_write_b32 a207, v179
)"
R"(mm v[40:43],a[112:115],a[72:75],v[40:43]
v_mul_f32 v0, s58, v0
v_mul_f32 v1, s58, v1
mm v[40:43],a[116:119],a[76:79],v[40:43]
v_mul_f32 v32, s58, v32
v_mul_f32 v33, s58, v33
mm v[72:75],a[112:115],a[80:83],v[72:75]
v_mul_f32 v64, s58, v64
v_mul_f32 v65, s58, v65
mm v[72:75],a[116:119],a[84:87],v[72:75]
v_mul_f32 v96, s58, v96
v_mul_f32 v97, s58, v97
mm v[104:107],a[112:115],a[88:91],v[104:107]
v_mul_f32 v2, s58, v2
v_mul_f32 v3, s58, v3
mm v[104:107],a[116:119],a[92:95],v[104:107]
)"
R"(s_waitcnt lgkmcnt(15)
cv v0,v0,v1
v_mul_f32 v34, s58, v34
mm v[12:15],a[120:123],a[64:67],v[12:15]
v_mul_f32 v35, s58, v35
cv v32,v32,v33
mm v[12:15],a[124:127],a[68:71],v[12:15]
v_mul_f32 v66, s58, v66
v_mul_f32 v67, s58, v67
mm v[44:47],a[120:123],a[72:75],v[44:47]
cv v64,v64,v65
v_mul_f32 v98, s58, v98
mm v[44:47],a[124:127],a[76:79],v[44:47]
v_mul_f32 v99, s58, v99
cv v96,v96,v97
mm v[76:79],a[120:123],a[80:83],v[76:79]
)"
R"(v_mul_f32 v8, s58, v8
v_mul_f32 v9, s58, v9
mm v[76:79],a[124:127],a[84:87],v[76:79]
v_mul_f32 v10, s58, v10
v_mul_f32 v11, s58, v11
mm v[108:111],a[120:123],a[88:91],v[108:111]
v_mul_f32 v40, s58, v40
v_mul_f32 v41, s58, v41
mm v[108:111],a[124:127],a[92:95],v[108:111]
s_waitcnt lgkmcnt(14)
cv v8,v8,v9
v_mul_f32 v42, s58, v42
mm v[16:19],a[128:131],a[64:67],v[16:19]
v_mul_f32 v72, s58, v72
v_mul_f32 v73, s58, v73
mm v[16:19],a[132:135],a[68:71],v[16:19]
)"
R"(v_mul_f32 v43, s58, v43
cv v40,v40,v41
mm v[48:51],a[128:131],a[72:75],v[48:51]
v_mul_f32 v104, s58, v104
v_mul_f32 v105, s58, v105
mm v[48:51],a[132:135],a[76:79],v[48:51]
v_mul_f32 v74, s58, v74
v_mul_f32 v75, s58, v75
mm v[80:83],a[128:131],a[80:83],v[80:83]
cv v72,v72,v73
v_mul_f32 v106, s58, v106
mm v[80:83],a[132:135],a[84:87],v[80:83]
v_mul_f32 v107, s58, v107
cv v104,v104,v105
mm v[112:115],a[128:131],a[88:91],v[112:115]
v_mul_f32 v4, s58, v4
)"
R"(v_mul_f32 v5, s58, v5
mm v[112:115],a[132:135],a[92:95],v[112:115]
s_waitcnt lgkmcnt(8)
cv v1,v2,v3
v_mul_f32 v36, s58, v36
mm v[20:23],a[136:139],a[64:67],v[20:23]
v_mul_f32 v37, s58, v37
cv v33,v34,v35
mm v[20:23],a[140:143],a[68:71],v[20:23]
v_mul_f32 v68, s58, v68
v_mul_f32 v69, s58, v69
mm v[52:55],a[136:139],a[72:75],v[52:55]
cv v65,v66,v67
v_mul_f32 v100, s58, v100
mm v[52:55],a[140:143],a[76:79],v[52:55]
v_mul_f32 v101, s58, v101
)"
R"(cv v97,v98,v99
mm v[84:87],a[136:139],a[80:83],v[84:87]
v_mul_f32 v16, s58, v16
v_mul_f32 v17, s58, v17
mm v[84:87],a[140:143],a[84:87],v[84:87]
v_mul_f32 v18, s58, v18
v_mul_f32 v19, s58, v19
mm v[116:119],a[136:139],a[88:91],v[116:119]
v_mul_f32 v48, s58, v48
v_mul_f32 v49, s58, v49
mm v[116:119],a[140:143],a[92:95],v[116:119]
s_waitcnt lgkmcnt(4)
cv v16,v16,v17
v_mul_f32 v50, s58, v50
mm v[24:27],a[144:147],a[64:67],v[24:27]
v_mul_f32 v80, s58, v80
)"
R"(v_mul_f32 v81, s58, v81
mm v[24:27],a[148:151],a[68:71],v[24:27]
v_mul_f32 v51, s58, v51
cv v48,v48,v49
mm v[56:59],a[144:147],a[72:75],v[56:59]
v_mul_f32 v112, s58, v112
v_mul_f32 v113, s58, v113
mm v[56:59],a[148:151],a[76:79],v[56:59]
v_mul_f32 v82, s58, v82
v_mul_f32 v83, s58, v83
mm v[88:91],a[144:147],a[80:83],v[88:91]
cv v80,v80,v81
v_mul_f32 v114, s58, v114
mm v[88:91],a[148:151],a[84:87],v[88:91]
v_mul_f32 v115, s58, v115
cv v112,v112,v113
)"
R"(mm v[120:123],a[144:147],a[88:91],v[120:123]
v_mul_f32 v12, s58, v12
v_mul_f32 v13, s58, v13
mm v[120:123],a[148:151],a[92:95],v[120:123]
s_waitcnt lgkmcnt(0)
cv v9,v10,v11
v_mul_f32 v44, s58, v44
mm v[28:31],a[152:155],a[64:67],v[28:31]
buffer_load_dword v156, v155, s[44:47], s60 offen sc1
mm v[28:31],a[156:159],a[68:71],v[28:31]
v_mul_f32 v45, s58, v45
cv v41,v42,v43
mm v[60:63],a[152:155],a[72:75],v[60:63]
v_mul_f32 v76, s58, v76
v_mul_f32 v77, s58, v77
mm v[60:63],a[156:159],a[76:79],v[60:63]
)"
R"(cv v73,v74,v75
v_mul_f32 v108, s58, v108
mm v[92:95],a[152:155],a[80:83],v[92:95]
v_mul_f32 v24, s58, v24
v_mul_f32 v25, s58, v25
mm v[92:95],a[156:159],a[84:87],v[92:95]
v_mul_f32 v26, s58, v26
v_mul_f32 v27, s58, v27
mm v[124:127],a[152:155],a[88:91],v[124:127]
v_mul_f32 v56, s58, v56
v_mul_f32 v57, s58, v57
mm v[124:127],a[156:159],a[92:95],v[124:127]
cv v24,v24,v25
v_mul_f32 v58, s58, v58
v_mul_f32 v88, s58, v88
v_mul_f32 v89, s58, v89
)"
R"(v_mul_f32 v59, s58, v59
cv v56,v56,v57
v_mul_f32 v90, s58, v90
v_mul_f32 v91, s58, v91
v_mul_f32 v120, s58, v120
v_mul_f32 v121, s58, v121
cv v88,v88,v89
v_mul_f32 v122, s58, v122
v_mul_f32 v123, s58, v123
cv v120,v120,v121
v_mul_f32 v109, s58, v109
cv v105,v106,v107
v_mul_f32 v20, s58, v20
v_mul_f32 v21, s58, v21
cv v17,v18,v19
v_mul_f32 v52, s58, v52
)"
R"(v_mul_f32 v53, s58, v53
cv v49,v50,v51
v_mul_f32 v84, s58, v84
v_mul_f32 v85, s58, v85
cv v81,v82,v83
v_mul_f32 v116, s58, v116
v_mul_f32 v117, s58, v117
cv v113,v114,v115
v_mul_f32 v28, s58, v28
v_mul_f32 v29, s58, v29
cv v25,v26,v27
v_mul_f32 v60, s58, v60
v_mul_f32 v61, s58, v61
cv v57,v58,v59
v_mul_f32 v92, s58, v92
v_mul_f32 v93, s58, v93
)"
R"(cv v89,v90,v91
v_mul_f32 v124, s58, v124
v_mul_f32 v125, s58, v125
cv v121,v122,v123
v_mul_f32 v6, s58, v6
v_mul_f32 v7, s58, v7
cv v2,v4,v5
v_mul_f32 v38, s58, v38
v_mul_f32 v39, s58, v39
cv v34,v36,v37
v_mul_f32 v70, s58, v70
v_mul_f32 v71, s58, v71
cv v66,v68,v69
v_mul_f32 v102, s58, v102
v_mul_f32 v103, s58, v103
cv v98,v100,v101
)"
R"(v_mul_f32 v14, s58, v14
v_mul_f32 v15, s58, v15
cv v10,v12,v13
v_mul_f32 v46, s58, v46
v_mul_f32 v47, s58, v47
cv v42,v44,v45
v_mul_f32 v78, s58, v78
v_mul_f32 v79, s58, v79
cv v74,v76,v77
v_mul_f32 v110, s58, v110
v_mul_f32 v111, s58, v111
cv v106,v108,v109
v_mul_f32 v22, s58, v22
v_mul_f32 v23, s58, v23
cv v18,v20,v21
v_mul_f32 v54, s58, v54
)"
R"(v_mul_f32 v55, s58, v55
cv v50,v52,v53
v_mul_f32 v86, s58, v86
v_mul_f32 v87, s58, v87
cv v82,v84,v85
v_mul_f32 v118, s58, v118
v_mul_f32 v119, s58, v119
cv v114,v116,v117
v_mul_f32 v30, s58, v30
v_mul_f32 v31, s58, v31
cv v26,v28,v29
v_mul_f32 v62, s58, v62
v_mul_f32 v63, s58, v63
cv v58,v60,v61
v_mul_f32 v94, s58, v94
v_mul_f32 v95, s58, v95
)"
R"(cv v90,v92,v93
v_mul_f32 v126, s58, v126
v_mul_f32 v127, s58, v127
cv v122,v124,v125
cv v3,v6,v7
cv v35,v38,v39
cv v67,v70,v71
cv v99,v102,v103
cv v11,v14,v15
cv v43,v46,v47
cv v75,v78,v79
cv v107,v110,v111
cv v19,v22,v23
cv v51,v54,v55
cv v83,v86,v87
cv v115,v118,v119
)"
R"(cv v27,v30,v31
cv v59,v62,v63
cv v91,v94,v95
cv v123,v126,v127
v_accvgpr_write_b32 a0, v0
v_accvgpr_write_b32 a1, v1
v_accvgpr_write_b32 a2, v2
v_accvgpr_write_b32 a3, v3
v_accvgpr_write_b32 a16, v32
v_accvgpr_write_b32 a17, v33
v_accvgpr_write_b32 a18, v34
v_accvgpr_write_b32 a19, v35
v_accvgpr_write_b32 a32, v64
v_accvgpr_write_b32 a33, v65
v_accvgpr_write_b32 a34, v66
v_accvgpr_write_b32 a35, v67
)"
R"(v_accvgpr_write_b32 a48, v96
v_accvgpr_write_b32 a49, v97
v_accvgpr_write_b32 a50, v98
v_accvgpr_write_b32 a51, v99
v_accvgpr_write_b32 a4, v8
v_accvgpr_write_b32 a5, v9
v_accvgpr_write_b32 a6, v10
v_accvgpr_write_b32 a7, v11
v_accvgpr_write_b32 a20, v40
v_accvgpr_write_b32 a21, v41
v_accvgpr_write_b32 a22, v42
v_accvgpr_write_b32 a23, v43
v_accvgpr_write_b32 a36, v72
v_accvgpr_write_b32 a37, v73
v_accvgpr_write_b32 a38, v74
v_accvgpr_write_b32 a39, v75
)"
R"(v_accvgpr_write_b32 a52, v104
v_accvgpr_write_b32 a53, v105
v_accvgpr_write_b32 a54, v106
v_accvgpr_write_b32 a55, v107
v_accvgpr_write_b32 a8, v16
v_accvgpr_write_b32 a9, v17
v_accvgpr_write_b32 a10, v18
v_accvgpr_write_b32 a11, v19
v_accvgpr_write_b32 a24, v48
v_accvgpr_write_b32 a25, v49
v_accvgpr_write_b32 a26, v50
v_accvgpr_write_b32 a27, v51
v_accvgpr_write_b32 a40, v80
v_accvgpr_write_b32 a41, v81
v_accvgpr_write_b32 a42, v82
v_accvgpr_write_b32 a43, v83
)"
R"(v_accvgpr_write_b32 a56, v112
v_accvgpr_write_b32 a57, v113
v_accvgpr_write_b32 a58, v114
v_accvgpr_write_b32 a59, v115
v_accvgpr_write_b32 a12, v24
v_accvgpr_write_b32 a13, v25
v_accvgpr_write_b32 a14, v26
v_accvgpr_write_b32 a15, v27
v_accvgpr_write_b32 a28, v56
v_accvgpr_write_b32 a29, v57
v_accvgpr_write_b32 a30, v58
v_accvgpr_write_b32 a31, v59
v_accvgpr_write_b32 a44, v88
v_accvgpr_write_b32 a45, v89
v_accvgpr_write_b32 a46, v90
v_accvgpr_write_b32 a47, v91
)"
R"(v_accvgpr_write_b32 a60, v120
v_accvgpr_write_b32 a61, v121
v_accvgpr_write_b32 a62, v122
v_accvgpr_write_b32 a63, v123
s_waitcnt vmcnt(0)
v_cmp_eq_u32 vcc, 1, v156
s_nop 1
s_cmp_eq_u64 vcc, exec
s_cbranch_scc1 L_pollkv_ok
s_mov_b32 s62, 0x8000
L_pollkv:
buffer_load_dword v156, v155, s[44:47], s60 offen sc1
s_waitcnt vmcnt(0)
v_cmp_eq_u32 vcc, 1, v156
s_nop 1
s_cmp_eq_u64 vcc, exec
)"
R"(s_cbranch_scc1 L_pollkv_ok
s_sleep 1
s_sub_u32 s62, s62, 1
s_cmp_lg_u32 s62, 0
s_cbranch_scc1 L_pollkv
L_pollkv_ok:
s_lshl_b32 s74, s48, 20
sa s74,s74,0x100000
s_lshl_b32 s75, s50, 13
sa s74,s74,s75
sa m0,s75,0x0
sa s76,s74,0x0
bl v214,s[44:47],s76 offen sc1 lds
bl v214,s[44:47],s76 offen offset:1024 sc1 lds
bl v214,s[44:47],s76 offen offset:2048 sc1 lds
bl v214,s[44:47],s76 offen offset:3072 sc1 lds
)"
R"(sa m0,s75,0x1000
sa s76,s74,0x1000
bl v214,s[44:47],s76 offen sc1 lds
bl v214,s[44:47],s76 offen offset:1024 sc1 lds
bl v214,s[44:47],s76 offen offset:2048 sc1 lds
bl v214,s[44:47],s76 offen offset:3072 sc1 lds
s_lshl_b32 s77, s48, 5
sa s77,s77,s50
s_lshl_b32 s77, s77, 10
sa s77,s77,0x3300000
sa m0,s75,0x10000
sa s76,s74,0x8000
bl v214,s[44:47],s76 offen sc1 lds
s_lshr_b32 s56, s50, 1
s_lshl_b32 s56, s56, 10
s_and_b32 s62, s50, 1
)"
R"(bl v214,s[44:47],s76 offen offset:1024 sc1 lds
s_lshl_b32 s62, s62, 3
sa s56,s56,s62
sa s56,s56,0x18000
bl v214,s[44:47],s76 offen offset:2048 sc1 lds
v_add_u32 v230, s56, v214
v_add_u32 v231, 0x18000, v214
bl v214,s[44:47],s76 offen offset:3072 sc1 lds
v_lshrrev_b32 v232, 1, v214
s_lshl_b32 s63, s49, 2
sa s63,s63,s50
sa m0,s75,0x11000
sa s76,s74,0x9000
bl v214,s[44:47],s76 offen sc1 lds
s_lshl_b32 s63, s63, 7
sa s63,s63,s48
)"
R"(s_lshl_b32 s63, s63, 9
bl v214,s[44:47],s76 offen offset:1024 sc1 lds
sa s63,s63,0x3100000
v_mov_b32 v208, 0x3f803f80
bl v214,s[44:47],s76 offen offset:2048 sc1 lds
v_mov_b32 v209, 0x3f803f80
v_mov_b32 v210, 0x3f803f80
v_mov_b32 v211, 0x3f803f80
bl v214,s[44:47],s76 offen offset:3072 sc1 lds
v_mov_b32 v213, 0xff800000
v_bfe_u32 v220, v214, 4, 4
bl v[96:99],v214,s[44:47],s77 offen sc1
sa s77,s77,0x1000
v_lshrrev_b32 v221, 8, v214
v_lshlrev_b32 v221, 2, v221
v_cmp_le_u32 s[88:89], v221, v220
)"
R"(bl v[100:103],v214,s[44:47],s77 offen sc1
sa s77,s77,0x1000
v_add_u32 v221, 1, v221
v_cmp_le_u32 s[90:91], v221, v220
v_add_u32 v221, 1, v221
bl v[104:107],v214,s[44:47],s77 offen sc1
sa s77,s77,0x1000
v_cmp_le_u32 s[92:93], v221, v220
v_add_u32 v221, 1, v221
bl v[108:111],v214,s[44:47],s77 offen sc1
sa s77,s77,0x1000
v_cmp_le_u32 s[94:95], v221, v220
v_add_u32 v221, 1, v221
s_getpc_b64 s[40:41]
L_ct_att:
sa s40,s40,(L_att_full8-L_ct_att)
)"
R"(s_addc_u32 s41, s41, 0
bl v[112:115],v214,s[44:47],s77 offen sc1
sa s77,s77,0x1000
bl v[116:119],v214,s[44:47],s77 offen sc1
sa s77,s77,0x1000
s_and_b32 s41, s41, 0xffff
s_mov_b32 s42, (L_end-L_att_full8)
bl v[120:123],v214,s[44:47],s77 offen sc1
sa s77,s77,0x1000
s_mov_b32 s43, 0x20000
s_setreg_imm32_b32 hwreg(HW_REG_MODE, 4, 2), 0
s_getpc_b64 s[60:61]
L_att_base:
s_mov_b32 s52, 0
bl v[124:127],v214,s[44:47],s77 offen sc1
sa s77,s77,0x1000
)"
R"(s_cmp_ge_u32 s50, 2
s_cbranch_scc1 L_q_nostash
s_lshl_b32 s56, s50, 14
sa s56,s56,0x20000
v_add_u32 v228, s56, v214
ds_write_b128 v228, a[0:3] offset:0
ds_write_b128 v228, a[4:7] offset:1024
ds_write_b128 v228, a[8:11] offset:2048
ds_write_b128 v228, a[12:15] offset:3072
ds_write_b128 v228, a[16:19] offset:4096
ds_write_b128 v228, a[20:23] offset:5120
ds_write_b128 v228, a[24:27] offset:6144
ds_write_b128 v228, a[28:31] offset:7168
ds_write_b128 v228, a[32:35] offset:8192
ds_write_b128 v228, a[36:39] offset:9216
ds_write_b128 v228, a[40:43] offset:10240
)"
R"(ds_write_b128 v228, a[44:47] offset:11264
ds_write_b128 v228, a[48:51] offset:12288
ds_write_b128 v228, a[52:55] offset:13312
ds_write_b128 v228, a[56:59] offset:14336
ds_write_b128 v228, a[60:63] offset:15360
L_q_nostash:
s_waitcnt vmcnt(16) lgkmcnt(0)
s_barrier
v_mov_b32 v212, v214
dr a[64:67],v212 offset:0
dr a[68:71],v212 offset:1024
dr a[72:75],v212 offset:2048
dr a[76:79],v212 offset:3072
dr a[80:83],v212 offset:4096
dr a[84:87],v212 offset:5120
dr a[88:91],v212 offset:6144
)"
R"(dr a[92:95],v212 offset:7168
dr a[128:131],v212 offset:16384
dr a[132:135],v212 offset:17408
dr a[136:139],v212 offset:18432
dr a[140:143],v212 offset:19456
dr a[144:147],v212 offset:20480
dr a[148:151],v212 offset:21504
dr a[152:155],v212 offset:22528
dr a[156:159],v212 offset:23552
v_readlane_b32 s56, v216, 0
s_nop 3
sa s58,s60,s56
s_addc_u32 s59, s61, 0
s_waitcnt lgkmcnt(8)
s_setpc_b64 s[58:59]
.p2align 12
)"
R"(L_att_full8:
mm v[128:131],a[64:67],a[0:3],v[176:179]
dr a[96:99],v212 offset:8192
v_readlane_b32 s53, v217, s52
mm v[128:131],a[68:71],a[4:7],v[128:131]
ex v156,v156
mm v[128:131],a[72:75],a[8:11],v[128:131]
ex v157,v157
mm v[128:131],a[76:79],a[12:15],v[128:131]
ex v158,v158
mm v[132:135],a[80:83],a[0:3],v[176:179]
ex v159,v159
mm v[132:135],a[84:87],a[4:7],v[132:135]
dr a[100:103],v212 offset:9216
cv v174,v156,v157
mm v[132:135],a[88:91],a[8:11],v[132:135]
)"
R"(dr a[104:107],v212 offset:10240
cv v175,v158,v159
mm v[132:135],a[92:95],a[12:15],v[132:135]
dr a[108:111],v212 offset:11264
v_readlane_b32 s54, v218, s52
mm v[204:207],v[208:211],v[172:175],v[204:207]
dr a[112:115],v212 offset:12288
v_readlane_b32 s55, v219, s52
mm v[96:99],a[160:163],v[172:175],v[96:99]
sa s52,s52,1
ex v128,v128
mm v[100:103],a[164:167],v[172:175],v[100:103]
dr a[116:119],v212 offset:13312
v_readlane_b32 s56, v216, s52
mm v[104:107],a[168:171],v[172:175],v[104:107]
ex v129,v129
)"
R"(mm v[108:111],a[172:175],v[172:175],v[108:111]
dr a[120:123],v212 offset:14336
cv v160,v128,v129
mm v[112:115],a[176:179],v[172:175],v[112:115]
ex v130,v130
mm v[116:119],a[180:183],v[172:175],v[116:119]
sa s58,s60,s56
s_addc_u32 s59, s61, 0
dr a[124:127],v212 offset:15360
mm v[120:123],a[184:187],v[172:175],v[120:123]
ex v131,v131
mm v[124:127],a[188:191],v[172:175],v[124:127]
dr a[160:163],v212 offset:24576
cv v161,v130,v131
mm v[136:139],a[64:67],a[16:19],v[180:183]
dr a[164:167],v212 offset:25600
)"
R"(mm v[136:139],a[68:71],a[20:23],v[136:139]
ex v132,v132
mm v[136:139],a[72:75],a[24:27],v[136:139]
ex v133,v133
mm v[136:139],a[76:79],a[28:31],v[136:139]
ex v134,v134
mm v[140:143],a[80:83],a[16:19],v[180:183]
ex v135,v135
mm v[140:143],a[84:87],a[20:23],v[140:143]
dr a[168:171],v212 offset:26624
cv v162,v132,v133
mm v[140:143],a[88:91],a[24:27],v[140:143]
s_waitcnt lgkmcnt(11)
dr a[172:175],v212 offset:27648
cv v163,v134,v135
mm v[140:143],a[92:95],a[28:31],v[140:143]
)"
R"(dr a[176:179],v212 offset:28672
mm v[192:195],v[208:211],v[160:163],v[192:195]
dr a[180:183],v212 offset:29696
mm v[0:3],a[128:131],v[160:163],v[0:3]
dr a[184:187],v212 offset:30720
mm v[4:7],a[132:135],v[160:163],v[4:7]
ex v136,v136
mm v[8:11],a[136:139],v[160:163],v[8:11]
dr a[188:191],v212 offset:31744
mm v[12:15],a[140:143],v[160:163],v[12:15]
ex v137,v137
mm v[16:19],a[144:147],v[160:163],v[16:19]
ex v138,v138
mm v[20:23],a[148:151],v[160:163],v[20:23]
ex v139,v139
mm v[24:27],a[152:155],v[160:163],v[24:27]
)"
R"(ex v140,v140
mm v[28:31],a[156:159],v[160:163],v[28:31]
ex v141,v141
mm v[144:147],a[64:67],a[32:35],v[184:187]
ex v142,v142
mm v[144:147],a[68:71],a[36:39],v[144:147]
ex v143,v143
mm v[144:147],a[72:75],a[40:43],v[144:147]
cv v164,v136,v137
cv v165,v138,v139
mm v[144:147],a[76:79],a[44:47],v[144:147]
cv v166,v140,v141
cv v167,v142,v143
mm v[148:151],a[80:83],a[32:35],v[184:187]
v_add_u32 v212, s55, v214
mm v[148:151],a[84:87],a[36:39],v[148:151]
)"
R"(mm v[148:151],a[88:91],a[40:43],v[148:151]
s_waitcnt vmcnt(0) lgkmcnt(0)
s_barrier
mm v[148:151],a[92:95],a[44:47],v[148:151]
sa m0,s54,0x0
sa s62,s53,0x0
mm v[196:199],v[208:211],v[164:167],v[196:199]
bl v214,s[44:47],s62 offen sc1 lds
mm v[32:35],a[128:131],v[164:167],v[32:35]
ex v144,v144
mm v[36:39],a[132:135],v[164:167],v[36:39]
ex v145,v145
mm v[40:43],a[136:139],v[164:167],v[40:43]
ex v146,v146
mm v[44:47],a[140:143],v[164:167],v[44:47]
ex v147,v147
)"
R"(mm v[48:51],a[144:147],v[164:167],v[48:51]
ex v148,v148
mm v[52:55],a[148:151],v[164:167],v[52:55]
ex v149,v149
mm v[56:59],a[152:155],v[164:167],v[56:59]
ex v150,v150
mm v[60:63],a[156:159],v[164:167],v[60:63]
ex v151,v151
mm v[152:155],a[64:67],a[48:51],v[188:191]
cv v168,v144,v145
cv v169,v146,v147
mm v[152:155],a[68:71],a[52:55],v[152:155]
cv v170,v148,v149
cv v171,v150,v151
mm v[152:155],a[72:75],a[56:59],v[152:155]
bl v214,s[44:47],s62 offen offset:1024 sc1 lds
)"
R"(mm v[152:155],a[76:79],a[60:63],v[152:155]
mm v[156:159],a[80:83],a[48:51],v[188:191]
mm v[156:159],a[84:87],a[52:55],v[156:159]
mm v[156:159],a[88:91],a[56:59],v[156:159]
mm v[156:159],a[92:95],a[60:63],v[156:159]
dr a[64:67],v212 offset:0
mm v[200:203],v[208:211],v[168:171],v[200:203]
mm v[64:67],a[128:131],v[168:171],v[64:67]
mm v[68:71],a[132:135],v[168:171],v[68:71]
dr a[68:71],v212 offset:1024
mm v[72:75],a[136:139],v[168:171],v[72:75]
ex v152,v152
mm v[76:79],a[140:143],v[168:171],v[76:79]
ex v153,v153
mm v[80:83],a[144:147],v[168:171],v[80:83]
dr a[72:75],v212 offset:2048
)"
R"(cv v172,v152,v153
mm v[84:87],a[148:151],v[168:171],v[84:87]
bl v214,s[44:47],s62 offen offset:2048 sc1 lds
mm v[88:91],a[152:155],v[168:171],v[88:91]
ex v154,v154
mm v[92:95],a[156:159],v[168:171],v[92:95]
dr a[76:79],v212 offset:3072
mm v[128:131],a[96:99],a[0:3],v[176:179]
ex v155,v155
mm v[128:131],a[100:103],a[4:7],v[128:131]
ex v156,v156
mm v[128:131],a[104:107],a[8:11],v[128:131]
ex v157,v157
mm v[128:131],a[108:111],a[12:15],v[128:131]
ex v158,v158
mm v[132:135],a[112:115],a[0:3],v[176:179]
)"
R"(ex v159,v159
mm v[132:135],a[116:119],a[4:7],v[132:135]
cv v173,v154,v155
cv v174,v156,v157
mm v[132:135],a[120:123],a[8:11],v[132:135]
dr a[80:83],v212 offset:4096
cv v175,v158,v159
mm v[132:135],a[124:127],a[12:15],v[132:135]
dr a[84:87],v212 offset:5120
mm v[204:207],v[208:211],v[172:175],v[204:207]
ex v128,v128
mm v[96:99],a[128:131],v[172:175],v[96:99]
dr a[88:91],v212 offset:6144
mm v[100:103],a[132:135],v[172:175],v[100:103]
bl v214,s[44:47],s62 offen offset:3072 sc1 lds
mm v[104:107],a[136:139],v[172:175],v[104:107]
)"
R"(ex v129,v129
mm v[108:111],a[140:143],v[172:175],v[108:111]
dr a[92:95],v212 offset:7168
cv v160,v128,v129
mm v[112:115],a[144:147],v[172:175],v[112:115]
ex v130,v130
mm v[116:119],a[148:151],v[172:175],v[116:119]
ex v131,v131
mm v[120:123],a[152:155],v[172:175],v[120:123]
ex v132,v132
mm v[124:127],a[156:159],v[172:175],v[124:127]
dr a[128:131],v212 offset:16384
cv v161,v130,v131
mm v[136:139],a[96:99],a[16:19],v[180:183]
ex v133,v133
mm v[136:139],a[100:103],a[20:23],v[136:139]
)"
R"(ex v134,v134
mm v[136:139],a[104:107],a[24:27],v[136:139]
ex v135,v135
mm v[136:139],a[108:111],a[28:31],v[136:139]
cv v162,v132,v133
cv v163,v134,v135
mm v[140:143],a[112:115],a[16:19],v[180:183]
sa m0,s54,0x1000
sa s62,s53,0x1000
mm v[140:143],a[116:119],a[20:23],v[140:143]
dr a[132:135],v212 offset:17408
mm v[140:143],a[120:123],a[24:27],v[140:143]
s_waitcnt lgkmcnt(10)
bl v214,s[44:47],s62 offen sc1 lds
mm v[140:143],a[124:127],a[28:31],v[140:143]
ex v136,v136
)"
R"(mm v[192:195],v[208:211],v[160:163],v[192:195]
ex v137,v137
mm v[0:3],a[160:163],v[160:163],v[0:3]
ex v138,v138
mm v[4:7],a[164:167],v[160:163],v[4:7]
ex v139,v139
mm v[8:11],a[168:171],v[160:163],v[8:11]
dr a[136:139],v212 offset:18432
cv v164,v136,v137
mm v[12:15],a[172:175],v[160:163],v[12:15]
ex v140,v140
mm v[16:19],a[176:179],v[160:163],v[16:19]
ex v141,v141
mm v[20:23],a[180:183],v[160:163],v[20:23]
ex v142,v142
mm v[24:27],a[184:187],v[160:163],v[24:27]
)"
R"(bl v214,s[44:47],s62 offen offset:1024 sc1 lds
mm v[28:31],a[188:191],v[160:163],v[28:31]
ex v143,v143
mm v[144:147],a[96:99],a[32:35],v[184:187]
cv v165,v138,v139
cv v166,v140,v141
mm v[144:147],a[100:103],a[36:39],v[144:147]
dr a[140:143],v212 offset:19456
cv v167,v142,v143
mm v[144:147],a[104:107],a[40:43],v[144:147]
buffer_load_dword v248, v215, s[40:43], 0 offen
mm v[144:147],a[108:111],a[44:47],v[144:147]
mm v[148:151],a[112:115],a[32:35],v[184:187]
mm v[148:151],a[116:119],a[36:39],v[148:151]
mm v[148:151],a[120:123],a[40:43],v[148:151]
mm v[148:151],a[124:127],a[44:47],v[148:151]
)"
R"(dr a[144:147],v212 offset:20480
mm v[196:199],v[208:211],v[164:167],v[196:199]
mm v[32:35],a[160:163],v[164:167],v[32:35]
mm v[36:39],a[164:167],v[164:167],v[36:39]
bl v214,s[44:47],s62 offen offset:2048 sc1 lds
mm v[40:43],a[168:171],v[164:167],v[40:43]
ex v144,v144
mm v[44:47],a[172:175],v[164:167],v[44:47]
ex v145,v145
mm v[48:51],a[176:179],v[164:167],v[48:51]
dr a[148:151],v212 offset:21504
cv v168,v144,v145
mm v[52:55],a[180:183],v[164:167],v[52:55]
ex v146,v146
mm v[56:59],a[184:187],v[164:167],v[56:59]
ex v147,v147
)"
R"(mm v[60:63],a[188:191],v[164:167],v[60:63]
ex v148,v148
mm v[152:155],a[96:99],a[48:51],v[188:191]
ex v149,v149
mm v[152:155],a[100:103],a[52:55],v[152:155]
ex v150,v150
mm v[152:155],a[104:107],a[56:59],v[152:155]
ex v151,v151
mm v[152:155],a[108:111],a[60:63],v[152:155]
cv v169,v146,v147
cv v170,v148,v149
mm v[156:159],a[112:115],a[48:51],v[188:191]
dr a[152:155],v212 offset:22528
cv v171,v150,v151
mm v[156:159],a[116:119],a[52:55],v[156:159]
bl v214,s[44:47],s62 offen offset:3072 sc1 lds
)"
R"(mm v[156:159],a[120:123],a[56:59],v[156:159]
ex v152,v152
mm v[156:159],a[124:127],a[60:63],v[156:159]
ex v153,v153
mm v[200:203],v[208:211],v[168:171],v[200:203]
ex v154,v154
mm v[64:67],a[160:163],v[168:171],v[64:67]
dr a[156:159],v212 offset:23552
cv v172,v152,v153
mm v[68:71],a[164:167],v[168:171],v[68:71]
ex v155,v155
mm v[72:75],a[168:171],v[168:171],v[72:75]
cv v173,v154,v155
mm v[76:79],a[172:175],v[168:171],v[76:79]
mm v[80:83],a[176:179],v[168:171],v[80:83]
mm v[84:87],a[180:183],v[168:171],v[84:87]
)"
R"(mm v[88:91],a[184:187],v[168:171],v[88:91]
mm v[92:95],a[188:191],v[168:171],v[92:95]
s_waitcnt lgkmcnt(8)
s_setpc_b64 s[58:59]
L_att_a0_16:
mm v[128:131],a[64:67],a[0:3],v[176:179]
ex v156,v156
mm v[128:131],a[68:71],a[4:7],v[128:131]
ex v157,v157
mm v[128:131],a[72:75],a[8:11],v[128:131]
ex v158,v158
mm v[128:131],a[76:79],a[12:15],v[128:131]
ex v159,v159
cv v174,v156,v157
cv v175,v158,v159
v_mov_b32 v162, 0
)"
R"(v_mov_b32 v163, 0
mm v[204:207],v[208:211],v[172:175],v[204:207]
dr a[96:99],v212 offset:8192
v_readlane_b32 s53, v217, s52
mm v[96:99],a[160:163],v[172:175],v[96:99]
dr a[100:103],v212 offset:9216
v_cndmask_b32 v128, v213, v128, s[88:89]
mm v[100:103],a[164:167],v[172:175],v[100:103]
dr a[104:107],v212 offset:10240
v_cndmask_b32 v129, v213, v129, s[90:91]
mm v[104:107],a[168:171],v[172:175],v[104:107]
dr a[108:111],v212 offset:11264
v_cndmask_b32 v130, v213, v130, s[92:93]
mm v[108:111],a[172:175],v[172:175],v[108:111]
dr a[112:115],v212 offset:12288
v_cndmask_b32 v131, v213, v131, s[94:95]
)"
R"(mm v[112:115],a[176:179],v[172:175],v[112:115]
dr a[116:119],v212 offset:13312
v_readlane_b32 s54, v218, s52
mm v[116:119],a[180:183],v[172:175],v[116:119]
dr a[120:123],v212 offset:14336
v_readlane_b32 s55, v219, s52
mm v[120:123],a[184:187],v[172:175],v[120:123]
sa s52,s52,1
dr a[124:127],v212 offset:15360
mm v[124:127],a[188:191],v[172:175],v[124:127]
dr a[160:163],v212 offset:24576
v_readlane_b32 s56, v216, s52
mm v[136:139],a[64:67],a[16:19],v[180:183]
sa s58,s60,s56
s_addc_u32 s59, s61, 0
dr a[164:167],v212 offset:25600
)"
R"(mm v[136:139],a[68:71],a[20:23],v[136:139]
ex v128,v128
mm v[136:139],a[72:75],a[24:27],v[136:139]
ex v129,v129
mm v[136:139],a[76:79],a[28:31],v[136:139]
ex v130,v130
mm v[140:143],a[80:83],a[16:19],v[180:183]
ex v131,v131
mm v[140:143],a[84:87],a[20:23],v[140:143]
dr a[168:171],v212 offset:26624
cv v160,v128,v129
mm v[140:143],a[88:91],a[24:27],v[140:143]
s_waitcnt lgkmcnt(11)
dr a[172:175],v212 offset:27648
cv v161,v130,v131
mm v[140:143],a[92:95],a[28:31],v[140:143]
)"
R"(dr a[176:179],v212 offset:28672
mm v[192:195],v[208:211],v[160:163],v[192:195]
dr a[180:183],v212 offset:29696
mm v[0:3],a[128:131],v[160:163],v[0:3]
dr a[184:187],v212 offset:30720
mm v[4:7],a[132:135],v[160:163],v[4:7]
ex v136,v136
mm v[8:11],a[136:139],v[160:163],v[8:11]
dr a[188:191],v212 offset:31744
mm v[12:15],a[140:143],v[160:163],v[12:15]
ex v137,v137
mm v[16:19],a[144:147],v[160:163],v[16:19]
ex v138,v138
mm v[20:23],a[148:151],v[160:163],v[20:23]
ex v139,v139
mm v[24:27],a[152:155],v[160:163],v[24:27]
)"
R"(ex v140,v140
mm v[28:31],a[156:159],v[160:163],v[28:31]
ex v141,v141
mm v[144:147],a[64:67],a[32:35],v[184:187]
ex v142,v142
mm v[144:147],a[68:71],a[36:39],v[144:147]
ex v143,v143
mm v[144:147],a[72:75],a[40:43],v[144:147]
cv v164,v136,v137
cv v165,v138,v139
mm v[144:147],a[76:79],a[44:47],v[144:147]
cv v166,v140,v141
cv v167,v142,v143
mm v[148:151],a[80:83],a[32:35],v[184:187]
v_add_u32 v212, s55, v214
mm v[148:151],a[84:87],a[36:39],v[148:151]
)"
R"(mm v[148:151],a[88:91],a[40:43],v[148:151]
s_waitcnt vmcnt(0) lgkmcnt(0)
s_barrier
mm v[148:151],a[92:95],a[44:47],v[148:151]
sa m0,s54,0x0
sa s62,s53,0x0
mm v[196:199],v[208:211],v[164:167],v[196:199]
bl v214,s[44:47],s62 offen sc1 lds
mm v[32:35],a[128:131],v[164:167],v[32:35]
ex v144,v144
mm v[36:39],a[132:135],v[164:167],v[36:39]
ex v145,v145
mm v[40:43],a[136:139],v[164:167],v[40:43]
ex v146,v146
mm v[44:47],a[140:143],v[164:167],v[44:47]
bl v214,s[44:47],s62 offen offset:1024 sc1 lds
)"
R"(mm v[48:51],a[144:147],v[164:167],v[48:51]
ex v147,v147
mm v[52:55],a[148:151],v[164:167],v[52:55]
ex v148,v148
mm v[56:59],a[152:155],v[164:167],v[56:59]
ex v149,v149
mm v[60:63],a[156:159],v[164:167],v[60:63]
ex v150,v150
mm v[152:155],a[64:67],a[48:51],v[188:191]
bl v214,s[44:47],s62 offen offset:2048 sc1 lds
mm v[152:155],a[68:71],a[52:55],v[152:155]
ex v151,v151
mm v[152:155],a[72:75],a[56:59],v[152:155]
cv v168,v144,v145
cv v169,v146,v147
mm v[152:155],a[76:79],a[60:63],v[152:155]
)"
R"(cv v170,v148,v149
cv v171,v150,v151
mm v[156:159],a[80:83],a[48:51],v[188:191]
mm v[156:159],a[84:87],a[52:55],v[156:159]
bl v214,s[44:47],s62 offen offset:3072 sc1 lds
mm v[156:159],a[88:91],a[56:59],v[156:159]
mm v[156:159],a[92:95],a[60:63],v[156:159]
dr a[64:67],v212 offset:0
mm v[200:203],v[208:211],v[168:171],v[200:203]
ex v152,v152
mm v[64:67],a[128:131],v[168:171],v[64:67]
sa m0,s54,0x1000
sa s62,s53,0x1000
dr a[68:71],v212 offset:1024
mm v[68:71],a[132:135],v[168:171],v[68:71]
ex v153,v153
)"
R"(mm v[72:75],a[136:139],v[168:171],v[72:75]
ex v154,v154
mm v[76:79],a[140:143],v[168:171],v[76:79]
ex v155,v155
mm v[80:83],a[144:147],v[168:171],v[80:83]
ex v156,v156
mm v[84:87],a[148:151],v[168:171],v[84:87]
ex v157,v157
mm v[88:91],a[152:155],v[168:171],v[88:91]
ex v158,v158
mm v[92:95],a[156:159],v[168:171],v[92:95]
ex v159,v159
cv v172,v152,v153
cv v173,v154,v155
cv v174,v156,v157
cv v175,v158,v159
)"
R"(s_nop 1
mm v[204:207],v[208:211],v[172:175],v[204:207]
dr a[72:75],v212 offset:2048
mm v[96:99],a[128:131],v[172:175],v[96:99]
bl v214,s[44:47],s62 offen sc1 lds
mm v[100:103],a[132:135],v[172:175],v[100:103]
dr a[76:79],v212 offset:3072
mm v[104:107],a[136:139],v[172:175],v[104:107]
dr a[80:83],v212 offset:4096
mm v[108:111],a[140:143],v[172:175],v[108:111]
bl v214,s[44:47],s62 offen offset:1024 sc1 lds
mm v[112:115],a[144:147],v[172:175],v[112:115]
dr a[84:87],v212 offset:5120
mm v[116:119],a[148:151],v[172:175],v[116:119]
dr a[88:91],v212 offset:6144
mm v[120:123],a[152:155],v[172:175],v[120:123]
)"
R"(bl v214,s[44:47],s62 offen offset:2048 sc1 lds
mm v[124:127],a[156:159],v[172:175],v[124:127]
dr a[92:95],v212 offset:7168
mm v[136:139],a[96:99],a[16:19],v[180:183]
dr a[128:131],v212 offset:16384
mm v[136:139],a[100:103],a[20:23],v[136:139]
bl v214,s[44:47],s62 offen offset:3072 sc1 lds
mm v[136:139],a[104:107],a[24:27],v[136:139]
sa m0,s54,0x2000
sa s62,s53,0x2000
mm v[136:139],a[108:111],a[28:31],v[136:139]
bl v214,s[44:47],s62 offen sc1 lds
mm v[140:143],a[112:115],a[16:19],v[180:183]
dr a[132:135],v212 offset:17408
mm v[140:143],a[116:119],a[20:23],v[140:143]
mm v[140:143],a[120:123],a[24:27],v[140:143]
)"
R"(s_waitcnt lgkmcnt(10)
mm v[140:143],a[124:127],a[28:31],v[140:143]
mm v[144:147],a[96:99],a[32:35],v[184:187]
bl v214,s[44:47],s62 offen offset:1024 sc1 lds
mm v[144:147],a[100:103],a[36:39],v[144:147]
dr a[136:139],v212 offset:18432
mm v[144:147],a[104:107],a[40:43],v[144:147]
ex v136,v136
mm v[144:147],a[108:111],a[44:47],v[144:147]
ex v137,v137
mm v[148:151],a[112:115],a[32:35],v[184:187]
v_cndmask_b32 v140, v213, v140, s[88:89]
v_cndmask_b32 v141, v213, v141, s[90:91]
mm v[148:151],a[116:119],a[36:39],v[148:151]
v_cndmask_b32 v142, v213, v142, s[92:93]
v_cndmask_b32 v143, v213, v143, s[94:95]
)"
R"(mm v[148:151],a[120:123],a[40:43],v[148:151]
ex v138,v138
mm v[148:151],a[124:127],a[44:47],v[148:151]
ex v139,v139
cv v164,v136,v137
cv v165,v138,v139
ex v140,v140
ex v141,v141
ex v142,v142
cv v166,v140,v141
ex v143,v143
ex v144,v144
cv v167,v142,v143
ex v145,v145
ex v146,v146
mm v[196:199],v[208:211],v[164:167],v[196:199]
)"
R"(dr a[140:143],v212 offset:19456
cv v168,v144,v145
mm v[32:35],a[160:163],v[164:167],v[32:35]
bl v214,s[44:47],s62 offen offset:2048 sc1 lds
mm v[36:39],a[164:167],v[164:167],v[36:39]
bl v214,s[44:47],s62 offen offset:3072 sc1 lds
mm v[40:43],a[168:171],v[164:167],v[40:43]
ex v147,v147
mm v[44:47],a[172:175],v[164:167],v[44:47]
dr a[144:147],v212 offset:20480
cv v169,v146,v147
mm v[48:51],a[176:179],v[164:167],v[48:51]
sa m0,s54,0x3000
sa s62,s53,0x3000
mm v[52:55],a[180:183],v[164:167],v[52:55]
bl v214,s[44:47],s62 offen sc1 lds
)"
R"(mm v[56:59],a[184:187],v[164:167],v[56:59]
ex v148,v148
mm v[60:63],a[188:191],v[164:167],v[60:63]
ex v149,v149
mm v[152:155],a[96:99],a[48:51],v[188:191]
dr a[148:151],v212 offset:21504
cv v170,v148,v149
mm v[152:155],a[100:103],a[52:55],v[152:155]
ex v150,v150
mm v[152:155],a[104:107],a[56:59],v[152:155]
ex v151,v151
mm v[152:155],a[108:111],a[60:63],v[152:155]
cv v171,v150,v151
mm v[156:159],a[112:115],a[48:51],v[188:191]
bl v214,s[44:47],s62 offen offset:1024 sc1 lds
mm v[156:159],a[116:119],a[52:55],v[156:159]
)"
R"(dr a[152:155],v212 offset:22528
mm v[156:159],a[120:123],a[56:59],v[156:159]
mm v[156:159],a[124:127],a[60:63],v[156:159]
bl v214,s[44:47],s62 offen offset:2048 sc1 lds
mm v[200:203],v[208:211],v[168:171],v[200:203]
ex v152,v152
mm v[64:67],a[160:163],v[168:171],v[64:67]
ex v153,v153
mm v[68:71],a[164:167],v[168:171],v[68:71]
dr a[156:159],v212 offset:23552
cv v172,v152,v153
mm v[72:75],a[168:171],v[168:171],v[72:75]
ex v154,v154
mm v[76:79],a[172:175],v[168:171],v[76:79]
bl v214,s[44:47],s62 offen offset:3072 sc1 lds
mm v[80:83],a[176:179],v[168:171],v[80:83]
)"
R"(ex v155,v155
mm v[84:87],a[180:183],v[168:171],v[84:87]
cv v173,v154,v155
mm v[88:91],a[184:187],v[168:171],v[88:91]
mm v[92:95],a[188:191],v[168:171],v[92:95]
s_waitcnt lgkmcnt(8)
s_setpc_b64 s[58:59]
.p2align 12
L_att_full16:
mm v[128:131],a[64:67],a[0:3],v[176:179]
dr a[96:99],v212 offset:8192
v_readlane_b32 s53, v217, s52
mm v[128:131],a[68:71],a[4:7],v[128:131]
ex v156,v156
mm v[128:131],a[72:75],a[8:11],v[128:131]
ex v157,v157
)"
R"(mm v[128:131],a[76:79],a[12:15],v[128:131]
ex v158,v158
mm v[132:135],a[80:83],a[0:3],v[176:179]
ex v159,v159
mm v[132:135],a[84:87],a[4:7],v[132:135]
dr a[100:103],v212 offset:9216
cv v174,v156,v157
mm v[132:135],a[88:91],a[8:11],v[132:135]
dr a[104:107],v212 offset:10240
cv v175,v158,v159
mm v[132:135],a[92:95],a[12:15],v[132:135]
dr a[108:111],v212 offset:11264
v_readlane_b32 s54, v218, s52
mm v[204:207],v[208:211],v[172:175],v[204:207]
dr a[112:115],v212 offset:12288
v_readlane_b32 s55, v219, s52
)"
R"(mm v[96:99],a[160:163],v[172:175],v[96:99]
sa s52,s52,1
ex v128,v128
mm v[100:103],a[164:167],v[172:175],v[100:103]
dr a[116:119],v212 offset:13312
v_readlane_b32 s56, v216, s52
mm v[104:107],a[168:171],v[172:175],v[104:107]
ex v129,v129
mm v[108:111],a[172:175],v[172:175],v[108:111]
dr a[120:123],v212 offset:14336
cv v160,v128,v129
mm v[112:115],a[176:179],v[172:175],v[112:115]
ex v130,v130
mm v[116:119],a[180:183],v[172:175],v[116:119]
sa s58,s60,s56
s_addc_u32 s59, s61, 0
)"
R"(dr a[124:127],v212 offset:15360
mm v[120:123],a[184:187],v[172:175],v[120:123]
ex v131,v131
mm v[124:127],a[188:191],v[172:175],v[124:127]
dr a[160:163],v212 offset:24576
cv v161,v130,v131
mm v[136:139],a[64:67],a[16:19],v[180:183]
dr a[164:167],v212 offset:25600
mm v[136:139],a[68:71],a[20:23],v[136:139]
ex v132,v132
mm v[136:139],a[72:75],a[24:27],v[136:139]
ex v133,v133
mm v[136:139],a[76:79],a[28:31],v[136:139]
ex v134,v134
mm v[140:143],a[80:83],a[16:19],v[180:183]
ex v135,v135
)"
R"(mm v[140:143],a[84:87],a[20:23],v[140:143]
dr a[168:171],v212 offset:26624
cv v162,v132,v133
mm v[140:143],a[88:91],a[24:27],v[140:143]
s_waitcnt lgkmcnt(11)
dr a[172:175],v212 offset:27648
cv v163,v134,v135
mm v[140:143],a[92:95],a[28:31],v[140:143]
dr a[176:179],v212 offset:28672
mm v[192:195],v[208:211],v[160:163],v[192:195]
dr a[180:183],v212 offset:29696
mm v[0:3],a[128:131],v[160:163],v[0:3]
dr a[184:187],v212 offset:30720
mm v[4:7],a[132:135],v[160:163],v[4:7]
ex v136,v136
mm v[8:11],a[136:139],v[160:163],v[8:11]
)"
R"(dr a[188:191],v212 offset:31744
mm v[12:15],a[140:143],v[160:163],v[12:15]
ex v137,v137
mm v[16:19],a[144:147],v[160:163],v[16:19]
ex v138,v138
mm v[20:23],a[148:151],v[160:163],v[20:23]
ex v139,v139
mm v[24:27],a[152:155],v[160:163],v[24:27]
ex v140,v140
mm v[28:31],a[156:159],v[160:163],v[28:31]
ex v141,v141
mm v[144:147],a[64:67],a[32:35],v[184:187]
ex v142,v142
mm v[144:147],a[68:71],a[36:39],v[144:147]
ex v143,v143
mm v[144:147],a[72:75],a[40:43],v[144:147]
)"
R"(cv v164,v136,v137
cv v165,v138,v139
mm v[144:147],a[76:79],a[44:47],v[144:147]
cv v166,v140,v141
cv v167,v142,v143
mm v[148:151],a[80:83],a[32:35],v[184:187]
v_add_u32 v212, s55, v214
mm v[148:151],a[84:87],a[36:39],v[148:151]
mm v[148:151],a[88:91],a[40:43],v[148:151]
s_waitcnt vmcnt(0) lgkmcnt(0)
s_barrier
mm v[148:151],a[92:95],a[44:47],v[148:151]
sa m0,s54,0x0
sa s62,s53,0x0
mm v[196:199],v[208:211],v[164:167],v[196:199]
bl v214,s[44:47],s62 offen sc1 lds
)"
R"(mm v[32:35],a[128:131],v[164:167],v[32:35]
ex v144,v144
mm v[36:39],a[132:135],v[164:167],v[36:39]
ex v145,v145
mm v[40:43],a[136:139],v[164:167],v[40:43]
ex v146,v146
mm v[44:47],a[140:143],v[164:167],v[44:47]
ex v147,v147
mm v[48:51],a[144:147],v[164:167],v[48:51]
bl v214,s[44:47],s62 offen offset:1024 sc1 lds
mm v[52:55],a[148:151],v[164:167],v[52:55]
ex v148,v148
mm v[56:59],a[152:155],v[164:167],v[56:59]
ex v149,v149
mm v[60:63],a[156:159],v[164:167],v[60:63]
ex v150,v150
)"
R"(mm v[152:155],a[64:67],a[48:51],v[188:191]
ex v151,v151
mm v[152:155],a[68:71],a[52:55],v[152:155]
cv v168,v144,v145
cv v169,v146,v147
mm v[152:155],a[72:75],a[56:59],v[152:155]
cv v170,v148,v149
cv v171,v150,v151
mm v[152:155],a[76:79],a[60:63],v[152:155]
bl v214,s[44:47],s62 offen offset:2048 sc1 lds
mm v[156:159],a[80:83],a[48:51],v[188:191]
mm v[156:159],a[84:87],a[52:55],v[156:159]
mm v[156:159],a[88:91],a[56:59],v[156:159]
mm v[156:159],a[92:95],a[60:63],v[156:159]
dr a[64:67],v212 offset:0
mm v[200:203],v[208:211],v[168:171],v[200:203]
)"
R"(bl v214,s[44:47],s62 offen offset:3072 sc1 lds
mm v[64:67],a[128:131],v[168:171],v[64:67]
ex v152,v152
mm v[68:71],a[132:135],v[168:171],v[68:71]
dr a[68:71],v212 offset:1024
mm v[72:75],a[136:139],v[168:171],v[72:75]
ex v153,v153
mm v[76:79],a[140:143],v[168:171],v[76:79]
ex v154,v154
mm v[80:83],a[144:147],v[168:171],v[80:83]
sa m0,s54,0x1000
sa s62,s53,0x1000
dr a[72:75],v212 offset:2048
mm v[84:87],a[148:151],v[168:171],v[84:87]
bl v214,s[44:47],s62 offen sc1 lds
mm v[88:91],a[152:155],v[168:171],v[88:91]
)"
R"(ex v155,v155
mm v[92:95],a[156:159],v[168:171],v[92:95]
dr a[76:79],v212 offset:3072
cv v172,v152,v153
mm v[128:131],a[96:99],a[0:3],v[176:179]
ex v156,v156
mm v[128:131],a[100:103],a[4:7],v[128:131]
ex v157,v157
mm v[128:131],a[104:107],a[8:11],v[128:131]
ex v158,v158
mm v[128:131],a[108:111],a[12:15],v[128:131]
ex v159,v159
mm v[132:135],a[112:115],a[0:3],v[176:179]
cv v173,v154,v155
cv v174,v156,v157
mm v[132:135],a[116:119],a[4:7],v[132:135]
)"
R"(dr a[80:83],v212 offset:4096
cv v175,v158,v159
mm v[132:135],a[120:123],a[8:11],v[132:135]
bl v214,s[44:47],s62 offen offset:1024 sc1 lds
mm v[132:135],a[124:127],a[12:15],v[132:135]
dr a[84:87],v212 offset:5120
mm v[204:207],v[208:211],v[172:175],v[204:207]
ex v128,v128
mm v[96:99],a[128:131],v[172:175],v[96:99]
dr a[88:91],v212 offset:6144
mm v[100:103],a[132:135],v[172:175],v[100:103]
bl v214,s[44:47],s62 offen offset:2048 sc1 lds
mm v[104:107],a[136:139],v[172:175],v[104:107]
ex v129,v129
mm v[108:111],a[140:143],v[172:175],v[108:111]
dr a[92:95],v212 offset:7168
)"
R"(cv v160,v128,v129
mm v[112:115],a[144:147],v[172:175],v[112:115]
ex v130,v130
mm v[116:119],a[148:151],v[172:175],v[116:119]
ex v131,v131
mm v[120:123],a[152:155],v[172:175],v[120:123]
bl v214,s[44:47],s62 offen offset:3072 sc1 lds
mm v[124:127],a[156:159],v[172:175],v[124:127]
dr a[128:131],v212 offset:16384
cv v161,v130,v131
mm v[136:139],a[96:99],a[16:19],v[180:183]
ex v132,v132
mm v[136:139],a[100:103],a[20:23],v[136:139]
ex v133,v133
mm v[136:139],a[104:107],a[24:27],v[136:139]
ex v134,v134
)"
R"(mm v[136:139],a[108:111],a[28:31],v[136:139]
ex v135,v135
mm v[140:143],a[112:115],a[16:19],v[180:183]
cv v162,v132,v133
cv v163,v134,v135
mm v[140:143],a[116:119],a[20:23],v[140:143]
sa m0,s54,0x2000
sa s62,s53,0x2000
dr a[132:135],v212 offset:17408
mm v[140:143],a[120:123],a[24:27],v[140:143]
s_waitcnt lgkmcnt(10)
bl v214,s[44:47],s62 offen sc1 lds
mm v[140:143],a[124:127],a[28:31],v[140:143]
ex v136,v136
mm v[192:195],v[208:211],v[160:163],v[192:195]
ex v137,v137
)"
R"(mm v[0:3],a[160:163],v[160:163],v[0:3]
bl v214,s[44:47],s62 offen offset:1024 sc1 lds
mm v[4:7],a[164:167],v[160:163],v[4:7]
ex v138,v138
mm v[8:11],a[168:171],v[160:163],v[8:11]
dr a[136:139],v212 offset:18432
cv v164,v136,v137
mm v[12:15],a[172:175],v[160:163],v[12:15]
ex v139,v139
mm v[16:19],a[176:179],v[160:163],v[16:19]
ex v140,v140
mm v[20:23],a[180:183],v[160:163],v[20:23]
ex v141,v141
mm v[24:27],a[184:187],v[160:163],v[24:27]
bl v214,s[44:47],s62 offen offset:2048 sc1 lds
mm v[28:31],a[188:191],v[160:163],v[28:31]
)"
R"(ex v142,v142
mm v[144:147],a[96:99],a[32:35],v[184:187]
ex v143,v143
mm v[144:147],a[100:103],a[36:39],v[144:147]
dr a[140:143],v212 offset:19456
cv v165,v138,v139
mm v[144:147],a[104:107],a[40:43],v[144:147]
cv v166,v140,v141
cv v167,v142,v143
mm v[144:147],a[108:111],a[44:47],v[144:147]
buffer_load_dword v248, v215, s[40:43], 0 offen
mm v[148:151],a[112:115],a[32:35],v[184:187]
bl v214,s[44:47],s62 offen offset:3072 sc1 lds
mm v[148:151],a[116:119],a[36:39],v[148:151]
mm v[148:151],a[120:123],a[40:43],v[148:151]
mm v[148:151],a[124:127],a[44:47],v[148:151]
)"
R"(dr a[144:147],v212 offset:20480
mm v[196:199],v[208:211],v[164:167],v[196:199]
ex v144,v144
mm v[32:35],a[160:163],v[164:167],v[32:35]
sa m0,s54,0x3000
sa s62,s53,0x3000
mm v[36:39],a[164:167],v[164:167],v[36:39]
bl v214,s[44:47],s62 offen sc1 lds
mm v[40:43],a[168:171],v[164:167],v[40:43]
ex v145,v145
mm v[44:47],a[172:175],v[164:167],v[44:47]
ex v146,v146
mm v[48:51],a[176:179],v[164:167],v[48:51]
dr a[148:151],v212 offset:21504
cv v168,v144,v145
mm v[52:55],a[180:183],v[164:167],v[52:55]
)"
R"(ex v147,v147
mm v[56:59],a[184:187],v[164:167],v[56:59]
ex v148,v148
mm v[60:63],a[188:191],v[164:167],v[60:63]
bl v214,s[44:47],s62 offen offset:1024 sc1 lds
mm v[152:155],a[96:99],a[48:51],v[188:191]
ex v149,v149
mm v[152:155],a[100:103],a[52:55],v[152:155]
ex v150,v150
mm v[152:155],a[104:107],a[56:59],v[152:155]
ex v151,v151
mm v[152:155],a[108:111],a[60:63],v[152:155]
cv v169,v146,v147
cv v170,v148,v149
mm v[156:159],a[112:115],a[48:51],v[188:191]
dr a[152:155],v212 offset:22528
)"
R"(cv v171,v150,v151
mm v[156:159],a[116:119],a[52:55],v[156:159]
bl v214,s[44:47],s62 offen offset:2048 sc1 lds
mm v[156:159],a[120:123],a[56:59],v[156:159]
ex v152,v152
mm v[156:159],a[124:127],a[60:63],v[156:159]
ex v153,v153
mm v[200:203],v[208:211],v[168:171],v[200:203]
ex v154,v154
mm v[64:67],a[160:163],v[168:171],v[64:67]
dr a[156:159],v212 offset:23552
cv v172,v152,v153
mm v[68:71],a[164:167],v[168:171],v[68:71]
ex v155,v155
mm v[72:75],a[168:171],v[168:171],v[72:75]
bl v214,s[44:47],s62 offen offset:3072 sc1 lds
)"
R"(mm v[76:79],a[172:175],v[168:171],v[76:79]
cv v173,v154,v155
mm v[80:83],a[176:179],v[168:171],v[80:83]
mm v[84:87],a[180:183],v[168:171],v[84:87]
mm v[88:91],a[184:187],v[168:171],v[88:91]
mm v[92:95],a[188:191],v[168:171],v[92:95]
s_waitcnt lgkmcnt(8)
s_setpc_b64 s[58:59]
L_att_a1_16:
mm v[128:131],a[64:67],a[0:3],v[176:179]
dr a[96:99],v212 offset:8192
v_readlane_b32 s53, v217, s52
mm v[128:131],a[68:71],a[4:7],v[128:131]
ex v156,v156
mm v[128:131],a[72:75],a[8:11],v[128:131]
ex v157,v157
)"
R"(mm v[128:131],a[76:79],a[12:15],v[128:131]
ex v158,v158
mm v[132:135],a[80:83],a[0:3],v[176:179]
ex v159,v159
mm v[132:135],a[84:87],a[4:7],v[132:135]
dr a[100:103],v212 offset:9216
cv v174,v156,v157
mm v[132:135],a[88:91],a[8:11],v[132:135]
dr a[104:107],v212 offset:10240
cv v175,v158,v159
mm v[132:135],a[92:95],a[12:15],v[132:135]
dr a[108:111],v212 offset:11264
v_readlane_b32 s54, v218, s52
mm v[204:207],v[208:211],v[172:175],v[204:207]
dr a[112:115],v212 offset:12288
v_readlane_b32 s55, v219, s52
)"
R"(mm v[96:99],a[160:163],v[172:175],v[96:99]
sa s52,s52,1
ex v128,v128
mm v[100:103],a[164:167],v[172:175],v[100:103]
dr a[116:119],v212 offset:13312
v_cndmask_b32 v132, v213, v132, s[88:89]
mm v[104:107],a[168:171],v[172:175],v[104:107]
v_cndmask_b32 v133, v213, v133, s[90:91]
v_cndmask_b32 v134, v213, v134, s[92:93]
mm v[108:111],a[172:175],v[172:175],v[108:111]
dr a[120:123],v212 offset:14336
v_cndmask_b32 v135, v213, v135, s[94:95]
mm v[112:115],a[176:179],v[172:175],v[112:115]
ex v129,v129
mm v[116:119],a[180:183],v[172:175],v[116:119]
dr a[124:127],v212 offset:15360
)"
R"(cv v160,v128,v129
mm v[120:123],a[184:187],v[172:175],v[120:123]
ex v130,v130
mm v[124:127],a[188:191],v[172:175],v[124:127]
dr a[160:163],v212 offset:24576
v_readlane_b32 s56, v216, s52
mm v[136:139],a[64:67],a[16:19],v[180:183]
sa s58,s60,s56
s_addc_u32 s59, s61, 0
dr a[164:167],v212 offset:25600
mm v[136:139],a[68:71],a[20:23],v[136:139]
ex v131,v131
mm v[136:139],a[72:75],a[24:27],v[136:139]
ex v132,v132
mm v[136:139],a[76:79],a[28:31],v[136:139]
ex v133,v133
)"
R"(mm v[140:143],a[80:83],a[16:19],v[180:183]
ex v134,v134
mm v[140:143],a[84:87],a[20:23],v[140:143]
ex v135,v135
mm v[140:143],a[88:91],a[24:27],v[140:143]
s_waitcnt lgkmcnt(10)
dr a[168:171],v212 offset:26624
cv v161,v130,v131
mm v[140:143],a[92:95],a[28:31],v[140:143]
cv v162,v132,v133
cv v163,v134,v135
ex v136,v136
ex v137,v137
mm v[192:195],v[208:211],v[160:163],v[192:195]
dr a[172:175],v212 offset:27648
cv v164,v136,v137
)"
R"(mm v[0:3],a[128:131],v[160:163],v[0:3]
dr a[176:179],v212 offset:28672
mm v[4:7],a[132:135],v[160:163],v[4:7]
dr a[180:183],v212 offset:29696
mm v[8:11],a[136:139],v[160:163],v[8:11]
dr a[184:187],v212 offset:30720
mm v[12:15],a[140:143],v[160:163],v[12:15]
dr a[188:191],v212 offset:31744
mm v[16:19],a[144:147],v[160:163],v[16:19]
ex v138,v138
mm v[20:23],a[148:151],v[160:163],v[20:23]
ex v139,v139
mm v[24:27],a[152:155],v[160:163],v[24:27]
ex v140,v140
mm v[28:31],a[156:159],v[160:163],v[28:31]
ex v141,v141
)"
R"(mm v[144:147],a[64:67],a[32:35],v[184:187]
ex v142,v142
mm v[144:147],a[68:71],a[36:39],v[144:147]
ex v143,v143
mm v[144:147],a[72:75],a[40:43],v[144:147]
cv v165,v138,v139
cv v166,v140,v141
mm v[144:147],a[76:79],a[44:47],v[144:147]
cv v167,v142,v143
v_add_u32 v212, s55, v214
mm v[148:151],a[80:83],a[32:35],v[184:187]
mm v[148:151],a[84:87],a[36:39],v[148:151]
mm v[148:151],a[88:91],a[40:43],v[148:151]
s_waitcnt vmcnt(0) lgkmcnt(0)
s_barrier
mm v[148:151],a[92:95],a[44:47],v[148:151]
)"
R"(sa m0,s54,0x0
sa s62,s53,0x0
mm v[196:199],v[208:211],v[164:167],v[196:199]
bl v214,s[44:47],s62 offen sc1 lds
mm v[32:35],a[128:131],v[164:167],v[32:35]
ex v144,v144
mm v[36:39],a[132:135],v[164:167],v[36:39]
ex v145,v145
mm v[40:43],a[136:139],v[164:167],v[40:43]
ex v146,v146
mm v[44:47],a[140:143],v[164:167],v[44:47]
bl v214,s[44:47],s62 offen offset:1024 sc1 lds
mm v[48:51],a[144:147],v[164:167],v[48:51]
ex v147,v147
mm v[52:55],a[148:151],v[164:167],v[52:55]
ex v148,v148
)"
R"(mm v[56:59],a[152:155],v[164:167],v[56:59]
ex v149,v149
mm v[60:63],a[156:159],v[164:167],v[60:63]
ex v150,v150
mm v[152:155],a[64:67],a[48:51],v[188:191]
bl v214,s[44:47],s62 offen offset:2048 sc1 lds
mm v[152:155],a[68:71],a[52:55],v[152:155]
ex v151,v151
mm v[152:155],a[72:75],a[56:59],v[152:155]
cv v168,v144,v145
cv v169,v146,v147
mm v[152:155],a[76:79],a[60:63],v[152:155]
cv v170,v148,v149
cv v171,v150,v151
mm v[156:159],a[80:83],a[48:51],v[188:191]
bl v214,s[44:47],s62 offen offset:3072 sc1 lds
)"
R"(mm v[156:159],a[84:87],a[52:55],v[156:159]
mm v[156:159],a[88:91],a[56:59],v[156:159]
mm v[156:159],a[92:95],a[60:63],v[156:159]
dr a[64:67],v212 offset:0
mm v[200:203],v[208:211],v[168:171],v[200:203]
sa m0,s54,0x1000
sa s62,s53,0x1000
mm v[64:67],a[128:131],v[168:171],v[64:67]
dr a[68:71],v212 offset:1024
mm v[68:71],a[132:135],v[168:171],v[68:71]
bl v214,s[44:47],s62 offen sc1 lds
mm v[72:75],a[136:139],v[168:171],v[72:75]
ex v152,v152
mm v[76:79],a[140:143],v[168:171],v[76:79]
ex v153,v153
mm v[80:83],a[144:147],v[168:171],v[80:83]
)"
R"(ex v154,v154
mm v[84:87],a[148:151],v[168:171],v[84:87]
ex v155,v155
mm v[88:91],a[152:155],v[168:171],v[88:91]
ex v156,v156
mm v[92:95],a[156:159],v[168:171],v[92:95]
ex v157,v157
cv v172,v152,v153
cv v173,v154,v155
cv v174,v156,v157
ex v158,v158
ex v159,v159
s_nop 0
cv v175,v158,v159
s_nop 1
mm v[204:207],v[208:211],v[172:175],v[204:207]
)"
R"(dr a[72:75],v212 offset:2048
mm v[96:99],a[128:131],v[172:175],v[96:99]
dr a[76:79],v212 offset:3072
mm v[100:103],a[132:135],v[172:175],v[100:103]
bl v214,s[44:47],s62 offen offset:1024 sc1 lds
mm v[104:107],a[136:139],v[172:175],v[104:107]
dr a[80:83],v212 offset:4096
mm v[108:111],a[140:143],v[172:175],v[108:111]
dr a[84:87],v212 offset:5120
mm v[112:115],a[144:147],v[172:175],v[112:115]
bl v214,s[44:47],s62 offen offset:2048 sc1 lds
mm v[116:119],a[148:151],v[172:175],v[116:119]
dr a[88:91],v212 offset:6144
mm v[120:123],a[152:155],v[172:175],v[120:123]
dr a[92:95],v212 offset:7168
mm v[124:127],a[156:159],v[172:175],v[124:127]
)"
R"(bl v214,s[44:47],s62 offen offset:3072 sc1 lds
mm v[136:139],a[96:99],a[16:19],v[180:183]
sa m0,s54,0x2000
sa s62,s53,0x2000
dr a[128:131],v212 offset:16384
mm v[136:139],a[100:103],a[20:23],v[136:139]
bl v214,s[44:47],s62 offen sc1 lds
mm v[136:139],a[104:107],a[24:27],v[136:139]
s_waitcnt lgkmcnt(9)
v_mov_b32 v166, 0
v_mov_b32 v167, 0
mm v[136:139],a[108:111],a[28:31],v[136:139]
dr a[132:135],v212 offset:17408
mm v[144:147],a[96:99],a[32:35],v[184:187]
mm v[144:147],a[100:103],a[36:39],v[144:147]
mm v[144:147],a[104:107],a[40:43],v[144:147]
)"
R"(bl v214,s[44:47],s62 offen offset:1024 sc1 lds
mm v[144:147],a[108:111],a[44:47],v[144:147]
mm v[148:151],a[112:115],a[32:35],v[184:187]
dr a[136:139],v212 offset:18432
mm v[148:151],a[116:119],a[36:39],v[148:151]
v_cndmask_b32 v136, v213, v136, s[88:89]
v_cndmask_b32 v137, v213, v137, s[90:91]
mm v[148:151],a[120:123],a[40:43],v[148:151]
v_cndmask_b32 v138, v213, v138, s[92:93]
v_cndmask_b32 v139, v213, v139, s[94:95]
mm v[148:151],a[124:127],a[44:47],v[148:151]
ex v136,v136
ex v137,v137
ex v138,v138
cv v164,v136,v137
ex v139,v139
)"
R"(ex v144,v144
cv v165,v138,v139
ex v145,v145
ex v146,v146
mm v[196:199],v[208:211],v[164:167],v[196:199]
bl v214,s[44:47],s62 offen offset:2048 sc1 lds
mm v[32:35],a[160:163],v[164:167],v[32:35]
dr a[140:143],v212 offset:19456
cv v168,v144,v145
mm v[36:39],a[164:167],v[164:167],v[36:39]
ex v147,v147
mm v[40:43],a[168:171],v[164:167],v[40:43]
bl v214,s[44:47],s62 offen offset:3072 sc1 lds
mm v[44:47],a[172:175],v[164:167],v[44:47]
ex v148,v148
mm v[48:51],a[176:179],v[164:167],v[48:51]
)"
R"(ex v149,v149
mm v[52:55],a[180:183],v[164:167],v[52:55]
sa m0,s54,0x3000
sa s62,s53,0x3000
dr a[144:147],v212 offset:20480
mm v[56:59],a[184:187],v[164:167],v[56:59]
bl v214,s[44:47],s62 offen sc1 lds
mm v[60:63],a[188:191],v[164:167],v[60:63]
ex v150,v150
mm v[152:155],a[96:99],a[48:51],v[188:191]
ex v151,v151
mm v[152:155],a[100:103],a[52:55],v[152:155]
dr a[148:151],v212 offset:21504
cv v169,v146,v147
mm v[152:155],a[104:107],a[56:59],v[152:155]
cv v170,v148,v149
)"
R"(cv v171,v150,v151
mm v[152:155],a[108:111],a[60:63],v[152:155]
bl v214,s[44:47],s62 offen offset:1024 sc1 lds
mm v[156:159],a[112:115],a[48:51],v[188:191]
mm v[156:159],a[116:119],a[52:55],v[156:159]
mm v[156:159],a[120:123],a[56:59],v[156:159]
dr a[152:155],v212 offset:22528
mm v[156:159],a[124:127],a[60:63],v[156:159]
bl v214,s[44:47],s62 offen offset:2048 sc1 lds
mm v[200:203],v[208:211],v[168:171],v[200:203]
ex v152,v152
mm v[64:67],a[160:163],v[168:171],v[64:67]
ex v153,v153
mm v[68:71],a[164:167],v[168:171],v[68:71]
ex v154,v154
mm v[72:75],a[168:171],v[168:171],v[72:75]
)"
R"(dr a[156:159],v212 offset:23552
cv v172,v152,v153
mm v[76:79],a[172:175],v[168:171],v[76:79]
bl v214,s[44:47],s62 offen offset:3072 sc1 lds
mm v[80:83],a[176:179],v[168:171],v[80:83]
ex v155,v155
mm v[84:87],a[180:183],v[168:171],v[84:87]
cv v173,v154,v155
mm v[88:91],a[184:187],v[168:171],v[88:91]
mm v[92:95],a[188:191],v[168:171],v[92:95]
s_waitcnt lgkmcnt(8)
s_setpc_b64 s[58:59]
.p2align 12
L_att_first8:
mm v[128:131],a[64:67],a[0:3],0
dr a[96:99],v212 offset:8192
)"
R"(v_readlane_b32 s55, v219, s52
mm v[128:131],a[68:71],a[4:7],v[128:131]
dr a[100:103],v212 offset:9216
v_readlane_b32 s53, v217, s52
mm v[128:131],a[72:75],a[8:11],v[128:131]
dr a[104:107],v212 offset:10240
v_readlane_b32 s54, v218, s52
mm v[128:131],a[76:79],a[12:15],v[128:131]
sa s52,s52,1
dr a[108:111],v212 offset:11264
mm v[132:135],a[80:83],a[0:3],0
dr a[112:115],v212 offset:12288
v_readlane_b32 s56, v216, s52
mm v[132:135],a[84:87],a[4:7],v[132:135]
sa s58,s60,s56
s_addc_u32 s59, s61, 0
)"
R"(dr a[116:119],v212 offset:13312
mm v[132:135],a[88:91],a[8:11],v[132:135]
s_waitcnt vmcnt(0)
dr a[120:123],v212 offset:14336
v_max3_f32 v220, v128, v129, v130
mm v[132:135],a[92:95],a[12:15],v[132:135]
dr a[124:127],v212 offset:15360
v_add_f32 v96, v96, v100
mm v[136:139],a[64:67],a[16:19],0
dr a[160:163],v212 offset:24576
v_add_f32 v97, v97, v101
mm v[136:139],a[68:71],a[20:23],v[136:139]
dr a[164:167],v212 offset:25600
v_add_f32 v96, v96, v104
mm v[136:139],a[72:75],a[24:27],v[136:139]
v_max3_f32 v220, v220, v131, v132
)"
R"(v_max3_f32 v220, v220, v133, v134
mm v[136:139],a[76:79],a[28:31],v[136:139]
dr a[168:171],v212 offset:26624
v_max_f32 v220, v220, v135
mm v[140:143],a[80:83],a[16:19],0
v_mov_b32 v221, v220
v_add_f32 v97, v97, v105
mm v[140:143],a[84:87],a[20:23],v[140:143]
dr a[172:175],v212 offset:27648
v_add_f32 v98, v98, v102
mm v[140:143],a[88:91],a[24:27],v[140:143]
v_permlane32_swap_b32 v220, v221
mm v[140:143],a[92:95],a[28:31],v[140:143]
dr a[176:179],v212 offset:28672
v_add_f32 v99, v99, v103
mm v[144:147],a[64:67],a[32:35],0
)"
R"(dr a[180:183],v212 offset:29696
v_max_f32 v220, v220, v221
mm v[144:147],a[68:71],a[36:39],v[144:147]
v_add_f32 v96, v96, v108
v_add_f32 v97, v97, v109
mm v[144:147],a[72:75],a[40:43],v[144:147]
dr a[184:187],v212 offset:30720
v_add_f32 v98, v98, v106
mm v[144:147],a[76:79],a[44:47],v[144:147]
v_add_f32 v99, v99, v107
v_mov_b32 v221, v220
mm v[148:151],a[80:83],a[32:35],0
dr a[188:191],v212 offset:31744
v_add_f32 v96, v96, v112
mm v[148:151],a[84:87],a[36:39],v[148:151]
v_add_f32 v97, v97, v113
)"
R"(v_add_f32 v98, v98, v110
mm v[148:151],a[88:91],a[40:43],v[148:151]
v_add_f32 v99, v99, v111
v_max3_f32 v222, v136, v137, v138
mm v[148:151],a[92:95],a[44:47],v[148:151]
v_add_f32 v96, v96, v116
v_add_f32 v97, v97, v117
mm v[152:155],a[64:67],a[48:51],0
v_add_f32 v98, v98, v114
v_add_f32 v99, v99, v115
mm v[152:155],a[68:71],a[52:55],v[152:155]
v_permlane16_swap_b32 v220, v221
mm v[152:155],a[72:75],a[56:59],v[152:155]
v_max3_f32 v222, v222, v139, v140
v_add_f32 v96, v96, v120
mm v[152:155],a[76:79],a[60:63],v[152:155]
)"
R"(v_add_f32 v97, v97, v121
v_add_f32 v98, v98, v118
mm v[156:159],a[80:83],a[48:51],0
v_add_f32 v99, v99, v119
v_max_f32 v220, v220, v221
mm v[156:159],a[84:87],a[52:55],v[156:159]
v_max3_f32 v222, v222, v141, v142
v_add_f32 v96, v96, v124
v_add_f32 v97, v97, v125
cv v96,v96,v97
v_add_f32 v98, v98, v122
v_add_f32 v98, v98, v126
v_add_f32 v99, v99, v123
v_add_f32 v99, v99, v127
cv v97,v98,v99
ds_write_b64 v230, v[96:97]
)"
R"(mm v[156:159],a[88:91],a[56:59],v[156:159]
s_waitcnt lgkmcnt(15)
v_sub_f32 v176, 0xc2c80000, v220
v_max_f32 v222, v222, v143
mm v[156:159],a[92:95],a[60:63],v[156:159]
v_add_f32 v128, v128, v176
v_add_f32 v129, v129, v176
ex v128,v128
ex v129,v129
v_add_f32 v130, v130, v176
cv v160,v128,v129
ex v130,v130
v_add_f32 v131, v131, v176
ex v131,v131
v_add_f32 v132, v132, v176
cv v161,v130,v131
)"
R"(ex v132,v132
v_add_f32 v133, v133, v176
ex v133,v133
v_add_f32 v134, v134, v176
cv v162,v132,v133
ex v134,v134
v_add_f32 v135, v135, v176
ex v135,v135
v_mov_b32 v223, v222
cv v163,v134,v135
v_add_u32 v212, s55, v214
v_permlane32_swap_b32 v222, v223
mm v[192:195],v[208:211],v[160:163],0
s_waitcnt vmcnt(0) lgkmcnt(0)
s_barrier
v_max_f32 v222, v222, v223
)"
R"(v_mov_b32 v223, v222
mm v[0:3],a[128:131],v[160:163],0
sa m0,s54,0x0
sa s62,s53,0x0
dr a[208:211],v231
mm v[4:7],a[132:135],v[160:163],0
v_permlane16_swap_b32 v222, v223
mm v[8:11],a[136:139],v[160:163],0
dr a[212:215],v231 offset:1024
v_max_f32 v222, v222, v223
mm v[12:15],a[140:143],v[160:163],0
dr a[64:67],v212 offset:0
v_sub_f32 v180, 0xc2c80000, v222
mm v[16:19],a[144:147],v[160:163],0
v_add_f32 v136, v136, v180
v_add_f32 v137, v137, v180
)"
R"(mm v[20:23],a[148:151],v[160:163],0
v_add_f32 v138, v138, v180
v_add_f32 v139, v139, v180
mm v[24:27],a[152:155],v[160:163],0
v_add_f32 v140, v140, v180
v_add_f32 v141, v141, v180
mm v[28:31],a[156:159],v[160:163],0
v_add_f32 v142, v142, v180
v_add_f32 v143, v143, v180
ex v136,v136
ex v137,v137
ex v138,v138
cv v164,v136,v137
ex v139,v139
ex v140,v140
cv v165,v138,v139
)"
R"(ex v141,v141
ex v142,v142
cv v166,v140,v141
ex v143,v143
v_max3_f32 v224, v144, v145, v146
cv v167,v142,v143
v_max3_f32 v224, v224, v147, v148
v_max3_f32 v224, v224, v149, v150
mm v[196:199],v[208:211],v[164:167],0
bl v214,s[44:47],s62 offen sc1 lds
mm v[32:35],a[128:131],v[164:167],0
dr a[68:71],v212 offset:1024
v_max_f32 v224, v224, v151
mm v[36:39],a[132:135],v[164:167],0
v_mov_b32 v225, v224
v_sub_f32 v177, 0xc2c80000, v220
)"
R"(mm v[40:43],a[136:139],v[164:167],0
v_sub_f32 v178, 0xc2c80000, v220
v_sub_f32 v179, 0xc2c80000, v220
mm v[44:47],a[140:143],v[164:167],0
v_permlane32_swap_b32 v224, v225
mm v[48:51],a[144:147],v[164:167],0
bl v214,s[44:47],s62 offen offset:1024 sc1 lds
mm v[52:55],a[148:151],v[164:167],0
dr a[72:75],v212 offset:2048
v_max_f32 v224, v224, v225
mm v[56:59],a[152:155],v[164:167],0
s_waitcnt lgkmcnt(3)
v_mov_b32 v225, v224
v_max3_f32 v226, v152, v153, v154
mm v[60:63],a[156:159],v[164:167],0
v_permlane16_swap_b32 v224, v225
)"
R"(mm v[240:243],a[212:215],a[192:195],0
v_max_f32 v224, v224, v225
v_sub_f32 v184, 0xc2c80000, v224
mm v[236:239],a[208:211],a[192:195],0
v_add_f32 v144, v144, v184
v_add_f32 v145, v145, v184
mm v[128:131],a[96:99],a[0:3],v[176:179]
v_add_f32 v146, v146, v184
v_add_f32 v147, v147, v184
mm v[128:131],a[100:103],a[4:7],v[128:131]
v_add_f32 v148, v148, v184
v_add_f32 v149, v149, v184
mm v[128:131],a[104:107],a[8:11],v[128:131]
dr a[76:79],v212 offset:3072
v_add_f32 v150, v150, v184
mm v[128:131],a[108:111],a[12:15],v[128:131]
)"
R"(v_add_f32 v151, v151, v184
v_add_f32 v240, 0x358637bd, v240
mm v[132:135],a[112:115],a[0:3],v[176:179]
ex v144,v144
mm v[132:135],a[116:119],a[4:7],v[132:135]
ex v145,v145
mm v[132:135],a[120:123],a[8:11],v[132:135]
ex v146,v146
mm v[132:135],a[124:127],a[12:15],v[132:135]
ex v147,v147
mm v[248:251],a[212:215],a[196:199],0
ex v148,v148
mm v[244:247],a[208:211],a[196:199],0
ex v149,v149
cv v168,v144,v145
cv v169,v146,v147
)"
R"(cv v170,v148,v149
ex v150,v150
ex v151,v151
v_rcp_f32 v240, v240
cv v171,v150,v151
v_mul_f32 v236, v236, v240
v_mul_f32 v237, v237, v240
mm v[200:203],v[208:211],v[168:171],0
bl v214,s[44:47],s62 offen offset:2048 sc1 lds
mm v[64:67],a[128:131],v[168:171],0
v_mul_f32 v238, v238, v240
v_mul_f32 v239, v239, v240
mm v[68:71],a[132:135],v[168:171],0
cv v236,v236,v237
cv v237,v238,v239
mm v[72:75],a[136:139],v[168:171],0
)"
R"(dr a[80:83],v212 offset:4096
v_max3_f32 v226, v226, v155, v156
mm v[76:79],a[140:143],v[168:171],0
buffer_store_dwordx2 v[236:237], v232, s[44:47], s63 offen sc1
sa s63,s63,0x4000
mm v[80:83],a[144:147],v[168:171],0
v_max3_f32 v226, v226, v157, v158
v_max_f32 v226, v226, v159
mm v[84:87],a[148:151],v[168:171],0
v_mov_b32 v227, v226
v_sub_f32 v181, 0xc2c80000, v222
mm v[88:91],a[152:155],v[168:171],0
v_sub_f32 v182, 0xc2c80000, v222
v_sub_f32 v183, 0xc2c80000, v222
mm v[92:95],a[156:159],v[168:171],0
dr a[84:87],v212 offset:5120
)"
R"(v_add_f32 v248, 0x358637bd, v248
mm v[240:243],a[212:215],a[200:203],0
v_permlane32_swap_b32 v226, v227
mm v[236:239],a[208:211],a[200:203],0
v_rcp_f32 v248, v248
mm v[136:139],a[96:99],a[16:19],v[180:183]
v_max_f32 v226, v226, v227
v_mul_f32 v244, v244, v248
mm v[136:139],a[100:103],a[20:23],v[136:139]
v_mul_f32 v245, v245, v248
v_mov_b32 v227, v226
mm v[136:139],a[104:107],a[24:27],v[136:139]
v_mul_f32 v246, v246, v248
v_mul_f32 v247, v247, v248
mm v[136:139],a[108:111],a[28:31],v[136:139]
cv v244,v244,v245
)"
R"(cv v245,v246,v247
mm v[140:143],a[112:115],a[16:19],v[180:183]
bl v214,s[44:47],s62 offen offset:3072 sc1 lds
mm v[140:143],a[116:119],a[20:23],v[140:143]
v_permlane16_swap_b32 v226, v227
mm v[140:143],a[120:123],a[24:27],v[140:143]
buffer_store_dwordx2 v[244:245], v232, s[44:47], s63 offen sc1
sa s63,s63,0x4000
mm v[140:143],a[124:127],a[28:31],v[140:143]
v_max_f32 v226, v226, v227
v_sub_f32 v188, 0xc2c80000, v226
mm v[248:251],a[212:215],a[204:207],0
v_add_f32 v152, v152, v188
v_add_f32 v153, v153, v188
mm v[244:247],a[208:211],a[204:207],0
v_add_f32 v154, v154, v188
)"
R"(v_add_f32 v155, v155, v188
ex v152,v152
ex v153,v153
v_add_f32 v156, v156, v188
cv v172,v152,v153
ex v154,v154
ex v155,v155
v_add_f32 v157, v157, v188
cv v173,v154,v155
ex v156,v156
ex v157,v157
v_add_f32 v158, v158, v188
cv v174,v156,v157
ex v158,v158
v_add_f32 v159, v159, v188
ex v159,v159
)"
R"(v_add_f32 v240, 0x358637bd, v240
cv v175,v158,v159
v_rcp_f32 v240, v240
v_sub_f32 v185, 0xc2c80000, v224
mm v[204:207],v[208:211],v[172:175],0
dr a[88:91],v212 offset:6144
v_mul_f32 v236, v236, v240
mm v[96:99],a[128:131],v[172:175],0
v_mul_f32 v237, v237, v240
v_mul_f32 v238, v238, v240
mm v[100:103],a[132:135],v[172:175],0
v_mul_f32 v239, v239, v240
cv v236,v236,v237
mm v[104:107],a[136:139],v[172:175],0
dr a[92:95],v212 offset:7168
cv v237,v238,v239
)"
R"(mm v[108:111],a[140:143],v[172:175],0
buffer_store_dwordx2 v[236:237], v232, s[44:47], s63 offen sc1
sa s63,s63,0x4000
mm v[112:115],a[144:147],v[172:175],0
sa m0,s54,0x1000
sa s62,s53,0x1000
v_sub_f32 v186, 0xc2c80000, v224
mm v[116:119],a[148:151],v[172:175],0
bl v214,s[44:47],s62 offen sc1 lds
mm v[120:123],a[152:155],v[172:175],0
v_sub_f32 v187, 0xc2c80000, v224
v_add_f32 v248, 0x358637bd, v248
mm v[124:127],a[156:159],v[172:175],0
v_rcp_f32 v248, v248
mm v[144:147],a[96:99],a[32:35],v[184:187]
v_mul_f32 v244, v244, v248
)"
R"(v_mul_f32 v245, v245, v248
mm v[144:147],a[100:103],a[36:39],v[144:147]
dr a[128:131],v212 offset:16384
v_mul_f32 v246, v246, v248
mm v[144:147],a[104:107],a[40:43],v[144:147]
v_mul_f32 v247, v247, v248
cv v244,v244,v245
mm v[144:147],a[108:111],a[44:47],v[144:147]
cv v245,v246,v247
v_sub_f32 v189, 0xc2c80000, v226
mm v[148:151],a[112:115],a[32:35],v[184:187]
ex v128,v128
mm v[148:151],a[116:119],a[36:39],v[148:151]
ex v129,v129
mm v[148:151],a[120:123],a[40:43],v[148:151]
s_waitcnt lgkmcnt(12)
)"
R"(ex v130,v130
mm v[148:151],a[124:127],a[44:47],v[148:151]
ex v131,v131
cv v160,v128,v129
cv v161,v130,v131
ex v132,v132
ex v133,v133
ex v134,v134
cv v162,v132,v133
ex v135,v135
v_sub_f32 v190, 0xc2c80000, v226
cv v163,v134,v135
v_sub_f32 v191, 0xc2c80000, v226
ex v136,v136
mm v[192:195],v[208:211],v[160:163],v[192:195]
buffer_store_dwordx2 v[244:245], v232, s[44:47], s63 offen sc1
)"
R"(sa s63,s63,0x4000
mm v[0:3],a[160:163],v[160:163],v[0:3]
dr a[132:135],v212 offset:17408
mm v[4:7],a[164:167],v[160:163],v[4:7]
bl v214,s[44:47],s62 offen offset:1024 sc1 lds
mm v[8:11],a[168:171],v[160:163],v[8:11]
dr a[136:139],v212 offset:18432
mm v[12:15],a[172:175],v[160:163],v[12:15]
ex v137,v137
mm v[16:19],a[176:179],v[160:163],v[16:19]
ex v138,v138
mm v[20:23],a[180:183],v[160:163],v[20:23]
ex v139,v139
mm v[24:27],a[184:187],v[160:163],v[24:27]
dr a[140:143],v212 offset:19456
cv v164,v136,v137
)"
R"(mm v[28:31],a[188:191],v[160:163],v[28:31]
ex v140,v140
mm v[152:155],a[96:99],a[48:51],v[188:191]
bl v214,s[44:47],s62 offen offset:2048 sc1 lds
mm v[152:155],a[100:103],a[52:55],v[152:155]
ex v141,v141
mm v[152:155],a[104:107],a[56:59],v[152:155]
ex v142,v142
mm v[152:155],a[108:111],a[60:63],v[152:155]
ex v143,v143
mm v[156:159],a[112:115],a[48:51],v[188:191]
cv v165,v138,v139
cv v166,v140,v141
mm v[156:159],a[116:119],a[52:55],v[156:159]
dr a[144:147],v212 offset:20480
cv v167,v142,v143
)"
R"(mm v[156:159],a[120:123],a[56:59],v[156:159]
ex v144,v144
mm v[156:159],a[124:127],a[60:63],v[156:159]
ex v145,v145
mm v[196:199],v[208:211],v[164:167],v[196:199]
dr a[148:151],v212 offset:21504
cv v168,v144,v145
mm v[32:35],a[160:163],v[164:167],v[32:35]
ex v146,v146
mm v[36:39],a[164:167],v[164:167],v[36:39]
ex v147,v147
mm v[40:43],a[168:171],v[164:167],v[40:43]
ex v148,v148
mm v[44:47],a[172:175],v[164:167],v[44:47]
ex v149,v149
mm v[48:51],a[176:179],v[164:167],v[48:51]
)"
R"(ex v150,v150
mm v[52:55],a[180:183],v[164:167],v[52:55]
ex v151,v151
mm v[56:59],a[184:187],v[164:167],v[56:59]
cv v169,v146,v147
cv v170,v148,v149
mm v[60:63],a[188:191],v[164:167],v[60:63]
dr a[152:155],v212 offset:22528
cv v171,v150,v151
ex v152,v152
ex v153,v153
mm v[200:203],v[208:211],v[168:171],v[200:203]
bl v214,s[44:47],s62 offen offset:3072 sc1 lds
mm v[64:67],a[160:163],v[168:171],v[64:67]
ex v154,v154
mm v[68:71],a[164:167],v[168:171],v[68:71]
)"
R"(dr a[156:159],v212 offset:23552
cv v172,v152,v153
mm v[72:75],a[168:171],v[168:171],v[72:75]
ex v155,v155
mm v[76:79],a[172:175],v[168:171],v[76:79]
cv v173,v154,v155
mm v[80:83],a[176:179],v[168:171],v[80:83]
mm v[84:87],a[180:183],v[168:171],v[84:87]
mm v[88:91],a[184:187],v[168:171],v[88:91]
mm v[92:95],a[188:191],v[168:171],v[92:95]
s_waitcnt lgkmcnt(8)
s_setpc_b64 s[58:59]
.p2align 12
L_att_first16:
mm v[128:131],a[64:67],a[0:3],0
dr a[96:99],v212 offset:8192
)"
R"(v_readlane_b32 s55, v219, s52
mm v[128:131],a[68:71],a[4:7],v[128:131]
dr a[100:103],v212 offset:9216
v_readlane_b32 s53, v217, s52
mm v[128:131],a[72:75],a[8:11],v[128:131]
dr a[104:107],v212 offset:10240
v_readlane_b32 s54, v218, s52
mm v[128:131],a[76:79],a[12:15],v[128:131]
sa s52,s52,1
dr a[108:111],v212 offset:11264
mm v[132:135],a[80:83],a[0:3],0
dr a[112:115],v212 offset:12288
v_readlane_b32 s56, v216, s52
mm v[132:135],a[84:87],a[4:7],v[132:135]
sa s58,s60,s56
s_addc_u32 s59, s61, 0
)"
R"(dr a[116:119],v212 offset:13312
mm v[132:135],a[88:91],a[8:11],v[132:135]
s_waitcnt vmcnt(0)
dr a[120:123],v212 offset:14336
v_max3_f32 v220, v128, v129, v130
mm v[132:135],a[92:95],a[12:15],v[132:135]
dr a[124:127],v212 offset:15360
v_add_f32 v96, v96, v100
mm v[136:139],a[64:67],a[16:19],0
dr a[160:163],v212 offset:24576
v_add_f32 v97, v97, v101
mm v[136:139],a[68:71],a[20:23],v[136:139]
dr a[164:167],v212 offset:25600
v_add_f32 v96, v96, v104
mm v[136:139],a[72:75],a[24:27],v[136:139]
v_max3_f32 v220, v220, v131, v132
)"
R"(v_max3_f32 v220, v220, v133, v134
mm v[136:139],a[76:79],a[28:31],v[136:139]
dr a[168:171],v212 offset:26624
v_max_f32 v220, v220, v135
mm v[140:143],a[80:83],a[16:19],0
v_mov_b32 v221, v220
v_add_f32 v97, v97, v105
mm v[140:143],a[84:87],a[20:23],v[140:143]
dr a[172:175],v212 offset:27648
v_add_f32 v98, v98, v102
mm v[140:143],a[88:91],a[24:27],v[140:143]
v_permlane32_swap_b32 v220, v221
mm v[140:143],a[92:95],a[28:31],v[140:143]
dr a[176:179],v212 offset:28672
v_add_f32 v99, v99, v103
mm v[144:147],a[64:67],a[32:35],0
)"
R"(dr a[180:183],v212 offset:29696
v_max_f32 v220, v220, v221
mm v[144:147],a[68:71],a[36:39],v[144:147]
v_add_f32 v96, v96, v108
v_add_f32 v97, v97, v109
mm v[144:147],a[72:75],a[40:43],v[144:147]
dr a[184:187],v212 offset:30720
v_add_f32 v98, v98, v106
mm v[144:147],a[76:79],a[44:47],v[144:147]
v_add_f32 v99, v99, v107
v_mov_b32 v221, v220
mm v[148:151],a[80:83],a[32:35],0
dr a[188:191],v212 offset:31744
v_add_f32 v96, v96, v112
mm v[148:151],a[84:87],a[36:39],v[148:151]
v_add_f32 v97, v97, v113
)"
R"(v_add_f32 v98, v98, v110
mm v[148:151],a[88:91],a[40:43],v[148:151]
v_add_f32 v99, v99, v111
v_max3_f32 v222, v136, v137, v138
mm v[148:151],a[92:95],a[44:47],v[148:151]
v_add_f32 v96, v96, v116
v_add_f32 v97, v97, v117
mm v[152:155],a[64:67],a[48:51],0
v_add_f32 v98, v98, v114
v_add_f32 v99, v99, v115
mm v[152:155],a[68:71],a[52:55],v[152:155]
v_permlane16_swap_b32 v220, v221
mm v[152:155],a[72:75],a[56:59],v[152:155]
v_max3_f32 v222, v222, v139, v140
v_add_f32 v96, v96, v120
mm v[152:155],a[76:79],a[60:63],v[152:155]
)"
R"(v_add_f32 v97, v97, v121
v_add_f32 v98, v98, v118
mm v[156:159],a[80:83],a[48:51],0
v_add_f32 v99, v99, v119
v_max_f32 v220, v220, v221
mm v[156:159],a[84:87],a[52:55],v[156:159]
v_max3_f32 v222, v222, v141, v142
v_add_f32 v96, v96, v124
v_add_f32 v97, v97, v125
cv v96,v96,v97
v_add_f32 v98, v98, v122
v_add_f32 v98, v98, v126
v_add_f32 v99, v99, v123
v_add_f32 v99, v99, v127
cv v97,v98,v99
ds_write_b64 v230, v[96:97]
)"
R"(mm v[156:159],a[88:91],a[56:59],v[156:159]
s_waitcnt lgkmcnt(15)
v_sub_f32 v176, 0xc2c80000, v220
v_max_f32 v222, v222, v143
mm v[156:159],a[92:95],a[60:63],v[156:159]
v_add_f32 v128, v128, v176
v_add_f32 v129, v129, v176
ex v128,v128
ex v129,v129
v_add_f32 v130, v130, v176
cv v160,v128,v129
ex v130,v130
v_add_f32 v131, v131, v176
ex v131,v131
v_add_f32 v132, v132, v176
cv v161,v130,v131
)"
R"(ex v132,v132
v_add_f32 v133, v133, v176
ex v133,v133
v_add_f32 v134, v134, v176
cv v162,v132,v133
ex v134,v134
v_add_f32 v135, v135, v176
ex v135,v135
v_mov_b32 v223, v222
cv v163,v134,v135
v_add_u32 v212, s55, v214
v_permlane32_swap_b32 v222, v223
mm v[192:195],v[208:211],v[160:163],0
s_waitcnt vmcnt(0) lgkmcnt(0)
s_barrier
v_max_f32 v222, v222, v223
)"
R"(v_mov_b32 v223, v222
mm v[0:3],a[128:131],v[160:163],0
sa m0,s54,0x0
sa s62,s53,0x0
dr a[208:211],v231
mm v[4:7],a[132:135],v[160:163],0
v_permlane16_swap_b32 v222, v223
mm v[8:11],a[136:139],v[160:163],0
dr a[212:215],v231 offset:1024
v_max_f32 v222, v222, v223
mm v[12:15],a[140:143],v[160:163],0
dr a[64:67],v212 offset:0
v_sub_f32 v180, 0xc2c80000, v222
mm v[16:19],a[144:147],v[160:163],0
v_add_f32 v136, v136, v180
v_add_f32 v137, v137, v180
)"
R"(mm v[20:23],a[148:151],v[160:163],0
v_add_f32 v138, v138, v180
v_add_f32 v139, v139, v180
mm v[24:27],a[152:155],v[160:163],0
v_add_f32 v140, v140, v180
v_add_f32 v141, v141, v180
mm v[28:31],a[156:159],v[160:163],0
v_add_f32 v142, v142, v180
v_add_f32 v143, v143, v180
ex v136,v136
ex v137,v137
ex v138,v138
cv v164,v136,v137
ex v139,v139
ex v140,v140
cv v165,v138,v139
)"
R"(ex v141,v141
ex v142,v142
cv v166,v140,v141
ex v143,v143
v_max3_f32 v224, v144, v145, v146
cv v167,v142,v143
v_max3_f32 v224, v224, v147, v148
v_max3_f32 v224, v224, v149, v150
mm v[196:199],v[208:211],v[164:167],0
bl v214,s[44:47],s62 offen sc1 lds
mm v[32:35],a[128:131],v[164:167],0
dr a[68:71],v212 offset:1024
v_max_f32 v224, v224, v151
mm v[36:39],a[132:135],v[164:167],0
bl v214,s[44:47],s62 offen offset:1024 sc1 lds
mm v[40:43],a[136:139],v[164:167],0
)"
R"(v_mov_b32 v225, v224
v_sub_f32 v177, 0xc2c80000, v220
mm v[44:47],a[140:143],v[164:167],0
v_sub_f32 v178, 0xc2c80000, v220
v_sub_f32 v179, 0xc2c80000, v220
mm v[48:51],a[144:147],v[164:167],0
bl v214,s[44:47],s62 offen offset:2048 sc1 lds
mm v[52:55],a[148:151],v[164:167],0
dr a[72:75],v212 offset:2048
v_max3_f32 v226, v152, v153, v154
mm v[56:59],a[152:155],v[164:167],0
s_waitcnt lgkmcnt(3)
v_permlane32_swap_b32 v224, v225
mm v[60:63],a[156:159],v[164:167],0
v_max_f32 v224, v224, v225
v_mov_b32 v225, v224
)"
R"(mm v[240:243],a[212:215],a[192:195],0
v_max3_f32 v226, v226, v155, v156
v_max3_f32 v226, v226, v157, v158
mm v[236:239],a[208:211],a[192:195],0
v_permlane16_swap_b32 v224, v225
mm v[128:131],a[96:99],a[0:3],v[176:179]
v_max_f32 v224, v224, v225
v_sub_f32 v184, 0xc2c80000, v224
mm v[128:131],a[100:103],a[4:7],v[128:131]
bl v214,s[44:47],s62 offen offset:3072 sc1 lds
mm v[128:131],a[104:107],a[8:11],v[128:131]
dr a[76:79],v212 offset:3072
v_add_f32 v144, v144, v184
mm v[128:131],a[108:111],a[12:15],v[128:131]
v_add_f32 v145, v145, v184
v_add_f32 v146, v146, v184
)"
R"(mm v[132:135],a[112:115],a[0:3],v[176:179]
v_add_f32 v147, v147, v184
v_add_f32 v148, v148, v184
mm v[132:135],a[116:119],a[4:7],v[132:135]
v_add_f32 v149, v149, v184
v_add_f32 v150, v150, v184
mm v[132:135],a[120:123],a[8:11],v[132:135]
v_add_f32 v151, v151, v184
v_add_f32 v240, 0x358637bd, v240
mm v[132:135],a[124:127],a[12:15],v[132:135]
ex v144,v144
mm v[248:251],a[212:215],a[196:199],0
ex v145,v145
mm v[244:247],a[208:211],a[196:199],0
ex v146,v146
cv v168,v144,v145
)"
R"(ex v147,v147
ex v148,v148
cv v169,v146,v147
ex v149,v149
ex v150,v150
cv v170,v148,v149
ex v151,v151
v_rcp_f32 v240, v240
cv v171,v150,v151
v_mul_f32 v236, v236, v240
v_mul_f32 v237, v237, v240
mm v[200:203],v[208:211],v[168:171],0
sa m0,s54,0x1000
sa s62,s53,0x1000
v_mul_f32 v238, v238, v240
mm v[64:67],a[128:131],v[168:171],0
)"
R"(bl v214,s[44:47],s62 offen sc1 lds
mm v[68:71],a[132:135],v[168:171],0
v_mul_f32 v239, v239, v240
cv v236,v236,v237
mm v[72:75],a[136:139],v[168:171],0
dr a[80:83],v212 offset:4096
cv v237,v238,v239
mm v[76:79],a[140:143],v[168:171],0
buffer_store_dwordx2 v[236:237], v232, s[44:47], s63 offen sc1
sa s63,s63,0x4000
mm v[80:83],a[144:147],v[168:171],0
v_max_f32 v226, v226, v159
v_mov_b32 v227, v226
mm v[84:87],a[148:151],v[168:171],0
bl v214,s[44:47],s62 offen offset:1024 sc1 lds
mm v[88:91],a[152:155],v[168:171],0
)"
R"(v_sub_f32 v181, 0xc2c80000, v222
v_sub_f32 v182, 0xc2c80000, v222
mm v[92:95],a[156:159],v[168:171],0
v_sub_f32 v183, 0xc2c80000, v222
v_add_f32 v248, 0x358637bd, v248
mm v[240:243],a[212:215],a[200:203],0
v_permlane32_swap_b32 v226, v227
mm v[236:239],a[208:211],a[200:203],0
v_rcp_f32 v248, v248
mm v[136:139],a[96:99],a[16:19],v[180:183]
dr a[84:87],v212 offset:5120
v_max_f32 v226, v226, v227
mm v[136:139],a[100:103],a[20:23],v[136:139]
v_mul_f32 v244, v244, v248
v_mul_f32 v245, v245, v248
mm v[136:139],a[104:107],a[24:27],v[136:139]
)"
R"(v_mov_b32 v227, v226
v_mul_f32 v246, v246, v248
mm v[136:139],a[108:111],a[28:31],v[136:139]
v_mul_f32 v247, v247, v248
cv v244,v244,v245
mm v[140:143],a[112:115],a[16:19],v[180:183]
dr a[88:91],v212 offset:6144
cv v245,v246,v247
mm v[140:143],a[116:119],a[20:23],v[140:143]
bl v214,s[44:47],s62 offen offset:2048 sc1 lds
mm v[140:143],a[120:123],a[24:27],v[140:143]
v_permlane16_swap_b32 v226, v227
mm v[140:143],a[124:127],a[28:31],v[140:143]
buffer_store_dwordx2 v[244:245], v232, s[44:47], s63 offen sc1
sa s63,s63,0x4000
mm v[248:251],a[212:215],a[204:207],0
)"
R"(v_max_f32 v226, v226, v227
v_sub_f32 v188, 0xc2c80000, v226
mm v[244:247],a[208:211],a[204:207],0
v_add_f32 v152, v152, v188
v_add_f32 v153, v153, v188
ex v152,v152
ex v153,v153
v_add_f32 v154, v154, v188
cv v172,v152,v153
ex v154,v154
v_add_f32 v155, v155, v188
ex v155,v155
v_add_f32 v156, v156, v188
cv v173,v154,v155
ex v156,v156
v_add_f32 v157, v157, v188
)"
R"(ex v157,v157
v_add_f32 v158, v158, v188
cv v174,v156,v157
ex v158,v158
v_add_f32 v159, v159, v188
ex v159,v159
v_add_f32 v240, 0x358637bd, v240
cv v175,v158,v159
v_rcp_f32 v240, v240
v_sub_f32 v185, 0xc2c80000, v224
mm v[204:207],v[208:211],v[172:175],0
bl v214,s[44:47],s62 offen offset:3072 sc1 lds
mm v[96:99],a[128:131],v[172:175],0
v_mul_f32 v236, v236, v240
v_mul_f32 v237, v237, v240
mm v[100:103],a[132:135],v[172:175],0
)"
R"(v_mul_f32 v238, v238, v240
v_mul_f32 v239, v239, v240
mm v[104:107],a[136:139],v[172:175],0
cv v236,v236,v237
cv v237,v238,v239
mm v[108:111],a[140:143],v[172:175],0
sa m0,s54,0x2000
sa s62,s53,0x2000
dr a[92:95],v212 offset:7168
mm v[112:115],a[144:147],v[172:175],0
buffer_store_dwordx2 v[236:237], v232, s[44:47], s63 offen sc1
sa s63,s63,0x4000
mm v[116:119],a[148:151],v[172:175],0
bl v214,s[44:47],s62 offen sc1 lds
mm v[120:123],a[152:155],v[172:175],0
v_sub_f32 v186, 0xc2c80000, v224
)"
R"(v_sub_f32 v187, 0xc2c80000, v224
mm v[124:127],a[156:159],v[172:175],0
dr a[128:131],v212 offset:16384
v_add_f32 v248, 0x358637bd, v248
mm v[144:147],a[96:99],a[32:35],v[184:187]
v_rcp_f32 v248, v248
mm v[144:147],a[100:103],a[36:39],v[144:147]
v_mul_f32 v244, v244, v248
v_mul_f32 v245, v245, v248
mm v[144:147],a[104:107],a[40:43],v[144:147]
bl v214,s[44:47],s62 offen offset:1024 sc1 lds
mm v[144:147],a[108:111],a[44:47],v[144:147]
v_mul_f32 v246, v246, v248
v_mul_f32 v247, v247, v248
mm v[148:151],a[112:115],a[32:35],v[184:187]
cv v244,v244,v245
)"
R"(cv v245,v246,v247
mm v[148:151],a[116:119],a[36:39],v[148:151]
ex v128,v128
mm v[148:151],a[120:123],a[40:43],v[148:151]
s_waitcnt lgkmcnt(12)
ex v129,v129
mm v[148:151],a[124:127],a[44:47],v[148:151]
ex v130,v130
cv v160,v128,v129
ex v131,v131
ex v132,v132
cv v161,v130,v131
ex v133,v133
ex v134,v134
cv v162,v132,v133
ex v135,v135
)"
R"(v_sub_f32 v189, 0xc2c80000, v226
cv v163,v134,v135
v_sub_f32 v190, 0xc2c80000, v226
v_sub_f32 v191, 0xc2c80000, v226
mm v[192:195],v[208:211],v[160:163],v[192:195]
buffer_store_dwordx2 v[244:245], v232, s[44:47], s63 offen sc1
sa s63,s63,0x4000
mm v[0:3],a[160:163],v[160:163],v[0:3]
dr a[132:135],v212 offset:17408
mm v[4:7],a[164:167],v[160:163],v[4:7]
bl v214,s[44:47],s62 offen offset:2048 sc1 lds
mm v[8:11],a[168:171],v[160:163],v[8:11]
dr a[136:139],v212 offset:18432
mm v[12:15],a[172:175],v[160:163],v[12:15]
bl v214,s[44:47],s62 offen offset:3072 sc1 lds
mm v[16:19],a[176:179],v[160:163],v[16:19]
)"
R"(ex v136,v136
mm v[20:23],a[180:183],v[160:163],v[20:23]
ex v137,v137
mm v[24:27],a[184:187],v[160:163],v[24:27]
dr a[140:143],v212 offset:19456
cv v164,v136,v137
mm v[28:31],a[188:191],v[160:163],v[28:31]
sa m0,s54,0x3000
sa s62,s53,0x3000
mm v[152:155],a[96:99],a[48:51],v[188:191]
bl v214,s[44:47],s62 offen sc1 lds
mm v[152:155],a[100:103],a[52:55],v[152:155]
ex v138,v138
mm v[152:155],a[104:107],a[56:59],v[152:155]
ex v139,v139
mm v[152:155],a[108:111],a[60:63],v[152:155]
)"
R"(ex v140,v140
mm v[156:159],a[112:115],a[48:51],v[188:191]
ex v141,v141
mm v[156:159],a[116:119],a[52:55],v[156:159]
ex v142,v142
mm v[156:159],a[120:123],a[56:59],v[156:159]
ex v143,v143
mm v[156:159],a[124:127],a[60:63],v[156:159]
cv v165,v138,v139
cv v166,v140,v141
cv v167,v142,v143
ex v144,v144
ex v145,v145
mm v[196:199],v[208:211],v[164:167],v[196:199]
dr a[144:147],v212 offset:20480
cv v168,v144,v145
)"
R"(mm v[32:35],a[160:163],v[164:167],v[32:35]
dr a[148:151],v212 offset:21504
mm v[36:39],a[164:167],v[164:167],v[36:39]
bl v214,s[44:47],s62 offen offset:1024 sc1 lds
mm v[40:43],a[168:171],v[164:167],v[40:43]
ex v146,v146
mm v[44:47],a[172:175],v[164:167],v[44:47]
ex v147,v147
mm v[48:51],a[176:179],v[164:167],v[48:51]
ex v148,v148
mm v[52:55],a[180:183],v[164:167],v[52:55]
ex v149,v149
mm v[56:59],a[184:187],v[164:167],v[56:59]
ex v150,v150
mm v[60:63],a[188:191],v[164:167],v[60:63]
ex v151,v151
)"
R"(cv v169,v146,v147
cv v170,v148,v149
cv v171,v150,v151
ex v152,v152
ex v153,v153
mm v[200:203],v[208:211],v[168:171],v[200:203]
dr a[152:155],v212 offset:22528
cv v172,v152,v153
mm v[64:67],a[160:163],v[168:171],v[64:67]
bl v214,s[44:47],s62 offen offset:2048 sc1 lds
mm v[68:71],a[164:167],v[168:171],v[68:71]
dr a[156:159],v212 offset:23552
mm v[72:75],a[168:171],v[168:171],v[72:75]
bl v214,s[44:47],s62 offen offset:3072 sc1 lds
mm v[76:79],a[172:175],v[168:171],v[76:79]
ex v154,v154
)"
R"(mm v[80:83],a[176:179],v[168:171],v[80:83]
ex v155,v155
mm v[84:87],a[180:183],v[168:171],v[84:87]
cv v173,v154,v155
mm v[88:91],a[184:187],v[168:171],v[88:91]
mm v[92:95],a[188:191],v[168:171],v[92:95]
s_waitcnt lgkmcnt(8)
s_setpc_b64 s[58:59]
.p2align 12
L_att_fa0_16:
mm v[128:131],a[64:67],a[0:3],0
dr a[96:99],v212 offset:8192
v_mov_b32 v162, 0
mm v[128:131],a[68:71],a[4:7],v[128:131]
dr a[100:103],v212 offset:9216
v_mov_b32 v163, 0
)"
R"(mm v[128:131],a[72:75],a[8:11],v[128:131]
dr a[104:107],v212 offset:10240
v_readlane_b32 s55, v219, s52
mm v[128:131],a[76:79],a[12:15],v[128:131]
dr a[108:111],v212 offset:11264
v_readlane_b32 s53, v217, s52
mm v[136:139],a[64:67],a[16:19],0
dr a[112:115],v212 offset:12288
v_readlane_b32 s54, v218, s52
mm v[136:139],a[68:71],a[20:23],v[136:139]
sa s52,s52,1
dr a[116:119],v212 offset:13312
mm v[136:139],a[72:75],a[24:27],v[136:139]
s_waitcnt vmcnt(0)
dr a[120:123],v212 offset:14336
v_cndmask_b32 v128, v213, v128, s[88:89]
)"
R"(mm v[136:139],a[76:79],a[28:31],v[136:139]
dr a[124:127],v212 offset:15360
v_cndmask_b32 v129, v213, v129, s[90:91]
mm v[140:143],a[80:83],a[16:19],0
dr a[160:163],v212 offset:24576
v_cndmask_b32 v130, v213, v130, s[92:93]
mm v[140:143],a[84:87],a[20:23],v[140:143]
dr a[164:167],v212 offset:25600
v_cndmask_b32 v131, v213, v131, s[94:95]
mm v[140:143],a[88:91],a[24:27],v[140:143]
v_max3_f32 v220, v128, v129, v130
v_max_f32 v220, v220, v131
mm v[140:143],a[92:95],a[28:31],v[140:143]
dr a[168:171],v212 offset:26624
v_mov_b32 v221, v220
mm v[144:147],a[64:67],a[32:35],0
)"
R"(v_max3_f32 v222, v136, v137, v138
v_add_f32 v96, v96, v100
mm v[144:147],a[68:71],a[36:39],v[144:147]
dr a[172:175],v212 offset:27648
v_add_f32 v97, v97, v101
mm v[144:147],a[72:75],a[40:43],v[144:147]
v_permlane32_swap_b32 v220, v221
mm v[144:147],a[76:79],a[44:47],v[144:147]
dr a[176:179],v212 offset:28672
v_max_f32 v220, v220, v221
mm v[148:151],a[80:83],a[32:35],0
dr a[180:183],v212 offset:29696
v_mov_b32 v221, v220
mm v[148:151],a[84:87],a[36:39],v[148:151]
v_max3_f32 v222, v222, v139, v140
v_add_f32 v96, v96, v104
)"
R"(mm v[148:151],a[88:91],a[40:43],v[148:151]
v_permlane16_swap_b32 v220, v221
mm v[148:151],a[92:95],a[44:47],v[148:151]
dr a[184:187],v212 offset:30720
v_add_f32 v97, v97, v105
mm v[152:155],a[64:67],a[48:51],0
v_add_f32 v98, v98, v102
v_add_f32 v99, v99, v103
mm v[152:155],a[68:71],a[52:55],v[152:155]
v_max_f32 v220, v220, v221
v_max3_f32 v222, v222, v141, v142
mm v[152:155],a[72:75],a[56:59],v[152:155]
v_add_f32 v96, v96, v108
v_add_f32 v97, v97, v109
mm v[152:155],a[76:79],a[60:63],v[152:155]
v_add_f32 v98, v98, v106
)"
R"(v_add_f32 v99, v99, v107
mm v[156:159],a[80:83],a[48:51],0
v_sub_f32 v176, 0xc2c80000, v220
v_max_f32 v222, v222, v143
mm v[156:159],a[84:87],a[52:55],v[156:159]
dr a[188:191],v212 offset:31744
v_add_f32 v96, v96, v112
mm v[156:159],a[88:91],a[56:59],v[156:159]
s_waitcnt lgkmcnt(15)
v_add_f32 v97, v97, v113
v_add_f32 v98, v98, v110
mm v[156:159],a[92:95],a[60:63],v[156:159]
v_add_f32 v99, v99, v111
v_add_f32 v128, v128, v176
ex v128,v128
v_add_f32 v129, v129, v176
)"
R"(ex v129,v129
v_add_f32 v130, v130, v176
cv v160,v128,v129
ex v130,v130
v_add_f32 v131, v131, v176
ex v131,v131
v_mov_b32 v223, v222
cv v161,v130,v131
v_add_f32 v96, v96, v116
v_add_f32 v97, v97, v117
mm v[192:195],v[208:211],v[160:163],0
v_add_f32 v98, v98, v114
v_add_f32 v99, v99, v115
mm v[0:3],a[128:131],v[160:163],0
v_add_f32 v96, v96, v120
v_add_f32 v97, v97, v121
)"
R"(v_add_f32 v96, v96, v124
v_add_f32 v97, v97, v125
cv v96,v96,v97
v_add_f32 v98, v98, v118
v_add_f32 v98, v98, v122
v_add_f32 v98, v98, v126
v_add_f32 v99, v99, v119
v_add_f32 v99, v99, v123
v_add_f32 v99, v99, v127
cv v97,v98,v99
ds_write_b64 v230, v[96:97]
mm v[4:7],a[132:135],v[160:163],0
v_permlane32_swap_b32 v222, v223
mm v[8:11],a[136:139],v[160:163],0
v_max_f32 v222, v222, v223
v_mov_b32 v223, v222
)"
R"(mm v[12:15],a[140:143],v[160:163],0
s_waitcnt vmcnt(0) lgkmcnt(0)
s_barrier
v_max3_f32 v224, v144, v145, v146
mm v[16:19],a[144:147],v[160:163],0
v_permlane16_swap_b32 v222, v223
mm v[20:23],a[148:151],v[160:163],0
v_max_f32 v222, v222, v223
v_sub_f32 v180, 0xc2c80000, v222
mm v[24:27],a[152:155],v[160:163],0
v_add_f32 v136, v136, v180
v_add_f32 v137, v137, v180
mm v[28:31],a[156:159],v[160:163],0
v_add_f32 v138, v138, v180
v_add_f32 v139, v139, v180
ex v136,v136
)"
R"(ex v137,v137
v_add_f32 v140, v140, v180
cv v164,v136,v137
ex v138,v138
ex v139,v139
v_add_f32 v141, v141, v180
cv v165,v138,v139
ex v140,v140
ex v141,v141
v_add_f32 v142, v142, v180
cv v166,v140,v141
ex v142,v142
v_add_f32 v143, v143, v180
ex v143,v143
v_max3_f32 v224, v224, v147, v148
cv v167,v142,v143
)"
R"(v_max3_f32 v224, v224, v149, v150
v_add_u32 v212, s55, v214
mm v[196:199],v[208:211],v[164:167],0
dr a[208:211],v231
v_max_f32 v224, v224, v151
mm v[32:35],a[128:131],v[164:167],0
dr a[212:215],v231 offset:1024
v_mov_b32 v225, v224
mm v[36:39],a[132:135],v[164:167],0
sa m0,s54,0x0
sa s62,s53,0x0
dr a[64:67],v212 offset:0
mm v[40:43],a[136:139],v[164:167],0
bl v214,s[44:47],s62 offen sc1 lds
mm v[44:47],a[140:143],v[164:167],0
v_permlane32_swap_b32 v224, v225
)"
R"(mm v[48:51],a[144:147],v[164:167],0
v_max_f32 v224, v224, v225
v_mov_b32 v225, v224
mm v[52:55],a[148:151],v[164:167],0
dr a[68:71],v212 offset:1024
v_max3_f32 v226, v152, v153, v154
mm v[56:59],a[152:155],v[164:167],0
s_waitcnt lgkmcnt(2)
v_permlane16_swap_b32 v224, v225
mm v[60:63],a[156:159],v[164:167],0
bl v214,s[44:47],s62 offen offset:1024 sc1 lds
mm v[240:243],a[212:215],a[192:195],0
v_max_f32 v224, v224, v225
v_sub_f32 v184, 0xc2c80000, v224
mm v[236:239],a[208:211],a[192:195],0
v_add_f32 v144, v144, v184
)"
R"(v_add_f32 v145, v145, v184
mm v[248:251],a[212:215],a[196:199],0
v_add_f32 v146, v146, v184
v_add_f32 v147, v147, v184
mm v[244:247],a[208:211],a[196:199],0
v_add_f32 v148, v148, v184
v_add_f32 v149, v149, v184
ex v144,v144
ex v145,v145
v_add_f32 v150, v150, v184
cv v168,v144,v145
ex v146,v146
ex v147,v147
v_add_f32 v151, v151, v184
cv v169,v146,v147
ex v148,v148
)"
R"(ex v149,v149
ex v150,v150
cv v170,v148,v149
ex v151,v151
v_add_f32 v240, 0x358637bd, v240
cv v171,v150,v151
v_rcp_f32 v240, v240
v_max3_f32 v226, v226, v155, v156
mm v[200:203],v[208:211],v[168:171],0
dr a[72:75],v212 offset:2048
v_mul_f32 v236, v236, v240
mm v[64:67],a[128:131],v[168:171],0
bl v214,s[44:47],s62 offen offset:2048 sc1 lds
mm v[68:71],a[132:135],v[168:171],0
dr a[76:79],v212 offset:3072
v_mul_f32 v237, v237, v240
)"
R"(mm v[72:75],a[136:139],v[168:171],0
bl v214,s[44:47],s62 offen offset:3072 sc1 lds
mm v[76:79],a[140:143],v[168:171],0
v_max3_f32 v226, v226, v157, v158
v_mul_f32 v238, v238, v240
mm v[80:83],a[144:147],v[168:171],0
v_mul_f32 v239, v239, v240
cv v236,v236,v237
mm v[84:87],a[148:151],v[168:171],0
v_max_f32 v226, v226, v159
cv v237,v238,v239
mm v[88:91],a[152:155],v[168:171],0
sa m0,s54,0x1000
sa s62,s53,0x1000
v_mov_b32 v227, v226
mm v[92:95],a[156:159],v[168:171],0
)"
R"(buffer_store_dwordx2 v[236:237], v232, s[44:47], s63 offen sc1
sa s63,s63,0x4000
mm v[240:243],a[212:215],a[200:203],0
bl v214,s[44:47],s62 offen sc1 lds
mm v[236:239],a[208:211],a[200:203],0
v_sub_f32 v181, 0xc2c80000, v222
v_sub_f32 v182, 0xc2c80000, v222
v_sub_f32 v183, 0xc2c80000, v222
v_add_f32 v248, 0x358637bd, v248
v_readlane_b32 s56, v216, s52
mm v[136:139],a[96:99],a[16:19],v[180:183]
sa s58,s60,s56
s_addc_u32 s59, s61, 0
dr a[80:83],v212 offset:4096
mm v[136:139],a[100:103],a[20:23],v[136:139]
v_permlane32_swap_b32 v226, v227
)"
R"(mm v[136:139],a[104:107],a[24:27],v[136:139]
v_rcp_f32 v248, v248
mm v[136:139],a[108:111],a[28:31],v[136:139]
v_max_f32 v226, v226, v227
v_mul_f32 v244, v244, v248
mm v[140:143],a[112:115],a[16:19],v[180:183]
v_mul_f32 v245, v245, v248
v_mov_b32 v227, v226
mm v[140:143],a[116:119],a[20:23],v[140:143]
v_mul_f32 v246, v246, v248
v_mul_f32 v247, v247, v248
mm v[140:143],a[120:123],a[24:27],v[140:143]
cv v244,v244,v245
cv v245,v246,v247
mm v[140:143],a[124:127],a[28:31],v[140:143]
bl v214,s[44:47],s62 offen offset:1024 sc1 lds
)"
R"(buffer_store_dwordx2 v[244:245], v232, s[44:47], s63 offen sc1
sa s63,s63,0x4000
mm v[248:251],a[212:215],a[204:207],0
v_permlane16_swap_b32 v226, v227
mm v[244:247],a[208:211],a[204:207],0
dr a[84:87],v212 offset:5120
v_max_f32 v226, v226, v227
v_sub_f32 v188, 0xc2c80000, v226
v_add_f32 v152, v152, v188
ex v152,v152
v_add_f32 v153, v153, v188
ex v153,v153
v_add_f32 v154, v154, v188
cv v172,v152,v153
ex v154,v154
v_add_f32 v155, v155, v188
)"
R"(ex v155,v155
v_add_f32 v156, v156, v188
cv v173,v154,v155
ex v156,v156
v_add_f32 v157, v157, v188
ex v157,v157
v_add_f32 v158, v158, v188
cv v174,v156,v157
ex v158,v158
v_add_f32 v159, v159, v188
ex v159,v159
v_add_f32 v240, 0x358637bd, v240
cv v175,v158,v159
v_rcp_f32 v240, v240
v_sub_f32 v185, 0xc2c80000, v224
mm v[204:207],v[208:211],v[172:175],0
)"
R"(bl v214,s[44:47],s62 offen offset:2048 sc1 lds
mm v[96:99],a[128:131],v[172:175],0
dr a[88:91],v212 offset:6144
v_mul_f32 v236, v236, v240
mm v[100:103],a[132:135],v[172:175],0
v_mul_f32 v237, v237, v240
v_mul_f32 v238, v238, v240
mm v[104:107],a[136:139],v[172:175],0
v_mul_f32 v239, v239, v240
cv v236,v236,v237
mm v[108:111],a[140:143],v[172:175],0
bl v214,s[44:47],s62 offen offset:3072 sc1 lds
mm v[112:115],a[144:147],v[172:175],0
dr a[92:95],v212 offset:7168
cv v237,v238,v239
mm v[116:119],a[148:151],v[172:175],0
)"
R"(buffer_store_dwordx2 v[236:237], v232, s[44:47], s63 offen sc1
sa s63,s63,0x4000
mm v[120:123],a[152:155],v[172:175],0
sa m0,s54,0x2000
sa s62,s53,0x2000
v_sub_f32 v186, 0xc2c80000, v224
mm v[124:127],a[156:159],v[172:175],0
v_sub_f32 v187, 0xc2c80000, v224
v_add_f32 v248, 0x358637bd, v248
v_rcp_f32 v248, v248
mm v[144:147],a[96:99],a[32:35],v[184:187]
bl v214,s[44:47],s62 offen sc1 lds
mm v[144:147],a[100:103],a[36:39],v[144:147]
v_mul_f32 v244, v244, v248
v_mul_f32 v245, v245, v248
mm v[144:147],a[104:107],a[40:43],v[144:147]
)"
R"(dr a[128:131],v212 offset:16384
v_mul_f32 v246, v246, v248
mm v[144:147],a[108:111],a[44:47],v[144:147]
v_mul_f32 v247, v247, v248
cv v244,v244,v245
mm v[148:151],a[112:115],a[32:35],v[184:187]
bl v214,s[44:47],s62 offen offset:1024 sc1 lds
mm v[148:151],a[116:119],a[36:39],v[148:151]
cv v245,v246,v247
v_sub_f32 v189, 0xc2c80000, v226
mm v[148:151],a[120:123],a[40:43],v[148:151]
buffer_store_dwordx2 v[244:245], v232, s[44:47], s63 offen sc1
sa s63,s63,0x4000
mm v[148:151],a[124:127],a[44:47],v[148:151]
s_waitcnt lgkmcnt(12)
v_sub_f32 v190, 0xc2c80000, v226
)"
R"(v_sub_f32 v191, 0xc2c80000, v226
v_cndmask_b32 v140, v213, v140, s[88:89]
v_cndmask_b32 v141, v213, v141, s[90:91]
mm v[152:155],a[96:99],a[48:51],v[188:191]
dr a[132:135],v212 offset:17408
v_cndmask_b32 v142, v213, v142, s[92:93]
mm v[152:155],a[100:103],a[52:55],v[152:155]
bl v214,s[44:47],s62 offen offset:2048 sc1 lds
mm v[152:155],a[104:107],a[56:59],v[152:155]
dr a[136:139],v212 offset:18432
v_cndmask_b32 v143, v213, v143, s[94:95]
mm v[152:155],a[108:111],a[60:63],v[152:155]
ex v136,v136
mm v[156:159],a[112:115],a[48:51],v[188:191]
ex v137,v137
mm v[156:159],a[116:119],a[52:55],v[156:159]
)"
R"(ex v138,v138
mm v[156:159],a[120:123],a[56:59],v[156:159]
ex v139,v139
mm v[156:159],a[124:127],a[60:63],v[156:159]
ex v140,v140
cv v164,v136,v137
cv v165,v138,v139
ex v141,v141
ex v142,v142
cv v166,v140,v141
ex v143,v143
ex v144,v144
cv v167,v142,v143
ex v145,v145
ex v146,v146
mm v[196:199],v[208:211],v[164:167],v[196:199]
)"
R"(bl v214,s[44:47],s62 offen offset:3072 sc1 lds
mm v[32:35],a[160:163],v[164:167],v[32:35]
sa m0,s54,0x3000
sa s62,s53,0x3000
dr a[140:143],v212 offset:19456
mm v[36:39],a[164:167],v[164:167],v[36:39]
bl v214,s[44:47],s62 offen sc1 lds
mm v[40:43],a[168:171],v[164:167],v[40:43]
dr a[144:147],v212 offset:20480
cv v168,v144,v145
mm v[44:47],a[172:175],v[164:167],v[44:47]
ex v147,v147
mm v[48:51],a[176:179],v[164:167],v[48:51]
ex v148,v148
mm v[52:55],a[180:183],v[164:167],v[52:55]
ex v149,v149
)"
R"(mm v[56:59],a[184:187],v[164:167],v[56:59]
ex v150,v150
mm v[60:63],a[188:191],v[164:167],v[60:63]
ex v151,v151
cv v169,v146,v147
cv v170,v148,v149
cv v171,v150,v151
ex v152,v152
ex v153,v153
mm v[200:203],v[208:211],v[168:171],v[200:203]
dr a[148:151],v212 offset:21504
v_sub_f32 v177, 0xc2c80000, v220
mm v[64:67],a[160:163],v[168:171],v[64:67]
bl v214,s[44:47],s62 offen offset:1024 sc1 lds
mm v[68:71],a[164:167],v[168:171],v[68:71]
dr a[152:155],v212 offset:22528
)"
R"(v_sub_f32 v178, 0xc2c80000, v220
mm v[72:75],a[168:171],v[168:171],v[72:75]
bl v214,s[44:47],s62 offen offset:2048 sc1 lds
mm v[76:79],a[172:175],v[168:171],v[76:79]
dr a[156:159],v212 offset:23552
v_sub_f32 v179, 0xc2c80000, v220
mm v[80:83],a[176:179],v[168:171],v[80:83]
bl v214,s[44:47],s62 offen offset:3072 sc1 lds
mm v[84:87],a[180:183],v[168:171],v[84:87]
ex v154,v154
mm v[88:91],a[184:187],v[168:171],v[88:91]
ex v155,v155
mm v[92:95],a[188:191],v[168:171],v[92:95]
cv v172,v152,v153
cv v173,v154,v155
s_waitcnt lgkmcnt(8)
)"
R"(s_setpc_b64 s[58:59]
.p2align 12
L_att_fa1_16:
mm v[128:131],a[64:67],a[0:3],0
dr a[96:99],v212 offset:8192
v_readlane_b32 s55, v219, s52
mm v[128:131],a[68:71],a[4:7],v[128:131]
dr a[100:103],v212 offset:9216
v_readlane_b32 s53, v217, s52
mm v[128:131],a[72:75],a[8:11],v[128:131]
dr a[104:107],v212 offset:10240
v_readlane_b32 s54, v218, s52
mm v[128:131],a[76:79],a[12:15],v[128:131]
sa s52,s52,1
dr a[108:111],v212 offset:11264
mm v[132:135],a[80:83],a[0:3],0
)"
R"(dr a[112:115],v212 offset:12288
v_readlane_b32 s56, v216, s52
mm v[132:135],a[84:87],a[4:7],v[132:135]
sa s58,s60,s56
s_addc_u32 s59, s61, 0
dr a[116:119],v212 offset:13312
mm v[132:135],a[88:91],a[8:11],v[132:135]
s_waitcnt vmcnt(0)
dr a[120:123],v212 offset:14336
v_max3_f32 v220, v128, v129, v130
mm v[132:135],a[92:95],a[12:15],v[132:135]
dr a[124:127],v212 offset:15360
v_add_f32 v96, v96, v100
mm v[136:139],a[64:67],a[16:19],0
dr a[160:163],v212 offset:24576
v_add_f32 v97, v97, v101
)"
R"(mm v[136:139],a[68:71],a[20:23],v[136:139]
dr a[164:167],v212 offset:25600
v_add_f32 v96, v96, v104
mm v[136:139],a[72:75],a[24:27],v[136:139]
v_cndmask_b32 v132, v213, v132, s[88:89]
v_cndmask_b32 v133, v213, v133, s[90:91]
mm v[136:139],a[76:79],a[28:31],v[136:139]
dr a[168:171],v212 offset:26624
v_cndmask_b32 v134, v213, v134, s[92:93]
mm v[140:143],a[80:83],a[16:19],0
v_max3_f32 v220, v220, v131, v132
v_cndmask_b32 v135, v213, v135, s[94:95]
mm v[140:143],a[84:87],a[20:23],v[140:143]
dr a[172:175],v212 offset:27648
v_max3_f32 v220, v220, v133, v134
mm v[140:143],a[88:91],a[24:27],v[140:143]
)"
R"(v_max_f32 v220, v220, v135
v_mov_b32 v221, v220
mm v[140:143],a[92:95],a[28:31],v[140:143]
dr a[176:179],v212 offset:28672
v_add_f32 v97, v97, v105
mm v[144:147],a[64:67],a[32:35],0
dr a[180:183],v212 offset:29696
v_add_f32 v98, v98, v102
mm v[144:147],a[68:71],a[36:39],v[144:147]
v_permlane32_swap_b32 v220, v221
mm v[144:147],a[72:75],a[40:43],v[144:147]
dr a[184:187],v212 offset:30720
v_add_f32 v99, v99, v103
mm v[144:147],a[76:79],a[44:47],v[144:147]
v_max_f32 v220, v220, v221
v_add_f32 v96, v96, v108
)"
R"(mm v[148:151],a[80:83],a[32:35],0
v_add_f32 v97, v97, v109
v_add_f32 v98, v98, v106
mm v[148:151],a[84:87],a[36:39],v[148:151]
v_add_f32 v99, v99, v107
v_mov_b32 v221, v220
mm v[148:151],a[88:91],a[40:43],v[148:151]
dr a[188:191],v212 offset:31744
v_add_f32 v96, v96, v112
mm v[148:151],a[92:95],a[44:47],v[148:151]
v_add_f32 v97, v97, v113
v_add_f32 v98, v98, v110
mm v[152:155],a[64:67],a[48:51],0
v_add_f32 v99, v99, v111
v_max3_f32 v222, v136, v137, v138
mm v[152:155],a[68:71],a[52:55],v[152:155]
)"
R"(v_add_f32 v96, v96, v116
v_add_f32 v97, v97, v117
mm v[152:155],a[72:75],a[56:59],v[152:155]
v_add_f32 v98, v98, v114
v_add_f32 v99, v99, v115
mm v[152:155],a[76:79],a[60:63],v[152:155]
v_permlane16_swap_b32 v220, v221
mm v[156:159],a[80:83],a[48:51],0
v_max3_f32 v222, v222, v139, v140
v_add_f32 v96, v96, v120
mm v[156:159],a[84:87],a[52:55],v[156:159]
v_add_f32 v97, v97, v121
v_add_f32 v98, v98, v118
v_add_f32 v96, v96, v124
v_add_f32 v97, v97, v125
cv v96,v96,v97
)"
R"(v_add_f32 v98, v98, v122
v_add_f32 v98, v98, v126
v_add_f32 v99, v99, v119
v_add_f32 v99, v99, v123
v_add_f32 v99, v99, v127
cv v97,v98,v99
ds_write_b64 v230, v[96:97]
mm v[156:159],a[88:91],a[56:59],v[156:159]
s_waitcnt lgkmcnt(15)
v_max_f32 v220, v220, v221
v_max3_f32 v222, v222, v141, v142
mm v[156:159],a[92:95],a[60:63],v[156:159]
v_sub_f32 v176, 0xc2c80000, v220
v_max_f32 v222, v222, v143
v_add_f32 v128, v128, v176
ex v128,v128
)"
R"(v_add_f32 v129, v129, v176
ex v129,v129
v_add_f32 v130, v130, v176
cv v160,v128,v129
ex v130,v130
v_add_f32 v131, v131, v176
ex v131,v131
v_add_f32 v132, v132, v176
cv v161,v130,v131
ex v132,v132
v_add_f32 v133, v133, v176
ex v133,v133
v_add_f32 v134, v134, v176
cv v162,v132,v133
ex v134,v134
v_add_f32 v135, v135, v176
)"
R"(ex v135,v135
v_mov_b32 v223, v222
cv v163,v134,v135
v_add_u32 v212, s55, v214
v_permlane32_swap_b32 v222, v223
mm v[192:195],v[208:211],v[160:163],0
s_waitcnt vmcnt(0) lgkmcnt(0)
s_barrier
v_max_f32 v222, v222, v223
v_mov_b32 v223, v222
mm v[0:3],a[128:131],v[160:163],0
dr a[208:211],v231
v_max3_f32 v224, v144, v145, v146
mm v[4:7],a[132:135],v[160:163],0
v_permlane16_swap_b32 v222, v223
mm v[8:11],a[136:139],v[160:163],0
)"
R"(dr a[212:215],v231 offset:1024
v_max_f32 v222, v222, v223
mm v[12:15],a[140:143],v[160:163],0
dr a[64:67],v212 offset:0
v_sub_f32 v180, 0xc2c80000, v222
mm v[16:19],a[144:147],v[160:163],0
v_add_f32 v136, v136, v180
v_add_f32 v137, v137, v180
mm v[20:23],a[148:151],v[160:163],0
v_add_f32 v138, v138, v180
v_add_f32 v139, v139, v180
mm v[24:27],a[152:155],v[160:163],0
v_add_f32 v140, v140, v180
v_add_f32 v141, v141, v180
mm v[28:31],a[156:159],v[160:163],0
v_add_f32 v142, v142, v180
)"
R"(v_add_f32 v143, v143, v180
ex v136,v136
ex v137,v137
ex v138,v138
cv v164,v136,v137
ex v139,v139
ex v140,v140
cv v165,v138,v139
ex v141,v141
ex v142,v142
cv v166,v140,v141
ex v143,v143
v_max3_f32 v224, v224, v147, v148
cv v167,v142,v143
v_max3_f32 v224, v224, v149, v150
v_max_f32 v224, v224, v151
)"
R"(mm v[196:199],v[208:211],v[164:167],0
sa m0,s54,0x0
sa s62,s53,0x0
v_mov_b32 v225, v224
mm v[32:35],a[128:131],v[164:167],0
bl v214,s[44:47],s62 offen sc1 lds
mm v[36:39],a[132:135],v[164:167],0
dr a[68:71],v212 offset:1024
v_max3_f32 v226, v152, v153, v154
mm v[40:43],a[136:139],v[164:167],0
v_permlane32_swap_b32 v224, v225
mm v[44:47],a[140:143],v[164:167],0
bl v214,s[44:47],s62 offen offset:1024 sc1 lds
mm v[48:51],a[144:147],v[164:167],0
v_max_f32 v224, v224, v225
v_mov_b32 v225, v224
)"
R"(mm v[52:55],a[148:151],v[164:167],0
dr a[72:75],v212 offset:2048
v_max3_f32 v226, v226, v155, v156
mm v[56:59],a[152:155],v[164:167],0
s_waitcnt lgkmcnt(3)
v_permlane16_swap_b32 v224, v225
mm v[60:63],a[156:159],v[164:167],0
v_max_f32 v224, v224, v225
v_sub_f32 v184, 0xc2c80000, v224
mm v[240:243],a[212:215],a[192:195],0
bl v214,s[44:47],s62 offen offset:2048 sc1 lds
mm v[236:239],a[208:211],a[192:195],0
v_add_f32 v144, v144, v184
v_add_f32 v145, v145, v184
mm v[248:251],a[212:215],a[196:199],0
v_add_f32 v146, v146, v184
)"
R"(v_add_f32 v147, v147, v184
mm v[244:247],a[208:211],a[196:199],0
v_add_f32 v148, v148, v184
v_add_f32 v149, v149, v184
ex v144,v144
ex v145,v145
v_add_f32 v150, v150, v184
cv v168,v144,v145
ex v146,v146
ex v147,v147
v_add_f32 v151, v151, v184
cv v169,v146,v147
ex v148,v148
ex v149,v149
ex v150,v150
cv v170,v148,v149
)"
R"(ex v151,v151
v_max3_f32 v226, v226, v157, v158
cv v171,v150,v151
v_max_f32 v226, v226, v159
v_add_f32 v240, 0x358637bd, v240
mm v[200:203],v[208:211],v[168:171],0
dr a[76:79],v212 offset:3072
v_mov_b32 v227, v226
mm v[64:67],a[128:131],v[168:171],0
bl v214,s[44:47],s62 offen offset:3072 sc1 lds
mm v[68:71],a[132:135],v[168:171],0
sa m0,s54,0x1000
sa s62,s53,0x1000
dr a[80:83],v212 offset:4096
mm v[72:75],a[136:139],v[168:171],0
v_rcp_f32 v240, v240
)"
R"(mm v[76:79],a[140:143],v[168:171],0
bl v214,s[44:47],s62 offen sc1 lds
mm v[80:83],a[144:147],v[168:171],0
v_mul_f32 v236, v236, v240
v_mul_f32 v237, v237, v240
mm v[84:87],a[148:151],v[168:171],0
v_permlane32_swap_b32 v226, v227
mm v[88:91],a[152:155],v[168:171],0
v_mul_f32 v238, v238, v240
v_mul_f32 v239, v239, v240
mm v[92:95],a[156:159],v[168:171],0
cv v236,v236,v237
v_max_f32 v226, v226, v227
cv v237,v238,v239
buffer_store_dwordx2 v[236:237], v232, s[44:47], s63 offen sc1
sa s63,s63,0x4000
)"
R"(mm v[240:243],a[212:215],a[200:203],0
v_mov_b32 v227, v226
v_sub_f32 v181, 0xc2c80000, v222
mm v[236:239],a[208:211],a[200:203],0
v_sub_f32 v182, 0xc2c80000, v222
v_sub_f32 v183, 0xc2c80000, v222
v_add_f32 v248, 0x358637bd, v248
v_permlane16_swap_b32 v226, v227
mm v[136:139],a[96:99],a[16:19],v[180:183]
bl v214,s[44:47],s62 offen offset:1024 sc1 lds
mm v[136:139],a[100:103],a[20:23],v[136:139]
v_rcp_f32 v248, v248
mm v[136:139],a[104:107],a[24:27],v[136:139]
dr a[84:87],v212 offset:5120
v_max_f32 v226, v226, v227
mm v[136:139],a[108:111],a[28:31],v[136:139]
)"
R"(v_mul_f32 v244, v244, v248
v_mul_f32 v245, v245, v248
cv v244,v244,v245
v_mul_f32 v246, v246, v248
v_mul_f32 v247, v247, v248
cv v245,v246,v247
buffer_store_dwordx2 v[244:245], v232, s[44:47], s63 offen sc1
sa s63,s63,0x4000
mm v[248:251],a[212:215],a[204:207],0
v_sub_f32 v188, 0xc2c80000, v226
v_add_f32 v152, v152, v188
mm v[244:247],a[208:211],a[204:207],0
v_add_f32 v153, v153, v188
v_add_f32 v154, v154, v188
ex v152,v152
ex v153,v153
)"
R"(v_add_f32 v155, v155, v188
cv v172,v152,v153
ex v154,v154
ex v155,v155
v_add_f32 v156, v156, v188
cv v173,v154,v155
ex v156,v156
v_add_f32 v157, v157, v188
ex v157,v157
v_add_f32 v158, v158, v188
cv v174,v156,v157
ex v158,v158
v_add_f32 v159, v159, v188
ex v159,v159
v_add_f32 v240, 0x358637bd, v240
cv v175,v158,v159
)"
R"(v_rcp_f32 v240, v240
v_sub_f32 v185, 0xc2c80000, v224
mm v[204:207],v[208:211],v[172:175],0
bl v214,s[44:47],s62 offen offset:2048 sc1 lds
mm v[96:99],a[128:131],v[172:175],0
dr a[88:91],v212 offset:6144
v_mul_f32 v236, v236, v240
mm v[100:103],a[132:135],v[172:175],0
bl v214,s[44:47],s62 offen offset:3072 sc1 lds
mm v[104:107],a[136:139],v[172:175],0
dr a[92:95],v212 offset:7168
v_mul_f32 v237, v237, v240
mm v[108:111],a[140:143],v[172:175],0
v_mul_f32 v238, v238, v240
v_mul_f32 v239, v239, v240
mm v[112:115],a[144:147],v[172:175],0
)"
R"(sa m0,s54,0x2000
sa s62,s53,0x2000
cv v236,v236,v237
mm v[116:119],a[148:151],v[172:175],0
v_sub_f32 v186, 0xc2c80000, v224
v_sub_f32 v187, 0xc2c80000, v224
mm v[120:123],a[152:155],v[172:175],0
cv v237,v238,v239
v_add_f32 v248, 0x358637bd, v248
mm v[124:127],a[156:159],v[172:175],0
bl v214,s[44:47],s62 offen sc1 lds
mm v[144:147],a[96:99],a[32:35],v[184:187]
buffer_store_dwordx2 v[236:237], v232, s[44:47], s63 offen sc1
sa s63,s63,0x4000
mm v[144:147],a[100:103],a[36:39],v[144:147]
v_rcp_f32 v248, v248
)"
R"(mm v[144:147],a[104:107],a[40:43],v[144:147]
v_mul_f32 v244, v244, v248
v_mul_f32 v245, v245, v248
mm v[144:147],a[108:111],a[44:47],v[144:147]
dr a[128:131],v212 offset:16384
v_mul_f32 v246, v246, v248
mm v[148:151],a[112:115],a[32:35],v[184:187]
v_mul_f32 v247, v247, v248
cv v244,v244,v245
mm v[148:151],a[116:119],a[36:39],v[148:151]
bl v214,s[44:47],s62 offen offset:1024 sc1 lds
mm v[148:151],a[120:123],a[40:43],v[148:151]
s_waitcnt lgkmcnt(12)
cv v245,v246,v247
v_sub_f32 v189, 0xc2c80000, v226
mm v[148:151],a[124:127],a[44:47],v[148:151]
)"
R"(buffer_store_dwordx2 v[244:245], v232, s[44:47], s63 offen sc1
sa s63,s63,0x4000
v_sub_f32 v190, 0xc2c80000, v226
v_sub_f32 v191, 0xc2c80000, v226
v_cndmask_b32 v136, v213, v136, s[88:89]
v_cndmask_b32 v137, v213, v137, s[90:91]
mm v[152:155],a[96:99],a[48:51],v[188:191]
dr a[132:135],v212 offset:17408
v_cndmask_b32 v138, v213, v138, s[92:93]
mm v[152:155],a[100:103],a[52:55],v[152:155]
bl v214,s[44:47],s62 offen offset:2048 sc1 lds
mm v[152:155],a[104:107],a[56:59],v[152:155]
dr a[136:139],v212 offset:18432
v_cndmask_b32 v139, v213, v139, s[94:95]
mm v[152:155],a[108:111],a[60:63],v[152:155]
ex v136,v136
)"
R"(mm v[156:159],a[112:115],a[48:51],v[188:191]
ex v137,v137
mm v[156:159],a[116:119],a[52:55],v[156:159]
ex v138,v138
mm v[156:159],a[120:123],a[56:59],v[156:159]
ex v139,v139
mm v[156:159],a[124:127],a[60:63],v[156:159]
bl v214,s[44:47],s62 offen offset:3072 sc1 lds
cv v164,v136,v137
cv v165,v138,v139
v_mov_b32 v166, 0
v_mov_b32 v167, 0
ex v144,v144
ex v145,v145
mm v[196:199],v[208:211],v[164:167],v[196:199]
sa m0,s54,0x3000
)"
R"(sa s62,s53,0x3000
dr a[140:143],v212 offset:19456
mm v[32:35],a[160:163],v[164:167],v[32:35]
bl v214,s[44:47],s62 offen sc1 lds
mm v[36:39],a[164:167],v[164:167],v[36:39]
dr a[144:147],v212 offset:20480
cv v168,v144,v145
mm v[40:43],a[168:171],v[164:167],v[40:43]
ex v146,v146
mm v[44:47],a[172:175],v[164:167],v[44:47]
ex v147,v147
mm v[48:51],a[176:179],v[164:167],v[48:51]
ex v148,v148
mm v[52:55],a[180:183],v[164:167],v[52:55]
ex v149,v149
mm v[56:59],a[184:187],v[164:167],v[56:59]
)"
R"(ex v150,v150
mm v[60:63],a[188:191],v[164:167],v[60:63]
ex v151,v151
cv v169,v146,v147
cv v170,v148,v149
cv v171,v150,v151
ex v152,v152
ex v153,v153
mm v[200:203],v[208:211],v[168:171],v[200:203]
dr a[148:151],v212 offset:21504
v_sub_f32 v177, 0xc2c80000, v220
mm v[64:67],a[160:163],v[168:171],v[64:67]
bl v214,s[44:47],s62 offen offset:1024 sc1 lds
mm v[68:71],a[164:167],v[168:171],v[68:71]
dr a[152:155],v212 offset:22528
v_sub_f32 v178, 0xc2c80000, v220
)"
R"(mm v[72:75],a[168:171],v[168:171],v[72:75]
bl v214,s[44:47],s62 offen offset:2048 sc1 lds
mm v[76:79],a[172:175],v[168:171],v[76:79]
dr a[156:159],v212 offset:23552
v_sub_f32 v179, 0xc2c80000, v220
mm v[80:83],a[176:179],v[168:171],v[80:83]
bl v214,s[44:47],s62 offen offset:3072 sc1 lds
mm v[84:87],a[180:183],v[168:171],v[84:87]
ex v154,v154
mm v[88:91],a[184:187],v[168:171],v[88:91]
ex v155,v155
mm v[92:95],a[188:191],v[168:171],v[92:95]
cv v172,v152,v153
cv v173,v154,v155
s_waitcnt lgkmcnt(8)
s_setpc_b64 s[58:59]
)"
R"(.p2align 12
L_att_seg0_16:
ex v156,v156
ex v157,v157
ex v158,v158
cv v174,v156,v157
ex v159,v159
s_nop 0
cv v175,v158,v159
s_nop 1
mm v[204:207],v[208:211],v[172:175],v[204:207]
dr a[96:99],v212 offset:8192
v_readlane_b32 s55, v219, s52
mm v[96:99],a[160:163],v[172:175],v[96:99]
dr a[100:103],v212 offset:9216
v_readlane_b32 s53, v217, s52
)"
R"(mm v[100:103],a[164:167],v[172:175],v[100:103]
dr a[104:107],v212 offset:10240
v_readlane_b32 s54, v218, s52
mm v[104:107],a[168:171],v[172:175],v[104:107]
sa s52,s52,1
dr a[108:111],v212 offset:11264
mm v[108:111],a[172:175],v[172:175],v[108:111]
dr a[112:115],v212 offset:12288
v_readlane_b32 s56, v216, s52
mm v[112:115],a[176:179],v[172:175],v[112:115]
sa s58,s60,s56
s_addc_u32 s59, s61, 0
dr a[116:119],v212 offset:13312
mm v[116:119],a[180:183],v[172:175],v[116:119]
dr a[120:123],v212 offset:14336
mm v[120:123],a[184:187],v[172:175],v[120:123]
)"
R"(s_waitcnt lgkmcnt(7)
dr a[124:127],v212 offset:15360
mm v[124:127],a[188:191],v[172:175],v[124:127]
dr a[160:163],v212 offset:24576
mm v[144:147],a[64:67],a[32:35],v[184:187]
dr a[164:167],v212 offset:25600
v_mov_b32 v170, 0
mm v[144:147],a[68:71],a[36:39],v[144:147]
v_mov_b32 v171, 0
mm v[144:147],a[72:75],a[40:43],v[144:147]
dr a[168:171],v212 offset:26624
mm v[144:147],a[76:79],a[44:47],v[144:147]
mm v[152:155],a[64:67],a[48:51],v[188:191]
dr a[172:175],v212 offset:27648
mm v[152:155],a[68:71],a[52:55],v[152:155]
mm v[152:155],a[72:75],a[56:59],v[152:155]
)"
R"(dr a[176:179],v212 offset:28672
mm v[152:155],a[76:79],a[60:63],v[152:155]
dr a[180:183],v212 offset:29696
mm v[156:159],a[80:83],a[48:51],v[188:191]
v_cndmask_b32 v144, v213, v144, s[88:89]
v_cndmask_b32 v145, v213, v145, s[90:91]
mm v[156:159],a[84:87],a[52:55],v[156:159]
v_cndmask_b32 v146, v213, v146, s[92:93]
v_cndmask_b32 v147, v213, v147, s[94:95]
mm v[156:159],a[88:91],a[56:59],v[156:159]
ex v144,v144
mm v[156:159],a[92:95],a[60:63],v[156:159]
ex v145,v145
ex v146,v146
cv v168,v144,v145
ex v147,v147
)"
R"(ex v152,v152
cv v169,v146,v147
ex v153,v153
ex v154,v154
mm v[200:203],v[208:211],v[168:171],v[200:203]
dr a[184:187],v212 offset:30720
cv v172,v152,v153
mm v[64:67],a[128:131],v[168:171],v[64:67]
dr a[188:191],v212 offset:31744
mm v[68:71],a[132:135],v[168:171],v[68:71]
ex v155,v155
mm v[72:75],a[136:139],v[168:171],v[72:75]
ex v156,v156
mm v[76:79],a[140:143],v[168:171],v[76:79]
ex v157,v157
mm v[80:83],a[144:147],v[168:171],v[80:83]
)"
R"(ex v158,v158
mm v[84:87],a[148:151],v[168:171],v[84:87]
ex v159,v159
mm v[88:91],a[152:155],v[168:171],v[88:91]
cv v173,v154,v155
cv v174,v156,v157
mm v[92:95],a[156:159],v[168:171],v[92:95]
cv v175,v158,v159
v_add_u32 v212, s55, v214
s_nop 0
mm v[204:207],v[208:211],v[172:175],v[204:207]
mm v[96:99],a[128:131],v[172:175],v[96:99]
mm v[100:103],a[132:135],v[172:175],v[100:103]
v_rcp_f32 v220, v192
v_rcp_f32 v222, v196
s_nop 0
)"
R"(v_pk_mul_f32 v[0:1], v[0:1], v[220:221] op_sel_hi:[1,0]
v_pk_mul_f32 v[2:3], v[2:3], v[220:221] op_sel_hi:[1,0]
v_pk_mul_f32 v[4:5], v[4:5], v[220:221] op_sel_hi:[1,0]
v_pk_mul_f32 v[6:7], v[6:7], v[220:221] op_sel_hi:[1,0]
cv v0,v0,v1
cv v1,v2,v3
cv v2,v4,v5
cv v3,v6,v7
v_pk_mul_f32 v[8:9], v[8:9], v[220:221] op_sel_hi:[1,0]
v_pk_mul_f32 v[10:11], v[10:11], v[220:221] op_sel_hi:[1,0]
v_pk_mul_f32 v[12:13], v[12:13], v[220:221] op_sel_hi:[1,0]
v_pk_mul_f32 v[14:15], v[14:15], v[220:221] op_sel_hi:[1,0]
cv v8,v8,v9
cv v9,v10,v11
cv v10,v12,v13
cv v11,v14,v15
)"
R"(v_pk_mul_f32 v[16:17], v[16:17], v[220:221] op_sel_hi:[1,0]
v_pk_mul_f32 v[18:19], v[18:19], v[220:221] op_sel_hi:[1,0]
v_pk_mul_f32 v[20:21], v[20:21], v[220:221] op_sel_hi:[1,0]
v_pk_mul_f32 v[22:23], v[22:23], v[220:221] op_sel_hi:[1,0]
cv v16,v16,v17
cv v17,v18,v19
cv v18,v20,v21
cv v19,v22,v23
v_pk_mul_f32 v[24:25], v[24:25], v[220:221] op_sel_hi:[1,0]
v_pk_mul_f32 v[26:27], v[26:27], v[220:221] op_sel_hi:[1,0]
v_pk_mul_f32 v[28:29], v[28:29], v[220:221] op_sel_hi:[1,0]
v_pk_mul_f32 v[30:31], v[30:31], v[220:221] op_sel_hi:[1,0]
cv v24,v24,v25
cv v25,v26,v27
cv v26,v28,v29
cv v27,v30,v31
)"
R"(v_pk_mul_f32 v[32:33], v[32:33], v[222:223] op_sel_hi:[1,0]
v_pk_mul_f32 v[34:35], v[34:35], v[222:223] op_sel_hi:[1,0]
v_pk_mul_f32 v[36:37], v[36:37], v[222:223] op_sel_hi:[1,0]
v_pk_mul_f32 v[38:39], v[38:39], v[222:223] op_sel_hi:[1,0]
cv v32,v32,v33
cv v33,v34,v35
cv v34,v36,v37
cv v35,v38,v39
v_pk_mul_f32 v[40:41], v[40:41], v[222:223] op_sel_hi:[1,0]
v_pk_mul_f32 v[42:43], v[42:43], v[222:223] op_sel_hi:[1,0]
v_pk_mul_f32 v[44:45], v[44:45], v[222:223] op_sel_hi:[1,0]
v_pk_mul_f32 v[46:47], v[46:47], v[222:223] op_sel_hi:[1,0]
cv v40,v40,v41
cv v41,v42,v43
cv v42,v44,v45
cv v43,v46,v47
)"
R"(v_pk_mul_f32 v[48:49], v[48:49], v[222:223] op_sel_hi:[1,0]
v_pk_mul_f32 v[50:51], v[50:51], v[222:223] op_sel_hi:[1,0]
v_pk_mul_f32 v[52:53], v[52:53], v[222:223] op_sel_hi:[1,0]
v_pk_mul_f32 v[54:55], v[54:55], v[222:223] op_sel_hi:[1,0]
cv v48,v48,v49
cv v49,v50,v51
cv v50,v52,v53
cv v51,v54,v55
v_pk_mul_f32 v[56:57], v[56:57], v[222:223] op_sel_hi:[1,0]
v_pk_mul_f32 v[58:59], v[58:59], v[222:223] op_sel_hi:[1,0]
v_pk_mul_f32 v[60:61], v[60:61], v[222:223] op_sel_hi:[1,0]
v_pk_mul_f32 v[62:63], v[62:63], v[222:223] op_sel_hi:[1,0]
cv v56,v56,v57
cv v57,v58,v59
cv v58,v60,v61
cv v59,v62,v63
)"
R"(s_lshl_b32 s56, s57, 5
sa s56,s56,s48
s_lshl_b32 s56, s56, 15
sa s56,s56,0x2100000
s_and_b32 s69, s50, 1
s_lshl_b32 s69, s69, 10
sa s68,s56,s69
s_sub_u32 s69, s56, s69
sa s56,s68,0x0
buffer_store_dwordx4 v[0:3], v214, s[44:47], s56 offen offset:0 sc1
sa s56,s68,0x2000
buffer_store_dwordx4 v[8:11], v214, s[44:47], s56 offen offset:0 sc1
sa s56,s68,0x4000
buffer_store_dwordx4 v[16:19], v214, s[44:47], s56 offen offset:0 sc1
sa s56,s68,0x6000
buffer_store_dwordx4 v[24:27], v214, s[44:47], s56 offen offset:0 sc1
)"
R"(sa s56,s69,0x0
buffer_store_dwordx4 v[32:35], v214, s[44:47], s56 offen offset:3072 sc1
sa s56,s69,0x2000
buffer_store_dwordx4 v[40:43], v214, s[44:47], s56 offen offset:3072 sc1
sa s56,s69,0x4000
buffer_store_dwordx4 v[48:51], v214, s[44:47], s56 offen offset:3072 sc1
sa s56,s69,0x6000
buffer_store_dwordx4 v[56:59], v214, s[44:47], s56 offen offset:3072 sc1
s_waitcnt vmcnt(8) lgkmcnt(0)
s_barrier
s_lshl_b32 s72, s49, 7
s_lshl_b32 s73, s48, 2
sa s72,s72,s73
sa s72,s72,0x12000
v_mov_b32 v228, 1
v_mov_b32 v229, 0
)"
R"(s_mov_b64 exec, 1
buffer_store_dword v228, v229, s[44:47], s72 offen sc1
s_mov_b64 exec, -1
mm v[104:107],a[136:139],v[172:175],v[104:107]
dr a[64:67],v212 offset:0
mm v[108:111],a[140:143],v[172:175],v[108:111]
dr a[68:71],v212 offset:1024
mm v[112:115],a[144:147],v[172:175],v[112:115]
dr a[72:75],v212 offset:2048
mm v[116:119],a[148:151],v[172:175],v[116:119]
dr a[76:79],v212 offset:3072
mm v[120:123],a[152:155],v[172:175],v[120:123]
s_waitcnt lgkmcnt(4)
dr a[80:83],v212 offset:4096
mm v[124:127],a[156:159],v[172:175],v[124:127]
dr a[84:87],v212 offset:5120
)"
R"(mm v[152:155],a[96:99],a[48:51],v[188:191]
dr a[88:91],v212 offset:6144
mm v[152:155],a[100:103],a[52:55],v[152:155]
dr a[92:95],v212 offset:7168
mm v[152:155],a[104:107],a[56:59],v[152:155]
dr a[128:131],v212 offset:16384
mm v[152:155],a[108:111],a[60:63],v[152:155]
dr a[132:135],v212 offset:17408
mm v[156:159],a[112:115],a[48:51],v[188:191]
dr a[136:139],v212 offset:18432
mm v[156:159],a[116:119],a[52:55],v[156:159]
dr a[140:143],v212 offset:19456
mm v[156:159],a[120:123],a[56:59],v[156:159]
dr a[144:147],v212 offset:20480
mm v[156:159],a[124:127],a[60:63],v[156:159]
dr a[148:151],v212 offset:21504
)"
R"(dr a[152:155],v212 offset:22528
dr a[156:159],v212 offset:23552
ex v152,v152
ex v153,v153
ex v154,v154
ex v155,v155
cv v172,v152,v153
cv v173,v154,v155
sa m0,s54,0x0
sa s62,s53,0x0
bl v214,s[44:47],s62 offen sc1 lds
s_nop 7
s_nop 1
v_cndmask_b32 v156, v213, v156, s[88:89]
bl v214,s[44:47],s62 offen offset:1024 sc1 lds
ex v156,v156
)"
R"(v_cndmask_b32 v157, v213, v157, s[90:91]
ex v157,v157
bl v214,s[44:47],s62 offen offset:2048 sc1 lds
v_cndmask_b32 v158, v213, v158, s[92:93]
cv v174,v156,v157
ex v158,v158
bl v214,s[44:47],s62 offen offset:3072 sc1 lds
v_cndmask_b32 v159, v213, v159, s[94:95]
ex v159,v159
s_nop 0
sa m0,s54,0x1000
sa s62,s53,0x1000
bl v214,s[44:47],s62 offen sc1 lds
cv v175,v158,v159
s_nop 1
mm v[204:207],v[208:211],v[172:175],v[204:207]
)"
R"(bl v214,s[44:47],s62 offen offset:1024 sc1 lds
mm v[96:99],a[160:163],v[172:175],v[96:99]
mm v[100:103],a[164:167],v[172:175],v[100:103]
mm v[104:107],a[168:171],v[172:175],v[104:107]
bl v214,s[44:47],s62 offen offset:2048 sc1 lds
mm v[108:111],a[172:175],v[172:175],v[108:111]
mm v[112:115],a[176:179],v[172:175],v[112:115]
mm v[116:119],a[180:183],v[172:175],v[116:119]
bl v214,s[44:47],s62 offen offset:3072 sc1 lds
mm v[120:123],a[184:187],v[172:175],v[120:123]
mm v[124:127],a[188:191],v[172:175],v[124:127]
s_nop 7
v_mov_b32 v220, 0x5d800000
v_cmp_nlt_f32 vcc, v192, v220
v_cmp_nlt_f32 s[62:63], v196, v220
s_or_b64 vcc, vcc, s[62:63]
)"
R"(v_cmp_nlt_f32 s[62:63], v200, v220
s_or_b64 vcc, vcc, s[62:63]
v_cmp_nlt_f32 s[62:63], v204, v220
s_or_b64 vcc, vcc, s[62:63]
s_cbranch_vccnz L_redo_seg0_16
L_ok_seg0_16:
s_and_b32 s56, s50, 1
s_lshl_b32 s56, s56, 14
sa s56,s56,0x20000
v_add_u32 v230, s56, v214
v_rcp_f32 v224, v200
v_rcp_f32 v226, v204
s_nop 0
sa m0,s54,0x2000
sa s62,s53,0x2000
bl v214,s[44:47],s62 offen sc1 lds
)"
R"(dr a[0:3],v230 offset:0
v_pk_mul_f32 v[64:65], v[64:65], v[224:225] op_sel_hi:[1,0]
v_pk_mul_f32 v[66:67], v[66:67], v[224:225] op_sel_hi:[1,0]
v_pk_mul_f32 v[68:69], v[68:69], v[224:225] op_sel_hi:[1,0]
v_pk_mul_f32 v[70:71], v[70:71], v[224:225] op_sel_hi:[1,0]
dr a[4:7],v230 offset:1024
cv v64,v64,v65
cv v65,v66,v67
cv v66,v68,v69
cv v67,v70,v71
bl v214,s[44:47],s62 offen offset:1024 sc1 lds
dr a[8:11],v230 offset:2048
v_pk_mul_f32 v[72:73], v[72:73], v[224:225] op_sel_hi:[1,0]
v_pk_mul_f32 v[74:75], v[74:75], v[224:225] op_sel_hi:[1,0]
v_pk_mul_f32 v[76:77], v[76:77], v[224:225] op_sel_hi:[1,0]
v_pk_mul_f32 v[78:79], v[78:79], v[224:225] op_sel_hi:[1,0]
)"
R"(dr a[12:15],v230 offset:3072
cv v72,v72,v73
cv v73,v74,v75
cv v74,v76,v77
cv v75,v78,v79
bl v214,s[44:47],s62 offen offset:2048 sc1 lds
dr a[16:19],v230 offset:4096
v_pk_mul_f32 v[80:81], v[80:81], v[224:225] op_sel_hi:[1,0]
v_pk_mul_f32 v[82:83], v[82:83], v[224:225] op_sel_hi:[1,0]
v_pk_mul_f32 v[84:85], v[84:85], v[224:225] op_sel_hi:[1,0]
v_pk_mul_f32 v[86:87], v[86:87], v[224:225] op_sel_hi:[1,0]
dr a[20:23],v230 offset:5120
cv v80,v80,v81
cv v81,v82,v83
cv v82,v84,v85
cv v83,v86,v87
)"
R"(bl v214,s[44:47],s62 offen offset:3072 sc1 lds
dr a[24:27],v230 offset:6144
v_pk_mul_f32 v[88:89], v[88:89], v[224:225] op_sel_hi:[1,0]
v_pk_mul_f32 v[90:91], v[90:91], v[224:225] op_sel_hi:[1,0]
v_pk_mul_f32 v[92:93], v[92:93], v[224:225] op_sel_hi:[1,0]
v_pk_mul_f32 v[94:95], v[94:95], v[224:225] op_sel_hi:[1,0]
dr a[28:31],v230 offset:7168
cv v88,v88,v89
cv v89,v90,v91
cv v90,v92,v93
cv v91,v94,v95
sa m0,s54,0x3000
sa s62,s53,0x3000
bl v214,s[44:47],s62 offen sc1 lds
dr a[32:35],v230 offset:8192
v_pk_mul_f32 v[96:97], v[96:97], v[226:227] op_sel_hi:[1,0]
)"
R"(v_pk_mul_f32 v[98:99], v[98:99], v[226:227] op_sel_hi:[1,0]
v_pk_mul_f32 v[100:101], v[100:101], v[226:227] op_sel_hi:[1,0]
v_pk_mul_f32 v[102:103], v[102:103], v[226:227] op_sel_hi:[1,0]
dr a[36:39],v230 offset:9216
cv v96,v96,v97
cv v97,v98,v99
cv v98,v100,v101
cv v99,v102,v103
bl v214,s[44:47],s62 offen offset:1024 sc1 lds
dr a[40:43],v230 offset:10240
v_pk_mul_f32 v[104:105], v[104:105], v[226:227] op_sel_hi:[1,0]
v_pk_mul_f32 v[106:107], v[106:107], v[226:227] op_sel_hi:[1,0]
v_pk_mul_f32 v[108:109], v[108:109], v[226:227] op_sel_hi:[1,0]
v_pk_mul_f32 v[110:111], v[110:111], v[226:227] op_sel_hi:[1,0]
dr a[44:47],v230 offset:11264
cv v104,v104,v105
)"
R"(cv v105,v106,v107
cv v106,v108,v109
cv v107,v110,v111
bl v214,s[44:47],s62 offen offset:2048 sc1 lds
dr a[48:51],v230 offset:12288
v_pk_mul_f32 v[112:113], v[112:113], v[226:227] op_sel_hi:[1,0]
v_pk_mul_f32 v[114:115], v[114:115], v[226:227] op_sel_hi:[1,0]
v_pk_mul_f32 v[116:117], v[116:117], v[226:227] op_sel_hi:[1,0]
v_pk_mul_f32 v[118:119], v[118:119], v[226:227] op_sel_hi:[1,0]
dr a[52:55],v230 offset:13312
cv v112,v112,v113
cv v113,v114,v115
cv v114,v116,v117
cv v115,v118,v119
bl v214,s[44:47],s62 offen offset:3072 sc1 lds
dr a[56:59],v230 offset:14336
)"
R"(v_pk_mul_f32 v[120:121], v[120:121], v[226:227] op_sel_hi:[1,0]
v_pk_mul_f32 v[122:123], v[122:123], v[226:227] op_sel_hi:[1,0]
v_pk_mul_f32 v[124:125], v[124:125], v[226:227] op_sel_hi:[1,0]
v_pk_mul_f32 v[126:127], v[126:127], v[226:227] op_sel_hi:[1,0]
dr a[60:63],v230 offset:15360
cv v120,v120,v121
cv v121,v122,v123
cv v122,v124,v125
cv v123,v126,v127
sa s56,s68,0x1000
buffer_store_dwordx4 v[64:67], v214, s[44:47], s56 offen offset:0 sc1
sa s56,s68,0x3000
buffer_store_dwordx4 v[72:75], v214, s[44:47], s56 offen offset:0 sc1
sa s56,s68,0x5000
buffer_store_dwordx4 v[80:83], v214, s[44:47], s56 offen offset:0 sc1
sa s56,s68,0x7000
)"
R"(buffer_store_dwordx4 v[88:91], v214, s[44:47], s56 offen offset:0 sc1
sa s56,s69,0x1000
buffer_store_dwordx4 v[96:99], v214, s[44:47], s56 offen offset:3072 sc1
sa s56,s69,0x3000
buffer_store_dwordx4 v[104:107], v214, s[44:47], s56 offen offset:3072 sc1
sa s56,s69,0x5000
buffer_store_dwordx4 v[112:115], v214, s[44:47], s56 offen offset:3072 sc1
sa s56,s69,0x7000
buffer_store_dwordx4 v[120:123], v214, s[44:47], s56 offen offset:3072 sc1
s_sub_u32 s57, 15, s49
s_waitcnt lgkmcnt(0)
s_setpc_b64 s[58:59]
.p2align 12
L_att_seg1_16:
ex v156,v156
ex v157,v157
)"
R"(ex v158,v158
cv v174,v156,v157
ex v159,v159
s_nop 0
cv v175,v158,v159
s_nop 1
mm v[204:207],v[208:211],v[172:175],v[204:207]
dr a[96:99],v212 offset:8192
v_readlane_b32 s55, v219, s52
mm v[96:99],a[160:163],v[172:175],v[96:99]
dr a[100:103],v212 offset:9216
v_readlane_b32 s53, v217, s52
mm v[100:103],a[164:167],v[172:175],v[100:103]
dr a[104:107],v212 offset:10240
v_readlane_b32 s54, v218, s52
mm v[104:107],a[168:171],v[172:175],v[104:107]
)"
R"(sa s52,s52,1
dr a[108:111],v212 offset:11264
mm v[108:111],a[172:175],v[172:175],v[108:111]
dr a[112:115],v212 offset:12288
v_readlane_b32 s56, v216, s52
mm v[112:115],a[176:179],v[172:175],v[112:115]
sa s58,s60,s56
s_addc_u32 s59, s61, 0
dr a[116:119],v212 offset:13312
mm v[116:119],a[180:183],v[172:175],v[116:119]
dr a[120:123],v212 offset:14336
mm v[120:123],a[184:187],v[172:175],v[120:123]
s_waitcnt lgkmcnt(7)
dr a[124:127],v212 offset:15360
mm v[124:127],a[188:191],v[172:175],v[124:127]
dr a[160:163],v212 offset:24576
)"
R"(mm v[144:147],a[64:67],a[32:35],v[184:187]
dr a[164:167],v212 offset:25600
mm v[144:147],a[68:71],a[36:39],v[144:147]
mm v[144:147],a[72:75],a[40:43],v[144:147]
dr a[168:171],v212 offset:26624
mm v[144:147],a[76:79],a[44:47],v[144:147]
mm v[148:151],a[80:83],a[32:35],v[184:187]
dr a[172:175],v212 offset:27648
mm v[148:151],a[84:87],a[36:39],v[148:151]
mm v[148:151],a[88:91],a[40:43],v[148:151]
dr a[176:179],v212 offset:28672
mm v[148:151],a[92:95],a[44:47],v[148:151]
dr a[180:183],v212 offset:29696
mm v[152:155],a[64:67],a[48:51],v[188:191]
ex v144,v144
mm v[152:155],a[68:71],a[52:55],v[152:155]
)"
R"(dr a[184:187],v212 offset:30720
mm v[152:155],a[72:75],a[56:59],v[152:155]
ex v145,v145
mm v[152:155],a[76:79],a[60:63],v[152:155]
v_cndmask_b32 v148, v213, v148, s[88:89]
v_cndmask_b32 v149, v213, v149, s[90:91]
mm v[156:159],a[80:83],a[48:51],v[188:191]
v_cndmask_b32 v150, v213, v150, s[92:93]
v_cndmask_b32 v151, v213, v151, s[94:95]
mm v[156:159],a[84:87],a[52:55],v[156:159]
ex v146,v146
mm v[156:159],a[88:91],a[56:59],v[156:159]
ex v147,v147
mm v[156:159],a[92:95],a[60:63],v[156:159]
ex v148,v148
cv v168,v144,v145
)"
R"(cv v169,v146,v147
ex v149,v149
ex v150,v150
cv v170,v148,v149
ex v151,v151
ex v152,v152
cv v171,v150,v151
ex v153,v153
ex v154,v154
mm v[200:203],v[208:211],v[168:171],v[200:203]
dr a[188:191],v212 offset:31744
cv v172,v152,v153
mm v[64:67],a[128:131],v[168:171],v[64:67]
ex v155,v155
mm v[68:71],a[132:135],v[168:171],v[68:71]
ex v156,v156
)"
R"(mm v[72:75],a[136:139],v[168:171],v[72:75]
ex v157,v157
mm v[76:79],a[140:143],v[168:171],v[76:79]
ex v158,v158
mm v[80:83],a[144:147],v[168:171],v[80:83]
ex v159,v159
mm v[84:87],a[148:151],v[168:171],v[84:87]
cv v173,v154,v155
cv v174,v156,v157
mm v[88:91],a[152:155],v[168:171],v[88:91]
v_rcp_f32 v220, v192
v_rcp_f32 v222, v196
s_nop 0
v_pk_mul_f32 v[0:1], v[0:1], v[220:221] op_sel_hi:[1,0]
v_pk_mul_f32 v[2:3], v[2:3], v[220:221] op_sel_hi:[1,0]
v_pk_mul_f32 v[4:5], v[4:5], v[220:221] op_sel_hi:[1,0]
)"
R"(v_pk_mul_f32 v[6:7], v[6:7], v[220:221] op_sel_hi:[1,0]
cv v0,v0,v1
cv v1,v2,v3
cv v2,v4,v5
cv v3,v6,v7
v_pk_mul_f32 v[8:9], v[8:9], v[220:221] op_sel_hi:[1,0]
v_pk_mul_f32 v[10:11], v[10:11], v[220:221] op_sel_hi:[1,0]
v_pk_mul_f32 v[12:13], v[12:13], v[220:221] op_sel_hi:[1,0]
v_pk_mul_f32 v[14:15], v[14:15], v[220:221] op_sel_hi:[1,0]
cv v8,v8,v9
cv v9,v10,v11
cv v10,v12,v13
cv v11,v14,v15
v_pk_mul_f32 v[16:17], v[16:17], v[220:221] op_sel_hi:[1,0]
v_pk_mul_f32 v[18:19], v[18:19], v[220:221] op_sel_hi:[1,0]
v_pk_mul_f32 v[20:21], v[20:21], v[220:221] op_sel_hi:[1,0]
)"
R"(v_pk_mul_f32 v[22:23], v[22:23], v[220:221] op_sel_hi:[1,0]
cv v16,v16,v17
cv v17,v18,v19
cv v18,v20,v21
cv v19,v22,v23
v_pk_mul_f32 v[24:25], v[24:25], v[220:221] op_sel_hi:[1,0]
v_pk_mul_f32 v[26:27], v[26:27], v[220:221] op_sel_hi:[1,0]
v_pk_mul_f32 v[28:29], v[28:29], v[220:221] op_sel_hi:[1,0]
v_pk_mul_f32 v[30:31], v[30:31], v[220:221] op_sel_hi:[1,0]
cv v24,v24,v25
cv v25,v26,v27
cv v26,v28,v29
cv v27,v30,v31
v_pk_mul_f32 v[32:33], v[32:33], v[222:223] op_sel_hi:[1,0]
v_pk_mul_f32 v[34:35], v[34:35], v[222:223] op_sel_hi:[1,0]
v_pk_mul_f32 v[36:37], v[36:37], v[222:223] op_sel_hi:[1,0]
)"
R"(v_pk_mul_f32 v[38:39], v[38:39], v[222:223] op_sel_hi:[1,0]
cv v32,v32,v33
cv v33,v34,v35
cv v34,v36,v37
cv v35,v38,v39
v_pk_mul_f32 v[40:41], v[40:41], v[222:223] op_sel_hi:[1,0]
v_pk_mul_f32 v[42:43], v[42:43], v[222:223] op_sel_hi:[1,0]
v_pk_mul_f32 v[44:45], v[44:45], v[222:223] op_sel_hi:[1,0]
v_pk_mul_f32 v[46:47], v[46:47], v[222:223] op_sel_hi:[1,0]
cv v40,v40,v41
cv v41,v42,v43
cv v42,v44,v45
cv v43,v46,v47
v_pk_mul_f32 v[48:49], v[48:49], v[222:223] op_sel_hi:[1,0]
v_pk_mul_f32 v[50:51], v[50:51], v[222:223] op_sel_hi:[1,0]
v_pk_mul_f32 v[52:53], v[52:53], v[222:223] op_sel_hi:[1,0]
)"
R"(v_pk_mul_f32 v[54:55], v[54:55], v[222:223] op_sel_hi:[1,0]
cv v48,v48,v49
cv v49,v50,v51
cv v50,v52,v53
cv v51,v54,v55
v_pk_mul_f32 v[56:57], v[56:57], v[222:223] op_sel_hi:[1,0]
v_pk_mul_f32 v[58:59], v[58:59], v[222:223] op_sel_hi:[1,0]
v_pk_mul_f32 v[60:61], v[60:61], v[222:223] op_sel_hi:[1,0]
v_pk_mul_f32 v[62:63], v[62:63], v[222:223] op_sel_hi:[1,0]
cv v56,v56,v57
cv v57,v58,v59
cv v58,v60,v61
cv v59,v62,v63
s_lshl_b32 s56, s57, 5
sa s56,s56,s48
s_lshl_b32 s56, s56, 15
)"
R"(sa s56,s56,0x2100000
s_and_b32 s69, s50, 1
s_lshl_b32 s69, s69, 10
sa s68,s56,s69
s_sub_u32 s69, s56, s69
sa s56,s68,0x0
buffer_store_dwordx4 v[0:3], v214, s[44:47], s56 offen offset:0 sc1
sa s56,s68,0x2000
buffer_store_dwordx4 v[8:11], v214, s[44:47], s56 offen offset:0 sc1
sa s56,s68,0x4000
buffer_store_dwordx4 v[16:19], v214, s[44:47], s56 offen offset:0 sc1
sa s56,s68,0x6000
buffer_store_dwordx4 v[24:27], v214, s[44:47], s56 offen offset:0 sc1
sa s56,s69,0x0
buffer_store_dwordx4 v[32:35], v214, s[44:47], s56 offen offset:3072 sc1
sa s56,s69,0x2000
)"
R"(buffer_store_dwordx4 v[40:43], v214, s[44:47], s56 offen offset:3072 sc1
sa s56,s69,0x4000
buffer_store_dwordx4 v[48:51], v214, s[44:47], s56 offen offset:3072 sc1
sa s56,s69,0x6000
buffer_store_dwordx4 v[56:59], v214, s[44:47], s56 offen offset:3072 sc1
s_waitcnt vmcnt(8) lgkmcnt(0)
s_barrier
cv v175,v158,v159
mm v[92:95],a[156:159],v[168:171],v[92:95]
v_add_u32 v212, s55, v214
mm v[204:207],v[208:211],v[172:175],v[204:207]
dr a[64:67],v212 offset:0
mm v[96:99],a[128:131],v[172:175],v[96:99]
dr a[68:71],v212 offset:1024
mm v[100:103],a[132:135],v[172:175],v[100:103]
dr a[72:75],v212 offset:2048
)"
R"(mm v[104:107],a[136:139],v[172:175],v[104:107]
dr a[76:79],v212 offset:3072
mm v[108:111],a[140:143],v[172:175],v[108:111]
dr a[80:83],v212 offset:4096
mm v[112:115],a[144:147],v[172:175],v[112:115]
dr a[84:87],v212 offset:5120
mm v[116:119],a[148:151],v[172:175],v[116:119]
dr a[88:91],v212 offset:6144
mm v[120:123],a[152:155],v[172:175],v[120:123]
s_waitcnt lgkmcnt(7)
dr a[92:95],v212 offset:7168
mm v[124:127],a[156:159],v[172:175],v[124:127]
dr a[128:131],v212 offset:16384
mm v[152:155],a[96:99],a[48:51],v[188:191]
dr a[132:135],v212 offset:17408
v_mov_b32 v174, 0
)"
R"(mm v[152:155],a[100:103],a[52:55],v[152:155]
dr a[136:139],v212 offset:18432
v_mov_b32 v175, 0
mm v[152:155],a[104:107],a[56:59],v[152:155]
dr a[140:143],v212 offset:19456
mm v[152:155],a[108:111],a[60:63],v[152:155]
dr a[144:147],v212 offset:20480
dr a[148:151],v212 offset:21504
dr a[152:155],v212 offset:22528
dr a[156:159],v212 offset:23552
s_nop 3
v_cndmask_b32 v152, v213, v152, s[88:89]
v_cndmask_b32 v153, v213, v153, s[90:91]
v_cndmask_b32 v154, v213, v154, s[92:93]
v_cndmask_b32 v155, v213, v155, s[94:95]
ex v152,v152
)"
R"(ex v153,v153
ex v154,v154
ex v155,v155
cv v172,v152,v153
cv v173,v154,v155
sa m0,s54,0x0
sa s62,s53,0x0
bl v214,s[44:47],s62 offen sc1 lds
s_nop 7
s_nop 1
bl v214,s[44:47],s62 offen offset:1024 sc1 lds
mm v[204:207],v[208:211],v[172:175],v[204:207]
bl v214,s[44:47],s62 offen offset:2048 sc1 lds
mm v[96:99],a[160:163],v[172:175],v[96:99]
mm v[100:103],a[164:167],v[172:175],v[100:103]
bl v214,s[44:47],s62 offen offset:3072 sc1 lds
)"
R"(mm v[104:107],a[168:171],v[172:175],v[104:107]
sa m0,s54,0x1000
sa s62,s53,0x1000
bl v214,s[44:47],s62 offen sc1 lds
mm v[108:111],a[172:175],v[172:175],v[108:111]
bl v214,s[44:47],s62 offen offset:1024 sc1 lds
mm v[112:115],a[176:179],v[172:175],v[112:115]
mm v[116:119],a[180:183],v[172:175],v[116:119]
bl v214,s[44:47],s62 offen offset:2048 sc1 lds
mm v[120:123],a[184:187],v[172:175],v[120:123]
bl v214,s[44:47],s62 offen offset:3072 sc1 lds
mm v[124:127],a[188:191],v[172:175],v[124:127]
s_nop 7
v_mov_b32 v220, 0x5d800000
v_cmp_nlt_f32 vcc, v192, v220
v_cmp_nlt_f32 s[62:63], v196, v220
)"
R"(s_or_b64 vcc, vcc, s[62:63]
v_cmp_nlt_f32 s[62:63], v200, v220
s_or_b64 vcc, vcc, s[62:63]
v_cmp_nlt_f32 s[62:63], v204, v220
s_or_b64 vcc, vcc, s[62:63]
s_cbranch_vccnz L_redo_seg1_16
L_ok_seg1_16:
s_and_b32 s56, s50, 1
s_lshl_b32 s56, s56, 14
sa s56,s56,0x20000
v_add_u32 v230, s56, v214
v_rcp_f32 v224, v200
v_rcp_f32 v226, v204
s_nop 0
sa m0,s54,0x2000
sa s62,s53,0x2000
)"
R"(bl v214,s[44:47],s62 offen sc1 lds
dr a[0:3],v230 offset:0
v_pk_mul_f32 v[64:65], v[64:65], v[224:225] op_sel_hi:[1,0]
v_pk_mul_f32 v[66:67], v[66:67], v[224:225] op_sel_hi:[1,0]
v_pk_mul_f32 v[68:69], v[68:69], v[224:225] op_sel_hi:[1,0]
v_pk_mul_f32 v[70:71], v[70:71], v[224:225] op_sel_hi:[1,0]
dr a[4:7],v230 offset:1024
cv v64,v64,v65
cv v65,v66,v67
cv v66,v68,v69
cv v67,v70,v71
bl v214,s[44:47],s62 offen offset:1024 sc1 lds
dr a[8:11],v230 offset:2048
v_pk_mul_f32 v[72:73], v[72:73], v[224:225] op_sel_hi:[1,0]
v_pk_mul_f32 v[74:75], v[74:75], v[224:225] op_sel_hi:[1,0]
v_pk_mul_f32 v[76:77], v[76:77], v[224:225] op_sel_hi:[1,0]
)"
R"(v_pk_mul_f32 v[78:79], v[78:79], v[224:225] op_sel_hi:[1,0]
dr a[12:15],v230 offset:3072
cv v72,v72,v73
cv v73,v74,v75
cv v74,v76,v77
cv v75,v78,v79
bl v214,s[44:47],s62 offen offset:2048 sc1 lds
dr a[16:19],v230 offset:4096
v_pk_mul_f32 v[80:81], v[80:81], v[224:225] op_sel_hi:[1,0]
v_pk_mul_f32 v[82:83], v[82:83], v[224:225] op_sel_hi:[1,0]
v_pk_mul_f32 v[84:85], v[84:85], v[224:225] op_sel_hi:[1,0]
v_pk_mul_f32 v[86:87], v[86:87], v[224:225] op_sel_hi:[1,0]
dr a[20:23],v230 offset:5120
cv v80,v80,v81
cv v81,v82,v83
cv v82,v84,v85
)"
R"(cv v83,v86,v87
bl v214,s[44:47],s62 offen offset:3072 sc1 lds
dr a[24:27],v230 offset:6144
v_pk_mul_f32 v[88:89], v[88:89], v[224:225] op_sel_hi:[1,0]
v_pk_mul_f32 v[90:91], v[90:91], v[224:225] op_sel_hi:[1,0]
v_pk_mul_f32 v[92:93], v[92:93], v[224:225] op_sel_hi:[1,0]
v_pk_mul_f32 v[94:95], v[94:95], v[224:225] op_sel_hi:[1,0]
dr a[28:31],v230 offset:7168
cv v88,v88,v89
cv v89,v90,v91
cv v90,v92,v93
cv v91,v94,v95
sa m0,s54,0x3000
sa s62,s53,0x3000
bl v214,s[44:47],s62 offen sc1 lds
dr a[32:35],v230 offset:8192
)"
R"(v_pk_mul_f32 v[96:97], v[96:97], v[226:227] op_sel_hi:[1,0]
v_pk_mul_f32 v[98:99], v[98:99], v[226:227] op_sel_hi:[1,0]
v_pk_mul_f32 v[100:101], v[100:101], v[226:227] op_sel_hi:[1,0]
v_pk_mul_f32 v[102:103], v[102:103], v[226:227] op_sel_hi:[1,0]
dr a[36:39],v230 offset:9216
cv v96,v96,v97
cv v97,v98,v99
cv v98,v100,v101
cv v99,v102,v103
bl v214,s[44:47],s62 offen offset:1024 sc1 lds
dr a[40:43],v230 offset:10240
v_pk_mul_f32 v[104:105], v[104:105], v[226:227] op_sel_hi:[1,0]
v_pk_mul_f32 v[106:107], v[106:107], v[226:227] op_sel_hi:[1,0]
v_pk_mul_f32 v[108:109], v[108:109], v[226:227] op_sel_hi:[1,0]
v_pk_mul_f32 v[110:111], v[110:111], v[226:227] op_sel_hi:[1,0]
dr a[44:47],v230 offset:11264
)"
R"(cv v104,v104,v105
cv v105,v106,v107
cv v106,v108,v109
cv v107,v110,v111
bl v214,s[44:47],s62 offen offset:2048 sc1 lds
dr a[48:51],v230 offset:12288
v_pk_mul_f32 v[112:113], v[112:113], v[226:227] op_sel_hi:[1,0]
v_pk_mul_f32 v[114:115], v[114:115], v[226:227] op_sel_hi:[1,0]
v_pk_mul_f32 v[116:117], v[116:117], v[226:227] op_sel_hi:[1,0]
v_pk_mul_f32 v[118:119], v[118:119], v[226:227] op_sel_hi:[1,0]
dr a[52:55],v230 offset:13312
cv v112,v112,v113
cv v113,v114,v115
cv v114,v116,v117
cv v115,v118,v119
bl v214,s[44:47],s62 offen offset:3072 sc1 lds
)"
R"(dr a[56:59],v230 offset:14336
v_pk_mul_f32 v[120:121], v[120:121], v[226:227] op_sel_hi:[1,0]
v_pk_mul_f32 v[122:123], v[122:123], v[226:227] op_sel_hi:[1,0]
v_pk_mul_f32 v[124:125], v[124:125], v[226:227] op_sel_hi:[1,0]
v_pk_mul_f32 v[126:127], v[126:127], v[226:227] op_sel_hi:[1,0]
dr a[60:63],v230 offset:15360
cv v120,v120,v121
cv v121,v122,v123
cv v122,v124,v125
cv v123,v126,v127
sa s56,s68,0x1000
buffer_store_dwordx4 v[64:67], v214, s[44:47], s56 offen offset:0 sc1
sa s56,s68,0x3000
buffer_store_dwordx4 v[72:75], v214, s[44:47], s56 offen offset:0 sc1
sa s56,s68,0x5000
buffer_store_dwordx4 v[80:83], v214, s[44:47], s56 offen offset:0 sc1
)"
R"(sa s56,s68,0x7000
buffer_store_dwordx4 v[88:91], v214, s[44:47], s56 offen offset:0 sc1
sa s56,s69,0x1000
buffer_store_dwordx4 v[96:99], v214, s[44:47], s56 offen offset:3072 sc1
sa s56,s69,0x3000
buffer_store_dwordx4 v[104:107], v214, s[44:47], s56 offen offset:3072 sc1
sa s56,s69,0x5000
buffer_store_dwordx4 v[112:115], v214, s[44:47], s56 offen offset:3072 sc1
sa s56,s69,0x7000
buffer_store_dwordx4 v[120:123], v214, s[44:47], s56 offen offset:3072 sc1
s_sub_u32 s57, 15, s49
s_waitcnt lgkmcnt(0)
s_setpc_b64 s[58:59]
.p2align 12
L_att_firstb16:
mm v[128:131],a[64:67],a[0:3],0
)"
R"(dr a[96:99],v212 offset:8192
v_readlane_b32 s55, v219, s52
mm v[128:131],a[68:71],a[4:7],v[128:131]
dr a[100:103],v212 offset:9216
v_readlane_b32 s53, v217, s52
mm v[128:131],a[72:75],a[8:11],v[128:131]
dr a[104:107],v212 offset:10240
v_readlane_b32 s54, v218, s52
mm v[128:131],a[76:79],a[12:15],v[128:131]
sa s52,s52,1
dr a[108:111],v212 offset:11264
mm v[132:135],a[80:83],a[0:3],0
dr a[112:115],v212 offset:12288
v_readlane_b32 s56, v216, s52
mm v[132:135],a[84:87],a[4:7],v[132:135]
sa s58,s60,s56
)"
R"(s_addc_u32 s59, s61, 0
dr a[116:119],v212 offset:13312
mm v[132:135],a[88:91],a[8:11],v[132:135]
dr a[120:123],v212 offset:14336
v_max3_f32 v220, v128, v129, v130
mm v[132:135],a[92:95],a[12:15],v[132:135]
dr a[124:127],v212 offset:15360
mm v[136:139],a[64:67],a[16:19],0
dr a[160:163],v212 offset:24576
mm v[136:139],a[68:71],a[20:23],v[136:139]
dr a[164:167],v212 offset:25600
mm v[136:139],a[72:75],a[24:27],v[136:139]
mm v[136:139],a[76:79],a[28:31],v[136:139]
dr a[168:171],v212 offset:26624
mm v[140:143],a[80:83],a[16:19],0
v_max3_f32 v220, v220, v131, v132
)"
R"(v_max3_f32 v220, v220, v133, v134
mm v[140:143],a[84:87],a[20:23],v[140:143]
dr a[172:175],v212 offset:27648
v_max_f32 v220, v220, v135
mm v[140:143],a[88:91],a[24:27],v[140:143]
v_mov_b32 v221, v220
v_max3_f32 v222, v136, v137, v138
mm v[140:143],a[92:95],a[28:31],v[140:143]
dr a[176:179],v212 offset:28672
mm v[144:147],a[64:67],a[32:35],0
dr a[180:183],v212 offset:29696
mm v[144:147],a[68:71],a[36:39],v[144:147]
v_permlane32_swap_b32 v220, v221
mm v[144:147],a[72:75],a[40:43],v[144:147]
dr a[184:187],v212 offset:30720
v_max_f32 v220, v220, v221
)"
R"(mm v[144:147],a[76:79],a[44:47],v[144:147]
v_mov_b32 v221, v220
v_max3_f32 v222, v222, v139, v140
mm v[148:151],a[80:83],a[32:35],0
dr a[188:191],v212 offset:31744
v_max3_f32 v222, v222, v141, v142
mm v[148:151],a[84:87],a[36:39],v[148:151]
v_permlane16_swap_b32 v220, v221
mm v[148:151],a[88:91],a[40:43],v[148:151]
v_max_f32 v220, v220, v221
v_sub_f32 v176, 0xc2c80000, v220
mm v[148:151],a[92:95],a[44:47],v[148:151]
v_max_f32 v222, v222, v143
v_add_f32 v128, v128, v176
mm v[152:155],a[64:67],a[48:51],0
v_add_f32 v129, v129, v176
)"
R"(v_add_f32 v130, v130, v176
mm v[152:155],a[68:71],a[52:55],v[152:155]
v_add_f32 v131, v131, v176
v_add_f32 v132, v132, v176
mm v[152:155],a[72:75],a[56:59],v[152:155]
v_add_f32 v133, v133, v176
v_add_f32 v134, v134, v176
mm v[152:155],a[76:79],a[60:63],v[152:155]
v_add_f32 v135, v135, v176
v_mov_b32 v223, v222
mm v[156:159],a[80:83],a[48:51],0
ex v128,v128
mm v[156:159],a[84:87],a[52:55],v[156:159]
ex v129,v129
mm v[156:159],a[88:91],a[56:59],v[156:159]
s_waitcnt lgkmcnt(15)
)"
R"(ex v130,v130
mm v[156:159],a[92:95],a[60:63],v[156:159]
ex v131,v131
cv v160,v128,v129
cv v161,v130,v131
ex v132,v132
ex v133,v133
ex v134,v134
cv v162,v132,v133
ex v135,v135
v_permlane32_swap_b32 v222, v223
cv v163,v134,v135
v_max_f32 v222, v222, v223
v_mov_b32 v223, v222
mm v[192:195],v[208:211],v[160:163],0
s_waitcnt vmcnt(8) lgkmcnt(0)
)"
R"(s_barrier
v_add_u32 v212, s55, v214
mm v[0:3],a[128:131],v[160:163],0
v_permlane16_swap_b32 v222, v223
mm v[4:7],a[132:135],v[160:163],0
v_max_f32 v222, v222, v223
v_sub_f32 v180, 0xc2c80000, v222
mm v[8:11],a[136:139],v[160:163],0
dr a[64:67],v212 offset:0
v_add_f32 v136, v136, v180
mm v[12:15],a[140:143],v[160:163],0
v_add_f32 v137, v137, v180
v_add_f32 v138, v138, v180
mm v[16:19],a[144:147],v[160:163],0
v_add_f32 v139, v139, v180
v_add_f32 v140, v140, v180
)"
R"(mm v[20:23],a[148:151],v[160:163],0
v_add_f32 v141, v141, v180
v_add_f32 v142, v142, v180
mm v[24:27],a[152:155],v[160:163],0
sa m0,s54,0x0
sa s62,s53,0x0
v_add_f32 v143, v143, v180
mm v[28:31],a[156:159],v[160:163],0
ex v136,v136
ex v137,v137
ex v138,v138
cv v164,v136,v137
ex v139,v139
ex v140,v140
cv v165,v138,v139
ex v141,v141
)"
R"(ex v142,v142
cv v166,v140,v141
ex v143,v143
v_max3_f32 v224, v144, v145, v146
cv v167,v142,v143
v_max3_f32 v224, v224, v147, v148
v_max3_f32 v224, v224, v149, v150
mm v[196:199],v[208:211],v[164:167],0
bl v214,s[44:47],s62 offen sc1 lds
mm v[32:35],a[128:131],v[164:167],0
dr a[68:71],v212 offset:1024
v_max_f32 v224, v224, v151
mm v[36:39],a[132:135],v[164:167],0
bl v214,s[44:47],s62 offen offset:1024 sc1 lds
mm v[40:43],a[136:139],v[164:167],0
v_mov_b32 v225, v224
)"
R"(v_sub_f32 v177, 0xc2c80000, v220
mm v[44:47],a[140:143],v[164:167],0
v_permlane32_swap_b32 v224, v225
mm v[48:51],a[144:147],v[164:167],0
v_sub_f32 v178, 0xc2c80000, v220
v_sub_f32 v179, 0xc2c80000, v220
mm v[52:55],a[148:151],v[164:167],0
dr a[72:75],v212 offset:2048
v_max_f32 v224, v224, v225
mm v[56:59],a[152:155],v[164:167],0
v_mov_b32 v225, v224
v_max3_f32 v226, v152, v153, v154
mm v[60:63],a[156:159],v[164:167],0
bl v214,s[44:47],s62 offen offset:2048 sc1 lds
mm v[128:131],a[96:99],a[0:3],v[176:179]
v_permlane16_swap_b32 v224, v225
)"
R"(mm v[128:131],a[100:103],a[4:7],v[128:131]
v_max_f32 v224, v224, v225
v_sub_f32 v184, 0xc2c80000, v224
mm v[128:131],a[104:107],a[8:11],v[128:131]
v_add_f32 v144, v144, v184
v_add_f32 v145, v145, v184
mm v[128:131],a[108:111],a[12:15],v[128:131]
v_add_f32 v146, v146, v184
v_add_f32 v147, v147, v184
mm v[132:135],a[112:115],a[0:3],v[176:179]
v_add_f32 v148, v148, v184
v_add_f32 v149, v149, v184
mm v[132:135],a[116:119],a[4:7],v[132:135]
v_add_f32 v150, v150, v184
v_add_f32 v151, v151, v184
mm v[132:135],a[120:123],a[8:11],v[132:135]
)"
R"(dr a[76:79],v212 offset:3072
v_max3_f32 v226, v226, v155, v156
mm v[132:135],a[124:127],a[12:15],v[132:135]
ex v144,v144
ex v145,v145
ex v146,v146
cv v168,v144,v145
ex v147,v147
ex v148,v148
cv v169,v146,v147
ex v149,v149
ex v150,v150
cv v170,v148,v149
ex v151,v151
v_max3_f32 v226, v226, v157, v158
cv v171,v150,v151
)"
R"(v_max_f32 v226, v226, v159
v_mov_b32 v227, v226
mm v[200:203],v[208:211],v[168:171],0
bl v214,s[44:47],s62 offen offset:3072 sc1 lds
mm v[64:67],a[128:131],v[168:171],0
sa m0,s54,0x1000
sa s62,s53,0x1000
dr a[80:83],v212 offset:4096
mm v[68:71],a[132:135],v[168:171],0
bl v214,s[44:47],s62 offen sc1 lds
mm v[72:75],a[136:139],v[168:171],0
v_permlane32_swap_b32 v226, v227
mm v[76:79],a[140:143],v[168:171],0
v_sub_f32 v181, 0xc2c80000, v222
v_sub_f32 v182, 0xc2c80000, v222
mm v[80:83],a[144:147],v[168:171],0
)"
R"(v_sub_f32 v183, 0xc2c80000, v222
v_max_f32 v226, v226, v227
mm v[84:87],a[148:151],v[168:171],0
dr a[84:87],v212 offset:5120
v_mov_b32 v227, v226
mm v[88:91],a[152:155],v[168:171],0
bl v214,s[44:47],s62 offen offset:1024 sc1 lds
mm v[92:95],a[156:159],v[168:171],0
v_permlane16_swap_b32 v226, v227
mm v[136:139],a[96:99],a[16:19],v[180:183]
v_max_f32 v226, v226, v227
v_sub_f32 v188, 0xc2c80000, v226
mm v[136:139],a[100:103],a[20:23],v[136:139]
v_add_f32 v152, v152, v188
v_add_f32 v153, v153, v188
mm v[136:139],a[104:107],a[24:27],v[136:139]
)"
R"(v_add_f32 v154, v154, v188
v_add_f32 v155, v155, v188
mm v[136:139],a[108:111],a[28:31],v[136:139]
v_add_f32 v156, v156, v188
v_add_f32 v157, v157, v188
mm v[140:143],a[112:115],a[16:19],v[180:183]
v_add_f32 v158, v158, v188
v_add_f32 v159, v159, v188
mm v[140:143],a[116:119],a[20:23],v[140:143]
ex v152,v152
mm v[140:143],a[120:123],a[24:27],v[140:143]
ex v153,v153
mm v[140:143],a[124:127],a[28:31],v[140:143]
ex v154,v154
cv v172,v152,v153
ex v155,v155
)"
R"(ex v156,v156
cv v173,v154,v155
ex v157,v157
ex v158,v158
cv v174,v156,v157
ex v159,v159
v_sub_f32 v185, 0xc2c80000, v224
cv v175,v158,v159
v_sub_f32 v186, 0xc2c80000, v224
v_sub_f32 v187, 0xc2c80000, v224
mm v[204:207],v[208:211],v[172:175],0
bl v214,s[44:47],s62 offen offset:2048 sc1 lds
mm v[96:99],a[128:131],v[172:175],0
dr a[88:91],v212 offset:6144
v_sub_f32 v189, 0xc2c80000, v226
mm v[100:103],a[132:135],v[172:175],0
)"
R"(bl v214,s[44:47],s62 offen offset:3072 sc1 lds
mm v[104:107],a[136:139],v[172:175],0
dr a[92:95],v212 offset:7168
v_sub_f32 v190, 0xc2c80000, v226
mm v[108:111],a[140:143],v[172:175],0
sa m0,s54,0x2000
sa s62,s53,0x2000
v_sub_f32 v191, 0xc2c80000, v226
mm v[112:115],a[144:147],v[172:175],0
bl v214,s[44:47],s62 offen sc1 lds
mm v[116:119],a[148:151],v[172:175],0
ex v128,v128
mm v[120:123],a[152:155],v[172:175],0
ex v129,v129
mm v[124:127],a[156:159],v[172:175],0
dr a[128:131],v212 offset:16384
)"
R"(cv v160,v128,v129
mm v[144:147],a[96:99],a[32:35],v[184:187]
ex v130,v130
mm v[144:147],a[100:103],a[36:39],v[144:147]
ex v131,v131
mm v[144:147],a[104:107],a[40:43],v[144:147]
ex v132,v132
mm v[144:147],a[108:111],a[44:47],v[144:147]
ex v133,v133
mm v[148:151],a[112:115],a[32:35],v[184:187]
ex v134,v134
mm v[148:151],a[116:119],a[36:39],v[148:151]
ex v135,v135
mm v[148:151],a[120:123],a[40:43],v[148:151]
s_waitcnt lgkmcnt(9)
cv v161,v130,v131
)"
R"(cv v162,v132,v133
mm v[148:151],a[124:127],a[44:47],v[148:151]
dr a[132:135],v212 offset:17408
cv v163,v134,v135
ex v136,v136
ex v137,v137
mm v[192:195],v[208:211],v[160:163],v[192:195]
bl v214,s[44:47],s62 offen offset:1024 sc1 lds
mm v[0:3],a[160:163],v[160:163],v[0:3]
dr a[136:139],v212 offset:18432
cv v164,v136,v137
mm v[4:7],a[164:167],v[160:163],v[4:7]
bl v214,s[44:47],s62 offen offset:2048 sc1 lds
mm v[8:11],a[168:171],v[160:163],v[8:11]
ex v138,v138
mm v[12:15],a[172:175],v[160:163],v[12:15]
)"
R"(ex v139,v139
mm v[16:19],a[176:179],v[160:163],v[16:19]
bl v214,s[44:47],s62 offen offset:3072 sc1 lds
mm v[20:23],a[180:183],v[160:163],v[20:23]
ex v140,v140
mm v[24:27],a[184:187],v[160:163],v[24:27]
dr a[140:143],v212 offset:19456
cv v165,v138,v139
mm v[28:31],a[188:191],v[160:163],v[28:31]
ex v141,v141
mm v[152:155],a[96:99],a[48:51],v[188:191]
ex v142,v142
mm v[152:155],a[100:103],a[52:55],v[152:155]
ex v143,v143
mm v[152:155],a[104:107],a[56:59],v[152:155]
sa m0,s54,0x3000
)"
R"(sa s62,s53,0x3000
cv v166,v140,v141
mm v[152:155],a[108:111],a[60:63],v[152:155]
dr a[144:147],v212 offset:20480
cv v167,v142,v143
mm v[156:159],a[112:115],a[48:51],v[188:191]
bl v214,s[44:47],s62 offen sc1 lds
mm v[156:159],a[116:119],a[52:55],v[156:159]
ex v144,v144
mm v[156:159],a[120:123],a[56:59],v[156:159]
ex v145,v145
mm v[156:159],a[124:127],a[60:63],v[156:159]
ex v146,v146
mm v[196:199],v[208:211],v[164:167],v[196:199]
dr a[148:151],v212 offset:21504
cv v168,v144,v145
)"
R"(mm v[32:35],a[160:163],v[164:167],v[32:35]
bl v214,s[44:47],s62 offen offset:1024 sc1 lds
mm v[36:39],a[164:167],v[164:167],v[36:39]
ex v147,v147
mm v[40:43],a[168:171],v[164:167],v[40:43]
ex v148,v148
mm v[44:47],a[172:175],v[164:167],v[44:47]
ex v149,v149
mm v[48:51],a[176:179],v[164:167],v[48:51]
ex v150,v150
mm v[52:55],a[180:183],v[164:167],v[52:55]
ex v151,v151
mm v[56:59],a[184:187],v[164:167],v[56:59]
cv v169,v146,v147
cv v170,v148,v149
mm v[60:63],a[188:191],v[164:167],v[60:63]
)"
R"(dr a[152:155],v212 offset:22528
cv v171,v150,v151
ex v152,v152
ex v153,v153
mm v[200:203],v[208:211],v[168:171],v[200:203]
bl v214,s[44:47],s62 offen offset:2048 sc1 lds
mm v[64:67],a[160:163],v[168:171],v[64:67]
ex v154,v154
mm v[68:71],a[164:167],v[168:171],v[68:71]
dr a[156:159],v212 offset:23552
cv v172,v152,v153
mm v[72:75],a[168:171],v[168:171],v[72:75]
bl v214,s[44:47],s62 offen offset:3072 sc1 lds
mm v[76:79],a[172:175],v[168:171],v[76:79]
ex v155,v155
mm v[80:83],a[176:179],v[168:171],v[80:83]
)"
R"(cv v173,v154,v155
mm v[84:87],a[180:183],v[168:171],v[84:87]
mm v[88:91],a[184:187],v[168:171],v[88:91]
mm v[92:95],a[188:191],v[168:171],v[92:95]
s_waitcnt lgkmcnt(8)
s_setpc_b64 s[58:59]
.p2align 12
L_att_seg0_0:
ex v156,v156
ex v157,v157
ex v158,v158
cv v174,v156,v157
ex v159,v159
s_nop 0
cv v175,v158,v159
s_nop 1
)"
R"(mm v[204:207],v[208:211],v[172:175],v[204:207]
dr a[96:99],v212 offset:8192
v_lshlrev_b32 v235, 3, v214
mm v[96:99],a[160:163],v[172:175],v[96:99]
s_lshl_b32 s62, s51, 2
dr a[100:103],v212 offset:9216
v_readlane_b32 s55, v219, s52
mm v[100:103],a[164:167],v[172:175],v[100:103]
sa s62,s62,s50
sa s52,s52,1
dr a[104:107],v212 offset:10240
mm v[104:107],a[168:171],v[172:175],v[104:107]
s_mul_i32 s63, s62, 4096
dr a[108:111],v212 offset:11264
v_readlane_b32 s56, v216, s52
mm v[108:111],a[172:175],v[172:175],v[108:111]
)"
R"(sa s36,s82,s63
s_addc_u32 s37, s83, 0
dr a[112:115],v212 offset:12288
mm v[112:115],a[176:179],v[172:175],v[112:115]
s_and_b32 s37, s37, 0xffff
dr a[116:119],v212 offset:13312
mm v[116:119],a[180:183],v[172:175],v[116:119]
s_mov_b32 s38, 4096
dr a[120:123],v212 offset:14336
mm v[120:123],a[184:187],v[172:175],v[120:123]
s_mov_b32 s39, 0x20000
s_waitcnt lgkmcnt(7)
dr a[124:127],v212 offset:15360
mm v[124:127],a[188:191],v[172:175],v[124:127]
s_mul_i32 s63, s62, 512
dr a[160:163],v212 offset:24576
)"
R"(mm v[144:147],a[64:67],a[32:35],v[184:187]
sa s64,s70,s63
s_addc_u32 s65, s71, 0
dr a[164:167],v212 offset:25600
mm v[144:147],a[68:71],a[36:39],v[144:147]
s_and_b32 s65, s65, 0xffff
s_bfe_u32 s80, s51, 0x20001
v_mov_b32 v170, 0
mm v[144:147],a[72:75],a[40:43],v[144:147]
s_mov_b32 s66, 512
s_bfe_u32 s79, s51, 0x20003
dr a[168:171],v212 offset:26624
mm v[144:147],a[76:79],a[44:47],v[144:147]
s_mov_b32 s67, 0x20000
s_lshr_b32 s78, s51, 5
v_mov_b32 v171, 0
)"
R"(mm v[152:155],a[64:67],a[48:51],v[188:191]
s_sub_u32 s81, 15, s78
buffer_load_dword v233, v235, s[36:39], 0 offen
mm v[152:155],a[68:71],a[52:55],v[152:155]
buffer_load_dword v234, v235, s[64:67], 0 offen
mm v[152:155],a[72:75],a[56:59],v[152:155]
dr a[172:175],v212 offset:27648
v_cndmask_b32 v144, v213, v144, s[88:89]
mm v[152:155],a[76:79],a[60:63],v[152:155]
v_cndmask_b32 v145, v213, v145, s[90:91]
v_cndmask_b32 v146, v213, v146, s[92:93]
mm v[156:159],a[80:83],a[48:51],v[188:191]
dr a[176:179],v212 offset:28672
v_cndmask_b32 v147, v213, v147, s[94:95]
mm v[156:159],a[84:87],a[52:55],v[156:159]
ex v144,v144
)"
R"(mm v[156:159],a[88:91],a[56:59],v[156:159]
ex v145,v145
mm v[156:159],a[92:95],a[60:63],v[156:159]
ex v146,v146
cv v168,v144,v145
ex v147,v147
ex v152,v152
cv v169,v146,v147
ex v153,v153
ex v154,v154
mm v[200:203],v[208:211],v[168:171],v[200:203]
s_bitcmp1_b32 s51, 0
s_cselect_b32 s78, s78, s81
dr a[180:183],v212 offset:29696
mm v[64:67],a[128:131],v[168:171],v[64:67]
s_lshl_b32 s78, s78, 2
)"
R"(dr a[184:187],v212 offset:30720
cv v172,v152,v153
mm v[68:71],a[132:135],v[168:171],v[68:71]
sa s78,s78,s79
dr a[188:191],v212 offset:31744
mm v[72:75],a[136:139],v[168:171],v[72:75]
s_lshl_b32 s78, s78, 2
ex v155,v155
mm v[76:79],a[140:143],v[168:171],v[76:79]
sa s78,s78,s80
ex v156,v156
mm v[80:83],a[144:147],v[168:171],v[80:83]
s_lshl_b32 s78, s78, 2
ex v157,v157
mm v[84:87],a[148:151],v[168:171],v[84:87]
sa s78,s78,s50
)"
R"(ex v158,v158
mm v[88:91],a[152:155],v[168:171],v[88:91]
s_lshl_b32 s78, s78, 13
ex v159,v159
mm v[92:95],a[156:159],v[168:171],v[92:95]
sa s78,s78,0x3400000
cv v173,v154,v155
cv v174,v156,v157
cv v175,v158,v159
v_add_u32 v212, s55, v214
s_nop 0
mm v[204:207],v[208:211],v[172:175],v[204:207]
sa s79,s78,0x1000
sa s58,s60,s56
s_addc_u32 s59, s61, 0
mm v[96:99],a[128:131],v[172:175],v[96:99]
)"
R"(v_accvgpr_write_b32 a216, -1
v_accvgpr_write_b32 a217, -1
mm v[100:103],a[132:135],v[172:175],v[100:103]
v_rcp_f32 v220, v192
v_rcp_f32 v222, v196
s_nop 0
v_pk_mul_f32 v[0:1], v[0:1], v[220:221] op_sel_hi:[1,0]
v_pk_mul_f32 v[2:3], v[2:3], v[220:221] op_sel_hi:[1,0]
v_pk_mul_f32 v[4:5], v[4:5], v[220:221] op_sel_hi:[1,0]
v_pk_mul_f32 v[6:7], v[6:7], v[220:221] op_sel_hi:[1,0]
cv v0,v0,v1
cv v1,v2,v3
cv v2,v4,v5
cv v3,v6,v7
v_pk_mul_f32 v[8:9], v[8:9], v[220:221] op_sel_hi:[1,0]
v_pk_mul_f32 v[10:11], v[10:11], v[220:221] op_sel_hi:[1,0]
)"
R"(v_pk_mul_f32 v[12:13], v[12:13], v[220:221] op_sel_hi:[1,0]
v_pk_mul_f32 v[14:15], v[14:15], v[220:221] op_sel_hi:[1,0]
cv v8,v8,v9
cv v9,v10,v11
cv v10,v12,v13
cv v11,v14,v15
v_pk_mul_f32 v[16:17], v[16:17], v[220:221] op_sel_hi:[1,0]
v_pk_mul_f32 v[18:19], v[18:19], v[220:221] op_sel_hi:[1,0]
v_pk_mul_f32 v[20:21], v[20:21], v[220:221] op_sel_hi:[1,0]
v_pk_mul_f32 v[22:23], v[22:23], v[220:221] op_sel_hi:[1,0]
cv v16,v16,v17
cv v17,v18,v19
cv v18,v20,v21
cv v19,v22,v23
v_pk_mul_f32 v[24:25], v[24:25], v[220:221] op_sel_hi:[1,0]
v_pk_mul_f32 v[26:27], v[26:27], v[220:221] op_sel_hi:[1,0]
)"
R"(v_pk_mul_f32 v[28:29], v[28:29], v[220:221] op_sel_hi:[1,0]
v_pk_mul_f32 v[30:31], v[30:31], v[220:221] op_sel_hi:[1,0]
cv v24,v24,v25
cv v25,v26,v27
cv v26,v28,v29
cv v27,v30,v31
v_pk_mul_f32 v[32:33], v[32:33], v[222:223] op_sel_hi:[1,0]
v_pk_mul_f32 v[34:35], v[34:35], v[222:223] op_sel_hi:[1,0]
v_pk_mul_f32 v[36:37], v[36:37], v[222:223] op_sel_hi:[1,0]
v_pk_mul_f32 v[38:39], v[38:39], v[222:223] op_sel_hi:[1,0]
cv v32,v32,v33
cv v33,v34,v35
cv v34,v36,v37
cv v35,v38,v39
v_pk_mul_f32 v[40:41], v[40:41], v[222:223] op_sel_hi:[1,0]
v_pk_mul_f32 v[42:43], v[42:43], v[222:223] op_sel_hi:[1,0]
)"
R"(v_pk_mul_f32 v[44:45], v[44:45], v[222:223] op_sel_hi:[1,0]
v_pk_mul_f32 v[46:47], v[46:47], v[222:223] op_sel_hi:[1,0]
cv v40,v40,v41
cv v41,v42,v43
cv v42,v44,v45
cv v43,v46,v47
v_pk_mul_f32 v[48:49], v[48:49], v[222:223] op_sel_hi:[1,0]
v_pk_mul_f32 v[50:51], v[50:51], v[222:223] op_sel_hi:[1,0]
v_pk_mul_f32 v[52:53], v[52:53], v[222:223] op_sel_hi:[1,0]
v_pk_mul_f32 v[54:55], v[54:55], v[222:223] op_sel_hi:[1,0]
cv v48,v48,v49
cv v49,v50,v51
cv v50,v52,v53
cv v51,v54,v55
v_pk_mul_f32 v[56:57], v[56:57], v[222:223] op_sel_hi:[1,0]
v_pk_mul_f32 v[58:59], v[58:59], v[222:223] op_sel_hi:[1,0]
)"
R"(v_pk_mul_f32 v[60:61], v[60:61], v[222:223] op_sel_hi:[1,0]
v_pk_mul_f32 v[62:63], v[62:63], v[222:223] op_sel_hi:[1,0]
cv v56,v56,v57
cv v57,v58,v59
cv v58,v60,v61
cv v59,v62,v63
s_lshl_b32 s56, s57, 5
sa s56,s56,s48
s_lshl_b32 s56, s56, 15
sa s56,s56,0x2100000
s_and_b32 s69, s50, 1
s_lshl_b32 s69, s69, 10
sa s68,s56,s69
s_sub_u32 s69, s56, s69
sa s56,s68,0x0
buffer_store_dwordx4 v[0:3], v214, s[44:47], s56 offen offset:0 sc1
)"
R"(sa s56,s68,0x2000
buffer_store_dwordx4 v[8:11], v214, s[44:47], s56 offen offset:0 sc1
sa s56,s68,0x4000
buffer_store_dwordx4 v[16:19], v214, s[44:47], s56 offen offset:0 sc1
sa s56,s68,0x6000
buffer_store_dwordx4 v[24:27], v214, s[44:47], s56 offen offset:0 sc1
sa s56,s69,0x0
buffer_store_dwordx4 v[32:35], v214, s[44:47], s56 offen offset:3072 sc1
sa s56,s69,0x2000
buffer_store_dwordx4 v[40:43], v214, s[44:47], s56 offen offset:3072 sc1
sa s56,s69,0x4000
buffer_store_dwordx4 v[48:51], v214, s[44:47], s56 offen offset:3072 sc1
sa s56,s69,0x6000
buffer_store_dwordx4 v[56:59], v214, s[44:47], s56 offen offset:3072 sc1
s_waitcnt vmcnt(10) lgkmcnt(0)
s_barrier
)"
R"(s_lshl_b32 s72, s49, 7
s_lshl_b32 s73, s48, 2
sa s72,s72,s73
sa s72,s72,0x12000
v_mov_b32 v228, 1
v_mov_b32 v229, 0
s_mov_b64 exec, 1
buffer_store_dword v228, v229, s[44:47], s72 offen sc1
s_mov_b64 exec, -1
mm v[104:107],a[136:139],v[172:175],v[104:107]
v_accvgpr_write_b32 a218, -1
v_accvgpr_write_b32 a219, -1
mm v[108:111],a[140:143],v[172:175],v[108:111]
dr a[64:67],v212 offset:0
mm v[112:115],a[144:147],v[172:175],v[112:115]
dr a[68:71],v212 offset:1024
)"
R"(mm v[116:119],a[148:151],v[172:175],v[116:119]
dr a[72:75],v212 offset:2048
mm v[120:123],a[152:155],v[172:175],v[120:123]
s_waitcnt lgkmcnt(3)
dr a[76:79],v212 offset:3072
mm v[124:127],a[156:159],v[172:175],v[124:127]
dr a[80:83],v212 offset:4096
mm v[152:155],a[96:99],a[48:51],v[188:191]
dr a[84:87],v212 offset:5120
mm v[152:155],a[100:103],a[52:55],v[152:155]
dr a[88:91],v212 offset:6144
buffer_store_dwordx4 a[216:219], v214, s[44:47], s78 offen offset:0 sc1
mm v[152:155],a[104:107],a[56:59],v[152:155]
dr a[92:95],v212 offset:7168
mm v[152:155],a[108:111],a[60:63],v[152:155]
dr a[128:131],v212 offset:16384
)"
R"(mm v[156:159],a[112:115],a[48:51],v[188:191]
dr a[132:135],v212 offset:17408
mm v[156:159],a[116:119],a[52:55],v[156:159]
dr a[136:139],v212 offset:18432
mm v[156:159],a[120:123],a[56:59],v[156:159]
dr a[140:143],v212 offset:19456
mm v[156:159],a[124:127],a[60:63],v[156:159]
buffer_store_dwordx4 a[216:219], v214, s[44:47], s78 offen offset:1024 sc1
buffer_store_dwordx4 a[216:219], v214, s[44:47], s78 offen offset:2048 sc1
buffer_store_dwordx4 a[216:219], v214, s[44:47], s78 offen offset:3072 sc1
buffer_store_dwordx4 a[216:219], v214, s[44:47], s79 offen offset:0 sc1
buffer_store_dwordx4 a[216:219], v214, s[44:47], s79 offen offset:1024 sc1
dr a[144:147],v212 offset:20480
dr a[148:151],v212 offset:21504
dr a[152:155],v212 offset:22528
dr a[156:159],v212 offset:23552
)"
R"(ex v152,v152
ex v153,v153
ex v154,v154
ex v155,v155
cv v172,v152,v153
cv v173,v154,v155
s_nop 7
s_nop 1
v_cndmask_b32 v156, v213, v156, s[88:89]
ex v156,v156
v_cndmask_b32 v157, v213, v157, s[90:91]
ex v157,v157
v_cndmask_b32 v158, v213, v158, s[92:93]
cv v174,v156,v157
ex v158,v158
v_cndmask_b32 v159, v213, v159, s[94:95]
)"
R"(ex v159,v159
s_nop 0
cv v175,v158,v159
s_nop 1
mm v[204:207],v[208:211],v[172:175],v[204:207]
mm v[96:99],a[160:163],v[172:175],v[96:99]
mm v[100:103],a[164:167],v[172:175],v[100:103]
mm v[104:107],a[168:171],v[172:175],v[104:107]
mm v[108:111],a[172:175],v[172:175],v[108:111]
mm v[112:115],a[176:179],v[172:175],v[112:115]
mm v[116:119],a[180:183],v[172:175],v[116:119]
mm v[120:123],a[184:187],v[172:175],v[120:123]
mm v[124:127],a[188:191],v[172:175],v[124:127]
s_nop 7
v_mov_b32 v220, 0x5d800000
v_cmp_nlt_f32 vcc, v192, v220
)"
R"(v_cmp_nlt_f32 s[62:63], v196, v220
s_or_b64 vcc, vcc, s[62:63]
v_cmp_nlt_f32 s[62:63], v200, v220
s_or_b64 vcc, vcc, s[62:63]
v_cmp_nlt_f32 s[62:63], v204, v220
s_or_b64 vcc, vcc, s[62:63]
s_cbranch_vccnz L_redo_seg0_0
L_ok_seg0_0:
s_and_b32 s56, s50, 1
s_lshl_b32 s56, s56, 14
sa s56,s56,0x20000
v_add_u32 v230, s56, v214
v_rcp_f32 v224, v200
v_rcp_f32 v226, v204
s_nop 0
dr a[0:3],v230 offset:0
)"
R"(v_pk_mul_f32 v[64:65], v[64:65], v[224:225] op_sel_hi:[1,0]
v_pk_mul_f32 v[66:67], v[66:67], v[224:225] op_sel_hi:[1,0]
v_pk_mul_f32 v[68:69], v[68:69], v[224:225] op_sel_hi:[1,0]
v_pk_mul_f32 v[70:71], v[70:71], v[224:225] op_sel_hi:[1,0]
dr a[4:7],v230 offset:1024
cv v64,v64,v65
cv v65,v66,v67
cv v66,v68,v69
cv v67,v70,v71
dr a[8:11],v230 offset:2048
v_pk_mul_f32 v[72:73], v[72:73], v[224:225] op_sel_hi:[1,0]
v_pk_mul_f32 v[74:75], v[74:75], v[224:225] op_sel_hi:[1,0]
v_pk_mul_f32 v[76:77], v[76:77], v[224:225] op_sel_hi:[1,0]
v_pk_mul_f32 v[78:79], v[78:79], v[224:225] op_sel_hi:[1,0]
dr a[12:15],v230 offset:3072
cv v72,v72,v73
)"
R"(cv v73,v74,v75
cv v74,v76,v77
cv v75,v78,v79
dr a[16:19],v230 offset:4096
v_pk_mul_f32 v[80:81], v[80:81], v[224:225] op_sel_hi:[1,0]
v_pk_mul_f32 v[82:83], v[82:83], v[224:225] op_sel_hi:[1,0]
v_pk_mul_f32 v[84:85], v[84:85], v[224:225] op_sel_hi:[1,0]
v_pk_mul_f32 v[86:87], v[86:87], v[224:225] op_sel_hi:[1,0]
dr a[20:23],v230 offset:5120
cv v80,v80,v81
cv v81,v82,v83
cv v82,v84,v85
cv v83,v86,v87
dr a[24:27],v230 offset:6144
v_pk_mul_f32 v[88:89], v[88:89], v[224:225] op_sel_hi:[1,0]
v_pk_mul_f32 v[90:91], v[90:91], v[224:225] op_sel_hi:[1,0]
)"
R"(v_pk_mul_f32 v[92:93], v[92:93], v[224:225] op_sel_hi:[1,0]
v_pk_mul_f32 v[94:95], v[94:95], v[224:225] op_sel_hi:[1,0]
dr a[28:31],v230 offset:7168
cv v88,v88,v89
cv v89,v90,v91
cv v90,v92,v93
cv v91,v94,v95
dr a[32:35],v230 offset:8192
v_pk_mul_f32 v[96:97], v[96:97], v[226:227] op_sel_hi:[1,0]
v_pk_mul_f32 v[98:99], v[98:99], v[226:227] op_sel_hi:[1,0]
v_pk_mul_f32 v[100:101], v[100:101], v[226:227] op_sel_hi:[1,0]
v_pk_mul_f32 v[102:103], v[102:103], v[226:227] op_sel_hi:[1,0]
dr a[36:39],v230 offset:9216
cv v96,v96,v97
cv v97,v98,v99
cv v98,v100,v101
)"
R"(cv v99,v102,v103
dr a[40:43],v230 offset:10240
v_pk_mul_f32 v[104:105], v[104:105], v[226:227] op_sel_hi:[1,0]
v_pk_mul_f32 v[106:107], v[106:107], v[226:227] op_sel_hi:[1,0]
v_pk_mul_f32 v[108:109], v[108:109], v[226:227] op_sel_hi:[1,0]
v_pk_mul_f32 v[110:111], v[110:111], v[226:227] op_sel_hi:[1,0]
dr a[44:47],v230 offset:11264
cv v104,v104,v105
cv v105,v106,v107
cv v106,v108,v109
cv v107,v110,v111
dr a[48:51],v230 offset:12288
v_pk_mul_f32 v[112:113], v[112:113], v[226:227] op_sel_hi:[1,0]
v_pk_mul_f32 v[114:115], v[114:115], v[226:227] op_sel_hi:[1,0]
v_pk_mul_f32 v[116:117], v[116:117], v[226:227] op_sel_hi:[1,0]
v_pk_mul_f32 v[118:119], v[118:119], v[226:227] op_sel_hi:[1,0]
)"
R"(dr a[52:55],v230 offset:13312
cv v112,v112,v113
cv v113,v114,v115
cv v114,v116,v117
cv v115,v118,v119
dr a[56:59],v230 offset:14336
v_pk_mul_f32 v[120:121], v[120:121], v[226:227] op_sel_hi:[1,0]
v_pk_mul_f32 v[122:123], v[122:123], v[226:227] op_sel_hi:[1,0]
v_pk_mul_f32 v[124:125], v[124:125], v[226:227] op_sel_hi:[1,0]
v_pk_mul_f32 v[126:127], v[126:127], v[226:227] op_sel_hi:[1,0]
dr a[60:63],v230 offset:15360
cv v120,v120,v121
cv v121,v122,v123
cv v122,v124,v125
cv v123,v126,v127
sa s56,s68,0x1000
)"
R"(buffer_store_dwordx4 v[64:67], v214, s[44:47], s56 offen offset:0 sc1
sa s56,s68,0x3000
buffer_store_dwordx4 v[72:75], v214, s[44:47], s56 offen offset:0 sc1
sa s56,s68,0x5000
buffer_store_dwordx4 v[80:83], v214, s[44:47], s56 offen offset:0 sc1
sa s56,s68,0x7000
buffer_store_dwordx4 v[88:91], v214, s[44:47], s56 offen offset:0 sc1
sa s56,s69,0x1000
buffer_store_dwordx4 v[96:99], v214, s[44:47], s56 offen offset:3072 sc1
sa s56,s69,0x3000
buffer_store_dwordx4 v[104:107], v214, s[44:47], s56 offen offset:3072 sc1
sa s56,s69,0x5000
buffer_store_dwordx4 v[112:115], v214, s[44:47], s56 offen offset:3072 sc1
sa s56,s69,0x7000
buffer_store_dwordx4 v[120:123], v214, s[44:47], s56 offen offset:3072 sc1
s_sub_u32 s57, 15, s49
)"
R"(s_waitcnt lgkmcnt(0)
s_setpc_b64 s[58:59]
.p2align 12
L_att_seg1_0:
ex v156,v156
ex v157,v157
ex v158,v158
cv v174,v156,v157
ex v159,v159
s_nop 0
cv v175,v158,v159
s_nop 1
mm v[204:207],v[208:211],v[172:175],v[204:207]
dr a[96:99],v212 offset:8192
v_lshlrev_b32 v235, 3, v214
mm v[96:99],a[160:163],v[172:175],v[96:99]
)"
R"(s_lshl_b32 s62, s51, 2
dr a[100:103],v212 offset:9216
v_readlane_b32 s55, v219, s52
mm v[100:103],a[164:167],v[172:175],v[100:103]
sa s62,s62,s50
sa s52,s52,1
dr a[104:107],v212 offset:10240
mm v[104:107],a[168:171],v[172:175],v[104:107]
s_mul_i32 s63, s62, 4096
dr a[108:111],v212 offset:11264
v_readlane_b32 s56, v216, s52
mm v[108:111],a[172:175],v[172:175],v[108:111]
sa s36,s82,s63
s_addc_u32 s37, s83, 0
dr a[112:115],v212 offset:12288
mm v[112:115],a[176:179],v[172:175],v[112:115]
)"
R"(s_and_b32 s37, s37, 0xffff
dr a[116:119],v212 offset:13312
mm v[116:119],a[180:183],v[172:175],v[116:119]
s_mov_b32 s38, 4096
dr a[120:123],v212 offset:14336
mm v[120:123],a[184:187],v[172:175],v[120:123]
s_mov_b32 s39, 0x20000
s_waitcnt lgkmcnt(7)
dr a[124:127],v212 offset:15360
mm v[124:127],a[188:191],v[172:175],v[124:127]
s_mul_i32 s63, s62, 512
dr a[160:163],v212 offset:24576
mm v[144:147],a[64:67],a[32:35],v[184:187]
sa s64,s70,s63
s_addc_u32 s65, s71, 0
dr a[164:167],v212 offset:25600
)"
R"(mm v[144:147],a[68:71],a[36:39],v[144:147]
s_and_b32 s65, s65, 0xffff
s_bfe_u32 s80, s51, 0x20001
mm v[144:147],a[72:75],a[40:43],v[144:147]
s_mov_b32 s66, 512
s_bfe_u32 s79, s51, 0x20003
dr a[168:171],v212 offset:26624
mm v[144:147],a[76:79],a[44:47],v[144:147]
s_mov_b32 s67, 0x20000
s_lshr_b32 s78, s51, 5
mm v[148:151],a[80:83],a[32:35],v[184:187]
s_sub_u32 s81, 15, s78
buffer_load_dword v233, v235, s[36:39], 0 offen
mm v[148:151],a[84:87],a[36:39],v[148:151]
buffer_load_dword v234, v235, s[64:67], 0 offen
mm v[148:151],a[88:91],a[40:43],v[148:151]
)"
R"(s_bitcmp1_b32 s51, 0
s_cselect_b32 s78, s78, s81
dr a[172:175],v212 offset:27648
mm v[148:151],a[92:95],a[44:47],v[148:151]
s_lshl_b32 s78, s78, 2
dr a[176:179],v212 offset:28672
mm v[152:155],a[64:67],a[48:51],v[188:191]
sa s78,s78,s79
dr a[180:183],v212 offset:29696
mm v[152:155],a[68:71],a[52:55],v[152:155]
s_lshl_b32 s78, s78, 2
dr a[184:187],v212 offset:30720
mm v[152:155],a[72:75],a[56:59],v[152:155]
sa s78,s78,s80
v_cndmask_b32 v148, v213, v148, s[88:89]
v_cndmask_b32 v149, v213, v149, s[90:91]
)"
R"(mm v[152:155],a[76:79],a[60:63],v[152:155]
s_lshl_b32 s78, s78, 2
v_cndmask_b32 v150, v213, v150, s[92:93]
v_cndmask_b32 v151, v213, v151, s[94:95]
mm v[156:159],a[80:83],a[48:51],v[188:191]
sa s78,s78,s50
ex v144,v144
mm v[156:159],a[84:87],a[52:55],v[156:159]
s_lshl_b32 s78, s78, 13
ex v145,v145
mm v[156:159],a[88:91],a[56:59],v[156:159]
sa s78,s78,0x3400000
ex v146,v146
mm v[156:159],a[92:95],a[60:63],v[156:159]
sa s79,s78,0x1000
ex v147,v147
)"
R"(cv v168,v144,v145
cv v169,v146,v147
ex v148,v148
ex v149,v149
ex v150,v150
cv v170,v148,v149
ex v151,v151
v_accvgpr_write_b32 a216, -1
cv v171,v150,v151
ex v152,v152
ex v153,v153
mm v[200:203],v[208:211],v[168:171],v[200:203]
sa s58,s60,s56
s_addc_u32 s59, s61, 0
dr a[188:191],v212 offset:31744
mm v[64:67],a[128:131],v[168:171],v[64:67]
)"
R"(ex v154,v154
mm v[68:71],a[132:135],v[168:171],v[68:71]
ex v155,v155
mm v[72:75],a[136:139],v[168:171],v[72:75]
ex v156,v156
mm v[76:79],a[140:143],v[168:171],v[76:79]
ex v157,v157
mm v[80:83],a[144:147],v[168:171],v[80:83]
ex v158,v158
mm v[84:87],a[148:151],v[168:171],v[84:87]
ex v159,v159
mm v[88:91],a[152:155],v[168:171],v[88:91]
v_rcp_f32 v220, v192
v_rcp_f32 v222, v196
s_nop 0
v_pk_mul_f32 v[0:1], v[0:1], v[220:221] op_sel_hi:[1,0]
)"
R"(v_pk_mul_f32 v[2:3], v[2:3], v[220:221] op_sel_hi:[1,0]
v_pk_mul_f32 v[4:5], v[4:5], v[220:221] op_sel_hi:[1,0]
v_pk_mul_f32 v[6:7], v[6:7], v[220:221] op_sel_hi:[1,0]
cv v0,v0,v1
cv v1,v2,v3
cv v2,v4,v5
cv v3,v6,v7
v_pk_mul_f32 v[8:9], v[8:9], v[220:221] op_sel_hi:[1,0]
v_pk_mul_f32 v[10:11], v[10:11], v[220:221] op_sel_hi:[1,0]
v_pk_mul_f32 v[12:13], v[12:13], v[220:221] op_sel_hi:[1,0]
v_pk_mul_f32 v[14:15], v[14:15], v[220:221] op_sel_hi:[1,0]
cv v8,v8,v9
cv v9,v10,v11
cv v10,v12,v13
cv v11,v14,v15
v_pk_mul_f32 v[16:17], v[16:17], v[220:221] op_sel_hi:[1,0]
)"
R"(v_pk_mul_f32 v[18:19], v[18:19], v[220:221] op_sel_hi:[1,0]
v_pk_mul_f32 v[20:21], v[20:21], v[220:221] op_sel_hi:[1,0]
v_pk_mul_f32 v[22:23], v[22:23], v[220:221] op_sel_hi:[1,0]
cv v16,v16,v17
cv v17,v18,v19
cv v18,v20,v21
cv v19,v22,v23
v_pk_mul_f32 v[24:25], v[24:25], v[220:221] op_sel_hi:[1,0]
v_pk_mul_f32 v[26:27], v[26:27], v[220:221] op_sel_hi:[1,0]
v_pk_mul_f32 v[28:29], v[28:29], v[220:221] op_sel_hi:[1,0]
v_pk_mul_f32 v[30:31], v[30:31], v[220:221] op_sel_hi:[1,0]
cv v24,v24,v25
cv v25,v26,v27
cv v26,v28,v29
cv v27,v30,v31
v_pk_mul_f32 v[32:33], v[32:33], v[222:223] op_sel_hi:[1,0]
)"
R"(v_pk_mul_f32 v[34:35], v[34:35], v[222:223] op_sel_hi:[1,0]
v_pk_mul_f32 v[36:37], v[36:37], v[222:223] op_sel_hi:[1,0]
v_pk_mul_f32 v[38:39], v[38:39], v[222:223] op_sel_hi:[1,0]
cv v32,v32,v33
cv v33,v34,v35
cv v34,v36,v37
cv v35,v38,v39
v_pk_mul_f32 v[40:41], v[40:41], v[222:223] op_sel_hi:[1,0]
v_pk_mul_f32 v[42:43], v[42:43], v[222:223] op_sel_hi:[1,0]
v_pk_mul_f32 v[44:45], v[44:45], v[222:223] op_sel_hi:[1,0]
v_pk_mul_f32 v[46:47], v[46:47], v[222:223] op_sel_hi:[1,0]
cv v40,v40,v41
cv v41,v42,v43
cv v42,v44,v45
cv v43,v46,v47
v_pk_mul_f32 v[48:49], v[48:49], v[222:223] op_sel_hi:[1,0]
)"
R"(v_pk_mul_f32 v[50:51], v[50:51], v[222:223] op_sel_hi:[1,0]
v_pk_mul_f32 v[52:53], v[52:53], v[222:223] op_sel_hi:[1,0]
v_pk_mul_f32 v[54:55], v[54:55], v[222:223] op_sel_hi:[1,0]
cv v48,v48,v49
cv v49,v50,v51
cv v50,v52,v53
cv v51,v54,v55
v_pk_mul_f32 v[56:57], v[56:57], v[222:223] op_sel_hi:[1,0]
v_pk_mul_f32 v[58:59], v[58:59], v[222:223] op_sel_hi:[1,0]
v_pk_mul_f32 v[60:61], v[60:61], v[222:223] op_sel_hi:[1,0]
v_pk_mul_f32 v[62:63], v[62:63], v[222:223] op_sel_hi:[1,0]
cv v56,v56,v57
cv v57,v58,v59
cv v58,v60,v61
cv v59,v62,v63
s_lshl_b32 s56, s57, 5
)"
R"(sa s56,s56,s48
s_lshl_b32 s56, s56, 15
sa s56,s56,0x2100000
s_and_b32 s69, s50, 1
s_lshl_b32 s69, s69, 10
sa s68,s56,s69
s_sub_u32 s69, s56, s69
sa s56,s68,0x0
buffer_store_dwordx4 v[0:3], v214, s[44:47], s56 offen offset:0 sc1
sa s56,s68,0x2000
buffer_store_dwordx4 v[8:11], v214, s[44:47], s56 offen offset:0 sc1
sa s56,s68,0x4000
buffer_store_dwordx4 v[16:19], v214, s[44:47], s56 offen offset:0 sc1
sa s56,s68,0x6000
buffer_store_dwordx4 v[24:27], v214, s[44:47], s56 offen offset:0 sc1
sa s56,s69,0x0
)"
R"(buffer_store_dwordx4 v[32:35], v214, s[44:47], s56 offen offset:3072 sc1
sa s56,s69,0x2000
buffer_store_dwordx4 v[40:43], v214, s[44:47], s56 offen offset:3072 sc1
sa s56,s69,0x4000
buffer_store_dwordx4 v[48:51], v214, s[44:47], s56 offen offset:3072 sc1
sa s56,s69,0x6000
buffer_store_dwordx4 v[56:59], v214, s[44:47], s56 offen offset:3072 sc1
s_waitcnt vmcnt(10) lgkmcnt(0)
s_barrier
v_accvgpr_write_b32 a217, -1
cv v172,v152,v153
mm v[92:95],a[156:159],v[168:171],v[92:95]
cv v173,v154,v155
cv v174,v156,v157
cv v175,v158,v159
v_add_u32 v212, s55, v214
)"
R"(v_accvgpr_write_b32 a218, -1
mm v[204:207],v[208:211],v[172:175],v[204:207]
dr a[64:67],v212 offset:0
v_accvgpr_write_b32 a219, -1
mm v[96:99],a[128:131],v[172:175],v[96:99]
dr a[68:71],v212 offset:1024
mm v[100:103],a[132:135],v[172:175],v[100:103]
dr a[72:75],v212 offset:2048
mm v[104:107],a[136:139],v[172:175],v[104:107]
dr a[76:79],v212 offset:3072
mm v[108:111],a[140:143],v[172:175],v[108:111]
dr a[80:83],v212 offset:4096
mm v[112:115],a[144:147],v[172:175],v[112:115]
dr a[84:87],v212 offset:5120
mm v[116:119],a[148:151],v[172:175],v[116:119]
dr a[88:91],v212 offset:6144
)"
R"(buffer_store_dwordx4 a[216:219], v214, s[44:47], s78 offen offset:0 sc1
mm v[120:123],a[152:155],v[172:175],v[120:123]
s_waitcnt lgkmcnt(7)
dr a[92:95],v212 offset:7168
mm v[124:127],a[156:159],v[172:175],v[124:127]
dr a[128:131],v212 offset:16384
mm v[152:155],a[96:99],a[48:51],v[188:191]
dr a[132:135],v212 offset:17408
v_mov_b32 v174, 0
mm v[152:155],a[100:103],a[52:55],v[152:155]
dr a[136:139],v212 offset:18432
v_mov_b32 v175, 0
mm v[152:155],a[104:107],a[56:59],v[152:155]
dr a[140:143],v212 offset:19456
mm v[152:155],a[108:111],a[60:63],v[152:155]
buffer_store_dwordx4 a[216:219], v214, s[44:47], s78 offen offset:1024 sc1
)"
R"(buffer_store_dwordx4 a[216:219], v214, s[44:47], s78 offen offset:2048 sc1
buffer_store_dwordx4 a[216:219], v214, s[44:47], s78 offen offset:3072 sc1
buffer_store_dwordx4 a[216:219], v214, s[44:47], s79 offen offset:0 sc1
buffer_store_dwordx4 a[216:219], v214, s[44:47], s79 offen offset:1024 sc1
dr a[144:147],v212 offset:20480
dr a[148:151],v212 offset:21504
dr a[152:155],v212 offset:22528
dr a[156:159],v212 offset:23552
v_cndmask_b32 v152, v213, v152, s[88:89]
v_cndmask_b32 v153, v213, v153, s[90:91]
v_cndmask_b32 v154, v213, v154, s[92:93]
v_cndmask_b32 v155, v213, v155, s[94:95]
ex v152,v152
ex v153,v153
ex v154,v154
ex v155,v155
)"
R"(cv v172,v152,v153
cv v173,v154,v155
s_nop 7
s_nop 1
mm v[204:207],v[208:211],v[172:175],v[204:207]
mm v[96:99],a[160:163],v[172:175],v[96:99]
mm v[100:103],a[164:167],v[172:175],v[100:103]
mm v[104:107],a[168:171],v[172:175],v[104:107]
mm v[108:111],a[172:175],v[172:175],v[108:111]
mm v[112:115],a[176:179],v[172:175],v[112:115]
mm v[116:119],a[180:183],v[172:175],v[116:119]
mm v[120:123],a[184:187],v[172:175],v[120:123]
mm v[124:127],a[188:191],v[172:175],v[124:127]
s_nop 7
v_mov_b32 v220, 0x5d800000
v_cmp_nlt_f32 vcc, v192, v220
)"
R"(v_cmp_nlt_f32 s[62:63], v196, v220
s_or_b64 vcc, vcc, s[62:63]
v_cmp_nlt_f32 s[62:63], v200, v220
s_or_b64 vcc, vcc, s[62:63]
v_cmp_nlt_f32 s[62:63], v204, v220
s_or_b64 vcc, vcc, s[62:63]
s_cbranch_vccnz L_redo_seg1_0
L_ok_seg1_0:
s_and_b32 s56, s50, 1
s_lshl_b32 s56, s56, 14
sa s56,s56,0x20000
v_add_u32 v230, s56, v214
v_rcp_f32 v224, v200
v_rcp_f32 v226, v204
s_nop 0
dr a[0:3],v230 offset:0
)"
R"(v_pk_mul_f32 v[64:65], v[64:65], v[224:225] op_sel_hi:[1,0]
v_pk_mul_f32 v[66:67], v[66:67], v[224:225] op_sel_hi:[1,0]
v_pk_mul_f32 v[68:69], v[68:69], v[224:225] op_sel_hi:[1,0]
v_pk_mul_f32 v[70:71], v[70:71], v[224:225] op_sel_hi:[1,0]
dr a[4:7],v230 offset:1024
cv v64,v64,v65
cv v65,v66,v67
cv v66,v68,v69
cv v67,v70,v71
dr a[8:11],v230 offset:2048
v_pk_mul_f32 v[72:73], v[72:73], v[224:225] op_sel_hi:[1,0]
v_pk_mul_f32 v[74:75], v[74:75], v[224:225] op_sel_hi:[1,0]
v_pk_mul_f32 v[76:77], v[76:77], v[224:225] op_sel_hi:[1,0]
v_pk_mul_f32 v[78:79], v[78:79], v[224:225] op_sel_hi:[1,0]
dr a[12:15],v230 offset:3072
cv v72,v72,v73
)"
R"(cv v73,v74,v75
cv v74,v76,v77
cv v75,v78,v79
dr a[16:19],v230 offset:4096
v_pk_mul_f32 v[80:81], v[80:81], v[224:225] op_sel_hi:[1,0]
v_pk_mul_f32 v[82:83], v[82:83], v[224:225] op_sel_hi:[1,0]
v_pk_mul_f32 v[84:85], v[84:85], v[224:225] op_sel_hi:[1,0]
v_pk_mul_f32 v[86:87], v[86:87], v[224:225] op_sel_hi:[1,0]
dr a[20:23],v230 offset:5120
cv v80,v80,v81
cv v81,v82,v83
cv v82,v84,v85
cv v83,v86,v87
dr a[24:27],v230 offset:6144
v_pk_mul_f32 v[88:89], v[88:89], v[224:225] op_sel_hi:[1,0]
v_pk_mul_f32 v[90:91], v[90:91], v[224:225] op_sel_hi:[1,0]
)"
R"(v_pk_mul_f32 v[92:93], v[92:93], v[224:225] op_sel_hi:[1,0]
v_pk_mul_f32 v[94:95], v[94:95], v[224:225] op_sel_hi:[1,0]
dr a[28:31],v230 offset:7168
cv v88,v88,v89
cv v89,v90,v91
cv v90,v92,v93
cv v91,v94,v95
dr a[32:35],v230 offset:8192
v_pk_mul_f32 v[96:97], v[96:97], v[226:227] op_sel_hi:[1,0]
v_pk_mul_f32 v[98:99], v[98:99], v[226:227] op_sel_hi:[1,0]
v_pk_mul_f32 v[100:101], v[100:101], v[226:227] op_sel_hi:[1,0]
v_pk_mul_f32 v[102:103], v[102:103], v[226:227] op_sel_hi:[1,0]
dr a[36:39],v230 offset:9216
cv v96,v96,v97
cv v97,v98,v99
cv v98,v100,v101
)"
R"(cv v99,v102,v103
dr a[40:43],v230 offset:10240
v_pk_mul_f32 v[104:105], v[104:105], v[226:227] op_sel_hi:[1,0]
v_pk_mul_f32 v[106:107], v[106:107], v[226:227] op_sel_hi:[1,0]
v_pk_mul_f32 v[108:109], v[108:109], v[226:227] op_sel_hi:[1,0]
v_pk_mul_f32 v[110:111], v[110:111], v[226:227] op_sel_hi:[1,0]
dr a[44:47],v230 offset:11264
cv v104,v104,v105
cv v105,v106,v107
cv v106,v108,v109
cv v107,v110,v111
dr a[48:51],v230 offset:12288
v_pk_mul_f32 v[112:113], v[112:113], v[226:227] op_sel_hi:[1,0]
v_pk_mul_f32 v[114:115], v[114:115], v[226:227] op_sel_hi:[1,0]
v_pk_mul_f32 v[116:117], v[116:117], v[226:227] op_sel_hi:[1,0]
v_pk_mul_f32 v[118:119], v[118:119], v[226:227] op_sel_hi:[1,0]
)"
R"(dr a[52:55],v230 offset:13312
cv v112,v112,v113
cv v113,v114,v115
cv v114,v116,v117
cv v115,v118,v119
dr a[56:59],v230 offset:14336
v_pk_mul_f32 v[120:121], v[120:121], v[226:227] op_sel_hi:[1,0]
v_pk_mul_f32 v[122:123], v[122:123], v[226:227] op_sel_hi:[1,0]
v_pk_mul_f32 v[124:125], v[124:125], v[226:227] op_sel_hi:[1,0]
v_pk_mul_f32 v[126:127], v[126:127], v[226:227] op_sel_hi:[1,0]
dr a[60:63],v230 offset:15360
cv v120,v120,v121
cv v121,v122,v123
cv v122,v124,v125
cv v123,v126,v127
sa s56,s68,0x1000
)"
R"(buffer_store_dwordx4 v[64:67], v214, s[44:47], s56 offen offset:0 sc1
sa s56,s68,0x3000
buffer_store_dwordx4 v[72:75], v214, s[44:47], s56 offen offset:0 sc1
sa s56,s68,0x5000
buffer_store_dwordx4 v[80:83], v214, s[44:47], s56 offen offset:0 sc1
sa s56,s68,0x7000
buffer_store_dwordx4 v[88:91], v214, s[44:47], s56 offen offset:0 sc1
sa s56,s69,0x1000
buffer_store_dwordx4 v[96:99], v214, s[44:47], s56 offen offset:3072 sc1
sa s56,s69,0x3000
buffer_store_dwordx4 v[104:107], v214, s[44:47], s56 offen offset:3072 sc1
sa s56,s69,0x5000
buffer_store_dwordx4 v[112:115], v214, s[44:47], s56 offen offset:3072 sc1
sa s56,s69,0x7000
buffer_store_dwordx4 v[120:123], v214, s[44:47], s56 offen offset:3072 sc1
s_sub_u32 s57, 15, s49
)"
R"(s_waitcnt lgkmcnt(0)
s_setpc_b64 s[58:59]
.p2align 12
L_att_full0:
mm v[128:131],a[64:67],a[0:3],v[176:179]
dr a[96:99],v212 offset:8192
v_lshlrev_b32 v235, 3, v214
mm v[128:131],a[68:71],a[4:7],v[128:131]
s_lshl_b32 s62, s51, 2
ex v156,v156
mm v[128:131],a[72:75],a[8:11],v[128:131]
sa s62,s62,s50
ex v157,v157
mm v[128:131],a[76:79],a[12:15],v[128:131]
s_mul_i32 s63, s62, 4096
ex v158,v158
)"
R"(mm v[132:135],a[80:83],a[0:3],v[176:179]
sa s36,s82,s63
s_addc_u32 s37, s83, 0
dr a[100:103],v212 offset:9216
mm v[132:135],a[84:87],a[4:7],v[132:135]
s_and_b32 s37, s37, 0xffff
ex v159,v159
mm v[132:135],a[88:91],a[8:11],v[132:135]
s_mov_b32 s38, 4096
cv v174,v156,v157
cv v175,v158,v159
mm v[132:135],a[92:95],a[12:15],v[132:135]
s_mov_b32 s39, 0x20000
dr a[104:107],v212 offset:10240
v_readlane_b32 s55, v219, s52
mm v[204:207],v[208:211],v[172:175],v[204:207]
)"
R"(s_mul_i32 s63, s62, 512
sa s52,s52,1
dr a[108:111],v212 offset:11264
mm v[96:99],a[160:163],v[172:175],v[96:99]
sa s64,s70,s63
s_addc_u32 s65, s71, 0
dr a[112:115],v212 offset:12288
mm v[100:103],a[164:167],v[172:175],v[100:103]
s_and_b32 s65, s65, 0xffff
s_bfe_u32 s80, s51, 0x20001
dr a[116:119],v212 offset:13312
mm v[104:107],a[168:171],v[172:175],v[104:107]
s_mov_b32 s66, 512
s_bfe_u32 s79, s51, 0x20003
v_readlane_b32 s56, v216, s52
mm v[108:111],a[172:175],v[172:175],v[108:111]
)"
R"(s_mov_b32 s67, 0x20000
s_lshr_b32 s78, s51, 5
dr a[120:123],v212 offset:14336
mm v[112:115],a[176:179],v[172:175],v[112:115]
buffer_load_dword v233, v235, s[36:39], 0 offen
mm v[116:119],a[180:183],v[172:175],v[116:119]
s_sub_u32 s81, 15, s78
buffer_load_dword v234, v235, s[64:67], 0 offen
mm v[120:123],a[184:187],v[172:175],v[120:123]
s_bitcmp1_b32 s51, 0
s_cselect_b32 s78, s78, s81
dr a[124:127],v212 offset:15360
mm v[124:127],a[188:191],v[172:175],v[124:127]
sa s58,s60,s56
s_addc_u32 s59, s61, 0
dr a[160:163],v212 offset:24576
)"
R"(mm v[136:139],a[64:67],a[16:19],v[180:183]
s_lshl_b32 s78, s78, 2
dr a[164:167],v212 offset:25600
mm v[136:139],a[68:71],a[20:23],v[136:139]
sa s78,s78,s79
ex v128,v128
mm v[136:139],a[72:75],a[24:27],v[136:139]
ex v129,v129
mm v[136:139],a[76:79],a[28:31],v[136:139]
s_lshl_b32 s78, s78, 2
ex v130,v130
mm v[140:143],a[80:83],a[16:19],v[180:183]
sa s78,s78,s80
ex v131,v131
mm v[140:143],a[84:87],a[20:23],v[140:143]
ex v132,v132
)"
R"(mm v[140:143],a[88:91],a[24:27],v[140:143]
s_waitcnt lgkmcnt(10)
ex v133,v133
mm v[140:143],a[92:95],a[28:31],v[140:143]
s_lshl_b32 s78, s78, 2
ex v134,v134
cv v160,v128,v129
cv v161,v130,v131
cv v162,v132,v133
ex v135,v135
ex v136,v136
cv v163,v134,v135
ex v137,v137
ex v138,v138
mm v[192:195],v[208:211],v[160:163],v[192:195]
sa s78,s78,s50
)"
R"(dr a[168:171],v212 offset:26624
cv v164,v136,v137
mm v[0:3],a[128:131],v[160:163],v[0:3]
s_lshl_b32 s78, s78, 13
dr a[172:175],v212 offset:27648
mm v[4:7],a[132:135],v[160:163],v[4:7]
sa s78,s78,0x3400000
dr a[176:179],v212 offset:28672
mm v[8:11],a[136:139],v[160:163],v[8:11]
dr a[180:183],v212 offset:29696
mm v[12:15],a[140:143],v[160:163],v[12:15]
sa s79,s78,0x1000
dr a[184:187],v212 offset:30720
mm v[16:19],a[144:147],v[160:163],v[16:19]
dr a[188:191],v212 offset:31744
v_accvgpr_write_b32 a216, -1
)"
R"(mm v[20:23],a[148:151],v[160:163],v[20:23]
ex v139,v139
mm v[24:27],a[152:155],v[160:163],v[24:27]
v_accvgpr_write_b32 a217, -1
cv v165,v138,v139
mm v[28:31],a[156:159],v[160:163],v[28:31]
ex v140,v140
mm v[144:147],a[64:67],a[32:35],v[184:187]
ex v141,v141
mm v[144:147],a[68:71],a[36:39],v[144:147]
ex v142,v142
mm v[144:147],a[72:75],a[40:43],v[144:147]
ex v143,v143
mm v[144:147],a[76:79],a[44:47],v[144:147]
v_accvgpr_write_b32 a218, -1
cv v166,v140,v141
)"
R"(mm v[148:151],a[80:83],a[32:35],v[184:187]
cv v167,v142,v143
v_accvgpr_write_b32 a219, -1
mm v[148:151],a[84:87],a[36:39],v[148:151]
v_add_u32 v212, s55, v214
mm v[148:151],a[88:91],a[40:43],v[148:151]
s_waitcnt vmcnt(2) lgkmcnt(0)
s_barrier
mm v[148:151],a[92:95],a[44:47],v[148:151]
ex v144,v144
mm v[196:199],v[208:211],v[164:167],v[196:199]
ex v145,v145
mm v[32:35],a[128:131],v[164:167],v[32:35]
ex v146,v146
mm v[36:39],a[132:135],v[164:167],v[36:39]
buffer_store_dwordx4 a[216:219], v214, s[44:47], s78 offen offset:0 sc1
)"
R"(mm v[40:43],a[136:139],v[164:167],v[40:43]
ex v147,v147
mm v[44:47],a[140:143],v[164:167],v[44:47]
ex v148,v148
mm v[48:51],a[144:147],v[164:167],v[48:51]
ex v149,v149
mm v[52:55],a[148:151],v[164:167],v[52:55]
ex v150,v150
mm v[56:59],a[152:155],v[164:167],v[56:59]
ex v151,v151
mm v[60:63],a[156:159],v[164:167],v[60:63]
cv v168,v144,v145
cv v169,v146,v147
mm v[152:155],a[64:67],a[48:51],v[188:191]
cv v170,v148,v149
cv v171,v150,v151
)"
R"(mm v[152:155],a[68:71],a[52:55],v[152:155]
mm v[152:155],a[72:75],a[56:59],v[152:155]
mm v[152:155],a[76:79],a[60:63],v[152:155]
mm v[156:159],a[80:83],a[48:51],v[188:191]
mm v[156:159],a[84:87],a[52:55],v[156:159]
mm v[156:159],a[88:91],a[56:59],v[156:159]
mm v[156:159],a[92:95],a[60:63],v[156:159]
dr a[64:67],v212 offset:0
mm v[200:203],v[208:211],v[168:171],v[200:203]
buffer_store_dwordx4 a[216:219], v214, s[44:47], s78 offen offset:1024 sc1
mm v[64:67],a[128:131],v[168:171],v[64:67]
ex v152,v152
mm v[68:71],a[132:135],v[168:171],v[68:71]
dr a[68:71],v212 offset:1024
mm v[72:75],a[136:139],v[168:171],v[72:75]
ex v153,v153
)"
R"(mm v[76:79],a[140:143],v[168:171],v[76:79]
ex v154,v154
mm v[80:83],a[144:147],v[168:171],v[80:83]
dr a[72:75],v212 offset:2048
cv v172,v152,v153
mm v[84:87],a[148:151],v[168:171],v[84:87]
ex v155,v155
mm v[88:91],a[152:155],v[168:171],v[88:91]
ex v156,v156
mm v[92:95],a[156:159],v[168:171],v[92:95]
dr a[76:79],v212 offset:3072
cv v173,v154,v155
mm v[128:131],a[96:99],a[0:3],v[176:179]
ex v157,v157
mm v[128:131],a[100:103],a[4:7],v[128:131]
ex v158,v158
)"
R"(mm v[128:131],a[104:107],a[8:11],v[128:131]
ex v159,v159
mm v[128:131],a[108:111],a[12:15],v[128:131]
cv v174,v156,v157
cv v175,v158,v159
mm v[132:135],a[112:115],a[0:3],v[176:179]
dr a[80:83],v212 offset:4096
mm v[132:135],a[116:119],a[4:7],v[132:135]
mm v[132:135],a[120:123],a[8:11],v[132:135]
dr a[84:87],v212 offset:5120
mm v[132:135],a[124:127],a[12:15],v[132:135]
buffer_store_dwordx4 a[216:219], v214, s[44:47], s78 offen offset:2048 sc1
mm v[204:207],v[208:211],v[172:175],v[204:207]
ex v128,v128
mm v[96:99],a[128:131],v[172:175],v[96:99]
dr a[88:91],v212 offset:6144
)"
R"(mm v[100:103],a[132:135],v[172:175],v[100:103]
ex v129,v129
mm v[104:107],a[136:139],v[172:175],v[104:107]
ex v130,v130
mm v[108:111],a[140:143],v[172:175],v[108:111]
dr a[92:95],v212 offset:7168
cv v160,v128,v129
mm v[112:115],a[144:147],v[172:175],v[112:115]
ex v131,v131
mm v[116:119],a[148:151],v[172:175],v[116:119]
ex v132,v132
mm v[120:123],a[152:155],v[172:175],v[120:123]
ex v133,v133
mm v[124:127],a[156:159],v[172:175],v[124:127]
dr a[128:131],v212 offset:16384
cv v161,v130,v131
)"
R"(mm v[136:139],a[96:99],a[16:19],v[180:183]
ex v134,v134
mm v[136:139],a[100:103],a[20:23],v[136:139]
ex v135,v135
mm v[136:139],a[104:107],a[24:27],v[136:139]
cv v162,v132,v133
cv v163,v134,v135
mm v[136:139],a[108:111],a[28:31],v[136:139]
buffer_load_dword v248, v215, s[40:43], 0 offen
mm v[140:143],a[112:115],a[16:19],v[180:183]
buffer_store_dwordx4 a[216:219], v214, s[44:47], s78 offen offset:3072 sc1
mm v[140:143],a[116:119],a[20:23],v[140:143]
dr a[132:135],v212 offset:17408
mm v[140:143],a[120:123],a[24:27],v[140:143]
s_waitcnt lgkmcnt(10)
mm v[140:143],a[124:127],a[28:31],v[140:143]
)"
R"(ex v136,v136
mm v[192:195],v[208:211],v[160:163],v[192:195]
ex v137,v137
mm v[0:3],a[160:163],v[160:163],v[0:3]
ex v138,v138
mm v[4:7],a[164:167],v[160:163],v[4:7]
ex v139,v139
mm v[8:11],a[168:171],v[160:163],v[8:11]
dr a[136:139],v212 offset:18432
cv v164,v136,v137
mm v[12:15],a[172:175],v[160:163],v[12:15]
ex v140,v140
mm v[16:19],a[176:179],v[160:163],v[16:19]
ex v141,v141
mm v[20:23],a[180:183],v[160:163],v[20:23]
ex v142,v142
)"
R"(mm v[24:27],a[184:187],v[160:163],v[24:27]
ex v143,v143
mm v[28:31],a[188:191],v[160:163],v[28:31]
cv v165,v138,v139
cv v166,v140,v141
mm v[144:147],a[96:99],a[32:35],v[184:187]
cv v167,v142,v143
mm v[144:147],a[100:103],a[36:39],v[144:147]
dr a[140:143],v212 offset:19456
mm v[144:147],a[104:107],a[40:43],v[144:147]
buffer_store_dwordx4 a[216:219], v214, s[44:47], s79 offen offset:0 sc1
mm v[144:147],a[108:111],a[44:47],v[144:147]
mm v[148:151],a[112:115],a[32:35],v[184:187]
mm v[148:151],a[116:119],a[36:39],v[148:151]
mm v[148:151],a[120:123],a[40:43],v[148:151]
mm v[148:151],a[124:127],a[44:47],v[148:151]
)"
R"(dr a[144:147],v212 offset:20480
mm v[196:199],v[208:211],v[164:167],v[196:199]
mm v[32:35],a[160:163],v[164:167],v[32:35]
mm v[36:39],a[164:167],v[164:167],v[36:39]
ex v144,v144
mm v[40:43],a[168:171],v[164:167],v[40:43]
ex v145,v145
mm v[44:47],a[172:175],v[164:167],v[44:47]
ex v146,v146
mm v[48:51],a[176:179],v[164:167],v[48:51]
dr a[148:151],v212 offset:21504
cv v168,v144,v145
mm v[52:55],a[180:183],v[164:167],v[52:55]
ex v147,v147
mm v[56:59],a[184:187],v[164:167],v[56:59]
ex v148,v148
)"
R"(mm v[60:63],a[188:191],v[164:167],v[60:63]
ex v149,v149
mm v[152:155],a[96:99],a[48:51],v[188:191]
buffer_store_dwordx4 a[216:219], v214, s[44:47], s79 offen offset:1024 sc1
mm v[152:155],a[100:103],a[52:55],v[152:155]
ex v150,v150
mm v[152:155],a[104:107],a[56:59],v[152:155]
ex v151,v151
mm v[152:155],a[108:111],a[60:63],v[152:155]
cv v169,v146,v147
cv v170,v148,v149
mm v[156:159],a[112:115],a[48:51],v[188:191]
dr a[152:155],v212 offset:22528
cv v171,v150,v151
mm v[156:159],a[116:119],a[52:55],v[156:159]
mm v[156:159],a[120:123],a[56:59],v[156:159]
)"
R"(mm v[156:159],a[124:127],a[60:63],v[156:159]
ex v152,v152
mm v[200:203],v[208:211],v[168:171],v[200:203]
ex v153,v153
mm v[64:67],a[160:163],v[168:171],v[64:67]
dr a[156:159],v212 offset:23552
cv v172,v152,v153
mm v[68:71],a[164:167],v[168:171],v[68:71]
ex v154,v154
mm v[72:75],a[168:171],v[168:171],v[72:75]
ex v155,v155
mm v[76:79],a[172:175],v[168:171],v[76:79]
cv v173,v154,v155
mm v[80:83],a[176:179],v[168:171],v[80:83]
mm v[84:87],a[180:183],v[168:171],v[84:87]
mm v[88:91],a[184:187],v[168:171],v[88:91]
)"
R"(mm v[92:95],a[188:191],v[168:171],v[92:95]
s_waitcnt lgkmcnt(8)
s_setpc_b64 s[58:59]
L_att_fb0_0:
mm v[144:147],a[64:67],a[32:35],0
dr a[96:99],v212 offset:8192
v_mov_b32 v170, 0
mm v[144:147],a[68:71],a[36:39],v[144:147]
dr a[100:103],v212 offset:9216
v_bfe_u32 v241, v214, 4, 5
mm v[144:147],a[72:75],a[40:43],v[144:147]
s_and_b32 s74, s51, 7
dr a[104:107],v212 offset:10240
v_mov_b32 v171, 0
mm v[144:147],a[76:79],a[44:47],v[144:147]
s_lshl_b32 s74, s74, 7
)"
R"(dr a[108:111],v212 offset:11264
v_readlane_b32 s55, v219, s52
mm v[152:155],a[64:67],a[48:51],0
sa s74,s74,0x12000
sa s52,s52,1
dr a[112:115],v212 offset:12288
mm v[152:155],a[68:71],a[52:55],v[152:155]
v_lshl_add_u32 v249, v241, 2, s74
v_cndmask_b32 v144, v213, v144, s[88:89]
mm v[152:155],a[72:75],a[56:59],v[152:155]
s_lshr_b32 s74, s51, 5
v_cndmask_b32 v145, v213, v145, s[90:91]
v_cndmask_b32 v146, v213, v146, s[92:93]
mm v[152:155],a[76:79],a[60:63],v[152:155]
s_lshl_b32 s74, s74, 5
v_cndmask_b32 v147, v213, v147, s[94:95]
)"
R"(v_max3_f32 v224, v144, v145, v146
mm v[156:159],a[80:83],a[48:51],0
s_and_b32 s80, s51, 1
v_max_f32 v224, v224, v147
v_mov_b32 v225, v224
mm v[156:159],a[84:87],a[52:55],v[156:159]
s_lshl_b32 s80, s80, 2
dr a[116:119],v212 offset:13312
v_max3_f32 v226, v152, v153, v154
mm v[156:159],a[88:91],a[56:59],v[156:159]
sa s74,s74,s80
v_permlane32_swap_b32 v224, v225
mm v[156:159],a[92:95],a[60:63],v[156:159]
s_bfe_u32 s80, s51, 0x20003
s_waitcnt lgkmcnt(6)
v_max_f32 v224, v224, v225
)"
R"(v_mov_b32 v225, v224
v_readlane_b32 s56, v216, s52
v_mov_b32 v0, 0
v_permlane16_swap_b32 v224, v225
v_mov_b32 v1, 0
v_max_f32 v224, v224, v225
v_sub_f32 v184, 0xc2c80000, v224
v_add_f32 v144, v144, v184
ex v144,v144
v_add_f32 v145, v145, v184
ex v145,v145
v_max3_f32 v226, v226, v155, v156
cv v168,v144,v145
v_add_f32 v146, v146, v184
ex v146,v146
v_add_f32 v147, v147, v184
)"
R"(ex v147,v147
v_max3_f32 v226, v226, v157, v158
cv v169,v146,v147
v_max_f32 v226, v226, v159
v_mov_b32 v227, v226
mm v[200:203],v[208:211],v[168:171],0
sa s74,s74,s80
dr a[120:123],v212 offset:14336
v_mov_b32 v2, 0
mm v[64:67],a[128:131],v[168:171],0
s_lshl_b32 s74, s74, 4
v_permlane32_swap_b32 v226, v227
mm v[68:71],a[132:135],v[168:171],0
sa s74,s74,0x11000
dr a[124:127],v212 offset:15360
v_max_f32 v226, v226, v227
)"
R"(mm v[72:75],a[136:139],v[168:171],0
v_bfe_u32 v241, v214, 6, 2
v_lshl_add_u32 v244, v241, 7, s74
mm v[76:79],a[140:143],v[168:171],0
v_bfe_u32 v241, v214, 4, 2
v_lshl_add_u32 v244, v241, 2, v244
mm v[80:83],a[144:147],v[168:171],0
s_mov_b32 s40, s82
dr a[160:163],v212 offset:24576
v_mov_b32 v227, v226
mm v[84:87],a[148:151],v[168:171],0
s_and_b32 s41, s83, 0xffff
dr a[164:167],v212 offset:25600
v_mov_b32 v3, 0
mm v[88:91],a[152:155],v[168:171],0
s_mov_b32 s42, 0x400000
)"
R"(v_permlane16_swap_b32 v226, v227
mm v[92:95],a[156:159],v[168:171],0
s_mov_b32 s43, 0x20000
dr a[168:171],v212 offset:26624
v_max_f32 v226, v226, v227
v_sub_f32 v188, 0xc2c80000, v226
v_add_f32 v152, v152, v188
ex v152,v152
v_add_f32 v153, v153, v188
ex v153,v153
v_add_f32 v154, v154, v188
cv v172,v152,v153
ex v154,v154
v_add_f32 v155, v155, v188
ex v155,v155
v_add_f32 v156, v156, v188
)"
R"(cv v173,v154,v155
ex v156,v156
v_add_f32 v157, v157, v188
ex v157,v157
v_add_f32 v158, v158, v188
cv v174,v156,v157
ex v158,v158
v_add_f32 v159, v159, v188
ex v159,v159
v_sub_f32 v189, 0xc2c80000, v226
cv v175,v158,v159
v_sub_f32 v190, 0xc2c80000, v226
v_sub_f32 v191, 0xc2c80000, v226
mm v[204:207],v[208:211],v[172:175],0
s_lshl_b32 s77, s50, 12
dr a[172:175],v212 offset:27648
)"
R"(v_mov_b32 v4, 0
mm v[96:99],a[128:131],v[172:175],0
s_and_b32 s76, s51, 7
dr a[176:179],v212 offset:28672
v_mov_b32 v5, 0
mm v[100:103],a[132:135],v[172:175],0
s_lshl_b32 s76, s76, 19
dr a[180:183],v212 offset:29696
v_mov_b32 v6, 0
mm v[104:107],a[136:139],v[172:175],0
s_lshl_b32 s80, s50, 14
dr a[184:187],v212 offset:30720
v_mov_b32 v7, 0
mm v[108:111],a[140:143],v[172:175],0
sa s76,s76,s80
dr a[188:191],v212 offset:31744
)"
R"(v_mov_b32 v8, 0
mm v[112:115],a[144:147],v[172:175],0
s_bfe_u32 s80, s51, 0x20003
v_add_u32 v212, s55, v214
v_mov_b32 v9, 0
mm v[116:119],a[148:151],v[172:175],0
s_lshl_b32 s80, s80, 8
sa s58,s60,s56
s_addc_u32 s59, s61, 0
mm v[120:123],a[152:155],v[172:175],0
sa s76,s76,s80
s_waitcnt lgkmcnt(0)
v_mov_b32 v10, 0
mm v[124:127],a[156:159],v[172:175],0
v_lshrrev_b32 v241, 8, v214
v_lshlrev_b32 v238, 10, v241
)"
R"(mm v[152:155],a[96:99],a[48:51],v[188:191]
v_lshlrev_b32 v241, 1, v241
v_bfe_u32 v242, v214, 4, 4
mm v[152:155],a[100:103],a[52:55],v[152:155]
v_xor_b32 v242, v242, v241
v_lshl_add_u32 v238, v242, 4, v238
mm v[152:155],a[104:107],a[56:59],v[152:155]
v_xor_b32 v239, 128, v238
v_mov_b32 v11, 0
s_lshl_b32 s81, s49, 3
s_and_b32 s80, s51, 7
sa s81,s81,s80
s_lshl_b32 s81, s81, 6
s_and_b32 s80, s51, 1
s_mul_i32 s80, s80, 0x2000
sa s81,s81,s80
)"
R"(sa s81,s81,0x11000
v_bfe_u32 v250, v214, 4, 4
v_lshlrev_b32 v250, 2, v250
s_waitcnt vmcnt(0) lgkmcnt(0)
s_barrier
s_lshl_b32 s72, s49, 5
sa s72,s72,s48
s_lshl_b32 s72, s72, 1
s_and_b32 s73, s50, 1
sa s72,s72,s73
s_lshl_b32 s72, s72, 3
sa s72,s72,0x13000
v_mov_b32 v228, 1
v_mov_b32 v229, 1
v_mov_b32 v230, 0
s_mov_b64 exec, 1
)"
R"(buffer_store_dwordx2 v[228:229], v230, s[44:47], s72 offen sc1
s_mov_b64 exec, -1
mm v[152:155],a[108:111],a[60:63],v[152:155]
dr a[64:67],v212 offset:0
v_mov_b32 v12, 0
mm v[156:159],a[112:115],a[48:51],v[188:191]
dr a[68:71],v212 offset:1024
v_mov_b32 v13, 0
mm v[156:159],a[116:119],a[52:55],v[156:159]
sa m0,s77,0x14000
dr a[72:75],v212 offset:2048
v_mov_b32 v14, 0
mm v[156:159],a[120:123],a[56:59],v[156:159]
dr a[76:79],v212 offset:3072
v_mov_b32 v15, 0
mm v[156:159],a[124:127],a[60:63],v[156:159]
)"
R"(sa s79,s76,0x0
bl v238,s[40:43],s79 offen lds
sa s79,s76,0xc00
bl v239,s[40:43],s79 offen offset:1024 lds
dr a[80:83],v212 offset:4096
sa s79,s76,0x1800
bl v238,s[40:43],s79 offen offset:2048 lds
dr a[84:87],v212 offset:5120
sa s79,s76,0x2400
bl v239,s[40:43],s79 offen offset:3072 lds
dr a[88:91],v212 offset:6144
sa m0,s77,0x1c000
dr a[92:95],v212 offset:7168
sa s79,s76,0x10000
bl v238,s[40:43],s79 offen lds
dr a[128:131],v212 offset:16384
)"
R"(sa s79,s76,0x10c00
bl v239,s[40:43],s79 offen offset:1024 lds
dr a[132:135],v212 offset:17408
sa s79,s76,0x11800
bl v238,s[40:43],s79 offen offset:2048 lds
dr a[136:139],v212 offset:18432
sa s79,s76,0x12400
bl v239,s[40:43],s79 offen offset:3072 lds
dr a[140:143],v212 offset:19456
sa m0,s77,0x24000
dr a[144:147],v212 offset:20480
sa s79,s76,0x20000
bl v238,s[40:43],s79 offen lds
dr a[148:151],v212 offset:21504
sa s79,s76,0x20c00
bl v239,s[40:43],s79 offen offset:1024 lds
)"
R"(dr a[152:155],v212 offset:22528
sa s79,s76,0x21800
bl v238,s[40:43],s79 offen offset:2048 lds
dr a[156:159],v212 offset:23552
sa s79,s76,0x22400
bl v239,s[40:43],s79 offen offset:3072 lds
ex v152,v152
ex v153,v153
ex v154,v154
ex v155,v155
v_mov_b32 v16, 0
v_mov_b32 v17, 0
v_mov_b32 v18, 0
v_mov_b32 v19, 0
v_mov_b32 v20, 0
v_mov_b32 v21, 0
)"
R"(v_mov_b32 v22, 0
v_mov_b32 v23, 0
v_mov_b32 v24, 0
v_mov_b32 v25, 0
v_mov_b32 v26, 0
v_mov_b32 v27, 0
v_mov_b32 v28, 0
v_mov_b32 v29, 0
v_mov_b32 v30, 0
v_mov_b32 v31, 0
v_mov_b32 v192, 0
v_mov_b32 v176, 0x7f000000
v_mov_b32 v32, 0
v_mov_b32 v33, 0
v_mov_b32 v34, 0
v_mov_b32 v35, 0
)"
R"(v_mov_b32 v36, 0
v_mov_b32 v37, 0
v_mov_b32 v38, 0
v_mov_b32 v39, 0
v_mov_b32 v40, 0
v_mov_b32 v41, 0
v_mov_b32 v42, 0
v_mov_b32 v43, 0
v_mov_b32 v44, 0
v_mov_b32 v45, 0
v_mov_b32 v46, 0
v_mov_b32 v47, 0
v_mov_b32 v48, 0
v_mov_b32 v49, 0
v_mov_b32 v50, 0
v_mov_b32 v51, 0
)"
R"(v_mov_b32 v52, 0
v_mov_b32 v53, 0
v_mov_b32 v54, 0
v_mov_b32 v55, 0
v_mov_b32 v56, 0
v_mov_b32 v57, 0
v_mov_b32 v58, 0
v_mov_b32 v59, 0
v_mov_b32 v60, 0
v_mov_b32 v61, 0
v_mov_b32 v62, 0
v_mov_b32 v63, 0
v_mov_b32 v196, 0
v_mov_b32 v180, 0x7f000000
v_sub_f32 v185, 0xc2c80000, v224
v_sub_f32 v186, 0xc2c80000, v224
)"
R"(v_sub_f32 v187, 0xc2c80000, v224
cv v172,v152,v153
cv v173,v154,v155
s_waitcnt lgkmcnt(8)
s_setpc_b64 s[58:59]
.p2align 12
L_att_a0_0:
mm v[128:131],a[64:67],a[0:3],v[176:179]
ex v156,v156
mm v[128:131],a[68:71],a[4:7],v[128:131]
dr a[96:99],v212 offset:8192
v_bfe_u32 v241, v214, 4, 5
mm v[128:131],a[72:75],a[8:11],v[128:131]
s_and_b32 s74, s51, 7
ex v157,v157
mm v[128:131],a[76:79],a[12:15],v[128:131]
)"
R"(s_lshl_b32 s74, s74, 7
ex v158,v158
cv v174,v156,v157
ex v159,v159
v_mov_b32 v162, 0
cv v175,v158,v159
v_mov_b32 v163, 0
v_readlane_b32 s55, v219, s52
mm v[204:207],v[208:211],v[172:175],v[204:207]
sa s74,s74,0x12000
dr a[100:103],v212 offset:9216
v_cndmask_b32 v128, v213, v128, s[88:89]
mm v[96:99],a[160:163],v[172:175],v[96:99]
sa s52,s52,1
dr a[104:107],v212 offset:10240
v_lshl_add_u32 v249, v241, 2, s74
)"
R"(mm v[100:103],a[164:167],v[172:175],v[100:103]
s_lshr_b32 s74, s51, 5
dr a[108:111],v212 offset:11264
v_cndmask_b32 v129, v213, v129, s[90:91]
mm v[104:107],a[168:171],v[172:175],v[104:107]
s_lshl_b32 s74, s74, 5
dr a[112:115],v212 offset:12288
v_cndmask_b32 v130, v213, v130, s[92:93]
mm v[108:111],a[172:175],v[172:175],v[108:111]
s_and_b32 s80, s51, 1
dr a[116:119],v212 offset:13312
v_cndmask_b32 v131, v213, v131, s[94:95]
mm v[112:115],a[176:179],v[172:175],v[112:115]
s_lshl_b32 s80, s80, 2
dr a[120:123],v212 offset:14336
v_readlane_b32 s56, v216, s52
)"
R"(mm v[116:119],a[180:183],v[172:175],v[116:119]
sa s74,s74,s80
dr a[124:127],v212 offset:15360
mm v[120:123],a[184:187],v[172:175],v[120:123]
s_bfe_u32 s80, s51, 0x20003
ex v128,v128
mm v[124:127],a[188:191],v[172:175],v[124:127]
sa s74,s74,s80
dr a[160:163],v212 offset:24576
mm v[136:139],a[64:67],a[16:19],v[180:183]
s_lshl_b32 s74, s74, 4
dr a[164:167],v212 offset:25600
mm v[136:139],a[68:71],a[20:23],v[136:139]
sa s74,s74,0x11000
ex v129,v129
mm v[136:139],a[72:75],a[24:27],v[136:139]
)"
R"(v_bfe_u32 v241, v214, 6, 2
v_lshl_add_u32 v244, v241, 7, s74
mm v[136:139],a[76:79],a[28:31],v[136:139]
v_bfe_u32 v241, v214, 4, 2
v_lshl_add_u32 v244, v241, 2, v244
mm v[140:143],a[80:83],a[16:19],v[180:183]
s_mov_b32 s40, s82
ex v130,v130
mm v[140:143],a[84:87],a[20:23],v[140:143]
s_and_b32 s41, s83, 0xffff
ex v131,v131
mm v[140:143],a[88:91],a[24:27],v[140:143]
s_mov_b32 s42, 0x400000
dr a[168:171],v212 offset:26624
cv v160,v128,v129
mm v[140:143],a[92:95],a[28:31],v[140:143]
)"
R"(s_mov_b32 s43, 0x20000
s_waitcnt lgkmcnt(11)
cv v161,v130,v131
ex v136,v136
ex v137,v137
mm v[192:195],v[208:211],v[160:163],v[192:195]
s_lshl_b32 s77, s50, 12
dr a[172:175],v212 offset:27648
cv v164,v136,v137
mm v[0:3],a[128:131],v[160:163],v[0:3]
s_and_b32 s76, s51, 7
dr a[176:179],v212 offset:28672
mm v[4:7],a[132:135],v[160:163],v[4:7]
s_lshl_b32 s76, s76, 19
dr a[180:183],v212 offset:29696
mm v[8:11],a[136:139],v[160:163],v[8:11]
)"
R"(s_lshl_b32 s80, s50, 14
dr a[184:187],v212 offset:30720
mm v[12:15],a[140:143],v[160:163],v[12:15]
sa s76,s76,s80
dr a[188:191],v212 offset:31744
mm v[16:19],a[144:147],v[160:163],v[16:19]
s_bfe_u32 s80, s51, 0x20003
ex v138,v138
mm v[20:23],a[148:151],v[160:163],v[20:23]
s_lshl_b32 s80, s80, 8
ex v139,v139
mm v[24:27],a[152:155],v[160:163],v[24:27]
sa s76,s76,s80
ex v140,v140
mm v[28:31],a[156:159],v[160:163],v[28:31]
v_lshrrev_b32 v241, 8, v214
)"
R"(v_lshlrev_b32 v238, 10, v241
mm v[144:147],a[64:67],a[32:35],v[184:187]
v_lshlrev_b32 v241, 1, v241
v_bfe_u32 v242, v214, 4, 4
mm v[144:147],a[68:71],a[36:39],v[144:147]
v_xor_b32 v242, v242, v241
v_lshl_add_u32 v238, v242, 4, v238
mm v[144:147],a[72:75],a[40:43],v[144:147]
v_xor_b32 v239, 128, v238
cv v165,v138,v139
mm v[144:147],a[76:79],a[44:47],v[144:147]
s_lshl_b32 s81, s49, 3
ex v141,v141
mm v[148:151],a[80:83],a[32:35],v[184:187]
s_and_b32 s80, s51, 7
ex v142,v142
)"
R"(mm v[148:151],a[84:87],a[36:39],v[148:151]
sa s81,s81,s80
ex v143,v143
mm v[148:151],a[88:91],a[40:43],v[148:151]
s_lshl_b32 s81, s81, 6
cv v166,v140,v141
cv v167,v142,v143
s_and_b32 s80, s51, 1
s_mul_i32 s80, s80, 0x2000
sa s81,s81,s80
sa s81,s81,0x11000
v_bfe_u32 v250, v214, 4, 4
v_lshlrev_b32 v250, 2, v250
s_waitcnt vmcnt(0) lgkmcnt(0)
s_barrier
mm v[148:151],a[92:95],a[44:47],v[148:151]
)"
R"(ex v144,v144
mm v[196:199],v[208:211],v[164:167],v[196:199]
ex v145,v145
mm v[32:35],a[128:131],v[164:167],v[32:35]
sa m0,s77,0x14000
ex v146,v146
mm v[36:39],a[132:135],v[164:167],v[36:39]
ex v147,v147
mm v[40:43],a[136:139],v[164:167],v[40:43]
ex v148,v148
mm v[44:47],a[140:143],v[164:167],v[44:47]
ex v149,v149
mm v[48:51],a[144:147],v[164:167],v[48:51]
ex v150,v150
mm v[52:55],a[148:151],v[164:167],v[52:55]
sa s79,s76,0x0
)"
R"(bl v238,s[40:43],s79 offen lds
mm v[56:59],a[152:155],v[164:167],v[56:59]
ex v151,v151
mm v[60:63],a[156:159],v[164:167],v[60:63]
cv v168,v144,v145
cv v169,v146,v147
mm v[152:155],a[64:67],a[48:51],v[188:191]
cv v170,v148,v149
cv v171,v150,v151
mm v[152:155],a[68:71],a[52:55],v[152:155]
sa s58,s60,s56
s_addc_u32 s59, s61, 0
v_add_u32 v212, s55, v214
mm v[152:155],a[72:75],a[56:59],v[152:155]
sa s79,s76,0xc00
bl v239,s[40:43],s79 offen offset:1024 lds
)"
R"(mm v[152:155],a[76:79],a[60:63],v[152:155]
mm v[156:159],a[80:83],a[48:51],v[188:191]
mm v[156:159],a[84:87],a[52:55],v[156:159]
mm v[156:159],a[88:91],a[56:59],v[156:159]
mm v[156:159],a[92:95],a[60:63],v[156:159]
dr a[64:67],v212 offset:0
mm v[200:203],v[208:211],v[168:171],v[200:203]
sa s79,s76,0x1800
bl v238,s[40:43],s79 offen offset:2048 lds
mm v[64:67],a[128:131],v[168:171],v[64:67]
dr a[68:71],v212 offset:1024
mm v[68:71],a[132:135],v[168:171],v[68:71]
ex v152,v152
mm v[72:75],a[136:139],v[168:171],v[72:75]
ex v153,v153
mm v[76:79],a[140:143],v[168:171],v[76:79]
)"
R"(ex v154,v154
mm v[80:83],a[144:147],v[168:171],v[80:83]
ex v155,v155
mm v[84:87],a[148:151],v[168:171],v[84:87]
ex v156,v156
mm v[88:91],a[152:155],v[168:171],v[88:91]
ex v157,v157
mm v[92:95],a[156:159],v[168:171],v[92:95]
ex v158,v158
cv v172,v152,v153
cv v173,v154,v155
cv v174,v156,v157
ex v159,v159
s_nop 0
cv v175,v158,v159
s_nop 1
)"
R"(mm v[204:207],v[208:211],v[172:175],v[204:207]
dr a[72:75],v212 offset:2048
mm v[96:99],a[128:131],v[172:175],v[96:99]
sa s79,s76,0x2400
bl v239,s[40:43],s79 offen offset:3072 lds
mm v[100:103],a[132:135],v[172:175],v[100:103]
sa m0,s77,0x1c000
dr a[76:79],v212 offset:3072
mm v[104:107],a[136:139],v[172:175],v[104:107]
dr a[80:83],v212 offset:4096
mm v[108:111],a[140:143],v[172:175],v[108:111]
dr a[84:87],v212 offset:5120
mm v[112:115],a[144:147],v[172:175],v[112:115]
dr a[88:91],v212 offset:6144
mm v[116:119],a[148:151],v[172:175],v[116:119]
dr a[92:95],v212 offset:7168
)"
R"(mm v[120:123],a[152:155],v[172:175],v[120:123]
sa s79,s76,0x10000
bl v238,s[40:43],s79 offen lds
mm v[124:127],a[156:159],v[172:175],v[124:127]
dr a[128:131],v212 offset:16384
mm v[136:139],a[96:99],a[16:19],v[180:183]
mm v[136:139],a[100:103],a[20:23],v[136:139]
sa s79,s76,0x10c00
bl v239,s[40:43],s79 offen offset:1024 lds
mm v[136:139],a[104:107],a[24:27],v[136:139]
mm v[136:139],a[108:111],a[28:31],v[136:139]
mm v[140:143],a[112:115],a[16:19],v[180:183]
dr a[132:135],v212 offset:17408
mm v[140:143],a[116:119],a[20:23],v[140:143]
mm v[140:143],a[120:123],a[24:27],v[140:143]
s_waitcnt lgkmcnt(10)
)"
R"(mm v[140:143],a[124:127],a[28:31],v[140:143]
sa s79,s76,0x11800
bl v238,s[40:43],s79 offen offset:2048 lds
mm v[144:147],a[96:99],a[32:35],v[184:187]
ex v136,v136
mm v[144:147],a[100:103],a[36:39],v[144:147]
dr a[136:139],v212 offset:18432
mm v[144:147],a[104:107],a[40:43],v[144:147]
ex v137,v137
mm v[144:147],a[108:111],a[44:47],v[144:147]
v_cndmask_b32 v140, v213, v140, s[88:89]
v_cndmask_b32 v141, v213, v141, s[90:91]
mm v[148:151],a[112:115],a[32:35],v[184:187]
v_cndmask_b32 v142, v213, v142, s[92:93]
v_cndmask_b32 v143, v213, v143, s[94:95]
mm v[148:151],a[116:119],a[36:39],v[148:151]
)"
R"(ex v138,v138
mm v[148:151],a[120:123],a[40:43],v[148:151]
ex v139,v139
mm v[148:151],a[124:127],a[44:47],v[148:151]
ex v140,v140
cv v164,v136,v137
cv v165,v138,v139
ex v141,v141
ex v142,v142
cv v166,v140,v141
ex v143,v143
ex v144,v144
cv v167,v142,v143
ex v145,v145
ex v146,v146
mm v[196:199],v[208:211],v[164:167],v[196:199]
)"
R"(sa s79,s76,0x12400
bl v239,s[40:43],s79 offen offset:3072 lds
mm v[32:35],a[160:163],v[164:167],v[32:35]
sa m0,s77,0x24000
dr a[140:143],v212 offset:19456
cv v168,v144,v145
mm v[36:39],a[164:167],v[164:167],v[36:39]
ex v147,v147
mm v[40:43],a[168:171],v[164:167],v[40:43]
ex v148,v148
mm v[44:47],a[172:175],v[164:167],v[44:47]
dr a[144:147],v212 offset:20480
cv v169,v146,v147
mm v[48:51],a[176:179],v[164:167],v[48:51]
sa s79,s76,0x20000
bl v238,s[40:43],s79 offen lds
)"
R"(mm v[52:55],a[180:183],v[164:167],v[52:55]
ex v149,v149
mm v[56:59],a[184:187],v[164:167],v[56:59]
ex v150,v150
mm v[60:63],a[188:191],v[164:167],v[60:63]
ex v151,v151
mm v[152:155],a[96:99],a[48:51],v[188:191]
dr a[148:151],v212 offset:21504
cv v170,v148,v149
mm v[152:155],a[100:103],a[52:55],v[152:155]
sa s79,s76,0x20c00
bl v239,s[40:43],s79 offen offset:1024 lds
mm v[152:155],a[104:107],a[56:59],v[152:155]
cv v171,v150,v151
mm v[152:155],a[108:111],a[60:63],v[152:155]
mm v[156:159],a[112:115],a[48:51],v[188:191]
)"
R"(mm v[156:159],a[116:119],a[52:55],v[156:159]
dr a[152:155],v212 offset:22528
mm v[156:159],a[120:123],a[56:59],v[156:159]
sa s79,s76,0x21800
bl v238,s[40:43],s79 offen offset:2048 lds
mm v[156:159],a[124:127],a[60:63],v[156:159]
mm v[200:203],v[208:211],v[168:171],v[200:203]
ex v152,v152
mm v[64:67],a[160:163],v[168:171],v[64:67]
ex v153,v153
mm v[68:71],a[164:167],v[168:171],v[68:71]
dr a[156:159],v212 offset:23552
cv v172,v152,v153
mm v[72:75],a[168:171],v[168:171],v[72:75]
sa s79,s76,0x22400
bl v239,s[40:43],s79 offen offset:3072 lds
)"
R"(mm v[76:79],a[172:175],v[168:171],v[76:79]
ex v154,v154
mm v[80:83],a[176:179],v[168:171],v[80:83]
ex v155,v155
mm v[84:87],a[180:183],v[168:171],v[84:87]
cv v173,v154,v155
mm v[88:91],a[184:187],v[168:171],v[88:91]
mm v[92:95],a[188:191],v[168:171],v[92:95]
s_waitcnt lgkmcnt(8)
s_setpc_b64 s[58:59]
L_att_finlo:
s_mul_i32 s56, s50, 18432
v_add_u32 v230, s56, v214
s_xor_b32 s62, s50, 2
s_mul_i32 s56, s62, 18432
v_add_u32 v231, s56, v214
)"
R"(ds_write_b128 v230, v[0:3] offset:0
ds_write_b128 v230, v[4:7] offset:1024
ds_write_b128 v230, v[8:11] offset:2048
ds_write_b128 v230, v[12:15] offset:3072
ds_write_b128 v230, v[16:19] offset:4096
ds_write_b128 v230, v[20:23] offset:5120
s_nop 1
ex v156,v156
ex v157,v157
ex v158,v158
cv v174,v156,v157
ex v159,v159
s_nop 0
cv v175,v158,v159
s_nop 1
mm v[204:207],v[208:211],v[172:175],v[204:207]
)"
R"(ds_write_b128 v230, v[24:27] offset:6144
mm v[96:99],a[160:163],v[172:175],v[96:99]
ds_write_b128 v230, v[28:31] offset:7168
mm v[100:103],a[164:167],v[172:175],v[100:103]
ds_write_b128 v230, v[32:35] offset:8192
mm v[104:107],a[168:171],v[172:175],v[104:107]
ds_write_b128 v230, v[36:39] offset:9216
mm v[108:111],a[172:175],v[172:175],v[108:111]
ds_write_b128 v230, v[40:43] offset:10240
mm v[112:115],a[176:179],v[172:175],v[112:115]
ds_write_b128 v230, v[44:47] offset:11264
mm v[116:119],a[180:183],v[172:175],v[116:119]
ds_write_b128 v230, v[48:51] offset:12288
mm v[120:123],a[184:187],v[172:175],v[120:123]
ds_write_b128 v230, v[52:55] offset:13312
mm v[124:127],a[188:191],v[172:175],v[124:127]
)"
R"(ds_write_b128 v230, v[56:59] offset:14336
ds_write_b128 v230, v[60:63] offset:15360
s_nop 7
v_mov_b32 v220, 0x5d800000
v_cmp_nlt_f32 vcc, v192, v220
v_cmp_nlt_f32 s[62:63], v196, v220
s_or_b64 vcc, vcc, s[62:63]
v_cmp_nlt_f32 s[62:63], v200, v220
s_or_b64 vcc, vcc, s[62:63]
v_cmp_nlt_f32 s[62:63], v204, v220
s_or_b64 vcc, vcc, s[62:63]
s_cbranch_vccnz L_redo_finlo
L_ok_finlo:
v_mov_b32 v232, v176
v_mov_b32 v233, v192
v_mov_b32 v234, v180
)"
R"(v_mov_b32 v235, v196
ds_write_b128 v230, v[232:235] offset:16384
s_lshl_b32 s56, s57, 5
sa s56,s56,s48
s_lshl_b32 s56, s56, 15
sa s56,s56,0x2100000
s_and_b32 s69, s50, 1
s_lshl_b32 s69, s69, 10
sa s68,s56,s69
s_sub_u32 s69, s56, s69
s_waitcnt lgkmcnt(0)
s_barrier
dr v[232:235],v231 offset:16384
dr v[0:3],v231 offset:0
dr v[4:7],v231 offset:1024
dr v[8:11],v231 offset:2048
)"
R"(dr v[12:15],v231 offset:3072
dr v[16:19],v231 offset:4096
dr v[20:23],v231 offset:5120
dr v[24:27],v231 offset:6144
dr v[28:31],v231 offset:7168
dr v[32:35],v231 offset:8192
dr v[36:39],v231 offset:9216
dr v[40:43],v231 offset:10240
dr v[44:47],v231 offset:11264
dr v[48:51],v231 offset:12288
dr v[52:55],v231 offset:13312
dr v[56:59],v231 offset:14336
dr v[60:63],v231 offset:15360
buffer_load_dword v251, v250, s[44:47], s81 offen sc1
buffer_load_dword v245, v249, s[44:47], 0 offen sc1
s_waitcnt lgkmcnt(15)
)"
R"(v_min_f32 v228, v184, v232
v_sub_f32 v220, v228, v184
v_sub_f32 v222, v228, v232
v_mul_f32 v220, 0.5, v220
v_mul_f32 v222, 0.5, v222
ex v220,v220
ex v222,v222
v_min_f32 v229, v188, v234
v_sub_f32 v224, v229, v188
v_sub_f32 v226, v229, v234
v_mul_f32 v224, 0.5, v224
v_mul_f32 v226, 0.5, v226
ex v224,v224
ex v226,v226
v_mul_f32 v228, v220, v200
v_mul_f32 v233, v222, v233
)"
R"(v_mul_f32 v228, v228, v220
v_fmac_f32 v228, v222, v233
v_mul_f32 v229, v224, v204
v_mul_f32 v235, v226, v235
v_mul_f32 v229, v229, v224
v_fmac_f32 v229, v226, v235
v_rcp_f32 v228, v228
v_rcp_f32 v229, v229
v_mul_f32 v221, v220, v228
v_mul_f32 v223, v222, v228
v_mul_f32 v220, v220, v221
v_mul_f32 v222, v222, v223
v_mul_f32 v225, v224, v229
v_mul_f32 v227, v226, v229
v_mul_f32 v224, v224, v225
v_mul_f32 v226, v226, v227
)"
R"(v_pk_mul_f32 v[64:65], v[64:65], v[220:221] op_sel_hi:[1,0]
v_pk_mul_f32 v[66:67], v[66:67], v[220:221] op_sel_hi:[1,0]
v_pk_mul_f32 v[68:69], v[68:69], v[220:221] op_sel_hi:[1,0]
v_pk_mul_f32 v[70:71], v[70:71], v[220:221] op_sel_hi:[1,0]
v_pk_mul_f32 v[72:73], v[72:73], v[220:221] op_sel_hi:[1,0]
v_pk_mul_f32 v[74:75], v[74:75], v[220:221] op_sel_hi:[1,0]
v_pk_mul_f32 v[76:77], v[76:77], v[220:221] op_sel_hi:[1,0]
v_pk_mul_f32 v[78:79], v[78:79], v[220:221] op_sel_hi:[1,0]
v_pk_mul_f32 v[80:81], v[80:81], v[220:221] op_sel_hi:[1,0]
v_pk_mul_f32 v[82:83], v[82:83], v[220:221] op_sel_hi:[1,0]
v_pk_mul_f32 v[84:85], v[84:85], v[220:221] op_sel_hi:[1,0]
v_pk_mul_f32 v[86:87], v[86:87], v[220:221] op_sel_hi:[1,0]
v_pk_mul_f32 v[88:89], v[88:89], v[220:221] op_sel_hi:[1,0]
v_pk_mul_f32 v[90:91], v[90:91], v[220:221] op_sel_hi:[1,0]
v_pk_mul_f32 v[92:93], v[92:93], v[220:221] op_sel_hi:[1,0]
v_pk_mul_f32 v[94:95], v[94:95], v[220:221] op_sel_hi:[1,0]
)"
R"(v_pk_mul_f32 v[96:97], v[96:97], v[224:225] op_sel_hi:[1,0]
v_pk_mul_f32 v[98:99], v[98:99], v[224:225] op_sel_hi:[1,0]
v_pk_mul_f32 v[100:101], v[100:101], v[224:225] op_sel_hi:[1,0]
v_pk_mul_f32 v[102:103], v[102:103], v[224:225] op_sel_hi:[1,0]
v_pk_mul_f32 v[104:105], v[104:105], v[224:225] op_sel_hi:[1,0]
v_pk_mul_f32 v[106:107], v[106:107], v[224:225] op_sel_hi:[1,0]
v_pk_mul_f32 v[108:109], v[108:109], v[224:225] op_sel_hi:[1,0]
v_pk_mul_f32 v[110:111], v[110:111], v[224:225] op_sel_hi:[1,0]
v_pk_mul_f32 v[112:113], v[112:113], v[224:225] op_sel_hi:[1,0]
v_pk_mul_f32 v[114:115], v[114:115], v[224:225] op_sel_hi:[1,0]
v_pk_mul_f32 v[116:117], v[116:117], v[224:225] op_sel_hi:[1,0]
v_pk_mul_f32 v[118:119], v[118:119], v[224:225] op_sel_hi:[1,0]
v_pk_mul_f32 v[120:121], v[120:121], v[224:225] op_sel_hi:[1,0]
v_pk_mul_f32 v[122:123], v[122:123], v[224:225] op_sel_hi:[1,0]
v_pk_mul_f32 v[124:125], v[124:125], v[224:225] op_sel_hi:[1,0]
v_pk_mul_f32 v[126:127], v[126:127], v[224:225] op_sel_hi:[1,0]
)"
R"(s_waitcnt lgkmcnt(0)
v_pk_fma_f32 v[64:65], v[0:1], v[222:223], v[64:65] op_sel_hi:[1,0,1]
v_pk_fma_f32 v[66:67], v[2:3], v[222:223], v[66:67] op_sel_hi:[1,0,1]
v_pk_fma_f32 v[68:69], v[4:5], v[222:223], v[68:69] op_sel_hi:[1,0,1]
v_pk_fma_f32 v[70:71], v[6:7], v[222:223], v[70:71] op_sel_hi:[1,0,1]
cv v64,v64,v65
cv v65,v66,v67
cv v66,v68,v69
cv v67,v70,v71
sa s56,s68,0x1000
buffer_store_dwordx4 v[64:67], v214, s[44:47], s56 offen offset:0 sc1
v_pk_fma_f32 v[72:73], v[8:9], v[222:223], v[72:73] op_sel_hi:[1,0,1]
v_pk_fma_f32 v[74:75], v[10:11], v[222:223], v[74:75] op_sel_hi:[1,0,1]
v_pk_fma_f32 v[76:77], v[12:13], v[222:223], v[76:77] op_sel_hi:[1,0,1]
v_pk_fma_f32 v[78:79], v[14:15], v[222:223], v[78:79] op_sel_hi:[1,0,1]
cv v72,v72,v73
)"
R"(cv v73,v74,v75
cv v74,v76,v77
cv v75,v78,v79
sa s56,s68,0x3000
buffer_store_dwordx4 v[72:75], v214, s[44:47], s56 offen offset:0 sc1
v_pk_fma_f32 v[80:81], v[16:17], v[222:223], v[80:81] op_sel_hi:[1,0,1]
v_pk_fma_f32 v[82:83], v[18:19], v[222:223], v[82:83] op_sel_hi:[1,0,1]
v_pk_fma_f32 v[84:85], v[20:21], v[222:223], v[84:85] op_sel_hi:[1,0,1]
v_pk_fma_f32 v[86:87], v[22:23], v[222:223], v[86:87] op_sel_hi:[1,0,1]
cv v80,v80,v81
cv v81,v82,v83
cv v82,v84,v85
cv v83,v86,v87
sa s56,s68,0x5000
buffer_store_dwordx4 v[80:83], v214, s[44:47], s56 offen offset:0 sc1
v_pk_fma_f32 v[88:89], v[24:25], v[222:223], v[88:89] op_sel_hi:[1,0,1]
)"
R"(v_pk_fma_f32 v[90:91], v[26:27], v[222:223], v[90:91] op_sel_hi:[1,0,1]
v_pk_fma_f32 v[92:93], v[28:29], v[222:223], v[92:93] op_sel_hi:[1,0,1]
v_pk_fma_f32 v[94:95], v[30:31], v[222:223], v[94:95] op_sel_hi:[1,0,1]
cv v88,v88,v89
cv v89,v90,v91
cv v90,v92,v93
cv v91,v94,v95
sa s56,s68,0x7000
buffer_store_dwordx4 v[88:91], v214, s[44:47], s56 offen offset:0 sc1
v_pk_fma_f32 v[96:97], v[32:33], v[226:227], v[96:97] op_sel_hi:[1,0,1]
v_pk_fma_f32 v[98:99], v[34:35], v[226:227], v[98:99] op_sel_hi:[1,0,1]
v_pk_fma_f32 v[100:101], v[36:37], v[226:227], v[100:101] op_sel_hi:[1,0,1]
v_pk_fma_f32 v[102:103], v[38:39], v[226:227], v[102:103] op_sel_hi:[1,0,1]
cv v96,v96,v97
cv v97,v98,v99
cv v98,v100,v101
)"
R"(cv v99,v102,v103
sa s56,s69,0x1000
buffer_store_dwordx4 v[96:99], v214, s[44:47], s56 offen offset:3072 sc1
v_pk_fma_f32 v[104:105], v[40:41], v[226:227], v[104:105] op_sel_hi:[1,0,1]
v_pk_fma_f32 v[106:107], v[42:43], v[226:227], v[106:107] op_sel_hi:[1,0,1]
v_pk_fma_f32 v[108:109], v[44:45], v[226:227], v[108:109] op_sel_hi:[1,0,1]
v_pk_fma_f32 v[110:111], v[46:47], v[226:227], v[110:111] op_sel_hi:[1,0,1]
cv v104,v104,v105
cv v105,v106,v107
cv v106,v108,v109
cv v107,v110,v111
sa s56,s69,0x3000
buffer_store_dwordx4 v[104:107], v214, s[44:47], s56 offen offset:3072 sc1
v_pk_fma_f32 v[112:113], v[48:49], v[226:227], v[112:113] op_sel_hi:[1,0,1]
v_pk_fma_f32 v[114:115], v[50:51], v[226:227], v[114:115] op_sel_hi:[1,0,1]
v_pk_fma_f32 v[116:117], v[52:53], v[226:227], v[116:117] op_sel_hi:[1,0,1]
)"
R"(v_pk_fma_f32 v[118:119], v[54:55], v[226:227], v[118:119] op_sel_hi:[1,0,1]
cv v112,v112,v113
cv v113,v114,v115
cv v114,v116,v117
cv v115,v118,v119
sa s56,s69,0x5000
buffer_store_dwordx4 v[112:115], v214, s[44:47], s56 offen offset:3072 sc1
v_pk_fma_f32 v[120:121], v[56:57], v[226:227], v[120:121] op_sel_hi:[1,0,1]
v_pk_fma_f32 v[122:123], v[58:59], v[226:227], v[122:123] op_sel_hi:[1,0,1]
v_pk_fma_f32 v[124:125], v[60:61], v[226:227], v[124:125] op_sel_hi:[1,0,1]
v_pk_fma_f32 v[126:127], v[62:63], v[226:227], v[126:127] op_sel_hi:[1,0,1]
cv v120,v120,v121
cv v121,v122,v123
cv v122,v124,v125
cv v123,v126,v127
sa s56,s69,0x7000
)"
R"(buffer_store_dwordx4 v[120:123], v214, s[44:47], s56 offen offset:3072 sc1
s_branch L_att_end
.p2align 12
L_att_a1_0:
mm v[128:131],a[64:67],a[0:3],v[176:179]
dr a[96:99],v212 offset:8192
v_readlane_b32 s55, v219, s52
mm v[128:131],a[68:71],a[4:7],v[128:131]
sa s52,s52,1
v_bfe_u32 v241, v214, 4, 5
mm v[128:131],a[72:75],a[8:11],v[128:131]
s_and_b32 s74, s51, 7
ex v156,v156
mm v[128:131],a[76:79],a[12:15],v[128:131]
s_lshl_b32 s74, s74, 7
ex v157,v157
)"
R"(mm v[132:135],a[80:83],a[0:3],v[176:179]
sa s74,s74,0x12000
ex v158,v158
mm v[132:135],a[84:87],a[4:7],v[132:135]
dr a[100:103],v212 offset:9216
v_lshl_add_u32 v249, v241, 2, s74
mm v[132:135],a[88:91],a[8:11],v[132:135]
s_lshr_b32 s74, s51, 5
ex v159,v159
mm v[132:135],a[92:95],a[12:15],v[132:135]
s_lshl_b32 s74, s74, 5
cv v174,v156,v157
cv v175,v158,v159
ex v128,v128
ex v129,v129
mm v[204:207],v[208:211],v[172:175],v[204:207]
)"
R"(s_and_b32 s80, s51, 1
dr a[104:107],v212 offset:10240
cv v160,v128,v129
mm v[96:99],a[160:163],v[172:175],v[96:99]
s_lshl_b32 s80, s80, 2
dr a[108:111],v212 offset:11264
v_cndmask_b32 v132, v213, v132, s[88:89]
mm v[100:103],a[164:167],v[172:175],v[100:103]
sa s74,s74,s80
dr a[112:115],v212 offset:12288
v_cndmask_b32 v133, v213, v133, s[90:91]
mm v[104:107],a[168:171],v[172:175],v[104:107]
s_bfe_u32 s80, s51, 0x20003
dr a[116:119],v212 offset:13312
v_cndmask_b32 v134, v213, v134, s[92:93]
mm v[108:111],a[172:175],v[172:175],v[108:111]
)"
R"(sa s74,s74,s80
dr a[120:123],v212 offset:14336
v_cndmask_b32 v135, v213, v135, s[94:95]
mm v[112:115],a[176:179],v[172:175],v[112:115]
s_lshl_b32 s74, s74, 4
ex v130,v130
mm v[116:119],a[180:183],v[172:175],v[116:119]
sa s74,s74,0x11000
dr a[124:127],v212 offset:15360
v_readlane_b32 s56, v216, s52
mm v[120:123],a[184:187],v[172:175],v[120:123]
v_bfe_u32 v241, v214, 6, 2
v_lshl_add_u32 v244, v241, 7, s74
mm v[124:127],a[188:191],v[172:175],v[124:127]
v_bfe_u32 v241, v214, 4, 2
v_lshl_add_u32 v244, v241, 2, v244
)"
R"(mm v[136:139],a[64:67],a[16:19],v[180:183]
s_mov_b32 s40, s82
dr a[160:163],v212 offset:24576
mm v[136:139],a[68:71],a[20:23],v[136:139]
s_and_b32 s41, s83, 0xffff
dr a[164:167],v212 offset:25600
mm v[136:139],a[72:75],a[24:27],v[136:139]
s_mov_b32 s42, 0x400000
ex v131,v131
mm v[136:139],a[76:79],a[28:31],v[136:139]
s_mov_b32 s43, 0x20000
ex v132,v132
mm v[140:143],a[80:83],a[16:19],v[180:183]
s_lshl_b32 s77, s50, 12
ex v133,v133
mm v[140:143],a[84:87],a[20:23],v[140:143]
)"
R"(s_and_b32 s76, s51, 7
ex v134,v134
mm v[140:143],a[88:91],a[24:27],v[140:143]
s_lshl_b32 s76, s76, 19
ex v135,v135
mm v[140:143],a[92:95],a[28:31],v[140:143]
s_lshl_b32 s80, s50, 14
s_waitcnt lgkmcnt(10)
dr a[168:171],v212 offset:26624
cv v161,v130,v131
cv v162,v132,v133
cv v163,v134,v135
ex v136,v136
ex v137,v137
mm v[192:195],v[208:211],v[160:163],v[192:195]
sa s76,s76,s80
)"
R"(dr a[172:175],v212 offset:27648
cv v164,v136,v137
mm v[0:3],a[128:131],v[160:163],v[0:3]
s_bfe_u32 s80, s51, 0x20003
dr a[176:179],v212 offset:28672
mm v[4:7],a[132:135],v[160:163],v[4:7]
s_lshl_b32 s80, s80, 8
dr a[180:183],v212 offset:29696
mm v[8:11],a[136:139],v[160:163],v[8:11]
sa s76,s76,s80
dr a[184:187],v212 offset:30720
mm v[12:15],a[140:143],v[160:163],v[12:15]
v_lshrrev_b32 v241, 8, v214
v_lshlrev_b32 v238, 10, v241
mm v[16:19],a[144:147],v[160:163],v[16:19]
v_lshlrev_b32 v241, 1, v241
)"
R"(v_bfe_u32 v242, v214, 4, 4
mm v[20:23],a[148:151],v[160:163],v[20:23]
v_xor_b32 v242, v242, v241
v_lshl_add_u32 v238, v242, 4, v238
mm v[24:27],a[152:155],v[160:163],v[24:27]
dr a[188:191],v212 offset:31744
v_xor_b32 v239, 128, v238
mm v[28:31],a[156:159],v[160:163],v[28:31]
s_lshl_b32 s81, s49, 3
ex v138,v138
mm v[144:147],a[64:67],a[32:35],v[184:187]
s_and_b32 s80, s51, 7
ex v139,v139
mm v[144:147],a[68:71],a[36:39],v[144:147]
sa s81,s81,s80
ex v140,v140
)"
R"(mm v[144:147],a[72:75],a[40:43],v[144:147]
s_lshl_b32 s81, s81, 6
ex v141,v141
mm v[144:147],a[76:79],a[44:47],v[144:147]
s_and_b32 s80, s51, 1
ex v142,v142
mm v[148:151],a[80:83],a[32:35],v[184:187]
s_mul_i32 s80, s80, 0x2000
ex v143,v143
mm v[148:151],a[84:87],a[36:39],v[148:151]
sa s81,s81,s80
cv v165,v138,v139
cv v166,v140,v141
mm v[148:151],a[88:91],a[40:43],v[148:151]
sa s81,s81,0x11000
cv v167,v142,v143
)"
R"(v_add_u32 v212, s55, v214
v_bfe_u32 v250, v214, 4, 4
v_lshlrev_b32 v250, 2, v250
s_waitcnt vmcnt(0) lgkmcnt(0)
s_barrier
mm v[148:151],a[92:95],a[44:47],v[148:151]
ex v144,v144
mm v[196:199],v[208:211],v[164:167],v[196:199]
ex v145,v145
mm v[32:35],a[128:131],v[164:167],v[32:35]
sa m0,s77,0x14000
ex v146,v146
mm v[36:39],a[132:135],v[164:167],v[36:39]
ex v147,v147
mm v[40:43],a[136:139],v[164:167],v[40:43]
ex v148,v148
)"
R"(mm v[44:47],a[140:143],v[164:167],v[44:47]
ex v149,v149
mm v[48:51],a[144:147],v[164:167],v[48:51]
ex v150,v150
mm v[52:55],a[148:151],v[164:167],v[52:55]
sa s79,s76,0x0
bl v238,s[40:43],s79 offen lds
mm v[56:59],a[152:155],v[164:167],v[56:59]
ex v151,v151
mm v[60:63],a[156:159],v[164:167],v[60:63]
cv v168,v144,v145
cv v169,v146,v147
mm v[152:155],a[64:67],a[48:51],v[188:191]
cv v170,v148,v149
cv v171,v150,v151
mm v[152:155],a[68:71],a[52:55],v[152:155]
)"
R"(sa s58,s60,s56
s_addc_u32 s59, s61, 0
mm v[152:155],a[72:75],a[56:59],v[152:155]
sa s79,s76,0xc00
bl v239,s[40:43],s79 offen offset:1024 lds
mm v[152:155],a[76:79],a[60:63],v[152:155]
mm v[156:159],a[80:83],a[48:51],v[188:191]
mm v[156:159],a[84:87],a[52:55],v[156:159]
mm v[156:159],a[88:91],a[56:59],v[156:159]
mm v[156:159],a[92:95],a[60:63],v[156:159]
dr a[64:67],v212 offset:0
mm v[200:203],v[208:211],v[168:171],v[200:203]
sa s79,s76,0x1800
bl v238,s[40:43],s79 offen offset:2048 lds
mm v[64:67],a[128:131],v[168:171],v[64:67]
dr a[68:71],v212 offset:1024
)"
R"(mm v[68:71],a[132:135],v[168:171],v[68:71]
ex v152,v152
mm v[72:75],a[136:139],v[168:171],v[72:75]
ex v153,v153
mm v[76:79],a[140:143],v[168:171],v[76:79]
ex v154,v154
mm v[80:83],a[144:147],v[168:171],v[80:83]
ex v155,v155
mm v[84:87],a[148:151],v[168:171],v[84:87]
ex v156,v156
mm v[88:91],a[152:155],v[168:171],v[88:91]
ex v157,v157
mm v[92:95],a[156:159],v[168:171],v[92:95]
ex v158,v158
cv v172,v152,v153
cv v173,v154,v155
)"
R"(cv v174,v156,v157
ex v159,v159
s_nop 0
cv v175,v158,v159
s_nop 1
mm v[204:207],v[208:211],v[172:175],v[204:207]
dr a[72:75],v212 offset:2048
mm v[96:99],a[128:131],v[172:175],v[96:99]
sa s79,s76,0x2400
bl v239,s[40:43],s79 offen offset:3072 lds
mm v[100:103],a[132:135],v[172:175],v[100:103]
sa m0,s77,0x1c000
dr a[76:79],v212 offset:3072
mm v[104:107],a[136:139],v[172:175],v[104:107]
dr a[80:83],v212 offset:4096
mm v[108:111],a[140:143],v[172:175],v[108:111]
)"
R"(dr a[84:87],v212 offset:5120
mm v[112:115],a[144:147],v[172:175],v[112:115]
dr a[88:91],v212 offset:6144
mm v[116:119],a[148:151],v[172:175],v[116:119]
dr a[92:95],v212 offset:7168
mm v[120:123],a[152:155],v[172:175],v[120:123]
sa s79,s76,0x10000
bl v238,s[40:43],s79 offen lds
mm v[124:127],a[156:159],v[172:175],v[124:127]
dr a[128:131],v212 offset:16384
mm v[136:139],a[96:99],a[16:19],v[180:183]
v_mov_b32 v166, 0
v_mov_b32 v167, 0
mm v[136:139],a[100:103],a[20:23],v[136:139]
sa s79,s76,0x10c00
bl v239,s[40:43],s79 offen offset:1024 lds
)"
R"(mm v[136:139],a[104:107],a[24:27],v[136:139]
s_waitcnt lgkmcnt(9)
mm v[136:139],a[108:111],a[28:31],v[136:139]
dr a[132:135],v212 offset:17408
mm v[144:147],a[96:99],a[32:35],v[184:187]
mm v[144:147],a[100:103],a[36:39],v[144:147]
mm v[144:147],a[104:107],a[40:43],v[144:147]
sa s79,s76,0x11800
bl v238,s[40:43],s79 offen offset:2048 lds
mm v[144:147],a[108:111],a[44:47],v[144:147]
mm v[148:151],a[112:115],a[32:35],v[184:187]
v_cndmask_b32 v136, v213, v136, s[88:89]
v_cndmask_b32 v137, v213, v137, s[90:91]
mm v[148:151],a[116:119],a[36:39],v[148:151]
v_cndmask_b32 v138, v213, v138, s[92:93]
v_cndmask_b32 v139, v213, v139, s[94:95]
)"
R"(mm v[148:151],a[120:123],a[40:43],v[148:151]
ex v136,v136
mm v[148:151],a[124:127],a[44:47],v[148:151]
ex v137,v137
ex v138,v138
cv v164,v136,v137
ex v139,v139
ex v144,v144
cv v165,v138,v139
ex v145,v145
ex v146,v146
mm v[196:199],v[208:211],v[164:167],v[196:199]
dr a[136:139],v212 offset:18432
cv v168,v144,v145
mm v[32:35],a[160:163],v[164:167],v[32:35]
sa s79,s76,0x12400
)"
R"(bl v239,s[40:43],s79 offen offset:3072 lds
mm v[36:39],a[164:167],v[164:167],v[36:39]
dr a[140:143],v212 offset:19456
mm v[40:43],a[168:171],v[164:167],v[40:43]
ex v147,v147
mm v[44:47],a[172:175],v[164:167],v[44:47]
sa m0,s77,0x24000
ex v148,v148
mm v[48:51],a[176:179],v[164:167],v[48:51]
ex v149,v149
mm v[52:55],a[180:183],v[164:167],v[52:55]
dr a[144:147],v212 offset:20480
cv v169,v146,v147
mm v[56:59],a[184:187],v[164:167],v[56:59]
ex v150,v150
mm v[60:63],a[188:191],v[164:167],v[60:63]
)"
R"(ex v151,v151
mm v[152:155],a[96:99],a[48:51],v[188:191]
sa s79,s76,0x20000
bl v238,s[40:43],s79 offen lds
mm v[152:155],a[100:103],a[52:55],v[152:155]
dr a[148:151],v212 offset:21504
cv v170,v148,v149
mm v[152:155],a[104:107],a[56:59],v[152:155]
cv v171,v150,v151
mm v[152:155],a[108:111],a[60:63],v[152:155]
mm v[156:159],a[112:115],a[48:51],v[188:191]
mm v[156:159],a[116:119],a[52:55],v[156:159]
sa s79,s76,0x20c00
bl v239,s[40:43],s79 offen offset:1024 lds
mm v[156:159],a[120:123],a[56:59],v[156:159]
dr a[152:155],v212 offset:22528
)"
R"(mm v[156:159],a[124:127],a[60:63],v[156:159]
mm v[200:203],v[208:211],v[168:171],v[200:203]
ex v152,v152
mm v[64:67],a[160:163],v[168:171],v[64:67]
ex v153,v153
mm v[68:71],a[164:167],v[168:171],v[68:71]
sa s79,s76,0x21800
bl v238,s[40:43],s79 offen offset:2048 lds
mm v[72:75],a[168:171],v[168:171],v[72:75]
dr a[156:159],v212 offset:23552
cv v172,v152,v153
mm v[76:79],a[172:175],v[168:171],v[76:79]
ex v154,v154
mm v[80:83],a[176:179],v[168:171],v[80:83]
ex v155,v155
mm v[84:87],a[180:183],v[168:171],v[84:87]
)"
R"(cv v173,v154,v155
mm v[88:91],a[184:187],v[168:171],v[88:91]
sa s79,s76,0x22400
bl v239,s[40:43],s79 offen offset:3072 lds
mm v[92:95],a[188:191],v[168:171],v[92:95]
s_waitcnt lgkmcnt(8)
s_setpc_b64 s[58:59]
L_att_fb1_0:
mm v[144:147],a[64:67],a[32:35],0
dr a[96:99],v212 offset:8192
v_readlane_b32 s55, v219, s52
mm v[144:147],a[68:71],a[36:39],v[144:147]
sa s52,s52,1
dr a[100:103],v212 offset:9216
v_bfe_u32 v241, v214, 4, 5
mm v[144:147],a[72:75],a[40:43],v[144:147]
)"
R"(s_and_b32 s74, s51, 7
dr a[104:107],v212 offset:10240
v_readlane_b32 s56, v216, s52
mm v[144:147],a[76:79],a[44:47],v[144:147]
s_lshl_b32 s74, s74, 7
dr a[108:111],v212 offset:11264
v_mov_b32 v0, 0
mm v[148:151],a[80:83],a[32:35],0
sa s74,s74,0x12000
dr a[112:115],v212 offset:12288
v_mov_b32 v1, 0
mm v[148:151],a[84:87],a[36:39],v[148:151]
v_lshl_add_u32 v249, v241, 2, s74
v_max3_f32 v224, v144, v145, v146
mm v[148:151],a[88:91],a[40:43],v[148:151]
s_lshr_b32 s74, s51, 5
)"
R"(dr a[116:119],v212 offset:13312
v_mov_b32 v2, 0
mm v[148:151],a[92:95],a[44:47],v[148:151]
s_lshl_b32 s74, s74, 5
dr a[120:123],v212 offset:14336
v_mov_b32 v3, 0
mm v[152:155],a[64:67],a[48:51],0
s_and_b32 s80, s51, 1
dr a[124:127],v212 offset:15360
v_mov_b32 v4, 0
mm v[152:155],a[68:71],a[52:55],v[152:155]
s_lshl_b32 s80, s80, 2
v_cndmask_b32 v148, v213, v148, s[88:89]
v_cndmask_b32 v149, v213, v149, s[90:91]
mm v[152:155],a[72:75],a[56:59],v[152:155]
sa s74,s74,s80
)"
R"(v_cndmask_b32 v150, v213, v150, s[92:93]
v_max3_f32 v224, v224, v147, v148
mm v[152:155],a[76:79],a[60:63],v[152:155]
s_bfe_u32 s80, s51, 0x20003
v_cndmask_b32 v151, v213, v151, s[94:95]
v_max3_f32 v224, v224, v149, v150
mm v[156:159],a[80:83],a[48:51],0
sa s74,s74,s80
v_max_f32 v224, v224, v151
v_mov_b32 v225, v224
mm v[156:159],a[84:87],a[52:55],v[156:159]
s_lshl_b32 s74, s74, 4
dr a[160:163],v212 offset:24576
v_max3_f32 v226, v152, v153, v154
mm v[156:159],a[88:91],a[56:59],v[156:159]
sa s74,s74,0x11000
)"
R"(v_permlane32_swap_b32 v224, v225
mm v[156:159],a[92:95],a[60:63],v[156:159]
s_waitcnt lgkmcnt(9)
v_bfe_u32 v241, v214, 6, 2
v_lshl_add_u32 v244, v241, 7, s74
v_max_f32 v224, v224, v225
v_mov_b32 v225, v224
v_bfe_u32 v241, v214, 4, 2
v_lshl_add_u32 v244, v241, 2, v244
v_permlane16_swap_b32 v224, v225
v_max3_f32 v226, v226, v155, v156
v_max_f32 v224, v224, v225
v_sub_f32 v184, 0xc2c80000, v224
v_add_f32 v144, v144, v184
ex v144,v144
v_add_f32 v145, v145, v184
)"
R"(ex v145,v145
v_max3_f32 v226, v226, v157, v158
cv v168,v144,v145
v_add_f32 v146, v146, v184
ex v146,v146
v_add_f32 v147, v147, v184
ex v147,v147
v_max_f32 v226, v226, v159
cv v169,v146,v147
v_add_f32 v148, v148, v184
ex v148,v148
v_add_f32 v149, v149, v184
ex v149,v149
v_add_f32 v150, v150, v184
cv v170,v148,v149
ex v150,v150
)"
R"(v_add_f32 v151, v151, v184
ex v151,v151
v_mov_b32 v227, v226
cv v171,v150,v151
v_mov_b32 v5, 0
v_permlane32_swap_b32 v226, v227
mm v[200:203],v[208:211],v[168:171],0
s_mov_b32 s40, s82
dr a[164:167],v212 offset:25600
v_max_f32 v226, v226, v227
mm v[64:67],a[128:131],v[168:171],0
s_and_b32 s41, s83, 0xffff
dr a[168:171],v212 offset:26624
v_mov_b32 v227, v226
mm v[68:71],a[132:135],v[168:171],0
s_mov_b32 s42, 0x400000
)"
R"(dr a[172:175],v212 offset:27648
v_mov_b32 v6, 0
mm v[72:75],a[136:139],v[168:171],0
s_mov_b32 s43, 0x20000
v_permlane16_swap_b32 v226, v227
mm v[76:79],a[140:143],v[168:171],0
s_lshl_b32 s77, s50, 12
dr a[176:179],v212 offset:28672
v_max_f32 v226, v226, v227
mm v[80:83],a[144:147],v[168:171],0
s_and_b32 s76, s51, 7
dr a[180:183],v212 offset:29696
v_sub_f32 v188, 0xc2c80000, v226
mm v[84:87],a[148:151],v[168:171],0
s_lshl_b32 s76, s76, 19
v_add_f32 v152, v152, v188
)"
R"(v_add_f32 v153, v153, v188
mm v[88:91],a[152:155],v[168:171],0
s_lshl_b32 s80, s50, 14
v_add_f32 v154, v154, v188
v_add_f32 v155, v155, v188
mm v[92:95],a[156:159],v[168:171],0
sa s76,s76,s80
v_add_f32 v156, v156, v188
v_add_f32 v157, v157, v188
ex v152,v152
ex v153,v153
v_add_f32 v158, v158, v188
cv v172,v152,v153
ex v154,v154
ex v155,v155
v_add_f32 v159, v159, v188
)"
R"(cv v173,v154,v155
ex v156,v156
ex v157,v157
ex v158,v158
cv v174,v156,v157
ex v159,v159
v_sub_f32 v189, 0xc2c80000, v226
cv v175,v158,v159
v_sub_f32 v190, 0xc2c80000, v226
v_sub_f32 v191, 0xc2c80000, v226
mm v[204:207],v[208:211],v[172:175],0
s_bfe_u32 s80, s51, 0x20003
dr a[184:187],v212 offset:30720
v_mov_b32 v7, 0
mm v[96:99],a[128:131],v[172:175],0
s_lshl_b32 s80, s80, 8
)"
R"(dr a[188:191],v212 offset:31744
v_mov_b32 v8, 0
mm v[100:103],a[132:135],v[172:175],0
sa s76,s76,s80
v_add_u32 v212, s55, v214
v_mov_b32 v9, 0
mm v[104:107],a[136:139],v[172:175],0
v_lshrrev_b32 v241, 8, v214
v_lshlrev_b32 v238, 10, v241
mm v[108:111],a[140:143],v[172:175],0
v_lshlrev_b32 v241, 1, v241
v_bfe_u32 v242, v214, 4, 4
mm v[112:115],a[144:147],v[172:175],0
v_xor_b32 v242, v242, v241
v_lshl_add_u32 v238, v242, 4, v238
mm v[116:119],a[148:151],v[172:175],0
)"
R"(sa s58,s60,s56
s_addc_u32 s59, s61, 0
v_xor_b32 v239, 128, v238
mm v[120:123],a[152:155],v[172:175],0
s_lshl_b32 s81, s49, 3
s_waitcnt lgkmcnt(0)
v_mov_b32 v10, 0
s_and_b32 s80, s51, 7
sa s81,s81,s80
s_lshl_b32 s81, s81, 6
s_and_b32 s80, s51, 1
s_mul_i32 s80, s80, 0x2000
sa s81,s81,s80
sa s81,s81,0x11000
v_bfe_u32 v250, v214, 4, 4
v_lshlrev_b32 v250, 2, v250
)"
R"(s_waitcnt vmcnt(0) lgkmcnt(0)
s_barrier
s_lshl_b32 s72, s49, 5
sa s72,s72,s48
s_lshl_b32 s72, s72, 1
s_and_b32 s73, s50, 1
sa s72,s72,s73
s_lshl_b32 s72, s72, 3
sa s72,s72,0x13000
v_mov_b32 v228, 1
v_mov_b32 v229, 1
v_mov_b32 v230, 0
s_mov_b64 exec, 1
buffer_store_dwordx2 v[228:229], v230, s[44:47], s72 offen sc1
s_mov_b64 exec, -1
mm v[124:127],a[156:159],v[172:175],0
)"
R"(dr a[64:67],v212 offset:0
v_mov_b32 v11, 0
mm v[152:155],a[96:99],a[48:51],v[188:191]
dr a[68:71],v212 offset:1024
v_mov_b32 v12, 0
mm v[152:155],a[100:103],a[52:55],v[152:155]
sa m0,s77,0x14000
dr a[72:75],v212 offset:2048
v_mov_b32 v13, 0
mm v[152:155],a[104:107],a[56:59],v[152:155]
dr a[76:79],v212 offset:3072
v_mov_b32 v14, 0
mm v[152:155],a[108:111],a[60:63],v[152:155]
sa s79,s76,0x0
bl v238,s[40:43],s79 offen lds
sa s79,s76,0xc00
)"
R"(bl v239,s[40:43],s79 offen offset:1024 lds
dr a[80:83],v212 offset:4096
sa s79,s76,0x1800
bl v238,s[40:43],s79 offen offset:2048 lds
dr a[84:87],v212 offset:5120
sa s79,s76,0x2400
bl v239,s[40:43],s79 offen offset:3072 lds
dr a[88:91],v212 offset:6144
sa m0,s77,0x1c000
dr a[92:95],v212 offset:7168
sa s79,s76,0x10000
bl v238,s[40:43],s79 offen lds
dr a[128:131],v212 offset:16384
sa s79,s76,0x10c00
bl v239,s[40:43],s79 offen offset:1024 lds
dr a[132:135],v212 offset:17408
)"
R"(sa s79,s76,0x11800
bl v238,s[40:43],s79 offen offset:2048 lds
dr a[136:139],v212 offset:18432
sa s79,s76,0x12400
bl v239,s[40:43],s79 offen offset:3072 lds
dr a[140:143],v212 offset:19456
sa m0,s77,0x24000
dr a[144:147],v212 offset:20480
sa s79,s76,0x20000
bl v238,s[40:43],s79 offen lds
dr a[148:151],v212 offset:21504
sa s79,s76,0x20c00
bl v239,s[40:43],s79 offen offset:1024 lds
dr a[152:155],v212 offset:22528
sa s79,s76,0x21800
bl v238,s[40:43],s79 offen offset:2048 lds
)"
R"(dr a[156:159],v212 offset:23552
sa s79,s76,0x22400
bl v239,s[40:43],s79 offen offset:3072 lds
v_cndmask_b32 v152, v213, v152, s[88:89]
v_cndmask_b32 v153, v213, v153, s[90:91]
v_cndmask_b32 v154, v213, v154, s[92:93]
v_cndmask_b32 v155, v213, v155, s[94:95]
ex v152,v152
ex v153,v153
ex v154,v154
ex v155,v155
v_mov_b32 v15, 0
v_mov_b32 v16, 0
v_mov_b32 v17, 0
v_mov_b32 v18, 0
v_mov_b32 v19, 0
)"
R"(v_mov_b32 v20, 0
v_mov_b32 v21, 0
v_mov_b32 v22, 0
v_mov_b32 v23, 0
v_mov_b32 v24, 0
v_mov_b32 v25, 0
v_mov_b32 v26, 0
v_mov_b32 v27, 0
v_mov_b32 v28, 0
v_mov_b32 v29, 0
v_mov_b32 v30, 0
v_mov_b32 v31, 0
v_mov_b32 v192, 0
v_mov_b32 v176, 0x7f000000
v_mov_b32 v32, 0
v_mov_b32 v33, 0
)"
R"(v_mov_b32 v34, 0
v_mov_b32 v35, 0
v_mov_b32 v36, 0
v_mov_b32 v37, 0
v_mov_b32 v38, 0
v_mov_b32 v39, 0
v_mov_b32 v40, 0
v_mov_b32 v41, 0
v_mov_b32 v42, 0
v_mov_b32 v43, 0
v_mov_b32 v44, 0
v_mov_b32 v45, 0
v_mov_b32 v46, 0
v_mov_b32 v47, 0
v_mov_b32 v48, 0
v_mov_b32 v49, 0
)"
R"(v_mov_b32 v50, 0
v_mov_b32 v51, 0
v_mov_b32 v52, 0
v_mov_b32 v53, 0
v_mov_b32 v54, 0
v_mov_b32 v55, 0
v_mov_b32 v56, 0
v_mov_b32 v57, 0
v_mov_b32 v58, 0
v_mov_b32 v59, 0
v_mov_b32 v60, 0
v_mov_b32 v61, 0
v_mov_b32 v62, 0
v_mov_b32 v63, 0
v_mov_b32 v196, 0
v_mov_b32 v180, 0x7f000000
)"
R"(v_sub_f32 v185, 0xc2c80000, v224
v_sub_f32 v186, 0xc2c80000, v224
v_sub_f32 v187, 0xc2c80000, v224
cv v172,v152,v153
cv v173,v154,v155
v_mov_b32 v174, 0
v_mov_b32 v175, 0
s_waitcnt lgkmcnt(8)
s_setpc_b64 s[58:59]
.p2align 12
L_att_b0_0:
ex v156,v156
ex v157,v157
ex v158,v158
cv v174,v156,v157
ex v159,v159
)"
R"(s_nop 0
cv v175,v158,v159
s_nop 1
mm v[204:207],v[208:211],v[172:175],v[204:207]
dr a[96:99],v212 offset:8192
v_readlane_b32 s55, v219, s52
mm v[96:99],a[160:163],v[172:175],v[96:99]
sa s52,s52,1
dr a[100:103],v212 offset:9216
v_bfe_u32 v241, v214, 4, 5
mm v[100:103],a[164:167],v[172:175],v[100:103]
s_and_b32 s74, s51, 7
dr a[104:107],v212 offset:10240
v_readlane_b32 s56, v216, s52
mm v[104:107],a[168:171],v[172:175],v[104:107]
s_lshl_b32 s74, s74, 7
)"
R"(dr a[108:111],v212 offset:11264
mm v[108:111],a[172:175],v[172:175],v[108:111]
sa s74,s74,0x12000
dr a[112:115],v212 offset:12288
mm v[112:115],a[176:179],v[172:175],v[112:115]
dr a[116:119],v212 offset:13312
v_lshl_add_u32 v249, v241, 2, s74
mm v[116:119],a[180:183],v[172:175],v[116:119]
s_lshr_b32 s74, s51, 5
dr a[120:123],v212 offset:14336
mm v[120:123],a[184:187],v[172:175],v[120:123]
s_lshl_b32 s74, s74, 5
s_waitcnt lgkmcnt(7)
dr a[124:127],v212 offset:15360
mm v[124:127],a[188:191],v[172:175],v[124:127]
s_and_b32 s80, s51, 1
)"
R"(dr a[160:163],v212 offset:24576
mm v[144:147],a[64:67],a[32:35],v[184:187]
s_lshl_b32 s80, s80, 2
dr a[164:167],v212 offset:25600
v_mov_b32 v170, 0
mm v[144:147],a[68:71],a[36:39],v[144:147]
sa s74,s74,s80
v_mov_b32 v171, 0
mm v[144:147],a[72:75],a[40:43],v[144:147]
s_bfe_u32 s80, s51, 0x20003
dr a[168:171],v212 offset:26624
mm v[144:147],a[76:79],a[44:47],v[144:147]
sa s74,s74,s80
sa s58,s60,s56
s_addc_u32 s59, s61, 0
mm v[152:155],a[64:67],a[48:51],v[188:191]
)"
R"(s_lshl_b32 s74, s74, 4
dr a[172:175],v212 offset:27648
mm v[152:155],a[68:71],a[52:55],v[152:155]
sa s74,s74,0x11000
mm v[152:155],a[72:75],a[56:59],v[152:155]
v_bfe_u32 v241, v214, 6, 2
v_lshl_add_u32 v244, v241, 7, s74
mm v[152:155],a[76:79],a[60:63],v[152:155]
v_bfe_u32 v241, v214, 4, 2
v_lshl_add_u32 v244, v241, 2, v244
mm v[156:159],a[80:83],a[48:51],v[188:191]
s_mov_b32 s40, s82
v_cndmask_b32 v144, v213, v144, s[88:89]
v_cndmask_b32 v145, v213, v145, s[90:91]
mm v[156:159],a[84:87],a[52:55],v[156:159]
s_and_b32 s41, s83, 0xffff
)"
R"(v_cndmask_b32 v146, v213, v146, s[92:93]
v_cndmask_b32 v147, v213, v147, s[94:95]
mm v[156:159],a[88:91],a[56:59],v[156:159]
s_mov_b32 s42, 0x400000
ex v144,v144
mm v[156:159],a[92:95],a[60:63],v[156:159]
s_mov_b32 s43, 0x20000
ex v145,v145
ex v146,v146
cv v168,v144,v145
ex v147,v147
ex v152,v152
cv v169,v146,v147
ex v153,v153
ex v154,v154
mm v[200:203],v[208:211],v[168:171],v[200:203]
)"
R"(s_lshl_b32 s77, s50, 12
dr a[176:179],v212 offset:28672
cv v172,v152,v153
mm v[64:67],a[128:131],v[168:171],v[64:67]
s_and_b32 s76, s51, 7
dr a[180:183],v212 offset:29696
mm v[68:71],a[132:135],v[168:171],v[68:71]
s_lshl_b32 s76, s76, 19
dr a[184:187],v212 offset:30720
mm v[72:75],a[136:139],v[168:171],v[72:75]
s_lshl_b32 s80, s50, 14
dr a[188:191],v212 offset:31744
mm v[76:79],a[140:143],v[168:171],v[76:79]
sa s76,s76,s80
ex v155,v155
mm v[80:83],a[144:147],v[168:171],v[80:83]
)"
R"(s_bfe_u32 s80, s51, 0x20003
ex v156,v156
mm v[84:87],a[148:151],v[168:171],v[84:87]
s_lshl_b32 s80, s80, 8
ex v157,v157
mm v[88:91],a[152:155],v[168:171],v[88:91]
sa s76,s76,s80
ex v158,v158
mm v[92:95],a[156:159],v[168:171],v[92:95]
v_lshrrev_b32 v241, 8, v214
v_lshlrev_b32 v238, 10, v241
cv v173,v154,v155
cv v174,v156,v157
ex v159,v159
v_lshlrev_b32 v241, 1, v241
cv v175,v158,v159
)"
R"(v_bfe_u32 v242, v214, 4, 4
v_xor_b32 v242, v242, v241
mm v[204:207],v[208:211],v[172:175],v[204:207]
v_lshl_add_u32 v238, v242, 4, v238
v_xor_b32 v239, 128, v238
mm v[96:99],a[128:131],v[172:175],v[96:99]
s_lshl_b32 s81, s49, 3
v_add_u32 v212, s55, v214
mm v[100:103],a[132:135],v[172:175],v[100:103]
s_and_b32 s80, s51, 7
sa s81,s81,s80
s_lshl_b32 s81, s81, 6
s_and_b32 s80, s51, 1
s_mul_i32 s80, s80, 0x2000
sa s81,s81,s80
sa s81,s81,0x11000
)"
R"(v_bfe_u32 v250, v214, 4, 4
v_lshlrev_b32 v250, 2, v250
s_waitcnt vmcnt(0) lgkmcnt(0)
s_barrier
s_lshl_b32 s72, s49, 5
sa s72,s72,s48
s_lshl_b32 s72, s72, 1
s_and_b32 s73, s50, 1
sa s72,s72,s73
s_lshl_b32 s72, s72, 3
sa s72,s72,0x13000
v_mov_b32 v228, 1
v_mov_b32 v229, 1
v_mov_b32 v230, 0
s_mov_b64 exec, 1
buffer_store_dwordx2 v[228:229], v230, s[44:47], s72 offen sc1
)"
R"(s_mov_b64 exec, -1
mm v[104:107],a[136:139],v[172:175],v[104:107]
dr a[64:67],v212 offset:0
mm v[108:111],a[140:143],v[172:175],v[108:111]
dr a[68:71],v212 offset:1024
mm v[112:115],a[144:147],v[172:175],v[112:115]
sa m0,s77,0x14000
dr a[72:75],v212 offset:2048
mm v[116:119],a[148:151],v[172:175],v[116:119]
dr a[76:79],v212 offset:3072
mm v[120:123],a[152:155],v[172:175],v[120:123]
s_waitcnt lgkmcnt(4)
dr a[80:83],v212 offset:4096
mm v[124:127],a[156:159],v[172:175],v[124:127]
dr a[84:87],v212 offset:5120
mm v[152:155],a[96:99],a[48:51],v[188:191]
)"
R"(dr a[88:91],v212 offset:6144
mm v[152:155],a[100:103],a[52:55],v[152:155]
sa s79,s76,0x0
bl v238,s[40:43],s79 offen lds
mm v[152:155],a[104:107],a[56:59],v[152:155]
dr a[92:95],v212 offset:7168
mm v[152:155],a[108:111],a[60:63],v[152:155]
dr a[128:131],v212 offset:16384
mm v[156:159],a[112:115],a[48:51],v[188:191]
dr a[132:135],v212 offset:17408
mm v[156:159],a[116:119],a[52:55],v[156:159]
dr a[136:139],v212 offset:18432
mm v[156:159],a[120:123],a[56:59],v[156:159]
sa s79,s76,0xc00
bl v239,s[40:43],s79 offen offset:1024 lds
mm v[156:159],a[124:127],a[60:63],v[156:159]
)"
R"(sa s79,s76,0x1800
bl v238,s[40:43],s79 offen offset:2048 lds
sa s79,s76,0x2400
bl v239,s[40:43],s79 offen offset:3072 lds
sa m0,s77,0x1c000
sa s79,s76,0x10000
bl v238,s[40:43],s79 offen lds
sa s79,s76,0x10c00
bl v239,s[40:43],s79 offen offset:1024 lds
sa s79,s76,0x11800
bl v238,s[40:43],s79 offen offset:2048 lds
dr a[140:143],v212 offset:19456
sa s79,s76,0x12400
bl v239,s[40:43],s79 offen offset:3072 lds
dr a[144:147],v212 offset:20480
sa m0,s77,0x24000
)"
R"(dr a[148:151],v212 offset:21504
sa s79,s76,0x20000
bl v238,s[40:43],s79 offen lds
dr a[152:155],v212 offset:22528
sa s79,s76,0x20c00
bl v239,s[40:43],s79 offen offset:1024 lds
dr a[156:159],v212 offset:23552
sa s79,s76,0x21800
bl v238,s[40:43],s79 offen offset:2048 lds
sa s79,s76,0x22400
bl v239,s[40:43],s79 offen offset:3072 lds
ex v152,v152
ex v153,v153
ex v154,v154
ex v155,v155
cv v172,v152,v153
)"
R"(cv v173,v154,v155
s_waitcnt lgkmcnt(8)
s_setpc_b64 s[58:59]
L_att_finhi0:
s_mul_i32 s56, s50, 18432
v_add_u32 v230, s56, v214
s_xor_b32 s62, s50, 2
s_mul_i32 s56, s62, 18432
v_add_u32 v231, s56, v214
s_nop 7
s_nop 1
v_cndmask_b32 v156, v213, v156, s[88:89]
ex v156,v156
v_cndmask_b32 v157, v213, v157, s[90:91]
ex v157,v157
v_cndmask_b32 v158, v213, v158, s[92:93]
)"
R"(cv v174,v156,v157
ex v158,v158
v_cndmask_b32 v159, v213, v159, s[94:95]
ex v159,v159
s_nop 0
cv v175,v158,v159
s_nop 1
mm v[204:207],v[208:211],v[172:175],v[204:207]
mm v[96:99],a[160:163],v[172:175],v[96:99]
mm v[100:103],a[164:167],v[172:175],v[100:103]
mm v[104:107],a[168:171],v[172:175],v[104:107]
mm v[108:111],a[172:175],v[172:175],v[108:111]
mm v[112:115],a[176:179],v[172:175],v[112:115]
mm v[116:119],a[180:183],v[172:175],v[116:119]
mm v[120:123],a[184:187],v[172:175],v[120:123]
mm v[124:127],a[188:191],v[172:175],v[124:127]
)"
R"(s_nop 7
v_mov_b32 v220, 0x5d800000
v_cmp_nlt_f32 vcc, v192, v220
v_cmp_nlt_f32 s[62:63], v196, v220
s_or_b64 vcc, vcc, s[62:63]
v_cmp_nlt_f32 s[62:63], v200, v220
s_or_b64 vcc, vcc, s[62:63]
v_cmp_nlt_f32 s[62:63], v204, v220
s_or_b64 vcc, vcc, s[62:63]
s_cbranch_vccnz L_redo_finhi0
L_ok_finhi0:
ds_write_b128 v230, v[64:67] offset:0
ds_write_b128 v230, v[68:71] offset:1024
ds_write_b128 v230, v[72:75] offset:2048
ds_write_b128 v230, v[76:79] offset:3072
ds_write_b128 v230, v[80:83] offset:4096
)"
R"(ds_write_b128 v230, v[84:87] offset:5120
ds_write_b128 v230, v[88:91] offset:6144
ds_write_b128 v230, v[92:95] offset:7168
ds_write_b128 v230, v[96:99] offset:8192
ds_write_b128 v230, v[100:103] offset:9216
ds_write_b128 v230, v[104:107] offset:10240
ds_write_b128 v230, v[108:111] offset:11264
ds_write_b128 v230, v[112:115] offset:12288
ds_write_b128 v230, v[116:119] offset:13312
ds_write_b128 v230, v[120:123] offset:14336
ds_write_b128 v230, v[124:127] offset:15360
v_mov_b32 v232, v184
v_mov_b32 v233, v200
v_mov_b32 v234, v188
v_mov_b32 v235, v204
ds_write_b128 v230, v[232:235] offset:16384
)"
R"(s_lshl_b32 s56, s57, 5
sa s56,s56,s48
s_lshl_b32 s56, s56, 15
sa s56,s56,0x2100000
s_and_b32 s69, s50, 1
s_lshl_b32 s69, s69, 10
sa s68,s56,s69
s_sub_u32 s69, s56, s69
s_waitcnt lgkmcnt(0)
s_barrier
dr v[232:235],v231 offset:16384
dr v[64:67],v231 offset:0
dr v[68:71],v231 offset:1024
dr v[72:75],v231 offset:2048
dr v[76:79],v231 offset:3072
dr v[80:83],v231 offset:4096
)"
R"(dr v[84:87],v231 offset:5120
dr v[88:91],v231 offset:6144
dr v[92:95],v231 offset:7168
dr v[96:99],v231 offset:8192
dr v[100:103],v231 offset:9216
dr v[104:107],v231 offset:10240
dr v[108:111],v231 offset:11264
dr v[112:115],v231 offset:12288
dr v[116:119],v231 offset:13312
dr v[120:123],v231 offset:14336
dr v[124:127],v231 offset:15360
buffer_load_dword v251, v250, s[44:47], s81 offen sc1
buffer_load_dword v245, v249, s[44:47], 0 offen sc1
s_waitcnt lgkmcnt(15)
v_min_f32 v228, v176, v232
v_sub_f32 v220, v228, v176
)"
R"(v_sub_f32 v222, v228, v232
v_mul_f32 v220, 0.5, v220
v_mul_f32 v222, 0.5, v222
ex v220,v220
ex v222,v222
v_min_f32 v229, v180, v234
v_sub_f32 v224, v229, v180
v_sub_f32 v226, v229, v234
v_mul_f32 v224, 0.5, v224
v_mul_f32 v226, 0.5, v226
ex v224,v224
ex v226,v226
v_mul_f32 v228, v220, v192
v_mul_f32 v233, v222, v233
v_mul_f32 v228, v228, v220
v_fmac_f32 v228, v222, v233
)"
R"(v_mul_f32 v229, v224, v196
v_mul_f32 v235, v226, v235
v_mul_f32 v229, v229, v224
v_fmac_f32 v229, v226, v235
v_rcp_f32 v228, v228
v_rcp_f32 v229, v229
v_mul_f32 v221, v220, v228
v_mul_f32 v223, v222, v228
v_mul_f32 v220, v220, v221
v_mul_f32 v222, v222, v223
v_mul_f32 v225, v224, v229
v_mul_f32 v227, v226, v229
v_mul_f32 v224, v224, v225
v_mul_f32 v226, v226, v227
v_pk_mul_f32 v[0:1], v[0:1], v[220:221] op_sel_hi:[1,0]
v_pk_mul_f32 v[2:3], v[2:3], v[220:221] op_sel_hi:[1,0]
)"
R"(v_pk_mul_f32 v[4:5], v[4:5], v[220:221] op_sel_hi:[1,0]
v_pk_mul_f32 v[6:7], v[6:7], v[220:221] op_sel_hi:[1,0]
v_pk_mul_f32 v[8:9], v[8:9], v[220:221] op_sel_hi:[1,0]
v_pk_mul_f32 v[10:11], v[10:11], v[220:221] op_sel_hi:[1,0]
v_pk_mul_f32 v[12:13], v[12:13], v[220:221] op_sel_hi:[1,0]
v_pk_mul_f32 v[14:15], v[14:15], v[220:221] op_sel_hi:[1,0]
v_pk_mul_f32 v[16:17], v[16:17], v[220:221] op_sel_hi:[1,0]
v_pk_mul_f32 v[18:19], v[18:19], v[220:221] op_sel_hi:[1,0]
v_pk_mul_f32 v[20:21], v[20:21], v[220:221] op_sel_hi:[1,0]
v_pk_mul_f32 v[22:23], v[22:23], v[220:221] op_sel_hi:[1,0]
v_pk_mul_f32 v[24:25], v[24:25], v[220:221] op_sel_hi:[1,0]
v_pk_mul_f32 v[26:27], v[26:27], v[220:221] op_sel_hi:[1,0]
v_pk_mul_f32 v[28:29], v[28:29], v[220:221] op_sel_hi:[1,0]
v_pk_mul_f32 v[30:31], v[30:31], v[220:221] op_sel_hi:[1,0]
v_pk_mul_f32 v[32:33], v[32:33], v[224:225] op_sel_hi:[1,0]
v_pk_mul_f32 v[34:35], v[34:35], v[224:225] op_sel_hi:[1,0]
)"
R"(v_pk_mul_f32 v[36:37], v[36:37], v[224:225] op_sel_hi:[1,0]
v_pk_mul_f32 v[38:39], v[38:39], v[224:225] op_sel_hi:[1,0]
v_pk_mul_f32 v[40:41], v[40:41], v[224:225] op_sel_hi:[1,0]
v_pk_mul_f32 v[42:43], v[42:43], v[224:225] op_sel_hi:[1,0]
v_pk_mul_f32 v[44:45], v[44:45], v[224:225] op_sel_hi:[1,0]
v_pk_mul_f32 v[46:47], v[46:47], v[224:225] op_sel_hi:[1,0]
v_pk_mul_f32 v[48:49], v[48:49], v[224:225] op_sel_hi:[1,0]
v_pk_mul_f32 v[50:51], v[50:51], v[224:225] op_sel_hi:[1,0]
v_pk_mul_f32 v[52:53], v[52:53], v[224:225] op_sel_hi:[1,0]
v_pk_mul_f32 v[54:55], v[54:55], v[224:225] op_sel_hi:[1,0]
v_pk_mul_f32 v[56:57], v[56:57], v[224:225] op_sel_hi:[1,0]
v_pk_mul_f32 v[58:59], v[58:59], v[224:225] op_sel_hi:[1,0]
v_pk_mul_f32 v[60:61], v[60:61], v[224:225] op_sel_hi:[1,0]
v_pk_mul_f32 v[62:63], v[62:63], v[224:225] op_sel_hi:[1,0]
s_waitcnt lgkmcnt(0)
v_pk_fma_f32 v[0:1], v[64:65], v[222:223], v[0:1] op_sel_hi:[1,0,1]
)"
R"(v_pk_fma_f32 v[2:3], v[66:67], v[222:223], v[2:3] op_sel_hi:[1,0,1]
v_pk_fma_f32 v[4:5], v[68:69], v[222:223], v[4:5] op_sel_hi:[1,0,1]
v_pk_fma_f32 v[6:7], v[70:71], v[222:223], v[6:7] op_sel_hi:[1,0,1]
cv v0,v0,v1
cv v1,v2,v3
cv v2,v4,v5
cv v3,v6,v7
sa s56,s68,0x0
buffer_store_dwordx4 v[0:3], v214, s[44:47], s56 offen offset:0 sc1
v_pk_fma_f32 v[8:9], v[72:73], v[222:223], v[8:9] op_sel_hi:[1,0,1]
v_pk_fma_f32 v[10:11], v[74:75], v[222:223], v[10:11] op_sel_hi:[1,0,1]
v_pk_fma_f32 v[12:13], v[76:77], v[222:223], v[12:13] op_sel_hi:[1,0,1]
v_pk_fma_f32 v[14:15], v[78:79], v[222:223], v[14:15] op_sel_hi:[1,0,1]
cv v8,v8,v9
cv v9,v10,v11
cv v10,v12,v13
)"
R"(cv v11,v14,v15
sa s56,s68,0x2000
buffer_store_dwordx4 v[8:11], v214, s[44:47], s56 offen offset:0 sc1
v_pk_fma_f32 v[16:17], v[80:81], v[222:223], v[16:17] op_sel_hi:[1,0,1]
v_pk_fma_f32 v[18:19], v[82:83], v[222:223], v[18:19] op_sel_hi:[1,0,1]
v_pk_fma_f32 v[20:21], v[84:85], v[222:223], v[20:21] op_sel_hi:[1,0,1]
v_pk_fma_f32 v[22:23], v[86:87], v[222:223], v[22:23] op_sel_hi:[1,0,1]
cv v16,v16,v17
cv v17,v18,v19
cv v18,v20,v21
cv v19,v22,v23
sa s56,s68,0x4000
buffer_store_dwordx4 v[16:19], v214, s[44:47], s56 offen offset:0 sc1
v_pk_fma_f32 v[24:25], v[88:89], v[222:223], v[24:25] op_sel_hi:[1,0,1]
v_pk_fma_f32 v[26:27], v[90:91], v[222:223], v[26:27] op_sel_hi:[1,0,1]
v_pk_fma_f32 v[28:29], v[92:93], v[222:223], v[28:29] op_sel_hi:[1,0,1]
)"
R"(v_pk_fma_f32 v[30:31], v[94:95], v[222:223], v[30:31] op_sel_hi:[1,0,1]
cv v24,v24,v25
cv v25,v26,v27
cv v26,v28,v29
cv v27,v30,v31
sa s56,s68,0x6000
buffer_store_dwordx4 v[24:27], v214, s[44:47], s56 offen offset:0 sc1
v_pk_fma_f32 v[32:33], v[96:97], v[226:227], v[32:33] op_sel_hi:[1,0,1]
v_pk_fma_f32 v[34:35], v[98:99], v[226:227], v[34:35] op_sel_hi:[1,0,1]
v_pk_fma_f32 v[36:37], v[100:101], v[226:227], v[36:37] op_sel_hi:[1,0,1]
v_pk_fma_f32 v[38:39], v[102:103], v[226:227], v[38:39] op_sel_hi:[1,0,1]
cv v32,v32,v33
cv v33,v34,v35
cv v34,v36,v37
cv v35,v38,v39
sa s56,s69,0x0
)"
R"(buffer_store_dwordx4 v[32:35], v214, s[44:47], s56 offen offset:3072 sc1
v_pk_fma_f32 v[40:41], v[104:105], v[226:227], v[40:41] op_sel_hi:[1,0,1]
v_pk_fma_f32 v[42:43], v[106:107], v[226:227], v[42:43] op_sel_hi:[1,0,1]
v_pk_fma_f32 v[44:45], v[108:109], v[226:227], v[44:45] op_sel_hi:[1,0,1]
v_pk_fma_f32 v[46:47], v[110:111], v[226:227], v[46:47] op_sel_hi:[1,0,1]
cv v40,v40,v41
cv v41,v42,v43
cv v42,v44,v45
cv v43,v46,v47
sa s56,s69,0x2000
buffer_store_dwordx4 v[40:43], v214, s[44:47], s56 offen offset:3072 sc1
v_pk_fma_f32 v[48:49], v[112:113], v[226:227], v[48:49] op_sel_hi:[1,0,1]
v_pk_fma_f32 v[50:51], v[114:115], v[226:227], v[50:51] op_sel_hi:[1,0,1]
v_pk_fma_f32 v[52:53], v[116:117], v[226:227], v[52:53] op_sel_hi:[1,0,1]
v_pk_fma_f32 v[54:55], v[118:119], v[226:227], v[54:55] op_sel_hi:[1,0,1]
cv v48,v48,v49
)"
R"(cv v49,v50,v51
cv v50,v52,v53
cv v51,v54,v55
sa s56,s69,0x4000
buffer_store_dwordx4 v[48:51], v214, s[44:47], s56 offen offset:3072 sc1
v_pk_fma_f32 v[56:57], v[120:121], v[226:227], v[56:57] op_sel_hi:[1,0,1]
v_pk_fma_f32 v[58:59], v[122:123], v[226:227], v[58:59] op_sel_hi:[1,0,1]
v_pk_fma_f32 v[60:61], v[124:125], v[226:227], v[60:61] op_sel_hi:[1,0,1]
v_pk_fma_f32 v[62:63], v[126:127], v[226:227], v[62:63] op_sel_hi:[1,0,1]
cv v56,v56,v57
cv v57,v58,v59
cv v58,v60,v61
cv v59,v62,v63
sa s56,s69,0x6000
buffer_store_dwordx4 v[56:59], v214, s[44:47], s56 offen offset:3072 sc1
s_branch L_att_end
)"
R"(.p2align 12
L_att_b1_0:
ex v156,v156
ex v157,v157
ex v158,v158
cv v174,v156,v157
ex v159,v159
s_nop 0
cv v175,v158,v159
s_nop 1
mm v[204:207],v[208:211],v[172:175],v[204:207]
dr a[96:99],v212 offset:8192
v_readlane_b32 s55, v219, s52
mm v[96:99],a[160:163],v[172:175],v[96:99]
sa s52,s52,1
dr a[100:103],v212 offset:9216
)"
R"(v_bfe_u32 v241, v214, 4, 5
mm v[100:103],a[164:167],v[172:175],v[100:103]
s_and_b32 s74, s51, 7
dr a[104:107],v212 offset:10240
v_readlane_b32 s56, v216, s52
mm v[104:107],a[168:171],v[172:175],v[104:107]
s_lshl_b32 s74, s74, 7
dr a[108:111],v212 offset:11264
mm v[108:111],a[172:175],v[172:175],v[108:111]
sa s74,s74,0x12000
dr a[112:115],v212 offset:12288
mm v[112:115],a[176:179],v[172:175],v[112:115]
dr a[116:119],v212 offset:13312
v_lshl_add_u32 v249, v241, 2, s74
mm v[116:119],a[180:183],v[172:175],v[116:119]
s_lshr_b32 s74, s51, 5
)"
R"(dr a[120:123],v212 offset:14336
mm v[120:123],a[184:187],v[172:175],v[120:123]
s_lshl_b32 s74, s74, 5
s_waitcnt lgkmcnt(7)
dr a[124:127],v212 offset:15360
mm v[124:127],a[188:191],v[172:175],v[124:127]
s_and_b32 s80, s51, 1
dr a[160:163],v212 offset:24576
mm v[144:147],a[64:67],a[32:35],v[184:187]
s_lshl_b32 s80, s80, 2
dr a[164:167],v212 offset:25600
mm v[144:147],a[68:71],a[36:39],v[144:147]
sa s74,s74,s80
sa s58,s60,s56
s_addc_u32 s59, s61, 0
mm v[144:147],a[72:75],a[40:43],v[144:147]
)"
R"(s_bfe_u32 s80, s51, 0x20003
dr a[168:171],v212 offset:26624
mm v[144:147],a[76:79],a[44:47],v[144:147]
sa s74,s74,s80
mm v[148:151],a[80:83],a[32:35],v[184:187]
s_lshl_b32 s74, s74, 4
dr a[172:175],v212 offset:27648
mm v[148:151],a[84:87],a[36:39],v[148:151]
sa s74,s74,0x11000
mm v[148:151],a[88:91],a[40:43],v[148:151]
v_bfe_u32 v241, v214, 6, 2
v_lshl_add_u32 v244, v241, 7, s74
mm v[148:151],a[92:95],a[44:47],v[148:151]
v_bfe_u32 v241, v214, 4, 2
v_lshl_add_u32 v244, v241, 2, v244
mm v[152:155],a[64:67],a[48:51],v[188:191]
)"
R"(s_mov_b32 s40, s82
dr a[176:179],v212 offset:28672
mm v[152:155],a[68:71],a[52:55],v[152:155]
s_and_b32 s41, s83, 0xffff
dr a[180:183],v212 offset:29696
mm v[152:155],a[72:75],a[56:59],v[152:155]
s_mov_b32 s42, 0x400000
v_cndmask_b32 v148, v213, v148, s[88:89]
v_cndmask_b32 v149, v213, v149, s[90:91]
mm v[152:155],a[76:79],a[60:63],v[152:155]
s_mov_b32 s43, 0x20000
v_cndmask_b32 v150, v213, v150, s[92:93]
v_cndmask_b32 v151, v213, v151, s[94:95]
mm v[156:159],a[80:83],a[48:51],v[188:191]
s_lshl_b32 s77, s50, 12
dr a[184:187],v212 offset:30720
)"
R"(mm v[156:159],a[84:87],a[52:55],v[156:159]
s_and_b32 s76, s51, 7
ex v144,v144
mm v[156:159],a[88:91],a[56:59],v[156:159]
s_lshl_b32 s76, s76, 19
ex v145,v145
mm v[156:159],a[92:95],a[60:63],v[156:159]
s_lshl_b32 s80, s50, 14
ex v146,v146
cv v168,v144,v145
ex v147,v147
ex v148,v148
cv v169,v146,v147
ex v149,v149
ex v150,v150
cv v170,v148,v149
)"
R"(ex v151,v151
ex v152,v152
cv v171,v150,v151
ex v153,v153
ex v154,v154
mm v[200:203],v[208:211],v[168:171],v[200:203]
sa s76,s76,s80
dr a[188:191],v212 offset:31744
cv v172,v152,v153
mm v[64:67],a[128:131],v[168:171],v[64:67]
s_bfe_u32 s80, s51, 0x20003
ex v155,v155
mm v[68:71],a[132:135],v[168:171],v[68:71]
s_lshl_b32 s80, s80, 8
ex v156,v156
mm v[72:75],a[136:139],v[168:171],v[72:75]
)"
R"(sa s76,s76,s80
ex v157,v157
mm v[76:79],a[140:143],v[168:171],v[76:79]
v_lshrrev_b32 v241, 8, v214
v_lshlrev_b32 v238, 10, v241
mm v[80:83],a[144:147],v[168:171],v[80:83]
v_lshlrev_b32 v241, 1, v241
v_bfe_u32 v242, v214, 4, 4
mm v[84:87],a[148:151],v[168:171],v[84:87]
v_xor_b32 v242, v242, v241
v_lshl_add_u32 v238, v242, 4, v238
mm v[88:91],a[152:155],v[168:171],v[88:91]
v_xor_b32 v239, 128, v238
cv v173,v154,v155
s_lshl_b32 s81, s49, 3
s_and_b32 s80, s51, 7
)"
R"(sa s81,s81,s80
s_lshl_b32 s81, s81, 6
s_and_b32 s80, s51, 1
s_mul_i32 s80, s80, 0x2000
sa s81,s81,s80
sa s81,s81,0x11000
v_bfe_u32 v250, v214, 4, 4
v_lshlrev_b32 v250, 2, v250
s_waitcnt vmcnt(0) lgkmcnt(0)
s_barrier
s_lshl_b32 s72, s49, 5
sa s72,s72,s48
s_lshl_b32 s72, s72, 1
s_and_b32 s73, s50, 1
sa s72,s72,s73
s_lshl_b32 s72, s72, 3
)"
R"(sa s72,s72,0x13000
v_mov_b32 v228, 1
v_mov_b32 v229, 1
v_mov_b32 v230, 0
s_mov_b64 exec, 1
buffer_store_dwordx2 v[228:229], v230, s[44:47], s72 offen sc1
s_mov_b64 exec, -1
mm v[92:95],a[156:159],v[168:171],v[92:95]
ex v158,v158
cv v174,v156,v157
ex v159,v159
v_add_u32 v212, s55, v214
cv v175,v158,v159
s_nop 1
mm v[204:207],v[208:211],v[172:175],v[204:207]
dr a[64:67],v212 offset:0
)"
R"(mm v[96:99],a[128:131],v[172:175],v[96:99]
sa m0,s77,0x14000
dr a[68:71],v212 offset:1024
mm v[100:103],a[132:135],v[172:175],v[100:103]
dr a[72:75],v212 offset:2048
mm v[104:107],a[136:139],v[172:175],v[104:107]
dr a[76:79],v212 offset:3072
mm v[108:111],a[140:143],v[172:175],v[108:111]
dr a[80:83],v212 offset:4096
mm v[112:115],a[144:147],v[172:175],v[112:115]
dr a[84:87],v212 offset:5120
mm v[116:119],a[148:151],v[172:175],v[116:119]
sa s79,s76,0x0
bl v238,s[40:43],s79 offen lds
mm v[120:123],a[152:155],v[172:175],v[120:123]
s_waitcnt lgkmcnt(6)
)"
R"(dr a[88:91],v212 offset:6144
mm v[124:127],a[156:159],v[172:175],v[124:127]
dr a[92:95],v212 offset:7168
mm v[152:155],a[96:99],a[48:51],v[188:191]
dr a[128:131],v212 offset:16384
v_mov_b32 v174, 0
mm v[152:155],a[100:103],a[52:55],v[152:155]
dr a[132:135],v212 offset:17408
v_mov_b32 v175, 0
mm v[152:155],a[104:107],a[56:59],v[152:155]
sa s79,s76,0xc00
bl v239,s[40:43],s79 offen offset:1024 lds
mm v[152:155],a[108:111],a[60:63],v[152:155]
sa s79,s76,0x1800
bl v238,s[40:43],s79 offen offset:2048 lds
sa s79,s76,0x2400
)"
R"(bl v239,s[40:43],s79 offen offset:3072 lds
sa m0,s77,0x1c000
sa s79,s76,0x10000
bl v238,s[40:43],s79 offen lds
sa s79,s76,0x10c00
bl v239,s[40:43],s79 offen offset:1024 lds
dr a[136:139],v212 offset:18432
sa s79,s76,0x11800
bl v238,s[40:43],s79 offen offset:2048 lds
dr a[140:143],v212 offset:19456
sa s79,s76,0x12400
bl v239,s[40:43],s79 offen offset:3072 lds
dr a[144:147],v212 offset:20480
sa m0,s77,0x24000
dr a[148:151],v212 offset:21504
sa s79,s76,0x20000
)"
R"(bl v238,s[40:43],s79 offen lds
dr a[152:155],v212 offset:22528
sa s79,s76,0x20c00
bl v239,s[40:43],s79 offen offset:1024 lds
dr a[156:159],v212 offset:23552
sa s79,s76,0x21800
bl v238,s[40:43],s79 offen offset:2048 lds
sa s79,s76,0x22400
bl v239,s[40:43],s79 offen offset:3072 lds
v_cndmask_b32 v152, v213, v152, s[88:89]
v_cndmask_b32 v153, v213, v153, s[90:91]
v_cndmask_b32 v154, v213, v154, s[92:93]
v_cndmask_b32 v155, v213, v155, s[94:95]
ex v152,v152
ex v153,v153
ex v154,v154
)"
R"(ex v155,v155
cv v172,v152,v153
cv v173,v154,v155
s_waitcnt lgkmcnt(8)
s_setpc_b64 s[58:59]
L_att_finhi1:
s_mul_i32 s56, s50, 18432
v_add_u32 v230, s56, v214
s_xor_b32 s62, s50, 2
s_mul_i32 s56, s62, 18432
v_add_u32 v231, s56, v214
s_nop 7
s_nop 1
mm v[204:207],v[208:211],v[172:175],v[204:207]
mm v[96:99],a[160:163],v[172:175],v[96:99]
mm v[100:103],a[164:167],v[172:175],v[100:103]
)"
R"(mm v[104:107],a[168:171],v[172:175],v[104:107]
mm v[108:111],a[172:175],v[172:175],v[108:111]
mm v[112:115],a[176:179],v[172:175],v[112:115]
mm v[116:119],a[180:183],v[172:175],v[116:119]
mm v[120:123],a[184:187],v[172:175],v[120:123]
mm v[124:127],a[188:191],v[172:175],v[124:127]
s_nop 7
v_mov_b32 v220, 0x5d800000
v_cmp_nlt_f32 vcc, v192, v220
v_cmp_nlt_f32 s[62:63], v196, v220
s_or_b64 vcc, vcc, s[62:63]
v_cmp_nlt_f32 s[62:63], v200, v220
s_or_b64 vcc, vcc, s[62:63]
v_cmp_nlt_f32 s[62:63], v204, v220
s_or_b64 vcc, vcc, s[62:63]
s_cbranch_vccnz L_redo_finhi1
)"
R"(L_ok_finhi1:
ds_write_b128 v230, v[64:67] offset:0
ds_write_b128 v230, v[68:71] offset:1024
ds_write_b128 v230, v[72:75] offset:2048
ds_write_b128 v230, v[76:79] offset:3072
ds_write_b128 v230, v[80:83] offset:4096
ds_write_b128 v230, v[84:87] offset:5120
ds_write_b128 v230, v[88:91] offset:6144
ds_write_b128 v230, v[92:95] offset:7168
ds_write_b128 v230, v[96:99] offset:8192
ds_write_b128 v230, v[100:103] offset:9216
ds_write_b128 v230, v[104:107] offset:10240
ds_write_b128 v230, v[108:111] offset:11264
ds_write_b128 v230, v[112:115] offset:12288
ds_write_b128 v230, v[116:119] offset:13312
ds_write_b128 v230, v[120:123] offset:14336
)"
R"(ds_write_b128 v230, v[124:127] offset:15360
v_mov_b32 v232, v184
v_mov_b32 v233, v200
v_mov_b32 v234, v188
v_mov_b32 v235, v204
ds_write_b128 v230, v[232:235] offset:16384
s_lshl_b32 s56, s57, 5
sa s56,s56,s48
s_lshl_b32 s56, s56, 15
sa s56,s56,0x2100000
s_and_b32 s69, s50, 1
s_lshl_b32 s69, s69, 10
sa s68,s56,s69
s_sub_u32 s69, s56, s69
s_waitcnt lgkmcnt(0)
s_barrier
)"
R"(dr v[232:235],v231 offset:16384
dr v[64:67],v231 offset:0
dr v[68:71],v231 offset:1024
dr v[72:75],v231 offset:2048
dr v[76:79],v231 offset:3072
dr v[80:83],v231 offset:4096
dr v[84:87],v231 offset:5120
dr v[88:91],v231 offset:6144
dr v[92:95],v231 offset:7168
dr v[96:99],v231 offset:8192
dr v[100:103],v231 offset:9216
dr v[104:107],v231 offset:10240
dr v[108:111],v231 offset:11264
dr v[112:115],v231 offset:12288
dr v[116:119],v231 offset:13312
dr v[120:123],v231 offset:14336
)"
R"(dr v[124:127],v231 offset:15360
buffer_load_dword v251, v250, s[44:47], s81 offen sc1
buffer_load_dword v245, v249, s[44:47], 0 offen sc1
s_waitcnt lgkmcnt(15)
v_min_f32 v228, v176, v232
v_sub_f32 v220, v228, v176
v_sub_f32 v222, v228, v232
v_mul_f32 v220, 0.5, v220
v_mul_f32 v222, 0.5, v222
ex v220,v220
ex v222,v222
v_min_f32 v229, v180, v234
v_sub_f32 v224, v229, v180
v_sub_f32 v226, v229, v234
v_mul_f32 v224, 0.5, v224
v_mul_f32 v226, 0.5, v226
)"
R"(ex v224,v224
ex v226,v226
v_mul_f32 v228, v220, v192
v_mul_f32 v233, v222, v233
v_mul_f32 v228, v228, v220
v_fmac_f32 v228, v222, v233
v_mul_f32 v229, v224, v196
v_mul_f32 v235, v226, v235
v_mul_f32 v229, v229, v224
v_fmac_f32 v229, v226, v235
v_rcp_f32 v228, v228
v_rcp_f32 v229, v229
v_mul_f32 v221, v220, v228
v_mul_f32 v223, v222, v228
v_mul_f32 v220, v220, v221
v_mul_f32 v222, v222, v223
)"
R"(v_mul_f32 v225, v224, v229
v_mul_f32 v227, v226, v229
v_mul_f32 v224, v224, v225
v_mul_f32 v226, v226, v227
v_pk_mul_f32 v[0:1], v[0:1], v[220:221] op_sel_hi:[1,0]
v_pk_mul_f32 v[2:3], v[2:3], v[220:221] op_sel_hi:[1,0]
v_pk_mul_f32 v[4:5], v[4:5], v[220:221] op_sel_hi:[1,0]
v_pk_mul_f32 v[6:7], v[6:7], v[220:221] op_sel_hi:[1,0]
v_pk_mul_f32 v[8:9], v[8:9], v[220:221] op_sel_hi:[1,0]
v_pk_mul_f32 v[10:11], v[10:11], v[220:221] op_sel_hi:[1,0]
v_pk_mul_f32 v[12:13], v[12:13], v[220:221] op_sel_hi:[1,0]
v_pk_mul_f32 v[14:15], v[14:15], v[220:221] op_sel_hi:[1,0]
v_pk_mul_f32 v[16:17], v[16:17], v[220:221] op_sel_hi:[1,0]
v_pk_mul_f32 v[18:19], v[18:19], v[220:221] op_sel_hi:[1,0]
v_pk_mul_f32 v[20:21], v[20:21], v[220:221] op_sel_hi:[1,0]
v_pk_mul_f32 v[22:23], v[22:23], v[220:221] op_sel_hi:[1,0]
)"
R"(v_pk_mul_f32 v[24:25], v[24:25], v[220:221] op_sel_hi:[1,0]
v_pk_mul_f32 v[26:27], v[26:27], v[220:221] op_sel_hi:[1,0]
v_pk_mul_f32 v[28:29], v[28:29], v[220:221] op_sel_hi:[1,0]
v_pk_mul_f32 v[30:31], v[30:31], v[220:221] op_sel_hi:[1,0]
v_pk_mul_f32 v[32:33], v[32:33], v[224:225] op_sel_hi:[1,0]
v_pk_mul_f32 v[34:35], v[34:35], v[224:225] op_sel_hi:[1,0]
v_pk_mul_f32 v[36:37], v[36:37], v[224:225] op_sel_hi:[1,0]
v_pk_mul_f32 v[38:39], v[38:39], v[224:225] op_sel_hi:[1,0]
v_pk_mul_f32 v[40:41], v[40:41], v[224:225] op_sel_hi:[1,0]
v_pk_mul_f32 v[42:43], v[42:43], v[224:225] op_sel_hi:[1,0]
v_pk_mul_f32 v[44:45], v[44:45], v[224:225] op_sel_hi:[1,0]
v_pk_mul_f32 v[46:47], v[46:47], v[224:225] op_sel_hi:[1,0]
v_pk_mul_f32 v[48:49], v[48:49], v[224:225] op_sel_hi:[1,0]
v_pk_mul_f32 v[50:51], v[50:51], v[224:225] op_sel_hi:[1,0]
v_pk_mul_f32 v[52:53], v[52:53], v[224:225] op_sel_hi:[1,0]
v_pk_mul_f32 v[54:55], v[54:55], v[224:225] op_sel_hi:[1,0]
)"
R"(v_pk_mul_f32 v[56:57], v[56:57], v[224:225] op_sel_hi:[1,0]
v_pk_mul_f32 v[58:59], v[58:59], v[224:225] op_sel_hi:[1,0]
v_pk_mul_f32 v[60:61], v[60:61], v[224:225] op_sel_hi:[1,0]
v_pk_mul_f32 v[62:63], v[62:63], v[224:225] op_sel_hi:[1,0]
s_waitcnt lgkmcnt(0)
v_pk_fma_f32 v[0:1], v[64:65], v[222:223], v[0:1] op_sel_hi:[1,0,1]
v_pk_fma_f32 v[2:3], v[66:67], v[222:223], v[2:3] op_sel_hi:[1,0,1]
v_pk_fma_f32 v[4:5], v[68:69], v[222:223], v[4:5] op_sel_hi:[1,0,1]
v_pk_fma_f32 v[6:7], v[70:71], v[222:223], v[6:7] op_sel_hi:[1,0,1]
cv v0,v0,v1
cv v1,v2,v3
cv v2,v4,v5
cv v3,v6,v7
sa s56,s68,0x0
buffer_store_dwordx4 v[0:3], v214, s[44:47], s56 offen offset:0 sc1
v_pk_fma_f32 v[8:9], v[72:73], v[222:223], v[8:9] op_sel_hi:[1,0,1]
)"
R"(v_pk_fma_f32 v[10:11], v[74:75], v[222:223], v[10:11] op_sel_hi:[1,0,1]
v_pk_fma_f32 v[12:13], v[76:77], v[222:223], v[12:13] op_sel_hi:[1,0,1]
v_pk_fma_f32 v[14:15], v[78:79], v[222:223], v[14:15] op_sel_hi:[1,0,1]
cv v8,v8,v9
cv v9,v10,v11
cv v10,v12,v13
cv v11,v14,v15
sa s56,s68,0x2000
buffer_store_dwordx4 v[8:11], v214, s[44:47], s56 offen offset:0 sc1
v_pk_fma_f32 v[16:17], v[80:81], v[222:223], v[16:17] op_sel_hi:[1,0,1]
v_pk_fma_f32 v[18:19], v[82:83], v[222:223], v[18:19] op_sel_hi:[1,0,1]
v_pk_fma_f32 v[20:21], v[84:85], v[222:223], v[20:21] op_sel_hi:[1,0,1]
v_pk_fma_f32 v[22:23], v[86:87], v[222:223], v[22:23] op_sel_hi:[1,0,1]
cv v16,v16,v17
cv v17,v18,v19
cv v18,v20,v21
)"
R"(cv v19,v22,v23
sa s56,s68,0x4000
buffer_store_dwordx4 v[16:19], v214, s[44:47], s56 offen offset:0 sc1
v_pk_fma_f32 v[24:25], v[88:89], v[222:223], v[24:25] op_sel_hi:[1,0,1]
v_pk_fma_f32 v[26:27], v[90:91], v[222:223], v[26:27] op_sel_hi:[1,0,1]
v_pk_fma_f32 v[28:29], v[92:93], v[222:223], v[28:29] op_sel_hi:[1,0,1]
v_pk_fma_f32 v[30:31], v[94:95], v[222:223], v[30:31] op_sel_hi:[1,0,1]
cv v24,v24,v25
cv v25,v26,v27
cv v26,v28,v29
cv v27,v30,v31
sa s56,s68,0x6000
buffer_store_dwordx4 v[24:27], v214, s[44:47], s56 offen offset:0 sc1
v_pk_fma_f32 v[32:33], v[96:97], v[226:227], v[32:33] op_sel_hi:[1,0,1]
v_pk_fma_f32 v[34:35], v[98:99], v[226:227], v[34:35] op_sel_hi:[1,0,1]
v_pk_fma_f32 v[36:37], v[100:101], v[226:227], v[36:37] op_sel_hi:[1,0,1]
)"
R"(v_pk_fma_f32 v[38:39], v[102:103], v[226:227], v[38:39] op_sel_hi:[1,0,1]
cv v32,v32,v33
cv v33,v34,v35
cv v34,v36,v37
cv v35,v38,v39
sa s56,s69,0x0
buffer_store_dwordx4 v[32:35], v214, s[44:47], s56 offen offset:3072 sc1
v_pk_fma_f32 v[40:41], v[104:105], v[226:227], v[40:41] op_sel_hi:[1,0,1]
v_pk_fma_f32 v[42:43], v[106:107], v[226:227], v[42:43] op_sel_hi:[1,0,1]
v_pk_fma_f32 v[44:45], v[108:109], v[226:227], v[44:45] op_sel_hi:[1,0,1]
v_pk_fma_f32 v[46:47], v[110:111], v[226:227], v[46:47] op_sel_hi:[1,0,1]
cv v40,v40,v41
cv v41,v42,v43
cv v42,v44,v45
cv v43,v46,v47
sa s56,s69,0x2000
)"
R"(buffer_store_dwordx4 v[40:43], v214, s[44:47], s56 offen offset:3072 sc1
v_pk_fma_f32 v[48:49], v[112:113], v[226:227], v[48:49] op_sel_hi:[1,0,1]
v_pk_fma_f32 v[50:51], v[114:115], v[226:227], v[50:51] op_sel_hi:[1,0,1]
v_pk_fma_f32 v[52:53], v[116:117], v[226:227], v[52:53] op_sel_hi:[1,0,1]
v_pk_fma_f32 v[54:55], v[118:119], v[226:227], v[54:55] op_sel_hi:[1,0,1]
cv v48,v48,v49
cv v49,v50,v51
cv v50,v52,v53
cv v51,v54,v55
sa s56,s69,0x4000
buffer_store_dwordx4 v[48:51], v214, s[44:47], s56 offen offset:3072 sc1
v_pk_fma_f32 v[56:57], v[120:121], v[226:227], v[56:57] op_sel_hi:[1,0,1]
v_pk_fma_f32 v[58:59], v[122:123], v[226:227], v[58:59] op_sel_hi:[1,0,1]
v_pk_fma_f32 v[60:61], v[124:125], v[226:227], v[60:61] op_sel_hi:[1,0,1]
v_pk_fma_f32 v[62:63], v[126:127], v[226:227], v[62:63] op_sel_hi:[1,0,1]
cv v56,v56,v57
)"
R"(cv v57,v58,v59
cv v58,v60,v61
cv v59,v62,v63
sa s56,s69,0x6000
buffer_store_dwordx4 v[56:59], v214, s[44:47], s56 offen offset:3072 sc1
s_branch L_att_end
L_att_end:
v_lshrrev_b32 v146, 4, v214
s_and_b32 s67, s51, 7
s_lshl_b32 s67, s67, 2
s_bfe_u32 s79, s51, 0x20003
sa s67,s67,s79
s_lshr_b32 s52, s51, 5
s_bfe_u32 s64, s51, 0x20001
s_bfe_u32 s66, s51, 0x20003
s_lshr_b32 s65, s51, 5
)"
R"(s_sub_u32 s79, 15, s65
s_bitcmp1_b32 s51, 0
s_cselect_b32 s65, s65, s79
s_mov_b32 s36, s70
s_and_b32 s37, s71, 0xffff
s_mov_b32 s38, 0x80000
s_mov_b32 s39, 0x20000
s_mov_b32 s88, s84
s_and_b32 s89, s85, 0xffff
s_mov_b32 s90, 0x400000
s_mov_b32 s91, 0x20000
s_and_b32 s80, s51, 7
s_lshl_b32 s79, s65, 3
sa s79,s79,s80
s_lshl_b32 s79, s79, 17
sa s79,s79,s77
)"
R"(sa s79,s79,0x2100000
s_xor_b32 s72, s64, 0
s_lshl_b32 s72, s72, 10
sa s72,s72,s79
s_sub_u32 s72, s72, 0
s_xor_b32 s73, s64, 1
s_lshl_b32 s73, s73, 10
sa s73,s73,s79
s_sub_u32 s73, s73, 1024
s_xor_b32 s74, s64, 2
s_lshl_b32 s74, s74, 10
sa s74,s74,s79
s_sub_u32 s74, s74, 2048
s_xor_b32 s75, s64, 3
s_lshl_b32 s75, s75, 10
sa s75,s75,s79
)"
R"(s_sub_u32 s75, s75, 3072
s_xor_b32 s53, s81, 64
s_lshl_b32 s55, s49, 5
sa s55,s55,s48
s_lshl_b32 s55, s55, 2
sa s55,s55,s50
s_lshl_b32 s55, s55, 2
sa s55,s55,0x11000
v_mov_b32 v246, 1
v_mov_b32 v247, 0
s_and_b32 s79, s50, 1
s_lshl_b32 s79, s79, 12
v_add_u32 v128, s79, v214
v_bfe_u32 v148, v146, 2, 4
v_lshlrev_b32 v149, 8, v148
v_and_b32 v147, 7, v148
)"
R"(v_lshl_add_u32 v149, v147, 5, v149
v_and_b32 v147, 3, v146
v_lshl_add_u32 v149, v147, 3, v149
s_lshr_b32 s79, s50, 1
s_lshl_b32 s79, s79, 7
v_xor_b32 v149, s79, v149
v_xor_b32 v131, 0, v149
v_xor_b32 v132, 32, v149
v_xor_b32 v133, 64, v149
v_xor_b32 v134, 96, v149
v_add_u32 v129, 0x10000, v128
v_add_u32 v135, 0x10000, v131
v_add_u32 v136, 0x10000, v132
v_add_u32 v137, 0x10000, v133
v_add_u32 v138, 0x10000, v134
v_add_u32 v130, 0x20000, v128
)"
R"(v_add_u32 v139, 0x20000, v131
v_add_u32 v140, 0x20000, v132
v_add_u32 v141, 0x20000, v133
v_add_u32 v142, 0x20000, v134
s_waitcnt vmcnt(8)
v_cmp_eq_u32 vcc, 1, v245
s_nop 1
s_cmp_eq_u64 vcc, exec
s_cbranch_scc0 L_f3
L_f3d:
s_bitcmp1_b32 s51, 0
s_cbranch_scc1 L_out_odd
v_bfe_u32 v148, v146, 2, 4
s_lshl_b32 s70, s67, 2
sa s70,s70,s50
s_lshl_b32 s70, s70, 14
)"
R"(sa s70,s70,0x3100000
s_lshl_b32 s78, s50, 15
s_lshl_b32 s56, s52, 7
sa s78,s78,s56
v_lshrrev_b32 v147, 3, v146
v_lshlrev_b32 v159, 10, v147
v_lshrrev_b32 v147, 4, v146
v_lshlrev_b32 v147, 1, v147
v_and_b32 v149, 7, v146
v_xor_b32 v149, v149, v147
v_lshl_add_u32 v159, v149, 4, v159
s_and_b32 s56, s50, 1
s_lshl_b32 s56, s56, 13
v_lshlrev_b32 v147, 3, v146
v_add_u32 v150, s56, v147
v_lshlrev_b32 v149, 7, v148
)"
R"(v_bfe_u32 v147, v148, 1, 2
v_lshl_add_u32 v149, v147, 5, v149
v_and_b32 v147, 3, v146
v_lshl_add_u32 v149, v147, 3, v149
s_lshr_b32 s56, s50, 1
s_lshl_b32 s56, s56, 6
v_xor_b32 v149, s56, v149
v_xor_b32 v153, 0, v149
v_xor_b32 v154, 32, v149
v_add_u32 v151, 0x10000, v150
v_add_u32 v155, 0x10000, v153
v_add_u32 v156, 0x10000, v154
v_add_u32 v152, 0x20000, v150
v_add_u32 v157, 0x20000, v153
v_add_u32 v158, 0x20000, v154
v_and_b32 v147, 15, v146
)"
R"(v_lshlrev_b32 v160, 11, v147
v_bfe_u32 v147, v146, 4, 1
v_lshl_add_u32 v160, v147, 5, v160
v_lshrrev_b32 v147, 5, v146
v_lshl_add_u32 v160, v147, 4, v160
s_barrier
sa m0,s77,0x18000
sa s79,s70,0x0
bl v214,s[44:47],s79 offen sc1 lds
sa s79,s70,0x0
bl v214,s[44:47],s79 offen offset:1024 sc1 lds
sa s79,s70,0x0
bl v214,s[44:47],s79 offen offset:2048 sc1 lds
sa s79,s70,0x0
bl v214,s[44:47],s79 offen offset:3072 sc1 lds
sa m0,s77,0x0
)"
R"(sa s79,s78,0x0
bl v159,s[36:39],s79 offen lds
sa s79,s78,0x1c00
bl v159,s[36:39],s79 offen offset:1024 lds
sa s79,s78,0x3800
bl v159,s[36:39],s79 offen offset:2048 lds
sa s79,s78,0x5400
bl v159,s[36:39],s79 offen offset:3072 lds
sa m0,s77,0x20000
sa s79,s70,0x1000
bl v214,s[44:47],s79 offen sc1 lds
sa s79,s70,0x1000
bl v214,s[44:47],s79 offen offset:1024 sc1 lds
sa s79,s70,0x1000
bl v214,s[44:47],s79 offen offset:2048 sc1 lds
sa s79,s70,0x1000
)"
R"(bl v214,s[44:47],s79 offen offset:3072 sc1 lds
sa m0,s77,0x4000
sa s79,s78,0x20000
bl v159,s[36:39],s79 offen lds
sa s79,s78,0x21c00
bl v159,s[36:39],s79 offen offset:1024 lds
sa s79,s78,0x23800
bl v159,s[36:39],s79 offen offset:2048 lds
sa s79,s78,0x25400
bl v159,s[36:39],s79 offen offset:3072 lds
s_waitcnt vmcnt(16)
s_mov_b64 exec, 1
buffer_store_dword v246, v247, s[44:47], s55 offen sc1
s_mov_b64 exec, -1
sa m0,s77,0x10000
sa s79,s70,0x2000
)"
R"(bl v214,s[44:47],s79 offen sc1 lds
sa s79,s70,0x2000
bl v214,s[44:47],s79 offen offset:1024 sc1 lds
sa s79,s70,0x2000
bl v214,s[44:47],s79 offen offset:2048 sc1 lds
sa s79,s70,0x2000
bl v214,s[44:47],s79 offen offset:3072 sc1 lds
s_waitcnt vmcnt(13)
s_barrier
dt v[72:73],v153 offset:0
ds_read_b64 v[64:65], v151 offset:32768
dt v[74:75],v153 offset:2048
ds_read_b64 v[66:67], v151 offset:33280
dt v[76:77],v154 offset:0
ds_read_b64 v[68:69], v151 offset:36864
dt v[78:79],v154 offset:2048
)"
R"(ds_read_b64 v[70:71], v151 offset:37376
dt v[88:89],v153 offset:4096
ds_read_b64 v[80:81], v151 offset:33792
dt v[90:91],v153 offset:6144
ds_read_b64 v[82:83], v151 offset:34304
dt v[92:93],v154 offset:4096
ds_read_b64 v[84:85], v151 offset:37888
dt v[94:95],v154 offset:6144
ds_read_b64 v[86:87], v151 offset:38400
s_waitcnt lgkmcnt(8)
mm v[164:167],v[72:75],v[64:67],0
dt v[104:105],v153 offset:8192
ds_read_b64 v[96:97], v151 offset:34816
sa m0,s77,0x8000
sa s79,s78,0x40000
bl v159,s[36:39],s79 offen lds
)"
R"(mm v[172:175],v[72:75],v[68:71],0
dt v[106:107],v153 offset:10240
ds_read_b64 v[98:99], v151 offset:35328
mm v[168:171],v[76:79],v[64:67],0
dt v[108:109],v154 offset:8192
ds_read_b64 v[100:101], v151 offset:38912
sa s79,s78,0x41c00
bl v159,s[36:39],s79 offen offset:1024 lds
mm v[176:179],v[76:79],v[68:71],0
dt v[110:111],v154 offset:10240
ds_read_b64 v[102:103], v151 offset:39424
sa s79,s78,0x43800
bl v159,s[36:39],s79 offen offset:2048 lds
s_waitcnt lgkmcnt(8)
mm v[164:167],v[88:91],v[80:83],v[164:167]
dt v[120:121],v153 offset:12288
)"
R"(ds_read_b64 v[112:113], v151 offset:35840
sa s79,s78,0x45400
bl v159,s[36:39],s79 offen offset:3072 lds
mm v[172:175],v[88:91],v[84:87],v[172:175]
dt v[122:123],v153 offset:14336
ds_read_b64 v[114:115], v151 offset:36352
mm v[168:171],v[92:95],v[80:83],v[168:171]
dt v[124:125],v154 offset:12288
ds_read_b64 v[116:117], v151 offset:39936
sa m0,s77,0xc000
sa s79,s78,0x60000
bl v159,s[36:39],s79 offen lds
mm v[176:179],v[92:95],v[84:87],v[176:179]
dt v[126:127],v154 offset:14336
ds_read_b64 v[118:119], v151 offset:40448
sa s79,s78,0x61c00
)"
R"(bl v159,s[36:39],s79 offen offset:1024 lds
s_waitcnt lgkmcnt(8)
mm v[164:167],v[104:107],v[96:99],v[164:167]
sa s79,s78,0x63800
bl v159,s[36:39],s79 offen offset:2048 lds
mm v[172:175],v[104:107],v[100:103],v[172:175]
mm v[168:171],v[108:111],v[96:99],v[168:171]
sa s79,s78,0x65400
bl v159,s[36:39],s79 offen offset:3072 lds
mm v[176:179],v[108:111],v[100:103],v[176:179]
s_waitcnt lgkmcnt(0)
mm v[164:167],v[120:123],v[112:115],v[164:167]
mm v[172:175],v[120:123],v[116:119],v[172:175]
mm v[168:171],v[124:127],v[112:115],v[168:171]
mm v[176:179],v[124:127],v[116:119],v[176:179]
s_waitcnt vmcnt(8)
)"
R"(s_barrier
dt v[72:73],v153 offset:16384
ds_read_b64 v[64:65], v152 offset:0
dt v[74:75],v153 offset:18432
ds_read_b64 v[66:67], v152 offset:512
dt v[76:77],v154 offset:16384
ds_read_b64 v[68:69], v152 offset:4096
dt v[78:79],v154 offset:18432
ds_read_b64 v[70:71], v152 offset:4608
dt v[88:89],v153 offset:20480
ds_read_b64 v[80:81], v152 offset:1024
dt v[90:91],v153 offset:22528
ds_read_b64 v[82:83], v152 offset:1536
dt v[92:93],v154 offset:20480
ds_read_b64 v[84:85], v152 offset:5120
dt v[94:95],v154 offset:22528
)"
R"(ds_read_b64 v[86:87], v152 offset:5632
s_waitcnt lgkmcnt(8)
mm v[164:167],v[72:75],v[64:67],v[164:167]
dt v[104:105],v153 offset:24576
ds_read_b64 v[96:97], v152 offset:2048
sa m0,s77,0x0
sa s79,s70,0x3000
bl v214,s[44:47],s79 offen sc1 lds
mm v[172:175],v[72:75],v[68:71],v[172:175]
dt v[106:107],v153 offset:26624
ds_read_b64 v[98:99], v152 offset:2560
mm v[168:171],v[76:79],v[64:67],v[168:171]
dt v[108:109],v154 offset:24576
ds_read_b64 v[100:101], v152 offset:6144
sa s79,s70,0x3000
bl v214,s[44:47],s79 offen offset:1024 sc1 lds
)"
R"(mm v[176:179],v[76:79],v[68:71],v[176:179]
dt v[110:111],v154 offset:26624
ds_read_b64 v[102:103], v152 offset:6656
sa s79,s70,0x3000
bl v214,s[44:47],s79 offen offset:2048 sc1 lds
s_waitcnt lgkmcnt(8)
mm v[164:167],v[88:91],v[80:83],v[164:167]
dt v[120:121],v153 offset:28672
ds_read_b64 v[112:113], v152 offset:3072
sa s79,s70,0x3000
bl v214,s[44:47],s79 offen offset:3072 sc1 lds
mm v[172:175],v[88:91],v[84:87],v[172:175]
dt v[122:123],v153 offset:30720
ds_read_b64 v[114:115], v152 offset:3584
mm v[168:171],v[92:95],v[80:83],v[168:171]
dt v[124:125],v154 offset:28672
)"
R"(ds_read_b64 v[116:117], v152 offset:7168
mm v[176:179],v[92:95],v[84:87],v[176:179]
dt v[126:127],v154 offset:30720
ds_read_b64 v[118:119], v152 offset:7680
s_waitcnt lgkmcnt(8)
mm v[164:167],v[104:107],v[96:99],v[164:167]
mm v[172:175],v[104:107],v[100:103],v[172:175]
mm v[168:171],v[108:111],v[96:99],v[168:171]
mm v[176:179],v[108:111],v[100:103],v[176:179]
s_waitcnt lgkmcnt(0)
mm v[164:167],v[120:123],v[112:115],v[164:167]
mm v[172:175],v[120:123],v[116:119],v[172:175]
mm v[168:171],v[124:127],v[112:115],v[168:171]
mm v[176:179],v[124:127],v[116:119],v[176:179]
buffer_load_dword v251, v250, s[44:47], s81 offen sc1
s_waitcnt vmcnt(1)
)"
R"(s_barrier
dt v[72:73],v153 offset:32768
ds_read_b64 v[64:65], v151 offset:0
dt v[74:75],v153 offset:34816
ds_read_b64 v[66:67], v151 offset:512
dt v[76:77],v154 offset:32768
ds_read_b64 v[68:69], v151 offset:4096
dt v[78:79],v154 offset:34816
ds_read_b64 v[70:71], v151 offset:4608
dt v[88:89],v153 offset:36864
ds_read_b64 v[80:81], v151 offset:1024
dt v[90:91],v153 offset:38912
ds_read_b64 v[82:83], v151 offset:1536
dt v[92:93],v154 offset:36864
ds_read_b64 v[84:85], v151 offset:5120
dt v[94:95],v154 offset:38912
)"
R"(ds_read_b64 v[86:87], v151 offset:5632
s_waitcnt lgkmcnt(8)
mm v[164:167],v[72:75],v[64:67],v[164:167]
dt v[104:105],v153 offset:40960
ds_read_b64 v[96:97], v151 offset:2048
mm v[172:175],v[72:75],v[68:71],v[172:175]
dt v[106:107],v153 offset:43008
ds_read_b64 v[98:99], v151 offset:2560
mm v[168:171],v[76:79],v[64:67],v[168:171]
dt v[108:109],v154 offset:40960
ds_read_b64 v[100:101], v151 offset:6144
mm v[176:179],v[76:79],v[68:71],v[176:179]
dt v[110:111],v154 offset:43008
ds_read_b64 v[102:103], v151 offset:6656
s_waitcnt lgkmcnt(8)
mm v[164:167],v[88:91],v[80:83],v[164:167]
)"
R"(dt v[120:121],v153 offset:45056
ds_read_b64 v[112:113], v151 offset:3072
mm v[172:175],v[88:91],v[84:87],v[172:175]
dt v[122:123],v153 offset:47104
ds_read_b64 v[114:115], v151 offset:3584
mm v[168:171],v[92:95],v[80:83],v[168:171]
dt v[124:125],v154 offset:45056
ds_read_b64 v[116:117], v151 offset:7168
mm v[176:179],v[92:95],v[84:87],v[176:179]
dt v[126:127],v154 offset:47104
ds_read_b64 v[118:119], v151 offset:7680
s_waitcnt lgkmcnt(8)
mm v[164:167],v[104:107],v[96:99],v[164:167]
dt v[72:73],v153 offset:49152
ds_read_b64 v[64:65], v150 offset:0
mm v[172:175],v[104:107],v[100:103],v[172:175]
)"
R"(dt v[74:75],v153 offset:51200
ds_read_b64 v[66:67], v150 offset:512
mm v[168:171],v[108:111],v[96:99],v[168:171]
dt v[76:77],v154 offset:49152
ds_read_b64 v[68:69], v150 offset:4096
mm v[176:179],v[108:111],v[100:103],v[176:179]
dt v[78:79],v154 offset:51200
ds_read_b64 v[70:71], v150 offset:4608
s_waitcnt lgkmcnt(8)
mm v[164:167],v[120:123],v[112:115],v[164:167]
dt v[88:89],v153 offset:53248
ds_read_b64 v[80:81], v150 offset:1024
mm v[172:175],v[120:123],v[116:119],v[172:175]
dt v[90:91],v153 offset:55296
ds_read_b64 v[82:83], v150 offset:1536
mm v[168:171],v[124:127],v[112:115],v[168:171]
)"
R"(dt v[92:93],v154 offset:53248
ds_read_b64 v[84:85], v150 offset:5120
mm v[176:179],v[124:127],v[116:119],v[176:179]
dt v[94:95],v154 offset:55296
ds_read_b64 v[86:87], v150 offset:5632
s_waitcnt lgkmcnt(8)
mm v[164:167],v[72:75],v[64:67],v[164:167]
dt v[104:105],v153 offset:57344
ds_read_b64 v[96:97], v150 offset:2048
mm v[172:175],v[72:75],v[68:71],v[172:175]
dt v[106:107],v153 offset:59392
ds_read_b64 v[98:99], v150 offset:2560
mm v[168:171],v[76:79],v[64:67],v[168:171]
dt v[108:109],v154 offset:57344
ds_read_b64 v[100:101], v150 offset:6144
mm v[176:179],v[76:79],v[68:71],v[176:179]
)"
R"(dt v[110:111],v154 offset:59392
ds_read_b64 v[102:103], v150 offset:6656
s_waitcnt lgkmcnt(8)
mm v[164:167],v[88:91],v[80:83],v[164:167]
dt v[120:121],v153 offset:61440
ds_read_b64 v[112:113], v150 offset:3072
mm v[172:175],v[88:91],v[84:87],v[172:175]
dt v[122:123],v153 offset:63488
ds_read_b64 v[114:115], v150 offset:3584
mm v[168:171],v[92:95],v[80:83],v[168:171]
dt v[124:125],v154 offset:61440
ds_read_b64 v[116:117], v150 offset:7168
mm v[176:179],v[92:95],v[84:87],v[176:179]
dt v[126:127],v154 offset:63488
ds_read_b64 v[118:119], v150 offset:7680
s_waitcnt lgkmcnt(8)
)"
R"(mm v[164:167],v[104:107],v[96:99],v[164:167]
mm v[172:175],v[104:107],v[100:103],v[172:175]
mm v[168:171],v[108:111],v[96:99],v[168:171]
mm v[176:179],v[108:111],v[100:103],v[176:179]
s_waitcnt lgkmcnt(0)
mm v[164:167],v[120:123],v[112:115],v[164:167]
mm v[172:175],v[120:123],v[116:119],v[172:175]
mm v[168:171],v[124:127],v[112:115],v[168:171]
mm v[176:179],v[124:127],v[116:119],v[176:179]
s_waitcnt vmcnt(0)
v_cmp_eq_u32 vcc, 1, v251
s_nop 1
s_cmp_eq_u64 vcc, exec
s_cbranch_scc0 L_f2e
L_f2ed:
s_barrier
)"
R"(sa m0,s77,0x10000
sa s79,s72,0x0
bl v214,s[44:47],s79 offen sc1 lds
sa s79,s73,0x0
bl v214,s[44:47],s79 offen offset:1024 sc1 lds
sa s79,s74,0x0
bl v214,s[44:47],s79 offen offset:2048 sc1 lds
sa s79,s75,0x0
bl v214,s[44:47],s79 offen offset:3072 sc1 lds
sa m0,s77,0x18000
sa s79,s72,0x4000
bl v214,s[44:47],s79 offen sc1 lds
sa s79,s73,0x4000
bl v214,s[44:47],s79 offen offset:1024 sc1 lds
sa s79,s74,0x4000
bl v214,s[44:47],s79 offen offset:2048 sc1 lds
)"
R"(sa s79,s75,0x4000
bl v214,s[44:47],s79 offen offset:3072 sc1 lds
s_lshl_b32 s79, s67, 1
s_and_b32 s80, s50, 1
sa s79,s79,s80
s_lshl_b32 s79, s79, 16
s_lshl_b32 s80, s52, 1
s_lshr_b32 s54, s50, 1
sa s80,s80,s54
s_lshl_b32 s80, s80, 6
sa s79,s79,s80
sa s80,s79,0x8000
cv v164,v164,v165
cv v165,v166,v167
cv v166,v168,v169
cv v167,v170,v171
)"
R"(s_nop 1
v_permlane16_swap_b32 v164, v166
v_permlane16_swap_b32 v165, v167
buffer_store_dwordx4 v[164:167], v160, s[88:91], s79 offen offset:0 sc1
cv v172,v172,v173
cv v173,v174,v175
cv v174,v176,v177
cv v175,v178,v179
s_nop 1
v_permlane16_swap_b32 v172, v174
v_permlane16_swap_b32 v173, v175
buffer_store_dwordx4 v[172:175], v160, s[88:91], s80 offen offset:0 sc1
s_waitcnt vmcnt(6)
s_barrier
dt a[32:33],v135 offset:16384
dt a[34:35],v135 offset:20480
)"
R"(dr a[0:3],v129 offset:0
dt a[36:37],v135 offset:24576
dt a[38:39],v135 offset:28672
dr a[4:7],v129 offset:8192
dt a[40:41],v136 offset:16384
dt a[42:43],v136 offset:20480
dr a[8:11],v129 offset:1024
dt a[44:45],v136 offset:24576
dt a[46:47],v136 offset:28672
dr a[12:15],v129 offset:9216
dt a[48:49],v137 offset:16384
dt a[50:51],v137 offset:20480
dr a[16:19],v129 offset:2048
dt a[52:53],v137 offset:24576
dt a[54:55],v137 offset:28672
dr a[20:23],v129 offset:10240
)"
R"(dt a[56:57],v138 offset:16384
dt a[58:59],v138 offset:20480
dr a[24:27],v129 offset:3072
dt a[60:61],v138 offset:24576
dt a[62:63],v138 offset:28672
dr a[28:31],v129 offset:11264
s_waitcnt vmcnt(2) lgkmcnt(15)
s_barrier
mm v[0:3],a[32:35],a[0:3],0
sa m0,s77,0x20000
dt a[96:97],v135 offset:49152
mm v[0:3],a[36:39],a[4:7],v[0:3]
s_waitcnt lgkmcnt(13)
dt a[98:99],v135 offset:53248
mm v[16:19],a[32:35],a[8:11],0
dr a[64:67],v129 offset:32768
)"
R"(mm v[16:19],a[36:39],a[12:15],v[16:19]
sa s79,s72,0x8000
bl v214,s[44:47],s79 offen sc1 lds
mm v[4:7],a[40:43],a[0:3],0
dt a[100:101],v135 offset:57344
mm v[4:7],a[44:47],a[4:7],v[4:7]
dt a[102:103],v135 offset:61440
mm v[20:23],a[40:43],a[8:11],0
dr a[68:71],v129 offset:40960
sa s79,s73,0x8000
bl v214,s[44:47],s79 offen offset:1024 sc1 lds
mm v[20:23],a[44:47],a[12:15],v[20:23]
s_waitcnt lgkmcnt(12)
mm v[32:35],a[32:35],a[16:19],0
dt a[104:105],v136 offset:49152
mm v[32:35],a[36:39],a[20:23],v[32:35]
)"
R"(dt a[106:107],v136 offset:53248
sa s79,s74,0x8000
bl v214,s[44:47],s79 offen offset:2048 sc1 lds
mm v[36:39],a[40:43],a[16:19],0
dr a[72:75],v129 offset:33792
mm v[36:39],a[44:47],a[20:23],v[36:39]
sa s79,s75,0x8000
bl v214,s[44:47],s79 offen offset:3072 sc1 lds
mm v[8:11],a[48:51],a[0:3],0
sa m0,s77,0x0
dt a[108:109],v136 offset:57344
mm v[8:11],a[52:55],a[4:7],v[8:11]
dt a[110:111],v136 offset:61440
mm v[24:27],a[48:51],a[8:11],0
dr a[76:79],v129 offset:41984
mm v[24:27],a[52:55],a[12:15],v[24:27]
)"
R"(sa s79,s72,0xc000
bl v214,s[44:47],s79 offen sc1 lds
mm v[40:43],a[48:51],a[16:19],0
dt a[112:113],v137 offset:49152
mm v[40:43],a[52:55],a[20:23],v[40:43]
s_waitcnt lgkmcnt(13)
dt a[114:115],v137 offset:53248
mm v[48:51],a[32:35],a[24:27],0
dr a[80:83],v129 offset:34816
mm v[48:51],a[36:39],a[28:31],v[48:51]
sa s79,s73,0xc000
bl v214,s[44:47],s79 offen offset:1024 sc1 lds
mm v[52:55],a[40:43],a[24:27],0
sa s79,s74,0xc000
bl v214,s[44:47],s79 offen offset:2048 sc1 lds
dt a[116:117],v137 offset:57344
)"
R"(mm v[52:55],a[44:47],a[28:31],v[52:55]
dt a[118:119],v137 offset:61440
mm v[56:59],a[48:51],a[24:27],0
dr a[84:87],v129 offset:43008
mm v[56:59],a[52:55],a[28:31],v[56:59]
sa s79,s75,0xc000
bl v214,s[44:47],s79 offen offset:3072 sc1 lds
mm v[12:15],a[56:59],a[0:3],0
dt a[120:121],v138 offset:49152
mm v[12:15],a[60:63],a[4:7],v[12:15]
sa m0,s77,0x4000
dt a[122:123],v138 offset:53248
mm v[28:31],a[56:59],a[8:11],0
sa s79,s76,0x30000
bl v238,s[40:43],s79 offen lds
dr a[88:91],v129 offset:35840
)"
R"(mm v[28:31],a[60:63],a[12:15],v[28:31]
mm v[44:47],a[56:59],a[16:19],0
dt a[124:125],v138 offset:57344
mm v[44:47],a[60:63],a[20:23],v[44:47]
sa s79,s76,0x30c00
bl v239,s[40:43],s79 offen offset:1024 lds
dt a[126:127],v138 offset:61440
mm v[60:63],a[56:59],a[24:27],0
dr a[92:95],v129 offset:44032
mm v[60:63],a[60:63],a[28:31],v[60:63]
s_waitcnt vmcnt(6) lgkmcnt(15)
s_barrier
mm v[0:3],a[96:99],a[64:67],v[0:3]
dt a[32:33],v139 offset:16384
mm v[0:3],a[100:103],a[68:71],v[0:3]
s_waitcnt lgkmcnt(13)
)"
R"(sa s79,s76,0x31800
bl v238,s[40:43],s79 offen offset:2048 lds
dt a[34:35],v139 offset:20480
mm v[16:19],a[96:99],a[72:75],v[16:19]
dr a[0:3],v130 offset:0
mm v[16:19],a[100:103],a[76:79],v[16:19]
mm v[4:7],a[104:107],a[64:67],v[4:7]
sa s79,s76,0x32400
bl v239,s[40:43],s79 offen offset:3072 lds
dt a[36:37],v139 offset:24576
mm v[4:7],a[108:111],a[68:71],v[4:7]
dt a[38:39],v139 offset:28672
mm v[20:23],a[104:107],a[72:75],v[20:23]
sa m0,s77,0x8000
dr a[4:7],v130 offset:8192
mm v[20:23],a[108:111],a[76:79],v[20:23]
)"
R"(s_waitcnt lgkmcnt(12)
sa s79,s72,0x10000
bl v214,s[44:47],s79 offen sc1 lds
mm v[32:35],a[96:99],a[80:83],v[32:35]
dt a[40:41],v140 offset:16384
mm v[32:35],a[100:103],a[84:87],v[32:35]
dt a[42:43],v140 offset:20480
mm v[36:39],a[104:107],a[80:83],v[36:39]
sa s79,s73,0x10000
bl v214,s[44:47],s79 offen offset:1024 sc1 lds
dr a[8:11],v130 offset:1024
mm v[36:39],a[108:111],a[84:87],v[36:39]
mm v[8:11],a[112:115],a[64:67],v[8:11]
dt a[44:45],v140 offset:24576
mm v[8:11],a[116:119],a[68:71],v[8:11]
dt a[46:47],v140 offset:28672
)"
R"(mm v[24:27],a[112:115],a[72:75],v[24:27]
sa s79,s74,0x10000
bl v214,s[44:47],s79 offen offset:2048 sc1 lds
dr a[12:15],v130 offset:9216
mm v[24:27],a[116:119],a[76:79],v[24:27]
mm v[40:43],a[112:115],a[80:83],v[40:43]
dt a[48:49],v141 offset:16384
mm v[40:43],a[116:119],a[84:87],v[40:43]
s_waitcnt lgkmcnt(13)
sa s79,s75,0x10000
bl v214,s[44:47],s79 offen offset:3072 sc1 lds
dt a[50:51],v141 offset:20480
mm v[48:51],a[96:99],a[88:91],v[48:51]
dr a[16:19],v130 offset:2048
mm v[48:51],a[100:103],a[92:95],v[48:51]
sa m0,s77,0xc000
)"
R"(mm v[52:55],a[104:107],a[88:91],v[52:55]
sa s79,s76,0x40000
bl v238,s[40:43],s79 offen lds
dt a[52:53],v141 offset:24576
mm v[52:55],a[108:111],a[92:95],v[52:55]
dt a[54:55],v141 offset:28672
mm v[56:59],a[112:115],a[88:91],v[56:59]
dr a[20:23],v130 offset:10240
mm v[56:59],a[116:119],a[92:95],v[56:59]
mm v[12:15],a[120:123],a[64:67],v[12:15]
sa s79,s76,0x40c00
bl v239,s[40:43],s79 offen offset:1024 lds
dt a[56:57],v142 offset:16384
mm v[12:15],a[124:127],a[68:71],v[12:15]
dt a[58:59],v142 offset:20480
mm v[28:31],a[120:123],a[72:75],v[28:31]
)"
R"(dr a[24:27],v130 offset:3072
mm v[28:31],a[124:127],a[76:79],v[28:31]
sa s79,s76,0x41800
bl v238,s[40:43],s79 offen offset:2048 lds
mm v[44:47],a[120:123],a[80:83],v[44:47]
dt a[60:61],v142 offset:24576
mm v[44:47],a[124:127],a[84:87],v[44:47]
dt a[62:63],v142 offset:28672
mm v[60:63],a[120:123],a[88:91],v[60:63]
sa s79,s76,0x42400
bl v239,s[40:43],s79 offen offset:3072 lds
dr a[28:31],v130 offset:11264
mm v[60:63],a[124:127],a[92:95],v[60:63]
s_waitcnt vmcnt(8) lgkmcnt(15)
s_barrier
buffer_load_dword v251, v250, s[44:47], s53 offen sc1
)"
R"(mm v[0:3],a[32:35],a[0:3],v[0:3]
dt a[96:97],v131 offset:16384
mm v[0:3],a[36:39],a[4:7],v[0:3]
s_waitcnt lgkmcnt(13)
sa m0,s77,0x10000
dt a[98:99],v131 offset:20480
mm v[16:19],a[32:35],a[8:11],v[16:19]
dr a[64:67],v128 offset:0
mm v[16:19],a[36:39],a[12:15],v[16:19]
sa s79,s72,0x14000
bl v214,s[44:47],s79 offen sc1 lds
mm v[4:7],a[40:43],a[0:3],v[4:7]
dt a[100:101],v131 offset:24576
mm v[4:7],a[44:47],a[4:7],v[4:7]
dt a[102:103],v131 offset:28672
mm v[20:23],a[40:43],a[8:11],v[20:23]
)"
R"(dr a[68:71],v128 offset:8192
mm v[20:23],a[44:47],a[12:15],v[20:23]
s_waitcnt lgkmcnt(12)
mm v[32:35],a[32:35],a[16:19],v[32:35]
dt a[104:105],v132 offset:16384
sa s79,s73,0x14000
bl v214,s[44:47],s79 offen offset:1024 sc1 lds
mm v[32:35],a[36:39],a[20:23],v[32:35]
dt a[106:107],v132 offset:20480
mm v[36:39],a[40:43],a[16:19],v[36:39]
dr a[72:75],v128 offset:1024
mm v[36:39],a[44:47],a[20:23],v[36:39]
sa s79,s74,0x14000
bl v214,s[44:47],s79 offen offset:2048 sc1 lds
mm v[8:11],a[48:51],a[0:3],v[8:11]
dt a[108:109],v132 offset:24576
)"
R"(mm v[8:11],a[52:55],a[4:7],v[8:11]
dt a[110:111],v132 offset:28672
mm v[24:27],a[48:51],a[8:11],v[24:27]
dr a[76:79],v128 offset:9216
sa s79,s75,0x14000
bl v214,s[44:47],s79 offen offset:3072 sc1 lds
mm v[24:27],a[52:55],a[12:15],v[24:27]
sa m0,s77,0x14000
mm v[40:43],a[48:51],a[16:19],v[40:43]
dt a[112:113],v133 offset:16384
mm v[40:43],a[52:55],a[20:23],v[40:43]
s_waitcnt lgkmcnt(13)
dt a[114:115],v133 offset:20480
mm v[48:51],a[32:35],a[24:27],v[48:51]
dr a[80:83],v128 offset:2048
sa s79,s76,0x50000
)"
R"(bl v238,s[40:43],s79 offen lds
mm v[48:51],a[36:39],a[28:31],v[48:51]
sa s79,s76,0x50c00
bl v239,s[40:43],s79 offen offset:1024 lds
mm v[52:55],a[40:43],a[24:27],v[52:55]
dt a[116:117],v133 offset:24576
mm v[52:55],a[44:47],a[28:31],v[52:55]
dt a[118:119],v133 offset:28672
mm v[56:59],a[48:51],a[24:27],v[56:59]
dr a[84:87],v128 offset:10240
mm v[56:59],a[52:55],a[28:31],v[56:59]
sa s79,s76,0x51800
bl v238,s[40:43],s79 offen offset:2048 lds
mm v[12:15],a[56:59],a[0:3],v[12:15]
dt a[120:121],v134 offset:16384
mm v[12:15],a[60:63],a[4:7],v[12:15]
)"
R"(dt a[122:123],v134 offset:20480
mm v[28:31],a[56:59],a[8:11],v[28:31]
dr a[88:91],v128 offset:3072
mm v[28:31],a[60:63],a[12:15],v[28:31]
sa s79,s76,0x52400
bl v239,s[40:43],s79 offen offset:3072 lds
mm v[44:47],a[56:59],a[16:19],v[44:47]
sa m0,s77,0x18000
dt a[124:125],v134 offset:24576
mm v[44:47],a[60:63],a[20:23],v[44:47]
dt a[126:127],v134 offset:28672
mm v[60:63],a[56:59],a[24:27],v[60:63]
dr a[92:95],v128 offset:11264
mm v[60:63],a[60:63],a[28:31],v[60:63]
s_waitcnt vmcnt(9) lgkmcnt(15)
s_barrier
)"
R"(sa s79,s72,0x18000
bl v214,s[44:47],s79 offen sc1 lds
mm v[0:3],a[96:99],a[64:67],v[0:3]
dt a[32:33],v131 offset:49152
mm v[0:3],a[100:103],a[68:71],v[0:3]
s_waitcnt lgkmcnt(13)
dt a[34:35],v131 offset:53248
mm v[16:19],a[96:99],a[72:75],v[16:19]
dr a[0:3],v128 offset:32768
sa s79,s73,0x18000
bl v214,s[44:47],s79 offen offset:1024 sc1 lds
mm v[16:19],a[100:103],a[76:79],v[16:19]
sa s79,s74,0x18000
bl v214,s[44:47],s79 offen offset:2048 sc1 lds
mm v[4:7],a[104:107],a[64:67],v[4:7]
dt a[36:37],v131 offset:57344
)"
R"(mm v[4:7],a[108:111],a[68:71],v[4:7]
dt a[38:39],v131 offset:61440
mm v[20:23],a[104:107],a[72:75],v[20:23]
dr a[4:7],v128 offset:40960
mm v[20:23],a[108:111],a[76:79],v[20:23]
s_waitcnt lgkmcnt(12)
mm v[32:35],a[96:99],a[80:83],v[32:35]
dt a[40:41],v132 offset:49152
mm v[32:35],a[100:103],a[84:87],v[32:35]
dt a[42:43],v132 offset:53248
sa s79,s75,0x18000
bl v214,s[44:47],s79 offen offset:3072 sc1 lds
mm v[36:39],a[104:107],a[80:83],v[36:39]
sa m0,s77,0x1c000
dr a[8:11],v128 offset:33792
mm v[36:39],a[108:111],a[84:87],v[36:39]
)"
R"(sa s79,s76,0x60000
bl v238,s[40:43],s79 offen lds
mm v[8:11],a[112:115],a[64:67],v[8:11]
dt a[44:45],v132 offset:57344
mm v[8:11],a[116:119],a[68:71],v[8:11]
dt a[46:47],v132 offset:61440
mm v[24:27],a[112:115],a[72:75],v[24:27]
dr a[12:15],v128 offset:41984
mm v[24:27],a[116:119],a[76:79],v[24:27]
sa s79,s76,0x60c00
bl v239,s[40:43],s79 offen offset:1024 lds
mm v[40:43],a[112:115],a[80:83],v[40:43]
dt a[48:49],v133 offset:49152
mm v[40:43],a[116:119],a[84:87],v[40:43]
s_waitcnt lgkmcnt(13)
dt a[50:51],v133 offset:53248
)"
R"(mm v[48:51],a[96:99],a[88:91],v[48:51]
dr a[16:19],v128 offset:34816
mm v[48:51],a[100:103],a[92:95],v[48:51]
sa s79,s76,0x61800
bl v238,s[40:43],s79 offen offset:2048 lds
mm v[52:55],a[104:107],a[88:91],v[52:55]
dt a[52:53],v133 offset:57344
mm v[52:55],a[108:111],a[92:95],v[52:55]
dt a[54:55],v133 offset:61440
mm v[56:59],a[112:115],a[88:91],v[56:59]
dr a[20:23],v128 offset:43008
sa s79,s76,0x62400
bl v239,s[40:43],s79 offen offset:3072 lds
mm v[56:59],a[116:119],a[92:95],v[56:59]
sa m0,s77,0x20000
mm v[12:15],a[120:123],a[64:67],v[12:15]
)"
R"(dt a[56:57],v134 offset:49152
mm v[12:15],a[124:127],a[68:71],v[12:15]
dt a[58:59],v134 offset:53248
sa s79,s72,0x1c000
bl v214,s[44:47],s79 offen sc1 lds
mm v[28:31],a[120:123],a[72:75],v[28:31]
dr a[24:27],v128 offset:35840
mm v[28:31],a[124:127],a[76:79],v[28:31]
sa s79,s73,0x1c000
bl v214,s[44:47],s79 offen offset:1024 sc1 lds
mm v[44:47],a[120:123],a[80:83],v[44:47]
dt a[60:61],v134 offset:57344
mm v[44:47],a[124:127],a[84:87],v[44:47]
dt a[62:63],v134 offset:61440
mm v[60:63],a[120:123],a[88:91],v[60:63]
dr a[28:31],v128 offset:44032
)"
R"(mm v[60:63],a[124:127],a[92:95],v[60:63]
s_waitcnt vmcnt(10) lgkmcnt(15)
s_barrier
v_cmp_eq_u32 vcc, 1, v251
s_nop 1
s_cmp_eq_u64 vcc, exec
s_cbranch_scc0 L_f2b0
L_f2b0d:
mm v[0:3],a[32:35],a[0:3],v[0:3]
dt a[96:97],v135 offset:16384
sa s79,s74,0x1c000
bl v214,s[44:47],s79 offen offset:2048 sc1 lds
mm v[0:3],a[36:39],a[4:7],v[0:3]
s_waitcnt lgkmcnt(13)
dt a[98:99],v135 offset:20480
mm v[16:19],a[32:35],a[8:11],v[16:19]
)"
R"(dr a[64:67],v129 offset:0
mm v[16:19],a[36:39],a[12:15],v[16:19]
sa s79,s75,0x1c000
bl v214,s[44:47],s79 offen offset:3072 sc1 lds
mm v[4:7],a[40:43],a[0:3],v[4:7]
sa m0,s77,0x24000
dt a[100:101],v135 offset:24576
mm v[4:7],a[44:47],a[4:7],v[4:7]
dt a[102:103],v135 offset:28672
mm v[20:23],a[40:43],a[8:11],v[20:23]
dr a[68:71],v129 offset:8192
mm v[20:23],a[44:47],a[12:15],v[20:23]
s_waitcnt lgkmcnt(12)
sa s79,s76,0x70000
bl v238,s[40:43],s79 offen lds
mm v[32:35],a[32:35],a[16:19],v[32:35]
)"
R"(dt a[104:105],v136 offset:16384
mm v[32:35],a[36:39],a[20:23],v[32:35]
dt a[106:107],v136 offset:20480
mm v[36:39],a[40:43],a[16:19],v[36:39]
dr a[72:75],v129 offset:1024
sa s79,s76,0x70c00
bl v239,s[40:43],s79 offen offset:1024 lds
mm v[36:39],a[44:47],a[20:23],v[36:39]
sa s79,s76,0x71800
bl v238,s[40:43],s79 offen offset:2048 lds
mm v[8:11],a[48:51],a[0:3],v[8:11]
dt a[108:109],v136 offset:24576
mm v[8:11],a[52:55],a[4:7],v[8:11]
dt a[110:111],v136 offset:28672
mm v[24:27],a[48:51],a[8:11],v[24:27]
dr a[76:79],v129 offset:9216
)"
R"(mm v[24:27],a[52:55],a[12:15],v[24:27]
sa s79,s76,0x72400
bl v239,s[40:43],s79 offen offset:3072 lds
mm v[40:43],a[48:51],a[16:19],v[40:43]
sa m0,s77,0x0
dt a[112:113],v137 offset:16384
mm v[40:43],a[52:55],a[20:23],v[40:43]
s_waitcnt lgkmcnt(13)
dt a[114:115],v137 offset:20480
mm v[48:51],a[32:35],a[24:27],v[48:51]
dr a[80:83],v129 offset:2048
mm v[48:51],a[36:39],a[28:31],v[48:51]
sa s79,s72,0x20000
bl v214,s[44:47],s79 offen sc1 lds
mm v[52:55],a[40:43],a[24:27],v[52:55]
dt a[116:117],v137 offset:24576
)"
R"(mm v[52:55],a[44:47],a[28:31],v[52:55]
dt a[118:119],v137 offset:28672
mm v[56:59],a[48:51],a[24:27],v[56:59]
dr a[84:87],v129 offset:10240
mm v[56:59],a[52:55],a[28:31],v[56:59]
sa s79,s73,0x20000
bl v214,s[44:47],s79 offen offset:1024 sc1 lds
mm v[12:15],a[56:59],a[0:3],v[12:15]
dt a[120:121],v138 offset:16384
mm v[12:15],a[60:63],a[4:7],v[12:15]
dt a[122:123],v138 offset:20480
mm v[28:31],a[56:59],a[8:11],v[28:31]
dr a[88:91],v129 offset:3072
sa s79,s74,0x20000
bl v214,s[44:47],s79 offen offset:2048 sc1 lds
mm v[28:31],a[60:63],a[12:15],v[28:31]
)"
R"(sa s79,s75,0x20000
bl v214,s[44:47],s79 offen offset:3072 sc1 lds
mm v[44:47],a[56:59],a[16:19],v[44:47]
dt a[124:125],v138 offset:24576
mm v[44:47],a[60:63],a[20:23],v[44:47]
sa m0,s77,0x4000
dt a[126:127],v138 offset:28672
mm v[60:63],a[56:59],a[24:27],v[60:63]
dr a[92:95],v129 offset:11264
mm v[60:63],a[60:63],a[28:31],v[60:63]
s_waitcnt vmcnt(12) lgkmcnt(15)
s_barrier
mm v[0:3],a[96:99],a[64:67],v[0:3]
dt a[32:33],v135 offset:49152
mm v[0:3],a[100:103],a[68:71],v[0:3]
s_waitcnt lgkmcnt(13)
)"
R"(dt a[34:35],v135 offset:53248
sa s79,s76,0x80000
bl v238,s[40:43],s79 offen lds
mm v[16:19],a[96:99],a[72:75],v[16:19]
dr a[0:3],v129 offset:32768
mm v[16:19],a[100:103],a[76:79],v[16:19]
sa s79,s76,0x80c00
bl v239,s[40:43],s79 offen offset:1024 lds
mm v[4:7],a[104:107],a[64:67],v[4:7]
dt a[36:37],v135 offset:57344
mm v[4:7],a[108:111],a[68:71],v[4:7]
dt a[38:39],v135 offset:61440
mm v[20:23],a[104:107],a[72:75],v[20:23]
dr a[4:7],v129 offset:40960
mm v[20:23],a[108:111],a[76:79],v[20:23]
s_waitcnt lgkmcnt(12)
)"
R"(mm v[32:35],a[96:99],a[80:83],v[32:35]
dt a[40:41],v136 offset:49152
sa s79,s76,0x81800
bl v238,s[40:43],s79 offen offset:2048 lds
mm v[32:35],a[100:103],a[84:87],v[32:35]
dt a[42:43],v136 offset:53248
mm v[36:39],a[104:107],a[80:83],v[36:39]
dr a[8:11],v129 offset:33792
mm v[36:39],a[108:111],a[84:87],v[36:39]
sa s79,s76,0x82400
bl v239,s[40:43],s79 offen offset:3072 lds
mm v[8:11],a[112:115],a[64:67],v[8:11]
sa m0,s77,0x8000
dt a[44:45],v136 offset:57344
mm v[8:11],a[116:119],a[68:71],v[8:11]
dt a[46:47],v136 offset:61440
)"
R"(mm v[24:27],a[112:115],a[72:75],v[24:27]
dr a[12:15],v129 offset:41984
sa s79,s72,0x24000
bl v214,s[44:47],s79 offen sc1 lds
mm v[24:27],a[116:119],a[76:79],v[24:27]
sa s79,s73,0x24000
bl v214,s[44:47],s79 offen offset:1024 sc1 lds
mm v[40:43],a[112:115],a[80:83],v[40:43]
dt a[48:49],v137 offset:49152
mm v[40:43],a[116:119],a[84:87],v[40:43]
s_waitcnt lgkmcnt(13)
dt a[50:51],v137 offset:53248
mm v[48:51],a[96:99],a[88:91],v[48:51]
dr a[16:19],v129 offset:34816
mm v[48:51],a[100:103],a[92:95],v[48:51]
sa s79,s74,0x24000
)"
R"(bl v214,s[44:47],s79 offen offset:2048 sc1 lds
mm v[52:55],a[104:107],a[88:91],v[52:55]
dt a[52:53],v137 offset:57344
mm v[52:55],a[108:111],a[92:95],v[52:55]
dt a[54:55],v137 offset:61440
mm v[56:59],a[112:115],a[88:91],v[56:59]
dr a[20:23],v129 offset:43008
mm v[56:59],a[116:119],a[92:95],v[56:59]
sa s79,s75,0x24000
bl v214,s[44:47],s79 offen offset:3072 sc1 lds
mm v[12:15],a[120:123],a[64:67],v[12:15]
sa m0,s77,0xc000
dt a[56:57],v138 offset:49152
mm v[12:15],a[124:127],a[68:71],v[12:15]
dt a[58:59],v138 offset:53248
mm v[28:31],a[120:123],a[72:75],v[28:31]
)"
R"(dr a[24:27],v129 offset:35840
mm v[28:31],a[124:127],a[76:79],v[28:31]
sa s79,s76,0x90000
bl v238,s[40:43],s79 offen lds
mm v[44:47],a[120:123],a[80:83],v[44:47]
dt a[60:61],v138 offset:57344
mm v[44:47],a[124:127],a[84:87],v[44:47]
dt a[62:63],v138 offset:61440
mm v[60:63],a[120:123],a[88:91],v[60:63]
dr a[28:31],v129 offset:44032
mm v[60:63],a[124:127],a[92:95],v[60:63]
s_waitcnt vmcnt(13) lgkmcnt(15)
s_barrier
sa s79,s76,0x90c00
bl v239,s[40:43],s79 offen offset:1024 lds
mm v[0:3],a[32:35],a[0:3],v[0:3]
)"
R"(dt a[96:97],v139 offset:16384
mm v[0:3],a[36:39],a[4:7],v[0:3]
s_waitcnt lgkmcnt(13)
dt a[98:99],v139 offset:20480
mm v[16:19],a[32:35],a[8:11],v[16:19]
dr a[64:67],v130 offset:0
sa s79,s76,0x91800
bl v238,s[40:43],s79 offen offset:2048 lds
mm v[16:19],a[36:39],a[12:15],v[16:19]
sa s79,s76,0x92400
bl v239,s[40:43],s79 offen offset:3072 lds
mm v[4:7],a[40:43],a[0:3],v[4:7]
dt a[100:101],v139 offset:24576
mm v[4:7],a[44:47],a[4:7],v[4:7]
sa m0,s77,0x10000
dt a[102:103],v139 offset:28672
)"
R"(mm v[20:23],a[40:43],a[8:11],v[20:23]
dr a[68:71],v130 offset:8192
mm v[20:23],a[44:47],a[12:15],v[20:23]
s_waitcnt lgkmcnt(12)
mm v[32:35],a[32:35],a[16:19],v[32:35]
dt a[104:105],v140 offset:16384
mm v[32:35],a[36:39],a[20:23],v[32:35]
dt a[106:107],v140 offset:20480
sa s79,s72,0x28000
bl v214,s[44:47],s79 offen sc1 lds
mm v[36:39],a[40:43],a[16:19],v[36:39]
dr a[72:75],v130 offset:1024
mm v[36:39],a[44:47],a[20:23],v[36:39]
sa s79,s73,0x28000
bl v214,s[44:47],s79 offen offset:1024 sc1 lds
mm v[8:11],a[48:51],a[0:3],v[8:11]
)"
R"(dt a[108:109],v140 offset:24576
mm v[8:11],a[52:55],a[4:7],v[8:11]
dt a[110:111],v140 offset:28672
mm v[24:27],a[48:51],a[8:11],v[24:27]
dr a[76:79],v130 offset:9216
mm v[24:27],a[52:55],a[12:15],v[24:27]
sa s79,s74,0x28000
bl v214,s[44:47],s79 offen offset:2048 sc1 lds
mm v[40:43],a[48:51],a[16:19],v[40:43]
dt a[112:113],v141 offset:16384
mm v[40:43],a[52:55],a[20:23],v[40:43]
s_waitcnt lgkmcnt(13)
dt a[114:115],v141 offset:20480
mm v[48:51],a[32:35],a[24:27],v[48:51]
dr a[80:83],v130 offset:2048
sa s79,s75,0x28000
)"
R"(bl v214,s[44:47],s79 offen offset:3072 sc1 lds
mm v[48:51],a[36:39],a[28:31],v[48:51]
sa m0,s77,0x14000
mm v[52:55],a[40:43],a[24:27],v[52:55]
dt a[116:117],v141 offset:24576
mm v[52:55],a[44:47],a[28:31],v[52:55]
dt a[118:119],v141 offset:28672
mm v[56:59],a[48:51],a[24:27],v[56:59]
dr a[84:87],v130 offset:10240
sa s79,s76,0xa0000
bl v238,s[40:43],s79 offen lds
mm v[56:59],a[52:55],a[28:31],v[56:59]
sa s79,s76,0xa0c00
bl v239,s[40:43],s79 offen offset:1024 lds
mm v[12:15],a[56:59],a[0:3],v[12:15]
dt a[120:121],v142 offset:16384
)"
R"(mm v[12:15],a[60:63],a[4:7],v[12:15]
dt a[122:123],v142 offset:20480
mm v[28:31],a[56:59],a[8:11],v[28:31]
dr a[88:91],v130 offset:3072
mm v[28:31],a[60:63],a[12:15],v[28:31]
sa s79,s76,0xa1800
bl v238,s[40:43],s79 offen offset:2048 lds
mm v[44:47],a[56:59],a[16:19],v[44:47]
dt a[124:125],v142 offset:24576
mm v[44:47],a[60:63],a[20:23],v[44:47]
dt a[126:127],v142 offset:28672
mm v[60:63],a[56:59],a[24:27],v[60:63]
dr a[92:95],v130 offset:11264
mm v[60:63],a[60:63],a[28:31],v[60:63]
s_waitcnt vmcnt(15) lgkmcnt(15)
s_barrier
)"
R"(mm v[0:3],a[96:99],a[64:67],v[0:3]
dt a[32:33],v131 offset:16384
sa s79,s76,0xa2400
bl v239,s[40:43],s79 offen offset:3072 lds
mm v[0:3],a[100:103],a[68:71],v[0:3]
s_waitcnt lgkmcnt(13)
sa m0,s77,0x18000
dt a[34:35],v131 offset:20480
mm v[16:19],a[96:99],a[72:75],v[16:19]
dr a[0:3],v128 offset:0
mm v[16:19],a[100:103],a[76:79],v[16:19]
sa s79,s72,0x2c000
bl v214,s[44:47],s79 offen sc1 lds
mm v[4:7],a[104:107],a[64:67],v[4:7]
dt a[36:37],v131 offset:24576
mm v[4:7],a[108:111],a[68:71],v[4:7]
)"
R"(dt a[38:39],v131 offset:28672
mm v[20:23],a[104:107],a[72:75],v[20:23]
dr a[4:7],v128 offset:8192
mm v[20:23],a[108:111],a[76:79],v[20:23]
s_waitcnt lgkmcnt(12)
sa s79,s73,0x2c000
bl v214,s[44:47],s79 offen offset:1024 sc1 lds
mm v[32:35],a[96:99],a[80:83],v[32:35]
dt a[40:41],v132 offset:16384
mm v[32:35],a[100:103],a[84:87],v[32:35]
dt a[42:43],v132 offset:20480
mm v[36:39],a[104:107],a[80:83],v[36:39]
dr a[8:11],v128 offset:1024
mm v[36:39],a[108:111],a[84:87],v[36:39]
sa s79,s74,0x2c000
bl v214,s[44:47],s79 offen offset:2048 sc1 lds
)"
R"(mm v[8:11],a[112:115],a[64:67],v[8:11]
dt a[44:45],v132 offset:24576
mm v[8:11],a[116:119],a[68:71],v[8:11]
dt a[46:47],v132 offset:28672
mm v[24:27],a[112:115],a[72:75],v[24:27]
dr a[12:15],v128 offset:9216
mm v[24:27],a[116:119],a[76:79],v[24:27]
sa s79,s75,0x2c000
bl v214,s[44:47],s79 offen offset:3072 sc1 lds
mm v[40:43],a[112:115],a[80:83],v[40:43]
sa m0,s77,0x1c000
dt a[48:49],v133 offset:16384
mm v[40:43],a[116:119],a[84:87],v[40:43]
s_waitcnt lgkmcnt(13)
dt a[50:51],v133 offset:20480
mm v[48:51],a[96:99],a[88:91],v[48:51]
)"
R"(dr a[16:19],v128 offset:2048
mm v[48:51],a[100:103],a[92:95],v[48:51]
sa s79,s76,0xb0000
bl v238,s[40:43],s79 offen lds
mm v[52:55],a[104:107],a[88:91],v[52:55]
dt a[52:53],v133 offset:24576
mm v[52:55],a[108:111],a[92:95],v[52:55]
dt a[54:55],v133 offset:28672
mm v[56:59],a[112:115],a[88:91],v[56:59]
dr a[20:23],v128 offset:10240
mm v[56:59],a[116:119],a[92:95],v[56:59]
sa s79,s76,0xb0c00
bl v239,s[40:43],s79 offen offset:1024 lds
mm v[12:15],a[120:123],a[64:67],v[12:15]
dt a[56:57],v134 offset:16384
mm v[12:15],a[124:127],a[68:71],v[12:15]
)"
R"(dt a[58:59],v134 offset:20480
mm v[28:31],a[120:123],a[72:75],v[28:31]
dr a[24:27],v128 offset:3072
mm v[28:31],a[124:127],a[76:79],v[28:31]
sa s79,s76,0xb1800
bl v238,s[40:43],s79 offen offset:2048 lds
mm v[44:47],a[120:123],a[80:83],v[44:47]
dt a[60:61],v134 offset:24576
mm v[44:47],a[124:127],a[84:87],v[44:47]
dt a[62:63],v134 offset:28672
mm v[60:63],a[120:123],a[88:91],v[60:63]
dr a[28:31],v128 offset:11264
mm v[60:63],a[124:127],a[92:95],v[60:63]
s_waitcnt vmcnt(15) lgkmcnt(15)
s_barrier
sa s79,s76,0xb2400
)"
R"(bl v239,s[40:43],s79 offen offset:3072 lds
mm v[0:3],a[32:35],a[0:3],v[0:3]
sa m0,s77,0x20000
dt a[96:97],v131 offset:49152
mm v[0:3],a[36:39],a[4:7],v[0:3]
s_waitcnt lgkmcnt(13)
dt a[98:99],v131 offset:53248
mm v[16:19],a[32:35],a[8:11],v[16:19]
dr a[64:67],v128 offset:32768
mm v[16:19],a[36:39],a[12:15],v[16:19]
sa s79,s72,0x30000
bl v214,s[44:47],s79 offen sc1 lds
mm v[4:7],a[40:43],a[0:3],v[4:7]
dt a[100:101],v131 offset:57344
mm v[4:7],a[44:47],a[4:7],v[4:7]
dt a[102:103],v131 offset:61440
)"
R"(mm v[20:23],a[40:43],a[8:11],v[20:23]
dr a[68:71],v128 offset:40960
mm v[20:23],a[44:47],a[12:15],v[20:23]
s_waitcnt lgkmcnt(12)
sa s79,s73,0x30000
bl v214,s[44:47],s79 offen offset:1024 sc1 lds
mm v[32:35],a[32:35],a[16:19],v[32:35]
dt a[104:105],v132 offset:49152
mm v[32:35],a[36:39],a[20:23],v[32:35]
dt a[106:107],v132 offset:53248
mm v[36:39],a[40:43],a[16:19],v[36:39]
dr a[72:75],v128 offset:33792
mm v[36:39],a[44:47],a[20:23],v[36:39]
sa s79,s74,0x30000
bl v214,s[44:47],s79 offen offset:2048 sc1 lds
mm v[8:11],a[48:51],a[0:3],v[8:11]
)"
R"(dt a[108:109],v132 offset:57344
mm v[8:11],a[52:55],a[4:7],v[8:11]
dt a[110:111],v132 offset:61440
mm v[24:27],a[48:51],a[8:11],v[24:27]
dr a[76:79],v128 offset:41984
mm v[24:27],a[52:55],a[12:15],v[24:27]
sa s79,s75,0x30000
bl v214,s[44:47],s79 offen offset:3072 sc1 lds
mm v[40:43],a[48:51],a[16:19],v[40:43]
sa m0,s77,0x24000
dt a[112:113],v133 offset:49152
mm v[40:43],a[52:55],a[20:23],v[40:43]
s_waitcnt lgkmcnt(13)
dt a[114:115],v133 offset:53248
mm v[48:51],a[32:35],a[24:27],v[48:51]
dr a[80:83],v128 offset:34816
)"
R"(mm v[48:51],a[36:39],a[28:31],v[48:51]
sa s79,s76,0xc0000
bl v238,s[40:43],s79 offen lds
mm v[52:55],a[40:43],a[24:27],v[52:55]
dt a[116:117],v133 offset:57344
mm v[52:55],a[44:47],a[28:31],v[52:55]
dt a[118:119],v133 offset:61440
mm v[56:59],a[48:51],a[24:27],v[56:59]
dr a[84:87],v128 offset:43008
mm v[56:59],a[52:55],a[28:31],v[56:59]
sa s79,s76,0xc0c00
bl v239,s[40:43],s79 offen offset:1024 lds
mm v[12:15],a[56:59],a[0:3],v[12:15]
dt a[120:121],v134 offset:49152
mm v[12:15],a[60:63],a[4:7],v[12:15]
dt a[122:123],v134 offset:53248
)"
R"(mm v[28:31],a[56:59],a[8:11],v[28:31]
dr a[88:91],v128 offset:35840
mm v[28:31],a[60:63],a[12:15],v[28:31]
sa s79,s76,0xc1800
bl v238,s[40:43],s79 offen offset:2048 lds
mm v[44:47],a[56:59],a[16:19],v[44:47]
dt a[124:125],v134 offset:57344
mm v[44:47],a[60:63],a[20:23],v[44:47]
dt a[126:127],v134 offset:61440
mm v[60:63],a[56:59],a[24:27],v[60:63]
dr a[92:95],v128 offset:44032
mm v[60:63],a[60:63],a[28:31],v[60:63]
s_waitcnt vmcnt(15) lgkmcnt(15)
s_barrier
sa s79,s76,0xc2400
bl v239,s[40:43],s79 offen offset:3072 lds
)"
R"(mm v[0:3],a[96:99],a[64:67],v[0:3]
dt a[32:33],v135 offset:16384
mm v[0:3],a[100:103],a[68:71],v[0:3]
s_waitcnt lgkmcnt(13)
dt a[34:35],v135 offset:20480
mm v[16:19],a[96:99],a[72:75],v[16:19]
dr a[0:3],v129 offset:0
mm v[16:19],a[100:103],a[76:79],v[16:19]
buffer_load_dword v251, v244, s[44:47], 0 offen sc1
sa m0,s77,0x0
sa s79,s72,0x34000
bl v214,s[44:47],s79 offen sc1 lds
mm v[4:7],a[104:107],a[64:67],v[4:7]
dt a[36:37],v135 offset:24576
mm v[4:7],a[108:111],a[68:71],v[4:7]
dt a[38:39],v135 offset:28672
)"
R"(mm v[20:23],a[104:107],a[72:75],v[20:23]
dr a[4:7],v129 offset:8192
mm v[20:23],a[108:111],a[76:79],v[20:23]
s_waitcnt lgkmcnt(12)
sa s79,s73,0x34000
bl v214,s[44:47],s79 offen offset:1024 sc1 lds
mm v[32:35],a[96:99],a[80:83],v[32:35]
dt a[40:41],v136 offset:16384
mm v[32:35],a[100:103],a[84:87],v[32:35]
dt a[42:43],v136 offset:20480
mm v[36:39],a[104:107],a[80:83],v[36:39]
dr a[8:11],v129 offset:1024
mm v[36:39],a[108:111],a[84:87],v[36:39]
sa s79,s74,0x34000
bl v214,s[44:47],s79 offen offset:2048 sc1 lds
mm v[8:11],a[112:115],a[64:67],v[8:11]
)"
R"(dt a[44:45],v136 offset:24576
mm v[8:11],a[116:119],a[68:71],v[8:11]
dt a[46:47],v136 offset:28672
mm v[24:27],a[112:115],a[72:75],v[24:27]
dr a[12:15],v129 offset:9216
mm v[24:27],a[116:119],a[76:79],v[24:27]
sa s79,s75,0x34000
bl v214,s[44:47],s79 offen offset:3072 sc1 lds
mm v[40:43],a[112:115],a[80:83],v[40:43]
sa m0,s77,0x4000
dt a[48:49],v137 offset:16384
mm v[40:43],a[116:119],a[84:87],v[40:43]
s_waitcnt lgkmcnt(13)
dt a[50:51],v137 offset:20480
mm v[48:51],a[96:99],a[88:91],v[48:51]
dr a[16:19],v129 offset:2048
)"
R"(mm v[48:51],a[100:103],a[92:95],v[48:51]
sa s79,s76,0xd0000
bl v238,s[40:43],s79 offen lds
mm v[52:55],a[104:107],a[88:91],v[52:55]
dt a[52:53],v137 offset:24576
mm v[52:55],a[108:111],a[92:95],v[52:55]
dt a[54:55],v137 offset:28672
mm v[56:59],a[112:115],a[88:91],v[56:59]
dr a[20:23],v129 offset:10240
mm v[56:59],a[116:119],a[92:95],v[56:59]
sa s79,s76,0xd0c00
bl v239,s[40:43],s79 offen offset:1024 lds
mm v[12:15],a[120:123],a[64:67],v[12:15]
dt a[56:57],v138 offset:16384
mm v[12:15],a[124:127],a[68:71],v[12:15]
dt a[58:59],v138 offset:20480
)"
R"(mm v[28:31],a[120:123],a[72:75],v[28:31]
dr a[24:27],v129 offset:3072
mm v[28:31],a[124:127],a[76:79],v[28:31]
sa s79,s76,0xd1800
bl v238,s[40:43],s79 offen offset:2048 lds
mm v[44:47],a[120:123],a[80:83],v[44:47]
dt a[60:61],v138 offset:24576
mm v[44:47],a[124:127],a[84:87],v[44:47]
dt a[62:63],v138 offset:28672
mm v[60:63],a[120:123],a[88:91],v[60:63]
dr a[28:31],v129 offset:11264
mm v[60:63],a[124:127],a[92:95],v[60:63]
s_waitcnt vmcnt(16) lgkmcnt(15)
s_barrier
sa s79,s76,0xd2400
bl v239,s[40:43],s79 offen offset:3072 lds
)"
R"(mm v[0:3],a[32:35],a[0:3],v[0:3]
sa m0,s77,0x8000
dt a[96:97],v135 offset:49152
mm v[0:3],a[36:39],a[4:7],v[0:3]
s_waitcnt lgkmcnt(13)
dt a[98:99],v135 offset:53248
mm v[16:19],a[32:35],a[8:11],v[16:19]
dr a[64:67],v129 offset:32768
mm v[16:19],a[36:39],a[12:15],v[16:19]
sa s79,s72,0x38000
bl v214,s[44:47],s79 offen sc1 lds
mm v[4:7],a[40:43],a[0:3],v[4:7]
dt a[100:101],v135 offset:57344
mm v[4:7],a[44:47],a[4:7],v[4:7]
dt a[102:103],v135 offset:61440
mm v[20:23],a[40:43],a[8:11],v[20:23]
)"
R"(dr a[68:71],v129 offset:40960
mm v[20:23],a[44:47],a[12:15],v[20:23]
s_waitcnt lgkmcnt(12)
sa s79,s73,0x38000
bl v214,s[44:47],s79 offen offset:1024 sc1 lds
mm v[32:35],a[32:35],a[16:19],v[32:35]
dt a[104:105],v136 offset:49152
mm v[32:35],a[36:39],a[20:23],v[32:35]
dt a[106:107],v136 offset:53248
mm v[36:39],a[40:43],a[16:19],v[36:39]
dr a[72:75],v129 offset:33792
mm v[36:39],a[44:47],a[20:23],v[36:39]
sa s79,s74,0x38000
bl v214,s[44:47],s79 offen offset:2048 sc1 lds
mm v[8:11],a[48:51],a[0:3],v[8:11]
dt a[108:109],v136 offset:57344
)"
R"(mm v[8:11],a[52:55],a[4:7],v[8:11]
dt a[110:111],v136 offset:61440
mm v[24:27],a[48:51],a[8:11],v[24:27]
dr a[76:79],v129 offset:41984
mm v[24:27],a[52:55],a[12:15],v[24:27]
sa s79,s75,0x38000
bl v214,s[44:47],s79 offen offset:3072 sc1 lds
mm v[40:43],a[48:51],a[16:19],v[40:43]
sa m0,s77,0xc000
dt a[112:113],v137 offset:49152
mm v[40:43],a[52:55],a[20:23],v[40:43]
s_waitcnt lgkmcnt(13)
dt a[114:115],v137 offset:53248
mm v[48:51],a[32:35],a[24:27],v[48:51]
dr a[80:83],v129 offset:34816
mm v[48:51],a[36:39],a[28:31],v[48:51]
)"
R"(sa s79,s76,0xe0000
bl v238,s[40:43],s79 offen lds
mm v[52:55],a[40:43],a[24:27],v[52:55]
dt a[116:117],v137 offset:57344
mm v[52:55],a[44:47],a[28:31],v[52:55]
dt a[118:119],v137 offset:61440
mm v[56:59],a[48:51],a[24:27],v[56:59]
dr a[84:87],v129 offset:43008
mm v[56:59],a[52:55],a[28:31],v[56:59]
sa s79,s76,0xe0c00
bl v239,s[40:43],s79 offen offset:1024 lds
mm v[12:15],a[56:59],a[0:3],v[12:15]
dt a[120:121],v138 offset:49152
mm v[12:15],a[60:63],a[4:7],v[12:15]
dt a[122:123],v138 offset:53248
mm v[28:31],a[56:59],a[8:11],v[28:31]
)"
R"(dr a[88:91],v129 offset:35840
mm v[28:31],a[60:63],a[12:15],v[28:31]
sa s79,s76,0xe1800
bl v238,s[40:43],s79 offen offset:2048 lds
mm v[44:47],a[56:59],a[16:19],v[44:47]
dt a[124:125],v138 offset:57344
mm v[44:47],a[60:63],a[20:23],v[44:47]
dt a[126:127],v138 offset:61440
mm v[60:63],a[56:59],a[24:27],v[60:63]
dr a[92:95],v129 offset:44032
mm v[60:63],a[60:63],a[28:31],v[60:63]
s_waitcnt vmcnt(16) lgkmcnt(15)
s_barrier
sa s79,s76,0xe2400
bl v239,s[40:43],s79 offen offset:3072 lds
mm v[0:3],a[96:99],a[64:67],v[0:3]
)"
R"(sa m0,s77,0x10000
dt a[32:33],v139 offset:16384
mm v[0:3],a[100:103],a[68:71],v[0:3]
s_waitcnt lgkmcnt(13)
dt a[34:35],v139 offset:20480
mm v[16:19],a[96:99],a[72:75],v[16:19]
dr a[0:3],v130 offset:0
mm v[16:19],a[100:103],a[76:79],v[16:19]
sa s79,s72,0x3c000
bl v214,s[44:47],s79 offen sc1 lds
mm v[4:7],a[104:107],a[64:67],v[4:7]
dt a[36:37],v139 offset:24576
mm v[4:7],a[108:111],a[68:71],v[4:7]
dt a[38:39],v139 offset:28672
mm v[20:23],a[104:107],a[72:75],v[20:23]
dr a[4:7],v130 offset:8192
)"
R"(mm v[20:23],a[108:111],a[76:79],v[20:23]
s_waitcnt lgkmcnt(12)
sa s79,s73,0x3c000
bl v214,s[44:47],s79 offen offset:1024 sc1 lds
mm v[32:35],a[96:99],a[80:83],v[32:35]
dt a[40:41],v140 offset:16384
mm v[32:35],a[100:103],a[84:87],v[32:35]
dt a[42:43],v140 offset:20480
mm v[36:39],a[104:107],a[80:83],v[36:39]
dr a[8:11],v130 offset:1024
mm v[36:39],a[108:111],a[84:87],v[36:39]
sa s79,s74,0x3c000
bl v214,s[44:47],s79 offen offset:2048 sc1 lds
mm v[8:11],a[112:115],a[64:67],v[8:11]
dt a[44:45],v140 offset:24576
mm v[8:11],a[116:119],a[68:71],v[8:11]
)"
R"(dt a[46:47],v140 offset:28672
mm v[24:27],a[112:115],a[72:75],v[24:27]
dr a[12:15],v130 offset:9216
mm v[24:27],a[116:119],a[76:79],v[24:27]
sa s79,s75,0x3c000
bl v214,s[44:47],s79 offen offset:3072 sc1 lds
mm v[40:43],a[112:115],a[80:83],v[40:43]
sa m0,s77,0x14000
dt a[48:49],v141 offset:16384
mm v[40:43],a[116:119],a[84:87],v[40:43]
s_waitcnt lgkmcnt(13)
dt a[50:51],v141 offset:20480
mm v[48:51],a[96:99],a[88:91],v[48:51]
dr a[16:19],v130 offset:2048
mm v[48:51],a[100:103],a[92:95],v[48:51]
sa s79,s76,0xf0000
)"
R"(bl v238,s[40:43],s79 offen lds
mm v[52:55],a[104:107],a[88:91],v[52:55]
dt a[52:53],v141 offset:24576
mm v[52:55],a[108:111],a[92:95],v[52:55]
dt a[54:55],v141 offset:28672
mm v[56:59],a[112:115],a[88:91],v[56:59]
dr a[20:23],v130 offset:10240
mm v[56:59],a[116:119],a[92:95],v[56:59]
sa s79,s76,0xf0c00
bl v239,s[40:43],s79 offen offset:1024 lds
mm v[12:15],a[120:123],a[64:67],v[12:15]
dt a[56:57],v142 offset:16384
mm v[12:15],a[124:127],a[68:71],v[12:15]
dt a[58:59],v142 offset:20480
mm v[28:31],a[120:123],a[72:75],v[28:31]
dr a[24:27],v130 offset:3072
)"
R"(mm v[28:31],a[124:127],a[76:79],v[28:31]
sa s79,s76,0xf1800
bl v238,s[40:43],s79 offen offset:2048 lds
mm v[44:47],a[120:123],a[80:83],v[44:47]
dt a[60:61],v142 offset:24576
mm v[44:47],a[124:127],a[84:87],v[44:47]
dt a[62:63],v142 offset:28672
mm v[60:63],a[120:123],a[88:91],v[60:63]
dr a[28:31],v130 offset:11264
mm v[60:63],a[124:127],a[92:95],v[60:63]
s_waitcnt vmcnt(15) lgkmcnt(15)
s_barrier
v_cmp_eq_u32 vcc, 1, v251
s_nop 1
s_cmp_eq_u64 vcc, exec
s_cbranch_scc0 L_fp0
)"
R"(L_fp0d:
sa s79,s76,0xf2400
bl v239,s[40:43],s79 offen offset:3072 lds
mm v[0:3],a[32:35],a[0:3],v[0:3]
dt a[96:97],v131 offset:16384
mm v[0:3],a[36:39],a[4:7],v[0:3]
s_waitcnt lgkmcnt(13)
dt a[98:99],v131 offset:20480
mm v[16:19],a[32:35],a[8:11],v[16:19]
dr a[64:67],v128 offset:0
mm v[16:19],a[36:39],a[12:15],v[16:19]
mm v[4:7],a[40:43],a[0:3],v[4:7]
dt a[100:101],v131 offset:24576
mm v[4:7],a[44:47],a[4:7],v[4:7]
dt a[102:103],v131 offset:28672
mm v[20:23],a[40:43],a[8:11],v[20:23]
)"
R"(dr a[68:71],v128 offset:8192
mm v[20:23],a[44:47],a[12:15],v[20:23]
s_waitcnt lgkmcnt(12)
mm v[32:35],a[32:35],a[16:19],v[32:35]
dt a[104:105],v132 offset:16384
mm v[32:35],a[36:39],a[20:23],v[32:35]
dt a[106:107],v132 offset:20480
mm v[36:39],a[40:43],a[16:19],v[36:39]
dr a[72:75],v128 offset:1024
mm v[36:39],a[44:47],a[20:23],v[36:39]
mm v[8:11],a[48:51],a[0:3],v[8:11]
dt a[108:109],v132 offset:24576
mm v[8:11],a[52:55],a[4:7],v[8:11]
dt a[110:111],v132 offset:28672
mm v[24:27],a[48:51],a[8:11],v[24:27]
dr a[76:79],v128 offset:9216
)"
R"(mm v[24:27],a[52:55],a[12:15],v[24:27]
mm v[40:43],a[48:51],a[16:19],v[40:43]
dt a[112:113],v133 offset:16384
mm v[40:43],a[52:55],a[20:23],v[40:43]
s_waitcnt lgkmcnt(13)
dt a[114:115],v133 offset:20480
mm v[48:51],a[32:35],a[24:27],v[48:51]
dr a[80:83],v128 offset:2048
mm v[48:51],a[36:39],a[28:31],v[48:51]
mm v[52:55],a[40:43],a[24:27],v[52:55]
dt a[116:117],v133 offset:24576
mm v[52:55],a[44:47],a[28:31],v[52:55]
dt a[118:119],v133 offset:28672
mm v[56:59],a[48:51],a[24:27],v[56:59]
dr a[84:87],v128 offset:10240
mm v[56:59],a[52:55],a[28:31],v[56:59]
)"
R"(mm v[12:15],a[56:59],a[0:3],v[12:15]
dt a[120:121],v134 offset:16384
mm v[12:15],a[60:63],a[4:7],v[12:15]
dt a[122:123],v134 offset:20480
mm v[28:31],a[56:59],a[8:11],v[28:31]
dr a[88:91],v128 offset:3072
mm v[28:31],a[60:63],a[12:15],v[28:31]
mm v[44:47],a[56:59],a[16:19],v[44:47]
dt a[124:125],v134 offset:24576
mm v[44:47],a[60:63],a[20:23],v[44:47]
dt a[126:127],v134 offset:28672
mm v[60:63],a[56:59],a[24:27],v[60:63]
dr a[92:95],v128 offset:11264
mm v[60:63],a[60:63],a[28:31],v[60:63]
s_waitcnt vmcnt(8) lgkmcnt(15)
s_barrier
)"
R"(mm v[0:3],a[96:99],a[64:67],v[0:3]
s_lshl_b32 s72, s65, 2
dt a[32:33],v131 offset:49152
mm v[0:3],a[100:103],a[68:71],v[0:3]
s_waitcnt lgkmcnt(13)
dt a[34:35],v131 offset:53248
mm v[16:19],a[96:99],a[72:75],v[16:19]
dr a[0:3],v128 offset:32768
mm v[16:19],a[100:103],a[76:79],v[16:19]
mm v[4:7],a[104:107],a[64:67],v[4:7]
dt a[36:37],v131 offset:57344
mm v[4:7],a[108:111],a[68:71],v[4:7]
dt a[38:39],v131 offset:61440
mm v[20:23],a[104:107],a[72:75],v[20:23]
dr a[4:7],v128 offset:40960
mm v[20:23],a[108:111],a[76:79],v[20:23]
)"
R"(s_waitcnt lgkmcnt(12)
mm v[32:35],a[96:99],a[80:83],v[32:35]
dt a[40:41],v132 offset:49152
mm v[32:35],a[100:103],a[84:87],v[32:35]
dt a[42:43],v132 offset:53248
mm v[36:39],a[104:107],a[80:83],v[36:39]
sa s72,s72,s66
dr a[8:11],v128 offset:33792
mm v[36:39],a[108:111],a[84:87],v[36:39]
mm v[8:11],a[112:115],a[64:67],v[8:11]
dt a[44:45],v132 offset:57344
mm v[8:11],a[116:119],a[68:71],v[8:11]
dt a[46:47],v132 offset:61440
mm v[24:27],a[112:115],a[72:75],v[24:27]
dr a[12:15],v128 offset:41984
mm v[24:27],a[116:119],a[76:79],v[24:27]
)"
R"(mm v[40:43],a[112:115],a[80:83],v[40:43]
dt a[48:49],v133 offset:49152
mm v[40:43],a[116:119],a[84:87],v[40:43]
s_waitcnt lgkmcnt(13)
dt a[50:51],v133 offset:53248
mm v[48:51],a[96:99],a[88:91],v[48:51]
dr a[16:19],v128 offset:34816
mm v[48:51],a[100:103],a[92:95],v[48:51]
mm v[52:55],a[104:107],a[88:91],v[52:55]
dt a[52:53],v133 offset:57344
mm v[52:55],a[108:111],a[92:95],v[52:55]
s_lshl_b32 s72, s72, 17
dt a[54:55],v133 offset:61440
mm v[56:59],a[112:115],a[88:91],v[56:59]
dr a[20:23],v128 offset:43008
mm v[56:59],a[116:119],a[92:95],v[56:59]
)"
R"(mm v[12:15],a[120:123],a[64:67],v[12:15]
dt a[56:57],v134 offset:49152
mm v[12:15],a[124:127],a[68:71],v[12:15]
dt a[58:59],v134 offset:53248
mm v[28:31],a[120:123],a[72:75],v[28:31]
dr a[24:27],v128 offset:35840
mm v[28:31],a[124:127],a[76:79],v[28:31]
mm v[44:47],a[120:123],a[80:83],v[44:47]
dt a[60:61],v134 offset:57344
mm v[44:47],a[124:127],a[84:87],v[44:47]
dt a[62:63],v134 offset:61440
mm v[60:63],a[120:123],a[88:91],v[60:63]
dr a[28:31],v128 offset:44032
mm v[60:63],a[124:127],a[92:95],v[60:63]
s_waitcnt vmcnt(0) lgkmcnt(15)
s_barrier
)"
R"(mm v[0:3],a[32:35],a[0:3],v[0:3]
s_lshl_b32 s80, s50, 13
dt a[96:97],v135 offset:16384
mm v[0:3],a[36:39],a[4:7],v[0:3]
s_waitcnt lgkmcnt(13)
dt a[98:99],v135 offset:20480
mm v[16:19],a[32:35],a[8:11],v[16:19]
dr a[64:67],v129 offset:0
mm v[16:19],a[36:39],a[12:15],v[16:19]
mm v[4:7],a[40:43],a[0:3],v[4:7]
dt a[100:101],v135 offset:24576
mm v[4:7],a[44:47],a[4:7],v[4:7]
dt a[102:103],v135 offset:28672
mm v[20:23],a[40:43],a[8:11],v[20:23]
dr a[68:71],v129 offset:8192
mm v[20:23],a[44:47],a[12:15],v[20:23]
)"
R"(s_waitcnt lgkmcnt(12)
mm v[32:35],a[32:35],a[16:19],v[32:35]
dt a[104:105],v136 offset:16384
mm v[32:35],a[36:39],a[20:23],v[32:35]
dt a[106:107],v136 offset:20480
mm v[36:39],a[40:43],a[16:19],v[36:39]
sa s72,s72,s80
dr a[72:75],v129 offset:1024
mm v[36:39],a[44:47],a[20:23],v[36:39]
mm v[8:11],a[48:51],a[0:3],v[8:11]
dt a[108:109],v136 offset:24576
mm v[8:11],a[52:55],a[4:7],v[8:11]
dt a[110:111],v136 offset:28672
mm v[24:27],a[48:51],a[8:11],v[24:27]
dr a[76:79],v129 offset:9216
mm v[24:27],a[52:55],a[12:15],v[24:27]
)"
R"(mm v[40:43],a[48:51],a[16:19],v[40:43]
dt a[112:113],v137 offset:16384
mm v[40:43],a[52:55],a[20:23],v[40:43]
s_waitcnt lgkmcnt(13)
dt a[114:115],v137 offset:20480
mm v[48:51],a[32:35],a[24:27],v[48:51]
dr a[80:83],v129 offset:2048
mm v[48:51],a[36:39],a[28:31],v[48:51]
mm v[52:55],a[40:43],a[24:27],v[52:55]
dt a[116:117],v137 offset:24576
mm v[52:55],a[44:47],a[28:31],v[52:55]
sa s72,s72,0x3400000
dt a[118:119],v137 offset:28672
mm v[56:59],a[48:51],a[24:27],v[56:59]
dr a[84:87],v129 offset:10240
mm v[56:59],a[52:55],a[28:31],v[56:59]
)"
R"(mm v[12:15],a[56:59],a[0:3],v[12:15]
dt a[120:121],v138 offset:16384
mm v[12:15],a[60:63],a[4:7],v[12:15]
dt a[122:123],v138 offset:20480
mm v[28:31],a[56:59],a[8:11],v[28:31]
dr a[88:91],v129 offset:3072
mm v[28:31],a[60:63],a[12:15],v[28:31]
mm v[44:47],a[56:59],a[16:19],v[44:47]
dt a[124:125],v138 offset:24576
mm v[44:47],a[60:63],a[20:23],v[44:47]
dt a[126:127],v138 offset:28672
mm v[60:63],a[56:59],a[24:27],v[60:63]
dr a[92:95],v129 offset:11264
mm v[60:63],a[60:63],a[28:31],v[60:63]
s_waitcnt lgkmcnt(15)
mm v[0:3],a[96:99],a[64:67],v[0:3]
)"
R"(mm v[0:3],a[100:103],a[68:71],v[0:3]
s_waitcnt lgkmcnt(12)
mm v[16:19],a[96:99],a[72:75],v[16:19]
mm v[16:19],a[100:103],a[76:79],v[16:19]
mm v[4:7],a[104:107],a[64:67],v[4:7]
mm v[4:7],a[108:111],a[68:71],v[4:7]
mm v[20:23],a[104:107],a[72:75],v[20:23]
mm v[20:23],a[108:111],a[76:79],v[20:23]
s_waitcnt lgkmcnt(6)
mm v[32:35],a[96:99],a[80:83],v[32:35]
mm v[32:35],a[100:103],a[84:87],v[32:35]
mm v[36:39],a[104:107],a[80:83],v[36:39]
mm v[36:39],a[108:111],a[84:87],v[36:39]
mm v[8:11],a[112:115],a[64:67],v[8:11]
mm v[8:11],a[116:119],a[68:71],v[8:11]
mm v[24:27],a[112:115],a[72:75],v[24:27]
)"
R"(mm v[24:27],a[116:119],a[76:79],v[24:27]
mm v[40:43],a[112:115],a[80:83],v[40:43]
mm v[40:43],a[116:119],a[84:87],v[40:43]
s_waitcnt lgkmcnt(0)
mm v[48:51],a[96:99],a[88:91],v[48:51]
mm v[48:51],a[100:103],a[92:95],v[48:51]
mm v[52:55],a[104:107],a[88:91],v[52:55]
mm v[52:55],a[108:111],a[92:95],v[52:55]
mm v[56:59],a[112:115],a[88:91],v[56:59]
mm v[56:59],a[116:119],a[92:95],v[56:59]
mm v[12:15],a[120:123],a[64:67],v[12:15]
mm v[12:15],a[124:127],a[68:71],v[12:15]
mm v[28:31],a[120:123],a[72:75],v[28:31]
mm v[28:31],a[124:127],a[76:79],v[28:31]
mm v[44:47],a[120:123],a[80:83],v[44:47]
mm v[44:47],a[124:127],a[84:87],v[44:47]
)"
R"(mm v[60:63],a[120:123],a[88:91],v[60:63]
mm v[60:63],a[124:127],a[92:95],v[60:63]
s_nop 7
cv v16,v16,v17
cv v17,v18,v19
cv v18,v20,v21
cv v19,v22,v23
cv v20,v24,v25
cv v21,v26,v27
cv v22,v28,v29
cv v23,v30,v31
s_xor_b32 s79, s64, 1
s_lshl_b32 s79, s79, 15
sa s79,s79,s72
sa s79,s79,0
buffer_store_dwordx4 v[16:19], v214, s[44:47], s79 offen offset:0 sc1
)"
R"(buffer_store_dwordx4 v[20:23], v214, s[44:47], s79 offen offset:1024 sc1
cv v32,v32,v33
cv v33,v34,v35
cv v34,v36,v37
cv v35,v38,v39
cv v36,v40,v41
cv v37,v42,v43
cv v38,v44,v45
cv v39,v46,v47
s_xor_b32 s79, s64, 2
s_lshl_b32 s79, s79, 15
sa s79,s79,s72
sa s79,s79,2048
buffer_store_dwordx4 v[32:35], v214, s[44:47], s79 offen offset:0 sc1
buffer_store_dwordx4 v[36:39], v214, s[44:47], s79 offen offset:1024 sc1
cv v48,v48,v49
)"
R"(cv v49,v50,v51
cv v50,v52,v53
cv v51,v54,v55
cv v52,v56,v57
cv v53,v58,v59
cv v54,v60,v61
cv v55,v62,v63
s_xor_b32 s79, s64, 3
s_lshl_b32 s79, s79, 15
sa s79,s79,s72
sa s79,s79,4096
buffer_store_dwordx4 v[48:51], v214, s[44:47], s79 offen offset:0 sc1
buffer_store_dwordx4 v[52:55], v214, s[44:47], s79 offen offset:1024 sc1
s_sleep 10
s_branch L_out_tail
L_out_odd:
)"
R"(v_cmp_eq_u32 vcc, 1, v251
s_nop 1
s_cmp_eq_u64 vcc, exec
s_cbranch_scc0 L_f2o
L_f2od:
s_barrier
sa m0,s77,0x10000
sa s79,s72,0x0
bl v214,s[44:47],s79 offen sc1 lds
sa s79,s73,0x0
bl v214,s[44:47],s79 offen offset:1024 sc1 lds
sa s79,s74,0x0
bl v214,s[44:47],s79 offen offset:2048 sc1 lds
sa s79,s75,0x0
bl v214,s[44:47],s79 offen offset:3072 sc1 lds
sa m0,s77,0x18000
)"
R"(sa s79,s72,0x4000
bl v214,s[44:47],s79 offen sc1 lds
sa s79,s73,0x4000
bl v214,s[44:47],s79 offen offset:1024 sc1 lds
sa s79,s74,0x4000
bl v214,s[44:47],s79 offen offset:2048 sc1 lds
sa s79,s75,0x4000
bl v214,s[44:47],s79 offen offset:3072 sc1 lds
s_waitcnt vmcnt(4)
s_barrier
dt a[32:33],v135 offset:16384
dt a[34:35],v135 offset:20480
dr a[0:3],v129 offset:0
dt a[36:37],v135 offset:24576
dt a[38:39],v135 offset:28672
dr a[4:7],v129 offset:8192
)"
R"(dt a[40:41],v136 offset:16384
dt a[42:43],v136 offset:20480
dr a[8:11],v129 offset:1024
dt a[44:45],v136 offset:24576
dt a[46:47],v136 offset:28672
dr a[12:15],v129 offset:9216
dt a[48:49],v137 offset:16384
dt a[50:51],v137 offset:20480
dr a[16:19],v129 offset:2048
dt a[52:53],v137 offset:24576
dt a[54:55],v137 offset:28672
dr a[20:23],v129 offset:10240
dt a[56:57],v138 offset:16384
dt a[58:59],v138 offset:20480
dr a[24:27],v129 offset:3072
dt a[60:61],v138 offset:24576
)"
R"(dt a[62:63],v138 offset:28672
dr a[28:31],v129 offset:11264
s_waitcnt vmcnt(0) lgkmcnt(15)
s_barrier
s_mov_b64 exec, 1
buffer_store_dword v246, v247, s[44:47], s55 offen sc1
s_mov_b64 exec, -1
mm v[0:3],a[32:35],a[0:3],0
sa m0,s77,0x20000
dt a[96:97],v135 offset:49152
mm v[0:3],a[36:39],a[4:7],v[0:3]
s_waitcnt lgkmcnt(13)
dt a[98:99],v135 offset:53248
mm v[16:19],a[32:35],a[8:11],0
dr a[64:67],v129 offset:32768
mm v[16:19],a[36:39],a[12:15],v[16:19]
)"
R"(sa s79,s72,0x8000
bl v214,s[44:47],s79 offen sc1 lds
mm v[4:7],a[40:43],a[0:3],0
dt a[100:101],v135 offset:57344
mm v[4:7],a[44:47],a[4:7],v[4:7]
dt a[102:103],v135 offset:61440
mm v[20:23],a[40:43],a[8:11],0
dr a[68:71],v129 offset:40960
sa s79,s73,0x8000
bl v214,s[44:47],s79 offen offset:1024 sc1 lds
mm v[20:23],a[44:47],a[12:15],v[20:23]
s_waitcnt lgkmcnt(12)
mm v[32:35],a[32:35],a[16:19],0
dt a[104:105],v136 offset:49152
v_bfe_u32 v148, v146, 2, 4
mm v[32:35],a[36:39],a[20:23],v[32:35]
)"
R"(dt a[106:107],v136 offset:53248
sa s79,s74,0x8000
bl v214,s[44:47],s79 offen offset:2048 sc1 lds
mm v[36:39],a[40:43],a[16:19],0
dr a[72:75],v129 offset:33792
mm v[36:39],a[44:47],a[20:23],v[36:39]
sa s79,s75,0x8000
bl v214,s[44:47],s79 offen offset:3072 sc1 lds
mm v[8:11],a[48:51],a[0:3],0
sa m0,s77,0x0
dt a[108:109],v136 offset:57344
mm v[8:11],a[52:55],a[4:7],v[8:11]
s_lshl_b32 s70, s67, 2
dt a[110:111],v136 offset:61440
mm v[24:27],a[48:51],a[8:11],0
dr a[76:79],v129 offset:41984
)"
R"(mm v[24:27],a[52:55],a[12:15],v[24:27]
sa s79,s72,0xc000
bl v214,s[44:47],s79 offen sc1 lds
mm v[40:43],a[48:51],a[16:19],0
dt a[112:113],v137 offset:49152
mm v[40:43],a[52:55],a[20:23],v[40:43]
s_waitcnt lgkmcnt(13)
dt a[114:115],v137 offset:53248
mm v[48:51],a[32:35],a[24:27],0
sa s70,s70,s50
dr a[80:83],v129 offset:34816
mm v[48:51],a[36:39],a[28:31],v[48:51]
sa s79,s73,0xc000
bl v214,s[44:47],s79 offen offset:1024 sc1 lds
mm v[52:55],a[40:43],a[24:27],0
sa s79,s74,0xc000
)"
R"(bl v214,s[44:47],s79 offen offset:2048 sc1 lds
dt a[116:117],v137 offset:57344
mm v[52:55],a[44:47],a[28:31],v[52:55]
dt a[118:119],v137 offset:61440
mm v[56:59],a[48:51],a[24:27],0
dr a[84:87],v129 offset:43008
mm v[56:59],a[52:55],a[28:31],v[56:59]
sa s79,s75,0xc000
bl v214,s[44:47],s79 offen offset:3072 sc1 lds
mm v[12:15],a[56:59],a[0:3],0
s_lshl_b32 s70, s70, 14
dt a[120:121],v138 offset:49152
mm v[12:15],a[60:63],a[4:7],v[12:15]
sa m0,s77,0x4000
dt a[122:123],v138 offset:53248
mm v[28:31],a[56:59],a[8:11],0
)"
R"(sa s79,s76,0x30000
bl v238,s[40:43],s79 offen lds
dr a[88:91],v129 offset:35840
mm v[28:31],a[60:63],a[12:15],v[28:31]
mm v[44:47],a[56:59],a[16:19],0
dt a[124:125],v138 offset:57344
mm v[44:47],a[60:63],a[20:23],v[44:47]
sa s79,s76,0x30c00
bl v239,s[40:43],s79 offen offset:1024 lds
dt a[126:127],v138 offset:61440
mm v[60:63],a[56:59],a[24:27],0
sa s70,s70,0x3100000
dr a[92:95],v129 offset:44032
mm v[60:63],a[60:63],a[28:31],v[60:63]
s_waitcnt vmcnt(6) lgkmcnt(15)
s_barrier
)"
R"(mm v[0:3],a[96:99],a[64:67],v[0:3]
dt a[32:33],v139 offset:16384
mm v[0:3],a[100:103],a[68:71],v[0:3]
s_waitcnt lgkmcnt(13)
sa s79,s76,0x31800
bl v238,s[40:43],s79 offen offset:2048 lds
dt a[34:35],v139 offset:20480
mm v[16:19],a[96:99],a[72:75],v[16:19]
s_lshl_b32 s78, s50, 15
dr a[0:3],v130 offset:0
mm v[16:19],a[100:103],a[76:79],v[16:19]
mm v[4:7],a[104:107],a[64:67],v[4:7]
sa s79,s76,0x32400
bl v239,s[40:43],s79 offen offset:3072 lds
dt a[36:37],v139 offset:24576
mm v[4:7],a[108:111],a[68:71],v[4:7]
)"
R"(dt a[38:39],v139 offset:28672
mm v[20:23],a[104:107],a[72:75],v[20:23]
sa m0,s77,0x8000
dr a[4:7],v130 offset:8192
mm v[20:23],a[108:111],a[76:79],v[20:23]
s_waitcnt lgkmcnt(12)
sa s79,s72,0x10000
bl v214,s[44:47],s79 offen sc1 lds
mm v[32:35],a[96:99],a[80:83],v[32:35]
s_lshl_b32 s56, s52, 7
dt a[40:41],v140 offset:16384
mm v[32:35],a[100:103],a[84:87],v[32:35]
dt a[42:43],v140 offset:20480
mm v[36:39],a[104:107],a[80:83],v[36:39]
sa s79,s73,0x10000
bl v214,s[44:47],s79 offen offset:1024 sc1 lds
)"
R"(dr a[8:11],v130 offset:1024
mm v[36:39],a[108:111],a[84:87],v[36:39]
mm v[8:11],a[112:115],a[64:67],v[8:11]
sa s78,s78,s56
dt a[44:45],v140 offset:24576
mm v[8:11],a[116:119],a[68:71],v[8:11]
dt a[46:47],v140 offset:28672
mm v[24:27],a[112:115],a[72:75],v[24:27]
sa s79,s74,0x10000
bl v214,s[44:47],s79 offen offset:2048 sc1 lds
dr a[12:15],v130 offset:9216
mm v[24:27],a[116:119],a[76:79],v[24:27]
mm v[40:43],a[112:115],a[80:83],v[40:43]
dt a[48:49],v141 offset:16384
mm v[40:43],a[116:119],a[84:87],v[40:43]
s_waitcnt lgkmcnt(13)
)"
R"(sa s79,s75,0x10000
bl v214,s[44:47],s79 offen offset:3072 sc1 lds
dt a[50:51],v141 offset:20480
mm v[48:51],a[96:99],a[88:91],v[48:51]
dr a[16:19],v130 offset:2048
v_lshrrev_b32 v147, 3, v146
mm v[48:51],a[100:103],a[92:95],v[48:51]
sa m0,s77,0xc000
mm v[52:55],a[104:107],a[88:91],v[52:55]
sa s79,s76,0x40000
bl v238,s[40:43],s79 offen lds
dt a[52:53],v141 offset:24576
mm v[52:55],a[108:111],a[92:95],v[52:55]
dt a[54:55],v141 offset:28672
mm v[56:59],a[112:115],a[88:91],v[56:59]
dr a[20:23],v130 offset:10240
)"
R"(mm v[56:59],a[116:119],a[92:95],v[56:59]
v_lshlrev_b32 v159, 10, v147
mm v[12:15],a[120:123],a[64:67],v[12:15]
sa s79,s76,0x40c00
bl v239,s[40:43],s79 offen offset:1024 lds
dt a[56:57],v142 offset:16384
mm v[12:15],a[124:127],a[68:71],v[12:15]
dt a[58:59],v142 offset:20480
mm v[28:31],a[120:123],a[72:75],v[28:31]
dr a[24:27],v130 offset:3072
mm v[28:31],a[124:127],a[76:79],v[28:31]
sa s79,s76,0x41800
bl v238,s[40:43],s79 offen offset:2048 lds
mm v[44:47],a[120:123],a[80:83],v[44:47]
dt a[60:61],v142 offset:24576
v_lshrrev_b32 v147, 4, v146
)"
R"(mm v[44:47],a[124:127],a[84:87],v[44:47]
dt a[62:63],v142 offset:28672
mm v[60:63],a[120:123],a[88:91],v[60:63]
sa s79,s76,0x42400
bl v239,s[40:43],s79 offen offset:3072 lds
dr a[28:31],v130 offset:11264
mm v[60:63],a[124:127],a[92:95],v[60:63]
s_waitcnt vmcnt(8) lgkmcnt(15)
s_barrier
buffer_load_dword v251, v250, s[44:47], s53 offen sc1
mm v[0:3],a[32:35],a[0:3],v[0:3]
dt a[96:97],v131 offset:16384
mm v[0:3],a[36:39],a[4:7],v[0:3]
s_waitcnt lgkmcnt(13)
sa m0,s77,0x10000
dt a[98:99],v131 offset:20480
)"
R"(mm v[16:19],a[32:35],a[8:11],v[16:19]
dr a[64:67],v128 offset:0
v_lshlrev_b32 v147, 1, v147
mm v[16:19],a[36:39],a[12:15],v[16:19]
sa s79,s72,0x14000
bl v214,s[44:47],s79 offen sc1 lds
mm v[4:7],a[40:43],a[0:3],v[4:7]
dt a[100:101],v131 offset:24576
mm v[4:7],a[44:47],a[4:7],v[4:7]
dt a[102:103],v131 offset:28672
mm v[20:23],a[40:43],a[8:11],v[20:23]
dr a[68:71],v128 offset:8192
mm v[20:23],a[44:47],a[12:15],v[20:23]
s_waitcnt lgkmcnt(12)
v_and_b32 v149, 7, v146
mm v[32:35],a[32:35],a[16:19],v[32:35]
)"
R"(dt a[104:105],v132 offset:16384
sa s79,s73,0x14000
bl v214,s[44:47],s79 offen offset:1024 sc1 lds
mm v[32:35],a[36:39],a[20:23],v[32:35]
dt a[106:107],v132 offset:20480
mm v[36:39],a[40:43],a[16:19],v[36:39]
dr a[72:75],v128 offset:1024
mm v[36:39],a[44:47],a[20:23],v[36:39]
sa s79,s74,0x14000
bl v214,s[44:47],s79 offen offset:2048 sc1 lds
mm v[8:11],a[48:51],a[0:3],v[8:11]
dt a[108:109],v132 offset:24576
v_xor_b32 v149, v149, v147
mm v[8:11],a[52:55],a[4:7],v[8:11]
sa s79,s75,0x14000
bl v214,s[44:47],s79 offen offset:3072 sc1 lds
)"
R"(dt a[110:111],v132 offset:28672
mm v[24:27],a[48:51],a[8:11],v[24:27]
sa m0,s77,0x14000
dr a[76:79],v128 offset:9216
mm v[24:27],a[52:55],a[12:15],v[24:27]
sa s79,s76,0x50000
bl v238,s[40:43],s79 offen lds
mm v[40:43],a[48:51],a[16:19],v[40:43]
dt a[112:113],v133 offset:16384
mm v[40:43],a[52:55],a[20:23],v[40:43]
s_waitcnt lgkmcnt(13)
dt a[114:115],v133 offset:20480
v_lshl_add_u32 v159, v149, 4, v159
mm v[48:51],a[32:35],a[24:27],v[48:51]
sa s79,s76,0x50c00
bl v239,s[40:43],s79 offen offset:1024 lds
)"
R"(dr a[80:83],v128 offset:2048
mm v[48:51],a[36:39],a[28:31],v[48:51]
mm v[52:55],a[40:43],a[24:27],v[52:55]
dt a[116:117],v133 offset:24576
mm v[52:55],a[44:47],a[28:31],v[52:55]
sa s79,s76,0x51800
bl v238,s[40:43],s79 offen offset:2048 lds
dt a[118:119],v133 offset:28672
mm v[56:59],a[48:51],a[24:27],v[56:59]
dr a[84:87],v128 offset:10240
mm v[56:59],a[52:55],a[28:31],v[56:59]
s_and_b32 s56, s50, 1
mm v[12:15],a[56:59],a[0:3],v[12:15]
dt a[120:121],v134 offset:16384
mm v[12:15],a[60:63],a[4:7],v[12:15]
sa s79,s76,0x52400
)"
R"(bl v239,s[40:43],s79 offen offset:3072 lds
dt a[122:123],v134 offset:20480
mm v[28:31],a[56:59],a[8:11],v[28:31]
dr a[88:91],v128 offset:3072
mm v[28:31],a[60:63],a[12:15],v[28:31]
sa m0,s77,0x18000
mm v[44:47],a[56:59],a[16:19],v[44:47]
sa s79,s72,0x18000
bl v214,s[44:47],s79 offen sc1 lds
dt a[124:125],v134 offset:24576
mm v[44:47],a[60:63],a[20:23],v[44:47]
s_lshl_b32 s56, s56, 13
dt a[126:127],v134 offset:28672
mm v[60:63],a[56:59],a[24:27],v[60:63]
dr a[92:95],v128 offset:11264
mm v[60:63],a[60:63],a[28:31],v[60:63]
)"
R"(s_waitcnt vmcnt(10) lgkmcnt(15)
s_barrier
sa s79,s73,0x18000
bl v214,s[44:47],s79 offen offset:1024 sc1 lds
mm v[0:3],a[96:99],a[64:67],v[0:3]
dt a[32:33],v131 offset:49152
mm v[0:3],a[100:103],a[68:71],v[0:3]
s_waitcnt lgkmcnt(13)
dt a[34:35],v131 offset:53248
v_lshlrev_b32 v147, 3, v146
mm v[16:19],a[96:99],a[72:75],v[16:19]
dr a[0:3],v128 offset:32768
mm v[16:19],a[100:103],a[76:79],v[16:19]
sa s79,s74,0x18000
bl v214,s[44:47],s79 offen offset:2048 sc1 lds
mm v[4:7],a[104:107],a[64:67],v[4:7]
)"
R"(dt a[36:37],v131 offset:57344
mm v[4:7],a[108:111],a[68:71],v[4:7]
dt a[38:39],v131 offset:61440
mm v[20:23],a[104:107],a[72:75],v[20:23]
sa s79,s75,0x18000
bl v214,s[44:47],s79 offen offset:3072 sc1 lds
dr a[4:7],v128 offset:40960
mm v[20:23],a[108:111],a[76:79],v[20:23]
s_waitcnt lgkmcnt(12)
v_add_u32 v150, s56, v147
mm v[32:35],a[96:99],a[80:83],v[32:35]
sa m0,s77,0x1c000
dt a[40:41],v132 offset:49152
mm v[32:35],a[100:103],a[84:87],v[32:35]
sa s79,s76,0x60000
bl v238,s[40:43],s79 offen lds
)"
R"(dt a[42:43],v132 offset:53248
mm v[36:39],a[104:107],a[80:83],v[36:39]
dr a[8:11],v128 offset:33792
mm v[36:39],a[108:111],a[84:87],v[36:39]
mm v[8:11],a[112:115],a[64:67],v[8:11]
sa s79,s76,0x60c00
bl v239,s[40:43],s79 offen offset:1024 lds
dt a[44:45],v132 offset:57344
mm v[8:11],a[116:119],a[68:71],v[8:11]
dt a[46:47],v132 offset:61440
v_lshlrev_b32 v149, 7, v148
mm v[24:27],a[112:115],a[72:75],v[24:27]
dr a[12:15],v128 offset:41984
mm v[24:27],a[116:119],a[76:79],v[24:27]
mm v[40:43],a[112:115],a[80:83],v[40:43]
sa s79,s76,0x61800
)"
R"(bl v238,s[40:43],s79 offen offset:2048 lds
dt a[48:49],v133 offset:49152
mm v[40:43],a[116:119],a[84:87],v[40:43]
s_waitcnt lgkmcnt(13)
dt a[50:51],v133 offset:53248
v_bfe_u32 v147, v148, 1, 2
mm v[48:51],a[96:99],a[88:91],v[48:51]
dr a[16:19],v128 offset:34816
mm v[48:51],a[100:103],a[92:95],v[48:51]
sa s79,s76,0x62400
bl v239,s[40:43],s79 offen offset:3072 lds
mm v[52:55],a[104:107],a[88:91],v[52:55]
dt a[52:53],v133 offset:57344
mm v[52:55],a[108:111],a[92:95],v[52:55]
sa m0,s77,0x20000
dt a[54:55],v133 offset:61440
)"
R"(mm v[56:59],a[112:115],a[88:91],v[56:59]
sa s79,s72,0x1c000
bl v214,s[44:47],s79 offen sc1 lds
dr a[20:23],v128 offset:43008
mm v[56:59],a[116:119],a[92:95],v[56:59]
v_lshl_add_u32 v149, v147, 5, v149
mm v[12:15],a[120:123],a[64:67],v[12:15]
dt a[56:57],v134 offset:49152
mm v[12:15],a[124:127],a[68:71],v[12:15]
dt a[58:59],v134 offset:53248
mm v[28:31],a[120:123],a[72:75],v[28:31]
sa s79,s73,0x1c000
bl v214,s[44:47],s79 offen offset:1024 sc1 lds
dr a[24:27],v128 offset:35840
mm v[28:31],a[124:127],a[76:79],v[28:31]
v_and_b32 v147, 3, v146
)"
R"(mm v[44:47],a[120:123],a[80:83],v[44:47]
dt a[60:61],v134 offset:57344
mm v[44:47],a[124:127],a[84:87],v[44:47]
sa s79,s74,0x1c000
bl v214,s[44:47],s79 offen offset:2048 sc1 lds
dt a[62:63],v134 offset:61440
mm v[60:63],a[120:123],a[88:91],v[60:63]
dr a[28:31],v128 offset:44032
mm v[60:63],a[124:127],a[92:95],v[60:63]
s_waitcnt vmcnt(11) lgkmcnt(15)
s_barrier
v_cmp_eq_u32 vcc, 1, v251
s_nop 1
s_cmp_eq_u64 vcc, exec
s_cbranch_scc0 L_f2b1
L_f2b1d:
)"
R"(mm v[0:3],a[32:35],a[0:3],v[0:3]
sa s79,s75,0x1c000
bl v214,s[44:47],s79 offen offset:3072 sc1 lds
dt a[96:97],v135 offset:16384
mm v[0:3],a[36:39],a[4:7],v[0:3]
s_waitcnt lgkmcnt(13)
dt a[98:99],v135 offset:20480
v_lshl_add_u32 v149, v147, 3, v149
mm v[16:19],a[32:35],a[8:11],v[16:19]
dr a[64:67],v129 offset:0
mm v[16:19],a[36:39],a[12:15],v[16:19]
sa m0,s77,0x24000
mm v[4:7],a[40:43],a[0:3],v[4:7]
sa s79,s76,0x70000
bl v238,s[40:43],s79 offen lds
dt a[100:101],v135 offset:24576
)"
R"(mm v[4:7],a[44:47],a[4:7],v[4:7]
dt a[102:103],v135 offset:28672
mm v[20:23],a[40:43],a[8:11],v[20:23]
s_lshr_b32 s56, s50, 1
dr a[68:71],v129 offset:8192
mm v[20:23],a[44:47],a[12:15],v[20:23]
s_waitcnt lgkmcnt(12)
sa s79,s76,0x70c00
bl v239,s[40:43],s79 offen offset:1024 lds
mm v[32:35],a[32:35],a[16:19],v[32:35]
dt a[104:105],v136 offset:16384
mm v[32:35],a[36:39],a[20:23],v[32:35]
dt a[106:107],v136 offset:20480
mm v[36:39],a[40:43],a[16:19],v[36:39]
sa s79,s76,0x71800
bl v238,s[40:43],s79 offen offset:2048 lds
)"
R"(dr a[72:75],v129 offset:1024
mm v[36:39],a[44:47],a[20:23],v[36:39]
s_lshl_b32 s56, s56, 6
mm v[8:11],a[48:51],a[0:3],v[8:11]
dt a[108:109],v136 offset:24576
mm v[8:11],a[52:55],a[4:7],v[8:11]
sa s79,s76,0x72400
bl v239,s[40:43],s79 offen offset:3072 lds
dt a[110:111],v136 offset:28672
mm v[24:27],a[48:51],a[8:11],v[24:27]
s_sub_u32 s72, s72, 0x40000
dr a[76:79],v129 offset:9216
mm v[24:27],a[52:55],a[12:15],v[24:27]
s_sub_u32 s73, s73, 0x40000
mm v[40:43],a[48:51],a[16:19],v[40:43]
s_sub_u32 s74, s74, 0x40000
)"
R"(dt a[112:113],v137 offset:16384
v_xor_b32 v149, s56, v149
mm v[40:43],a[52:55],a[20:23],v[40:43]
s_sub_u32 s75, s75, 0x40000
s_waitcnt lgkmcnt(13)
dt a[114:115],v137 offset:20480
mm v[48:51],a[32:35],a[24:27],v[48:51]
s_sub_u32 s76, s76, 0x100000
dr a[80:83],v129 offset:2048
mm v[48:51],a[36:39],a[28:31],v[48:51]
sa m0,s77,0x0
mm v[52:55],a[40:43],a[24:27],v[52:55]
dt a[116:117],v137 offset:24576
sa s79,s72,0x20000
bl v214,s[44:47],s79 offen sc1 lds
mm v[52:55],a[44:47],a[28:31],v[52:55]
)"
R"(dt a[118:119],v137 offset:28672
mm v[56:59],a[48:51],a[24:27],v[56:59]
dr a[84:87],v129 offset:10240
v_xor_b32 v153, 0, v149
mm v[56:59],a[52:55],a[28:31],v[56:59]
sa s79,s73,0x20000
bl v214,s[44:47],s79 offen offset:1024 sc1 lds
mm v[12:15],a[56:59],a[0:3],v[12:15]
dt a[120:121],v138 offset:16384
mm v[12:15],a[60:63],a[4:7],v[12:15]
dt a[122:123],v138 offset:20480
mm v[28:31],a[56:59],a[8:11],v[28:31]
dr a[88:91],v129 offset:3072
sa s79,s74,0x20000
bl v214,s[44:47],s79 offen offset:2048 sc1 lds
mm v[28:31],a[60:63],a[12:15],v[28:31]
)"
R"(sa s79,s75,0x20000
bl v214,s[44:47],s79 offen offset:3072 sc1 lds
mm v[44:47],a[56:59],a[16:19],v[44:47]
dt a[124:125],v138 offset:24576
v_xor_b32 v154, 32, v149
mm v[44:47],a[60:63],a[20:23],v[44:47]
sa m0,s77,0x4000
dt a[126:127],v138 offset:28672
mm v[60:63],a[56:59],a[24:27],v[60:63]
dr a[92:95],v129 offset:11264
mm v[60:63],a[60:63],a[28:31],v[60:63]
s_waitcnt vmcnt(12) lgkmcnt(15)
s_barrier
mm v[0:3],a[96:99],a[64:67],v[0:3]
dt a[32:33],v135 offset:49152
v_add_u32 v151, 0x10000, v150
)"
R"(mm v[0:3],a[100:103],a[68:71],v[0:3]
s_waitcnt lgkmcnt(13)
dt a[34:35],v135 offset:53248
sa s79,s76,0x80000
bl v238,s[40:43],s79 offen lds
mm v[16:19],a[96:99],a[72:75],v[16:19]
dr a[0:3],v129 offset:32768
mm v[16:19],a[100:103],a[76:79],v[16:19]
sa s79,s76,0x80c00
bl v239,s[40:43],s79 offen offset:1024 lds
mm v[4:7],a[104:107],a[64:67],v[4:7]
dt a[36:37],v135 offset:57344
mm v[4:7],a[108:111],a[68:71],v[4:7]
dt a[38:39],v135 offset:61440
mm v[20:23],a[104:107],a[72:75],v[20:23]
dr a[4:7],v129 offset:40960
)"
R"(v_add_u32 v155, 0x10000, v153
mm v[20:23],a[108:111],a[76:79],v[20:23]
s_waitcnt lgkmcnt(12)
mm v[32:35],a[96:99],a[80:83],v[32:35]
dt a[40:41],v136 offset:49152
sa s79,s76,0x81800
bl v238,s[40:43],s79 offen offset:2048 lds
mm v[32:35],a[100:103],a[84:87],v[32:35]
dt a[42:43],v136 offset:53248
mm v[36:39],a[104:107],a[80:83],v[36:39]
dr a[8:11],v129 offset:33792
mm v[36:39],a[108:111],a[84:87],v[36:39]
sa s79,s76,0x82400
bl v239,s[40:43],s79 offen offset:3072 lds
mm v[8:11],a[112:115],a[64:67],v[8:11]
sa m0,s77,0x8000
)"
R"(dt a[44:45],v136 offset:57344
v_add_u32 v156, 0x10000, v154
mm v[8:11],a[116:119],a[68:71],v[8:11]
dt a[46:47],v136 offset:61440
mm v[24:27],a[112:115],a[72:75],v[24:27]
dr a[12:15],v129 offset:41984
sa s79,s72,0x24000
bl v214,s[44:47],s79 offen sc1 lds
mm v[24:27],a[116:119],a[76:79],v[24:27]
sa s79,s73,0x24000
bl v214,s[44:47],s79 offen offset:1024 sc1 lds
mm v[40:43],a[112:115],a[80:83],v[40:43]
dt a[48:49],v137 offset:49152
v_add_u32 v152, 0x20000, v150
mm v[40:43],a[116:119],a[84:87],v[40:43]
s_waitcnt lgkmcnt(13)
)"
R"(dt a[50:51],v137 offset:53248
mm v[48:51],a[96:99],a[88:91],v[48:51]
dr a[16:19],v129 offset:34816
mm v[48:51],a[100:103],a[92:95],v[48:51]
sa s79,s74,0x24000
bl v214,s[44:47],s79 offen offset:2048 sc1 lds
mm v[52:55],a[104:107],a[88:91],v[52:55]
dt a[52:53],v137 offset:57344
mm v[52:55],a[108:111],a[92:95],v[52:55]
dt a[54:55],v137 offset:61440
v_add_u32 v157, 0x20000, v153
mm v[56:59],a[112:115],a[88:91],v[56:59]
dr a[20:23],v129 offset:43008
mm v[56:59],a[116:119],a[92:95],v[56:59]
sa s79,s75,0x24000
bl v214,s[44:47],s79 offen offset:3072 sc1 lds
)"
R"(mm v[12:15],a[120:123],a[64:67],v[12:15]
sa m0,s77,0xc000
dt a[56:57],v138 offset:49152
mm v[12:15],a[124:127],a[68:71],v[12:15]
dt a[58:59],v138 offset:53248
mm v[28:31],a[120:123],a[72:75],v[28:31]
dr a[24:27],v129 offset:35840
mm v[28:31],a[124:127],a[76:79],v[28:31]
sa s79,s76,0x90000
bl v238,s[40:43],s79 offen lds
mm v[44:47],a[120:123],a[80:83],v[44:47]
dt a[60:61],v138 offset:57344
v_add_u32 v158, 0x20000, v154
mm v[44:47],a[124:127],a[84:87],v[44:47]
dt a[62:63],v138 offset:61440
mm v[60:63],a[120:123],a[88:91],v[60:63]
)"
R"(dr a[28:31],v129 offset:44032
mm v[60:63],a[124:127],a[92:95],v[60:63]
s_waitcnt vmcnt(13) lgkmcnt(15)
s_barrier
sa s79,s76,0x90c00
bl v239,s[40:43],s79 offen offset:1024 lds
mm v[0:3],a[32:35],a[0:3],v[0:3]
dt a[96:97],v139 offset:16384
v_and_b32 v147, 15, v146
mm v[0:3],a[36:39],a[4:7],v[0:3]
s_waitcnt lgkmcnt(13)
dt a[98:99],v139 offset:20480
mm v[16:19],a[32:35],a[8:11],v[16:19]
dr a[64:67],v130 offset:0
sa s79,s76,0x91800
bl v238,s[40:43],s79 offen offset:2048 lds
)"
R"(mm v[16:19],a[36:39],a[12:15],v[16:19]
sa s79,s76,0x92400
bl v239,s[40:43],s79 offen offset:3072 lds
mm v[4:7],a[40:43],a[0:3],v[4:7]
dt a[100:101],v139 offset:24576
mm v[4:7],a[44:47],a[4:7],v[4:7]
sa m0,s77,0x10000
dt a[102:103],v139 offset:28672
v_lshlrev_b32 v160, 11, v147
mm v[20:23],a[40:43],a[8:11],v[20:23]
dr a[68:71],v130 offset:8192
mm v[20:23],a[44:47],a[12:15],v[20:23]
s_waitcnt lgkmcnt(12)
mm v[32:35],a[32:35],a[16:19],v[32:35]
dt a[104:105],v140 offset:16384
mm v[32:35],a[36:39],a[20:23],v[32:35]
)"
R"(dt a[106:107],v140 offset:20480
sa s79,s72,0x28000
bl v214,s[44:47],s79 offen sc1 lds
mm v[36:39],a[40:43],a[16:19],v[36:39]
dr a[72:75],v130 offset:1024
v_bfe_u32 v147, v146, 4, 1
mm v[36:39],a[44:47],a[20:23],v[36:39]
sa s79,s73,0x28000
bl v214,s[44:47],s79 offen offset:1024 sc1 lds
mm v[8:11],a[48:51],a[0:3],v[8:11]
dt a[108:109],v140 offset:24576
mm v[8:11],a[52:55],a[4:7],v[8:11]
dt a[110:111],v140 offset:28672
mm v[24:27],a[48:51],a[8:11],v[24:27]
dr a[76:79],v130 offset:9216
mm v[24:27],a[52:55],a[12:15],v[24:27]
)"
R"(sa s79,s74,0x28000
bl v214,s[44:47],s79 offen offset:2048 sc1 lds
mm v[40:43],a[48:51],a[16:19],v[40:43]
dt a[112:113],v141 offset:16384
v_lshl_add_u32 v160, v147, 5, v160
mm v[40:43],a[52:55],a[20:23],v[40:43]
s_waitcnt lgkmcnt(13)
dt a[114:115],v141 offset:20480
mm v[48:51],a[32:35],a[24:27],v[48:51]
dr a[80:83],v130 offset:2048
sa s79,s75,0x28000
bl v214,s[44:47],s79 offen offset:3072 sc1 lds
mm v[48:51],a[36:39],a[28:31],v[48:51]
sa m0,s77,0x14000
mm v[52:55],a[40:43],a[24:27],v[52:55]
dt a[116:117],v141 offset:24576
)"
R"(mm v[52:55],a[44:47],a[28:31],v[52:55]
dt a[118:119],v141 offset:28672
v_lshrrev_b32 v147, 5, v146
mm v[56:59],a[48:51],a[24:27],v[56:59]
dr a[84:87],v130 offset:10240
sa s79,s76,0xa0000
bl v238,s[40:43],s79 offen lds
mm v[56:59],a[52:55],a[28:31],v[56:59]
sa s79,s76,0xa0c00
bl v239,s[40:43],s79 offen offset:1024 lds
mm v[12:15],a[56:59],a[0:3],v[12:15]
dt a[120:121],v142 offset:16384
mm v[12:15],a[60:63],a[4:7],v[12:15]
dt a[122:123],v142 offset:20480
mm v[28:31],a[56:59],a[8:11],v[28:31]
dr a[88:91],v130 offset:3072
)"
R"(v_lshl_add_u32 v160, v147, 4, v160
mm v[28:31],a[60:63],a[12:15],v[28:31]
sa s79,s76,0xa1800
bl v238,s[40:43],s79 offen offset:2048 lds
mm v[44:47],a[56:59],a[16:19],v[44:47]
dt a[124:125],v142 offset:24576
mm v[44:47],a[60:63],a[20:23],v[44:47]
dt a[126:127],v142 offset:28672
mm v[60:63],a[56:59],a[24:27],v[60:63]
dr a[92:95],v130 offset:11264
mm v[60:63],a[60:63],a[28:31],v[60:63]
s_waitcnt vmcnt(15) lgkmcnt(15)
s_barrier
mm v[0:3],a[96:99],a[64:67],v[0:3]
dt a[32:33],v131 offset:16384
sa s79,s76,0xa2400
)"
R"(bl v239,s[40:43],s79 offen offset:3072 lds
mm v[0:3],a[100:103],a[68:71],v[0:3]
s_waitcnt lgkmcnt(13)
sa m0,s77,0x18000
dt a[34:35],v131 offset:20480
mm v[16:19],a[96:99],a[72:75],v[16:19]
dr a[0:3],v128 offset:0
mm v[16:19],a[100:103],a[76:79],v[16:19]
sa s79,s72,0x2c000
bl v214,s[44:47],s79 offen sc1 lds
mm v[4:7],a[104:107],a[64:67],v[4:7]
dt a[36:37],v131 offset:24576
mm v[4:7],a[108:111],a[68:71],v[4:7]
dt a[38:39],v131 offset:28672
mm v[20:23],a[104:107],a[72:75],v[20:23]
dr a[4:7],v128 offset:8192
)"
R"(mm v[20:23],a[108:111],a[76:79],v[20:23]
s_waitcnt lgkmcnt(12)
sa s79,s73,0x2c000
bl v214,s[44:47],s79 offen offset:1024 sc1 lds
mm v[32:35],a[96:99],a[80:83],v[32:35]
dt a[40:41],v132 offset:16384
mm v[32:35],a[100:103],a[84:87],v[32:35]
dt a[42:43],v132 offset:20480
mm v[36:39],a[104:107],a[80:83],v[36:39]
dr a[8:11],v128 offset:1024
mm v[36:39],a[108:111],a[84:87],v[36:39]
sa s79,s74,0x2c000
bl v214,s[44:47],s79 offen offset:2048 sc1 lds
mm v[8:11],a[112:115],a[64:67],v[8:11]
dt a[44:45],v132 offset:24576
mm v[8:11],a[116:119],a[68:71],v[8:11]
)"
R"(dt a[46:47],v132 offset:28672
mm v[24:27],a[112:115],a[72:75],v[24:27]
dr a[12:15],v128 offset:9216
mm v[24:27],a[116:119],a[76:79],v[24:27]
sa s79,s75,0x2c000
bl v214,s[44:47],s79 offen offset:3072 sc1 lds
mm v[40:43],a[112:115],a[80:83],v[40:43]
sa m0,s77,0x1c000
dt a[48:49],v133 offset:16384
mm v[40:43],a[116:119],a[84:87],v[40:43]
s_waitcnt lgkmcnt(13)
dt a[50:51],v133 offset:20480
mm v[48:51],a[96:99],a[88:91],v[48:51]
dr a[16:19],v128 offset:2048
mm v[48:51],a[100:103],a[92:95],v[48:51]
sa s79,s76,0xb0000
)"
R"(bl v238,s[40:43],s79 offen lds
mm v[52:55],a[104:107],a[88:91],v[52:55]
dt a[52:53],v133 offset:24576
mm v[52:55],a[108:111],a[92:95],v[52:55]
dt a[54:55],v133 offset:28672
mm v[56:59],a[112:115],a[88:91],v[56:59]
dr a[20:23],v128 offset:10240
mm v[56:59],a[116:119],a[92:95],v[56:59]
sa s79,s76,0xb0c00
bl v239,s[40:43],s79 offen offset:1024 lds
mm v[12:15],a[120:123],a[64:67],v[12:15]
dt a[56:57],v134 offset:16384
mm v[12:15],a[124:127],a[68:71],v[12:15]
dt a[58:59],v134 offset:20480
mm v[28:31],a[120:123],a[72:75],v[28:31]
dr a[24:27],v128 offset:3072
)"
R"(mm v[28:31],a[124:127],a[76:79],v[28:31]
sa s79,s76,0xb1800
bl v238,s[40:43],s79 offen offset:2048 lds
mm v[44:47],a[120:123],a[80:83],v[44:47]
dt a[60:61],v134 offset:24576
mm v[44:47],a[124:127],a[84:87],v[44:47]
dt a[62:63],v134 offset:28672
mm v[60:63],a[120:123],a[88:91],v[60:63]
dr a[28:31],v128 offset:11264
mm v[60:63],a[124:127],a[92:95],v[60:63]
s_waitcnt vmcnt(15) lgkmcnt(15)
s_barrier
sa s79,s76,0xb2400
bl v239,s[40:43],s79 offen offset:3072 lds
mm v[0:3],a[32:35],a[0:3],v[0:3]
sa m0,s77,0x20000
)"
R"(dt a[96:97],v131 offset:49152
mm v[0:3],a[36:39],a[4:7],v[0:3]
s_waitcnt lgkmcnt(13)
dt a[98:99],v131 offset:53248
mm v[16:19],a[32:35],a[8:11],v[16:19]
dr a[64:67],v128 offset:32768
mm v[16:19],a[36:39],a[12:15],v[16:19]
sa s79,s72,0x30000
bl v214,s[44:47],s79 offen sc1 lds
mm v[4:7],a[40:43],a[0:3],v[4:7]
dt a[100:101],v131 offset:57344
mm v[4:7],a[44:47],a[4:7],v[4:7]
dt a[102:103],v131 offset:61440
mm v[20:23],a[40:43],a[8:11],v[20:23]
dr a[68:71],v128 offset:40960
mm v[20:23],a[44:47],a[12:15],v[20:23]
)"
R"(s_waitcnt lgkmcnt(12)
sa s79,s73,0x30000
bl v214,s[44:47],s79 offen offset:1024 sc1 lds
mm v[32:35],a[32:35],a[16:19],v[32:35]
dt a[104:105],v132 offset:49152
mm v[32:35],a[36:39],a[20:23],v[32:35]
dt a[106:107],v132 offset:53248
mm v[36:39],a[40:43],a[16:19],v[36:39]
dr a[72:75],v128 offset:33792
mm v[36:39],a[44:47],a[20:23],v[36:39]
sa s79,s74,0x30000
bl v214,s[44:47],s79 offen offset:2048 sc1 lds
mm v[8:11],a[48:51],a[0:3],v[8:11]
dt a[108:109],v132 offset:57344
mm v[8:11],a[52:55],a[4:7],v[8:11]
dt a[110:111],v132 offset:61440
)"
R"(mm v[24:27],a[48:51],a[8:11],v[24:27]
dr a[76:79],v128 offset:41984
mm v[24:27],a[52:55],a[12:15],v[24:27]
sa s79,s75,0x30000
bl v214,s[44:47],s79 offen offset:3072 sc1 lds
mm v[40:43],a[48:51],a[16:19],v[40:43]
sa m0,s77,0x24000
dt a[112:113],v133 offset:49152
mm v[40:43],a[52:55],a[20:23],v[40:43]
s_waitcnt lgkmcnt(13)
dt a[114:115],v133 offset:53248
mm v[48:51],a[32:35],a[24:27],v[48:51]
dr a[80:83],v128 offset:34816
mm v[48:51],a[36:39],a[28:31],v[48:51]
sa s79,s76,0xc0000
bl v238,s[40:43],s79 offen lds
)"
R"(mm v[52:55],a[40:43],a[24:27],v[52:55]
dt a[116:117],v133 offset:57344
mm v[52:55],a[44:47],a[28:31],v[52:55]
dt a[118:119],v133 offset:61440
mm v[56:59],a[48:51],a[24:27],v[56:59]
dr a[84:87],v128 offset:43008
mm v[56:59],a[52:55],a[28:31],v[56:59]
sa s79,s76,0xc0c00
bl v239,s[40:43],s79 offen offset:1024 lds
mm v[12:15],a[56:59],a[0:3],v[12:15]
dt a[120:121],v134 offset:49152
mm v[12:15],a[60:63],a[4:7],v[12:15]
dt a[122:123],v134 offset:53248
mm v[28:31],a[56:59],a[8:11],v[28:31]
dr a[88:91],v128 offset:35840
mm v[28:31],a[60:63],a[12:15],v[28:31]
)"
R"(sa s79,s76,0xc1800
bl v238,s[40:43],s79 offen offset:2048 lds
mm v[44:47],a[56:59],a[16:19],v[44:47]
dt a[124:125],v134 offset:57344
mm v[44:47],a[60:63],a[20:23],v[44:47]
dt a[126:127],v134 offset:61440
mm v[60:63],a[56:59],a[24:27],v[60:63]
dr a[92:95],v128 offset:44032
mm v[60:63],a[60:63],a[28:31],v[60:63]
s_waitcnt vmcnt(15) lgkmcnt(15)
s_barrier
sa s79,s76,0xc2400
bl v239,s[40:43],s79 offen offset:3072 lds
mm v[0:3],a[96:99],a[64:67],v[0:3]
dt a[32:33],v135 offset:16384
mm v[0:3],a[100:103],a[68:71],v[0:3]
)"
R"(s_waitcnt lgkmcnt(13)
dt a[34:35],v135 offset:20480
mm v[16:19],a[96:99],a[72:75],v[16:19]
dr a[0:3],v129 offset:0
mm v[16:19],a[100:103],a[76:79],v[16:19]
buffer_load_dword v251, v244, s[44:47], 0 offen sc1
sa m0,s77,0x0
sa s79,s72,0x34000
bl v214,s[44:47],s79 offen sc1 lds
mm v[4:7],a[104:107],a[64:67],v[4:7]
dt a[36:37],v135 offset:24576
mm v[4:7],a[108:111],a[68:71],v[4:7]
dt a[38:39],v135 offset:28672
mm v[20:23],a[104:107],a[72:75],v[20:23]
dr a[4:7],v129 offset:8192
mm v[20:23],a[108:111],a[76:79],v[20:23]
)"
R"(s_waitcnt lgkmcnt(12)
sa s79,s73,0x34000
bl v214,s[44:47],s79 offen offset:1024 sc1 lds
mm v[32:35],a[96:99],a[80:83],v[32:35]
dt a[40:41],v136 offset:16384
mm v[32:35],a[100:103],a[84:87],v[32:35]
dt a[42:43],v136 offset:20480
mm v[36:39],a[104:107],a[80:83],v[36:39]
dr a[8:11],v129 offset:1024
mm v[36:39],a[108:111],a[84:87],v[36:39]
sa s79,s74,0x34000
bl v214,s[44:47],s79 offen offset:2048 sc1 lds
mm v[8:11],a[112:115],a[64:67],v[8:11]
dt a[44:45],v136 offset:24576
mm v[8:11],a[116:119],a[68:71],v[8:11]
dt a[46:47],v136 offset:28672
)"
R"(mm v[24:27],a[112:115],a[72:75],v[24:27]
dr a[12:15],v129 offset:9216
mm v[24:27],a[116:119],a[76:79],v[24:27]
sa s79,s75,0x34000
bl v214,s[44:47],s79 offen offset:3072 sc1 lds
mm v[40:43],a[112:115],a[80:83],v[40:43]
sa m0,s77,0x4000
dt a[48:49],v137 offset:16384
mm v[40:43],a[116:119],a[84:87],v[40:43]
s_waitcnt lgkmcnt(13)
dt a[50:51],v137 offset:20480
mm v[48:51],a[96:99],a[88:91],v[48:51]
dr a[16:19],v129 offset:2048
mm v[48:51],a[100:103],a[92:95],v[48:51]
sa s79,s76,0xd0000
bl v238,s[40:43],s79 offen lds
)"
R"(mm v[52:55],a[104:107],a[88:91],v[52:55]
dt a[52:53],v137 offset:24576
mm v[52:55],a[108:111],a[92:95],v[52:55]
dt a[54:55],v137 offset:28672
mm v[56:59],a[112:115],a[88:91],v[56:59]
dr a[20:23],v129 offset:10240
mm v[56:59],a[116:119],a[92:95],v[56:59]
sa s79,s76,0xd0c00
bl v239,s[40:43],s79 offen offset:1024 lds
mm v[12:15],a[120:123],a[64:67],v[12:15]
dt a[56:57],v138 offset:16384
mm v[12:15],a[124:127],a[68:71],v[12:15]
dt a[58:59],v138 offset:20480
mm v[28:31],a[120:123],a[72:75],v[28:31]
dr a[24:27],v129 offset:3072
mm v[28:31],a[124:127],a[76:79],v[28:31]
)"
R"(sa s79,s76,0xd1800
bl v238,s[40:43],s79 offen offset:2048 lds
mm v[44:47],a[120:123],a[80:83],v[44:47]
dt a[60:61],v138 offset:24576
mm v[44:47],a[124:127],a[84:87],v[44:47]
dt a[62:63],v138 offset:28672
mm v[60:63],a[120:123],a[88:91],v[60:63]
dr a[28:31],v129 offset:11264
mm v[60:63],a[124:127],a[92:95],v[60:63]
s_waitcnt vmcnt(16) lgkmcnt(15)
s_barrier
sa s79,s76,0xd2400
bl v239,s[40:43],s79 offen offset:3072 lds
mm v[0:3],a[32:35],a[0:3],v[0:3]
sa m0,s77,0x8000
dt a[96:97],v135 offset:49152
)"
R"(mm v[0:3],a[36:39],a[4:7],v[0:3]
s_waitcnt lgkmcnt(13)
dt a[98:99],v135 offset:53248
mm v[16:19],a[32:35],a[8:11],v[16:19]
dr a[64:67],v129 offset:32768
mm v[16:19],a[36:39],a[12:15],v[16:19]
sa s79,s72,0x38000
bl v214,s[44:47],s79 offen sc1 lds
mm v[4:7],a[40:43],a[0:3],v[4:7]
dt a[100:101],v135 offset:57344
mm v[4:7],a[44:47],a[4:7],v[4:7]
dt a[102:103],v135 offset:61440
mm v[20:23],a[40:43],a[8:11],v[20:23]
dr a[68:71],v129 offset:40960
mm v[20:23],a[44:47],a[12:15],v[20:23]
s_waitcnt lgkmcnt(12)
)"
R"(sa s79,s73,0x38000
bl v214,s[44:47],s79 offen offset:1024 sc1 lds
mm v[32:35],a[32:35],a[16:19],v[32:35]
dt a[104:105],v136 offset:49152
mm v[32:35],a[36:39],a[20:23],v[32:35]
dt a[106:107],v136 offset:53248
mm v[36:39],a[40:43],a[16:19],v[36:39]
dr a[72:75],v129 offset:33792
mm v[36:39],a[44:47],a[20:23],v[36:39]
sa s79,s74,0x38000
bl v214,s[44:47],s79 offen offset:2048 sc1 lds
mm v[8:11],a[48:51],a[0:3],v[8:11]
dt a[108:109],v136 offset:57344
mm v[8:11],a[52:55],a[4:7],v[8:11]
dt a[110:111],v136 offset:61440
mm v[24:27],a[48:51],a[8:11],v[24:27]
)"
R"(dr a[76:79],v129 offset:41984
mm v[24:27],a[52:55],a[12:15],v[24:27]
sa s79,s75,0x38000
bl v214,s[44:47],s79 offen offset:3072 sc1 lds
mm v[40:43],a[48:51],a[16:19],v[40:43]
sa m0,s77,0xc000
dt a[112:113],v137 offset:49152
mm v[40:43],a[52:55],a[20:23],v[40:43]
s_waitcnt lgkmcnt(13)
dt a[114:115],v137 offset:53248
mm v[48:51],a[32:35],a[24:27],v[48:51]
dr a[80:83],v129 offset:34816
mm v[48:51],a[36:39],a[28:31],v[48:51]
sa s79,s76,0xe0000
bl v238,s[40:43],s79 offen lds
mm v[52:55],a[40:43],a[24:27],v[52:55]
)"
R"(dt a[116:117],v137 offset:57344
mm v[52:55],a[44:47],a[28:31],v[52:55]
dt a[118:119],v137 offset:61440
mm v[56:59],a[48:51],a[24:27],v[56:59]
dr a[84:87],v129 offset:43008
mm v[56:59],a[52:55],a[28:31],v[56:59]
sa s79,s76,0xe0c00
bl v239,s[40:43],s79 offen offset:1024 lds
mm v[12:15],a[56:59],a[0:3],v[12:15]
dt a[120:121],v138 offset:49152
mm v[12:15],a[60:63],a[4:7],v[12:15]
dt a[122:123],v138 offset:53248
mm v[28:31],a[56:59],a[8:11],v[28:31]
dr a[88:91],v129 offset:35840
mm v[28:31],a[60:63],a[12:15],v[28:31]
sa s79,s76,0xe1800
)"
R"(bl v238,s[40:43],s79 offen offset:2048 lds
mm v[44:47],a[56:59],a[16:19],v[44:47]
dt a[124:125],v138 offset:57344
mm v[44:47],a[60:63],a[20:23],v[44:47]
dt a[126:127],v138 offset:61440
mm v[60:63],a[56:59],a[24:27],v[60:63]
dr a[92:95],v129 offset:44032
mm v[60:63],a[60:63],a[28:31],v[60:63]
s_waitcnt vmcnt(16) lgkmcnt(15)
s_barrier
sa s79,s76,0xe2400
bl v239,s[40:43],s79 offen offset:3072 lds
mm v[0:3],a[96:99],a[64:67],v[0:3]
sa m0,s77,0x10000
dt a[32:33],v139 offset:16384
mm v[0:3],a[100:103],a[68:71],v[0:3]
)"
R"(s_waitcnt lgkmcnt(13)
dt a[34:35],v139 offset:20480
mm v[16:19],a[96:99],a[72:75],v[16:19]
dr a[0:3],v130 offset:0
mm v[16:19],a[100:103],a[76:79],v[16:19]
sa s79,s72,0x3c000
bl v214,s[44:47],s79 offen sc1 lds
mm v[4:7],a[104:107],a[64:67],v[4:7]
dt a[36:37],v139 offset:24576
mm v[4:7],a[108:111],a[68:71],v[4:7]
dt a[38:39],v139 offset:28672
mm v[20:23],a[104:107],a[72:75],v[20:23]
dr a[4:7],v130 offset:8192
mm v[20:23],a[108:111],a[76:79],v[20:23]
s_waitcnt lgkmcnt(12)
sa s79,s73,0x3c000
)"
R"(bl v214,s[44:47],s79 offen offset:1024 sc1 lds
mm v[32:35],a[96:99],a[80:83],v[32:35]
dt a[40:41],v140 offset:16384
mm v[32:35],a[100:103],a[84:87],v[32:35]
dt a[42:43],v140 offset:20480
mm v[36:39],a[104:107],a[80:83],v[36:39]
dr a[8:11],v130 offset:1024
mm v[36:39],a[108:111],a[84:87],v[36:39]
sa s79,s74,0x3c000
bl v214,s[44:47],s79 offen offset:2048 sc1 lds
mm v[8:11],a[112:115],a[64:67],v[8:11]
dt a[44:45],v140 offset:24576
mm v[8:11],a[116:119],a[68:71],v[8:11]
dt a[46:47],v140 offset:28672
mm v[24:27],a[112:115],a[72:75],v[24:27]
dr a[12:15],v130 offset:9216
)"
R"(mm v[24:27],a[116:119],a[76:79],v[24:27]
sa s79,s75,0x3c000
bl v214,s[44:47],s79 offen offset:3072 sc1 lds
mm v[40:43],a[112:115],a[80:83],v[40:43]
sa m0,s77,0x14000
dt a[48:49],v141 offset:16384
mm v[40:43],a[116:119],a[84:87],v[40:43]
s_waitcnt lgkmcnt(13)
dt a[50:51],v141 offset:20480
mm v[48:51],a[96:99],a[88:91],v[48:51]
dr a[16:19],v130 offset:2048
mm v[48:51],a[100:103],a[92:95],v[48:51]
sa s79,s76,0xf0000
bl v238,s[40:43],s79 offen lds
mm v[52:55],a[104:107],a[88:91],v[52:55]
dt a[52:53],v141 offset:24576
)"
R"(mm v[52:55],a[108:111],a[92:95],v[52:55]
dt a[54:55],v141 offset:28672
mm v[56:59],a[112:115],a[88:91],v[56:59]
dr a[20:23],v130 offset:10240
mm v[56:59],a[116:119],a[92:95],v[56:59]
sa s79,s76,0xf0c00
bl v239,s[40:43],s79 offen offset:1024 lds
mm v[12:15],a[120:123],a[64:67],v[12:15]
dt a[56:57],v142 offset:16384
mm v[12:15],a[124:127],a[68:71],v[12:15]
dt a[58:59],v142 offset:20480
mm v[28:31],a[120:123],a[72:75],v[28:31]
dr a[24:27],v130 offset:3072
mm v[28:31],a[124:127],a[76:79],v[28:31]
sa s79,s76,0xf1800
bl v238,s[40:43],s79 offen offset:2048 lds
)"
R"(mm v[44:47],a[120:123],a[80:83],v[44:47]
dt a[60:61],v142 offset:24576
mm v[44:47],a[124:127],a[84:87],v[44:47]
dt a[62:63],v142 offset:28672
mm v[60:63],a[120:123],a[88:91],v[60:63]
dr a[28:31],v130 offset:11264
mm v[60:63],a[124:127],a[92:95],v[60:63]
s_waitcnt vmcnt(15) lgkmcnt(15)
s_barrier
v_cmp_eq_u32 vcc, 1, v251
s_nop 1
s_cmp_eq_u64 vcc, exec
s_cbranch_scc0 L_fp1
L_fp1d:
sa s79,s76,0xf2400
bl v239,s[40:43],s79 offen offset:3072 lds
)"
R"(mm v[0:3],a[32:35],a[0:3],v[0:3]
sa m0,s77,0x18000
dt a[96:97],v131 offset:16384
mm v[0:3],a[36:39],a[4:7],v[0:3]
s_waitcnt lgkmcnt(13)
dt a[98:99],v131 offset:20480
mm v[16:19],a[32:35],a[8:11],v[16:19]
dr a[64:67],v128 offset:0
mm v[16:19],a[36:39],a[12:15],v[16:19]
sa s79,s70,0x0
bl v214,s[44:47],s79 offen sc1 lds
mm v[4:7],a[40:43],a[0:3],v[4:7]
dt a[100:101],v131 offset:24576
mm v[4:7],a[44:47],a[4:7],v[4:7]
dt a[102:103],v131 offset:28672
mm v[20:23],a[40:43],a[8:11],v[20:23]
)"
R"(dr a[68:71],v128 offset:8192
mm v[20:23],a[44:47],a[12:15],v[20:23]
s_waitcnt lgkmcnt(12)
sa s79,s70,0x0
bl v214,s[44:47],s79 offen offset:1024 sc1 lds
mm v[32:35],a[32:35],a[16:19],v[32:35]
dt a[104:105],v132 offset:16384
mm v[32:35],a[36:39],a[20:23],v[32:35]
dt a[106:107],v132 offset:20480
mm v[36:39],a[40:43],a[16:19],v[36:39]
dr a[72:75],v128 offset:1024
mm v[36:39],a[44:47],a[20:23],v[36:39]
sa s79,s70,0x0
bl v214,s[44:47],s79 offen offset:2048 sc1 lds
mm v[8:11],a[48:51],a[0:3],v[8:11]
dt a[108:109],v132 offset:24576
)"
R"(mm v[8:11],a[52:55],a[4:7],v[8:11]
dt a[110:111],v132 offset:28672
mm v[24:27],a[48:51],a[8:11],v[24:27]
dr a[76:79],v128 offset:9216
mm v[24:27],a[52:55],a[12:15],v[24:27]
sa s79,s70,0x0
bl v214,s[44:47],s79 offen offset:3072 sc1 lds
mm v[40:43],a[48:51],a[16:19],v[40:43]
sa m0,s77,0x1c000
dt a[112:113],v133 offset:16384
mm v[40:43],a[52:55],a[20:23],v[40:43]
s_waitcnt lgkmcnt(13)
dt a[114:115],v133 offset:20480
mm v[48:51],a[32:35],a[24:27],v[48:51]
dr a[80:83],v128 offset:2048
mm v[48:51],a[36:39],a[28:31],v[48:51]
)"
R"(sa s79,s78,0x0
bl v159,s[36:39],s79 offen lds
mm v[52:55],a[40:43],a[24:27],v[52:55]
dt a[116:117],v133 offset:24576
mm v[52:55],a[44:47],a[28:31],v[52:55]
dt a[118:119],v133 offset:28672
mm v[56:59],a[48:51],a[24:27],v[56:59]
dr a[84:87],v128 offset:10240
mm v[56:59],a[52:55],a[28:31],v[56:59]
sa s79,s78,0x1c00
bl v159,s[36:39],s79 offen offset:1024 lds
mm v[12:15],a[56:59],a[0:3],v[12:15]
dt a[120:121],v134 offset:16384
mm v[12:15],a[60:63],a[4:7],v[12:15]
dt a[122:123],v134 offset:20480
mm v[28:31],a[56:59],a[8:11],v[28:31]
)"
R"(dr a[88:91],v128 offset:3072
mm v[28:31],a[60:63],a[12:15],v[28:31]
sa s79,s78,0x3800
bl v159,s[36:39],s79 offen offset:2048 lds
mm v[44:47],a[56:59],a[16:19],v[44:47]
dt a[124:125],v134 offset:24576
mm v[44:47],a[60:63],a[20:23],v[44:47]
dt a[126:127],v134 offset:28672
mm v[60:63],a[56:59],a[24:27],v[60:63]
dr a[92:95],v128 offset:11264
mm v[60:63],a[60:63],a[28:31],v[60:63]
s_waitcnt vmcnt(15) lgkmcnt(15)
s_barrier
sa s79,s78,0x5400
bl v159,s[36:39],s79 offen offset:3072 lds
mm v[0:3],a[96:99],a[64:67],v[0:3]
)"
R"(sa m0,s77,0x20000
s_lshl_b32 s72, s65, 2
dt a[32:33],v131 offset:49152
mm v[0:3],a[100:103],a[68:71],v[0:3]
s_waitcnt lgkmcnt(13)
dt a[34:35],v131 offset:53248
mm v[16:19],a[96:99],a[72:75],v[16:19]
dr a[0:3],v128 offset:32768
mm v[16:19],a[100:103],a[76:79],v[16:19]
sa s79,s70,0x1000
bl v214,s[44:47],s79 offen sc1 lds
mm v[4:7],a[104:107],a[64:67],v[4:7]
dt a[36:37],v131 offset:57344
mm v[4:7],a[108:111],a[68:71],v[4:7]
dt a[38:39],v131 offset:61440
mm v[20:23],a[104:107],a[72:75],v[20:23]
)"
R"(dr a[4:7],v128 offset:40960
mm v[20:23],a[108:111],a[76:79],v[20:23]
s_waitcnt lgkmcnt(12)
sa s79,s70,0x1000
bl v214,s[44:47],s79 offen offset:1024 sc1 lds
mm v[32:35],a[96:99],a[80:83],v[32:35]
dt a[40:41],v132 offset:49152
mm v[32:35],a[100:103],a[84:87],v[32:35]
dt a[42:43],v132 offset:53248
mm v[36:39],a[104:107],a[80:83],v[36:39]
sa s72,s72,s66
dr a[8:11],v128 offset:33792
mm v[36:39],a[108:111],a[84:87],v[36:39]
sa s79,s70,0x1000
bl v214,s[44:47],s79 offen offset:2048 sc1 lds
mm v[8:11],a[112:115],a[64:67],v[8:11]
)"
R"(dt a[44:45],v132 offset:57344
mm v[8:11],a[116:119],a[68:71],v[8:11]
dt a[46:47],v132 offset:61440
mm v[24:27],a[112:115],a[72:75],v[24:27]
dr a[12:15],v128 offset:41984
mm v[24:27],a[116:119],a[76:79],v[24:27]
sa s79,s70,0x1000
bl v214,s[44:47],s79 offen offset:3072 sc1 lds
mm v[40:43],a[112:115],a[80:83],v[40:43]
sa m0,s77,0x24000
dt a[48:49],v133 offset:49152
mm v[40:43],a[116:119],a[84:87],v[40:43]
s_waitcnt lgkmcnt(13)
dt a[50:51],v133 offset:53248
mm v[48:51],a[96:99],a[88:91],v[48:51]
dr a[16:19],v128 offset:34816
)"
R"(mm v[48:51],a[100:103],a[92:95],v[48:51]
sa s79,s78,0x20000
bl v159,s[36:39],s79 offen lds
mm v[52:55],a[104:107],a[88:91],v[52:55]
dt a[52:53],v133 offset:57344
mm v[52:55],a[108:111],a[92:95],v[52:55]
s_lshl_b32 s72, s72, 17
dt a[54:55],v133 offset:61440
mm v[56:59],a[112:115],a[88:91],v[56:59]
dr a[20:23],v128 offset:43008
mm v[56:59],a[116:119],a[92:95],v[56:59]
sa s79,s78,0x21c00
bl v159,s[36:39],s79 offen offset:1024 lds
mm v[12:15],a[120:123],a[64:67],v[12:15]
dt a[56:57],v134 offset:49152
mm v[12:15],a[124:127],a[68:71],v[12:15]
)"
R"(dt a[58:59],v134 offset:53248
mm v[28:31],a[120:123],a[72:75],v[28:31]
dr a[24:27],v128 offset:35840
mm v[28:31],a[124:127],a[76:79],v[28:31]
sa s79,s78,0x23800
bl v159,s[36:39],s79 offen offset:2048 lds
mm v[44:47],a[120:123],a[80:83],v[44:47]
dt a[60:61],v134 offset:57344
mm v[44:47],a[124:127],a[84:87],v[44:47]
dt a[62:63],v134 offset:61440
mm v[60:63],a[120:123],a[88:91],v[60:63]
dr a[28:31],v128 offset:44032
mm v[60:63],a[124:127],a[92:95],v[60:63]
s_waitcnt vmcnt(15) lgkmcnt(15)
s_barrier
sa s79,s78,0x25400
)"
R"(bl v159,s[36:39],s79 offen offset:3072 lds
mm v[0:3],a[32:35],a[0:3],v[0:3]
sa m0,s77,0x0
s_lshl_b32 s80, s50, 13
dt a[96:97],v135 offset:16384
mm v[0:3],a[36:39],a[4:7],v[0:3]
s_waitcnt lgkmcnt(13)
dt a[98:99],v135 offset:20480
mm v[16:19],a[32:35],a[8:11],v[16:19]
dr a[64:67],v129 offset:0
mm v[16:19],a[36:39],a[12:15],v[16:19]
sa s79,s70,0x2000
bl v214,s[44:47],s79 offen sc1 lds
mm v[4:7],a[40:43],a[0:3],v[4:7]
dt a[100:101],v135 offset:24576
mm v[4:7],a[44:47],a[4:7],v[4:7]
)"
R"(dt a[102:103],v135 offset:28672
mm v[20:23],a[40:43],a[8:11],v[20:23]
dr a[68:71],v129 offset:8192
mm v[20:23],a[44:47],a[12:15],v[20:23]
s_waitcnt lgkmcnt(12)
sa s79,s70,0x2000
bl v214,s[44:47],s79 offen offset:1024 sc1 lds
mm v[32:35],a[32:35],a[16:19],v[32:35]
dt a[104:105],v136 offset:16384
mm v[32:35],a[36:39],a[20:23],v[32:35]
dt a[106:107],v136 offset:20480
mm v[36:39],a[40:43],a[16:19],v[36:39]
sa s72,s72,s80
dr a[72:75],v129 offset:1024
mm v[36:39],a[44:47],a[20:23],v[36:39]
sa s79,s70,0x2000
)"
R"(bl v214,s[44:47],s79 offen offset:2048 sc1 lds
mm v[8:11],a[48:51],a[0:3],v[8:11]
dt a[108:109],v136 offset:24576
mm v[8:11],a[52:55],a[4:7],v[8:11]
dt a[110:111],v136 offset:28672
mm v[24:27],a[48:51],a[8:11],v[24:27]
dr a[76:79],v129 offset:9216
mm v[24:27],a[52:55],a[12:15],v[24:27]
sa s79,s70,0x2000
bl v214,s[44:47],s79 offen offset:3072 sc1 lds
mm v[40:43],a[48:51],a[16:19],v[40:43]
sa m0,s77,0x4000
dt a[112:113],v137 offset:16384
mm v[40:43],a[52:55],a[20:23],v[40:43]
s_waitcnt lgkmcnt(13)
dt a[114:115],v137 offset:20480
)"
R"(mm v[48:51],a[32:35],a[24:27],v[48:51]
dr a[80:83],v129 offset:2048
mm v[48:51],a[36:39],a[28:31],v[48:51]
sa s79,s78,0x40000
bl v159,s[36:39],s79 offen lds
mm v[52:55],a[40:43],a[24:27],v[52:55]
dt a[116:117],v137 offset:24576
mm v[52:55],a[44:47],a[28:31],v[52:55]
sa s72,s72,0x3400000
dt a[118:119],v137 offset:28672
mm v[56:59],a[48:51],a[24:27],v[56:59]
dr a[84:87],v129 offset:10240
mm v[56:59],a[52:55],a[28:31],v[56:59]
sa s79,s78,0x41c00
bl v159,s[36:39],s79 offen offset:1024 lds
mm v[12:15],a[56:59],a[0:3],v[12:15]
)"
R"(dt a[120:121],v138 offset:16384
mm v[12:15],a[60:63],a[4:7],v[12:15]
dt a[122:123],v138 offset:20480
mm v[28:31],a[56:59],a[8:11],v[28:31]
dr a[88:91],v129 offset:3072
mm v[28:31],a[60:63],a[12:15],v[28:31]
sa s79,s78,0x43800
bl v159,s[36:39],s79 offen offset:2048 lds
mm v[44:47],a[56:59],a[16:19],v[44:47]
dt a[124:125],v138 offset:24576
mm v[44:47],a[60:63],a[20:23],v[44:47]
dt a[126:127],v138 offset:28672
mm v[60:63],a[56:59],a[24:27],v[60:63]
dr a[92:95],v129 offset:11264
mm v[60:63],a[60:63],a[28:31],v[60:63]
s_waitcnt lgkmcnt(15)
)"
R"(s_barrier
sa s79,s78,0x45400
bl v159,s[36:39],s79 offen offset:3072 lds
mm v[0:3],a[96:99],a[64:67],v[0:3]
sa m0,s77,0x8000
mm v[0:3],a[100:103],a[68:71],v[0:3]
s_waitcnt lgkmcnt(12)
mm v[16:19],a[96:99],a[72:75],v[16:19]
sa s79,s70,0x3000
bl v214,s[44:47],s79 offen sc1 lds
mm v[16:19],a[100:103],a[76:79],v[16:19]
mm v[4:7],a[104:107],a[64:67],v[4:7]
sa s79,s70,0x3000
bl v214,s[44:47],s79 offen offset:1024 sc1 lds
mm v[4:7],a[108:111],a[68:71],v[4:7]
mm v[20:23],a[104:107],a[72:75],v[20:23]
)"
R"(mm v[20:23],a[108:111],a[76:79],v[20:23]
s_waitcnt lgkmcnt(6)
mm v[32:35],a[96:99],a[80:83],v[32:35]
sa s79,s70,0x3000
bl v214,s[44:47],s79 offen offset:2048 sc1 lds
mm v[32:35],a[100:103],a[84:87],v[32:35]
mm v[36:39],a[104:107],a[80:83],v[36:39]
mm v[36:39],a[108:111],a[84:87],v[36:39]
mm v[8:11],a[112:115],a[64:67],v[8:11]
sa s79,s70,0x3000
bl v214,s[44:47],s79 offen offset:3072 sc1 lds
mm v[8:11],a[116:119],a[68:71],v[8:11]
mm v[24:27],a[112:115],a[72:75],v[24:27]
mm v[24:27],a[116:119],a[76:79],v[24:27]
sa m0,s77,0xc000
mm v[40:43],a[112:115],a[80:83],v[40:43]
)"
R"(sa s79,s78,0x60000
bl v159,s[36:39],s79 offen lds
mm v[40:43],a[116:119],a[84:87],v[40:43]
s_waitcnt lgkmcnt(0)
mm v[48:51],a[96:99],a[88:91],v[48:51]
mm v[48:51],a[100:103],a[92:95],v[48:51]
mm v[52:55],a[104:107],a[88:91],v[52:55]
sa s79,s78,0x61c00
bl v159,s[36:39],s79 offen offset:1024 lds
mm v[52:55],a[108:111],a[92:95],v[52:55]
mm v[56:59],a[112:115],a[88:91],v[56:59]
mm v[56:59],a[116:119],a[92:95],v[56:59]
mm v[12:15],a[120:123],a[64:67],v[12:15]
sa s79,s78,0x63800
bl v159,s[36:39],s79 offen offset:2048 lds
mm v[12:15],a[124:127],a[68:71],v[12:15]
)"
R"(mm v[28:31],a[120:123],a[72:75],v[28:31]
mm v[28:31],a[124:127],a[76:79],v[28:31]
mm v[44:47],a[120:123],a[80:83],v[44:47]
sa s79,s78,0x65400
bl v159,s[36:39],s79 offen offset:3072 lds
mm v[44:47],a[124:127],a[84:87],v[44:47]
mm v[60:63],a[120:123],a[88:91],v[60:63]
mm v[60:63],a[124:127],a[92:95],v[60:63]
s_nop 7
cv v16,v16,v17
cv v17,v18,v19
cv v18,v20,v21
cv v19,v22,v23
cv v20,v24,v25
cv v21,v26,v27
cv v22,v28,v29
)"
R"(cv v23,v30,v31
s_xor_b32 s79, s64, 1
s_lshl_b32 s79, s79, 15
sa s79,s79,s72
sa s79,s79,0
buffer_store_dwordx4 v[16:19], v214, s[44:47], s79 offen offset:0 sc1
buffer_store_dwordx4 v[20:23], v214, s[44:47], s79 offen offset:1024 sc1
cv v32,v32,v33
cv v33,v34,v35
cv v34,v36,v37
cv v35,v38,v39
cv v36,v40,v41
cv v37,v42,v43
cv v38,v44,v45
cv v39,v46,v47
s_xor_b32 s79, s64, 2
)"
R"(s_lshl_b32 s79, s79, 15
sa s79,s79,s72
sa s79,s79,2048
buffer_store_dwordx4 v[32:35], v214, s[44:47], s79 offen offset:0 sc1
buffer_store_dwordx4 v[36:39], v214, s[44:47], s79 offen offset:1024 sc1
cv v48,v48,v49
cv v49,v50,v51
cv v50,v52,v53
cv v51,v54,v55
cv v52,v56,v57
cv v53,v58,v59
cv v54,v60,v61
cv v55,v62,v63
s_xor_b32 s79, s64, 3
s_lshl_b32 s79, s79, 15
sa s79,s79,s72
)"
R"(sa s79,s79,4096
buffer_store_dwordx4 v[48:51], v214, s[44:47], s79 offen offset:0 sc1
buffer_store_dwordx4 v[52:55], v214, s[44:47], s79 offen offset:1024 sc1
s_waitcnt vmcnt(22)
s_barrier
dt v[72:73],v155 offset:49152
ds_read_b64 v[64:65], v151 offset:32768
dt v[74:75],v155 offset:51200
ds_read_b64 v[66:67], v151 offset:33280
dt v[76:77],v156 offset:49152
ds_read_b64 v[68:69], v151 offset:36864
dt v[78:79],v156 offset:51200
ds_read_b64 v[70:71], v151 offset:37376
dt v[88:89],v155 offset:53248
ds_read_b64 v[80:81], v151 offset:33792
dt v[90:91],v155 offset:55296
)"
R"(ds_read_b64 v[82:83], v151 offset:34304
dt v[92:93],v156 offset:53248
ds_read_b64 v[84:85], v151 offset:37888
dt v[94:95],v156 offset:55296
ds_read_b64 v[86:87], v151 offset:38400
s_waitcnt lgkmcnt(8)
mm v[164:167],v[72:75],v[64:67],0
dt v[104:105],v155 offset:57344
ds_read_b64 v[96:97], v151 offset:34816
mm v[172:175],v[72:75],v[68:71],0
dt v[106:107],v155 offset:59392
ds_read_b64 v[98:99], v151 offset:35328
mm v[168:171],v[76:79],v[64:67],0
dt v[108:109],v156 offset:57344
ds_read_b64 v[100:101], v151 offset:38912
mm v[176:179],v[76:79],v[68:71],0
)"
R"(dt v[110:111],v156 offset:59392
ds_read_b64 v[102:103], v151 offset:39424
s_waitcnt lgkmcnt(8)
mm v[164:167],v[88:91],v[80:83],v[164:167]
dt v[120:121],v155 offset:61440
ds_read_b64 v[112:113], v151 offset:35840
mm v[172:175],v[88:91],v[84:87],v[172:175]
dt v[122:123],v155 offset:63488
ds_read_b64 v[114:115], v151 offset:36352
mm v[168:171],v[92:95],v[80:83],v[168:171]
dt v[124:125],v156 offset:61440
ds_read_b64 v[116:117], v151 offset:39936
mm v[176:179],v[92:95],v[84:87],v[176:179]
dt v[126:127],v156 offset:63488
ds_read_b64 v[118:119], v151 offset:40448
s_waitcnt lgkmcnt(8)
)"
R"(mm v[164:167],v[104:107],v[96:99],v[164:167]
dt v[72:73],v157 offset:16384
ds_read_b64 v[64:65], v152 offset:0
mm v[172:175],v[104:107],v[100:103],v[172:175]
dt v[74:75],v157 offset:18432
ds_read_b64 v[66:67], v152 offset:512
mm v[168:171],v[108:111],v[96:99],v[168:171]
dt v[76:77],v158 offset:16384
ds_read_b64 v[68:69], v152 offset:4096
mm v[176:179],v[108:111],v[100:103],v[176:179]
dt v[78:79],v158 offset:18432
ds_read_b64 v[70:71], v152 offset:4608
s_waitcnt lgkmcnt(8)
mm v[164:167],v[120:123],v[112:115],v[164:167]
dt v[88:89],v157 offset:20480
ds_read_b64 v[80:81], v152 offset:1024
)"
R"(mm v[172:175],v[120:123],v[116:119],v[172:175]
dt v[90:91],v157 offset:22528
ds_read_b64 v[82:83], v152 offset:1536
mm v[168:171],v[124:127],v[112:115],v[168:171]
dt v[92:93],v158 offset:20480
ds_read_b64 v[84:85], v152 offset:5120
mm v[176:179],v[124:127],v[116:119],v[176:179]
dt v[94:95],v158 offset:22528
ds_read_b64 v[86:87], v152 offset:5632
s_waitcnt lgkmcnt(8)
mm v[164:167],v[72:75],v[64:67],v[164:167]
dt v[104:105],v157 offset:24576
ds_read_b64 v[96:97], v152 offset:2048
mm v[172:175],v[72:75],v[68:71],v[172:175]
dt v[106:107],v157 offset:26624
ds_read_b64 v[98:99], v152 offset:2560
)"
R"(mm v[168:171],v[76:79],v[64:67],v[168:171]
dt v[108:109],v158 offset:24576
ds_read_b64 v[100:101], v152 offset:6144
mm v[176:179],v[76:79],v[68:71],v[176:179]
dt v[110:111],v158 offset:26624
ds_read_b64 v[102:103], v152 offset:6656
s_waitcnt lgkmcnt(8)
mm v[164:167],v[88:91],v[80:83],v[164:167]
dt v[120:121],v157 offset:28672
ds_read_b64 v[112:113], v152 offset:3072
mm v[172:175],v[88:91],v[84:87],v[172:175]
dt v[122:123],v157 offset:30720
ds_read_b64 v[114:115], v152 offset:3584
mm v[168:171],v[92:95],v[80:83],v[168:171]
dt v[124:125],v158 offset:28672
ds_read_b64 v[116:117], v152 offset:7168
)"
R"(mm v[176:179],v[92:95],v[84:87],v[176:179]
dt v[126:127],v158 offset:30720
ds_read_b64 v[118:119], v152 offset:7680
s_waitcnt lgkmcnt(8)
s_waitcnt vmcnt(6)
s_barrier
mm v[164:167],v[104:107],v[96:99],v[164:167]
dt v[72:73],v153 offset:16384
ds_read_b64 v[64:65], v150 offset:0
mm v[172:175],v[104:107],v[100:103],v[172:175]
dt v[74:75],v153 offset:18432
ds_read_b64 v[66:67], v150 offset:512
mm v[168:171],v[108:111],v[96:99],v[168:171]
dt v[76:77],v154 offset:16384
ds_read_b64 v[68:69], v150 offset:4096
mm v[176:179],v[108:111],v[100:103],v[176:179]
)"
R"(dt v[78:79],v154 offset:18432
ds_read_b64 v[70:71], v150 offset:4608
s_waitcnt lgkmcnt(8)
mm v[164:167],v[120:123],v[112:115],v[164:167]
dt v[88:89],v153 offset:20480
ds_read_b64 v[80:81], v150 offset:1024
mm v[172:175],v[120:123],v[116:119],v[172:175]
dt v[90:91],v153 offset:22528
ds_read_b64 v[82:83], v150 offset:1536
mm v[168:171],v[124:127],v[112:115],v[168:171]
dt v[92:93],v154 offset:20480
ds_read_b64 v[84:85], v150 offset:5120
mm v[176:179],v[124:127],v[116:119],v[176:179]
dt v[94:95],v154 offset:22528
ds_read_b64 v[86:87], v150 offset:5632
s_waitcnt lgkmcnt(8)
)"
R"(mm v[164:167],v[72:75],v[64:67],v[164:167]
dt v[104:105],v153 offset:24576
ds_read_b64 v[96:97], v150 offset:2048
mm v[172:175],v[72:75],v[68:71],v[172:175]
dt v[106:107],v153 offset:26624
ds_read_b64 v[98:99], v150 offset:2560
mm v[168:171],v[76:79],v[64:67],v[168:171]
dt v[108:109],v154 offset:24576
ds_read_b64 v[100:101], v150 offset:6144
mm v[176:179],v[76:79],v[68:71],v[176:179]
dt v[110:111],v154 offset:26624
ds_read_b64 v[102:103], v150 offset:6656
s_waitcnt lgkmcnt(8)
mm v[164:167],v[88:91],v[80:83],v[164:167]
dt v[120:121],v153 offset:28672
ds_read_b64 v[112:113], v150 offset:3072
)"
R"(mm v[172:175],v[88:91],v[84:87],v[172:175]
dt v[122:123],v153 offset:30720
ds_read_b64 v[114:115], v150 offset:3584
mm v[168:171],v[92:95],v[80:83],v[168:171]
dt v[124:125],v154 offset:28672
ds_read_b64 v[116:117], v150 offset:7168
mm v[176:179],v[92:95],v[84:87],v[176:179]
dt v[126:127],v154 offset:30720
ds_read_b64 v[118:119], v150 offset:7680
s_waitcnt lgkmcnt(8)
mm v[164:167],v[104:107],v[96:99],v[164:167]
dt v[72:73],v153 offset:49152
ds_read_b64 v[64:65], v150 offset:32768
mm v[172:175],v[104:107],v[100:103],v[172:175]
dt v[74:75],v153 offset:51200
ds_read_b64 v[66:67], v150 offset:33280
)"
R"(mm v[168:171],v[108:111],v[96:99],v[168:171]
dt v[76:77],v154 offset:49152
ds_read_b64 v[68:69], v150 offset:36864
mm v[176:179],v[108:111],v[100:103],v[176:179]
dt v[78:79],v154 offset:51200
ds_read_b64 v[70:71], v150 offset:37376
s_waitcnt lgkmcnt(8)
mm v[164:167],v[120:123],v[112:115],v[164:167]
dt v[88:89],v153 offset:53248
ds_read_b64 v[80:81], v150 offset:33792
mm v[172:175],v[120:123],v[116:119],v[172:175]
dt v[90:91],v153 offset:55296
ds_read_b64 v[82:83], v150 offset:34304
mm v[168:171],v[124:127],v[112:115],v[168:171]
dt v[92:93],v154 offset:53248
ds_read_b64 v[84:85], v150 offset:37888
)"
R"(mm v[176:179],v[124:127],v[116:119],v[176:179]
dt v[94:95],v154 offset:55296
ds_read_b64 v[86:87], v150 offset:38400
s_waitcnt lgkmcnt(8)
mm v[164:167],v[72:75],v[64:67],v[164:167]
dt v[104:105],v153 offset:57344
ds_read_b64 v[96:97], v150 offset:34816
mm v[172:175],v[72:75],v[68:71],v[172:175]
dt v[106:107],v153 offset:59392
ds_read_b64 v[98:99], v150 offset:35328
mm v[168:171],v[76:79],v[64:67],v[168:171]
dt v[108:109],v154 offset:57344
ds_read_b64 v[100:101], v150 offset:38912
mm v[176:179],v[76:79],v[68:71],v[176:179]
dt v[110:111],v154 offset:59392
ds_read_b64 v[102:103], v150 offset:39424
)"
R"(s_waitcnt lgkmcnt(8)
mm v[164:167],v[88:91],v[80:83],v[164:167]
dt v[120:121],v153 offset:61440
ds_read_b64 v[112:113], v150 offset:35840
mm v[172:175],v[88:91],v[84:87],v[172:175]
dt v[122:123],v153 offset:63488
ds_read_b64 v[114:115], v150 offset:36352
mm v[168:171],v[92:95],v[80:83],v[168:171]
dt v[124:125],v154 offset:61440
ds_read_b64 v[116:117], v150 offset:39936
mm v[176:179],v[92:95],v[84:87],v[176:179]
dt v[126:127],v154 offset:63488
ds_read_b64 v[118:119], v150 offset:40448
s_waitcnt lgkmcnt(8)
mm v[164:167],v[104:107],v[96:99],v[164:167]
mm v[172:175],v[104:107],v[100:103],v[172:175]
)"
R"(mm v[168:171],v[108:111],v[96:99],v[168:171]
mm v[176:179],v[108:111],v[100:103],v[176:179]
s_waitcnt lgkmcnt(0)
mm v[164:167],v[120:123],v[112:115],v[164:167]
mm v[172:175],v[120:123],v[116:119],v[172:175]
mm v[168:171],v[124:127],v[112:115],v[168:171]
mm v[176:179],v[124:127],v[116:119],v[176:179]
s_nop 7
s_lshl_b32 s79, s67, 1
s_and_b32 s80, s50, 1
sa s79,s79,s80
s_lshl_b32 s79, s79, 16
s_lshl_b32 s80, s52, 1
s_lshr_b32 s54, s50, 1
sa s80,s80,s54
s_lshl_b32 s80, s80, 6
)"
R"(sa s79,s79,s80
sa s80,s79,0x8000
cv v164,v164,v165
cv v165,v166,v167
cv v166,v168,v169
cv v167,v170,v171
s_nop 1
v_permlane16_swap_b32 v164, v166
v_permlane16_swap_b32 v165, v167
buffer_store_dwordx4 v[164:167], v160, s[88:91], s79 offen offset:0 sc1
cv v172,v172,v173
cv v173,v174,v175
cv v174,v176,v177
cv v175,v178,v179
s_nop 1
v_permlane16_swap_b32 v172, v174
)"
R"(v_permlane16_swap_b32 v173, v175
buffer_store_dwordx4 v[172:175], v160, s[88:91], s80 offen offset:0 sc1
L_out_tail:
s_lshl_b32 s79, s64, 15
sa s72,s72,s79
sa s73,s72,0x1000
s_mov_b32 s75, 0x8000
s_lshl_b32 s79, s65, 1
s_and_b32 s74, s50, 1
sa s79,s79,s74
s_lshl_b32 s79, s79, 2
sa s79,s79,s64
s_lshl_b32 s79, s79, 15
s_lshl_b32 s74, s66, 1
s_lshr_b32 s80, s50, 1
sa s74,s74,s80
)"
R"(s_lshl_b32 s74, s74, 7
sa s79,s79,s74
sa s79,s79,1024
L_out_poll:
bl v[180:183],v214,s[44:47],s72 offen offset:0 sc1
bl v[184:187],v214,s[44:47],s72 offen offset:1024 sc1
bl v[188:191],v214,s[44:47],s72 offen offset:2048 sc1
bl v[192:195],v214,s[44:47],s72 offen offset:3072 sc1
bl v[196:199],v214,s[44:47],s73 offen offset:0 sc1
bl v[200:203],v214,s[44:47],s73 offen offset:1024 sc1
L_out_wait:
s_waitcnt vmcnt(0)
v_max3_u32 v147, v180, v181, v182
v_max3_u32 v148, v183, v184, v185
v_max3_u32 v147, v147, v186, v187
v_max3_u32 v148, v148, v188, v189
)"
R"(v_max3_u32 v147, v147, v190, v191
v_max3_u32 v148, v148, v192, v193
v_max3_u32 v147, v147, v194, v195
v_max3_u32 v148, v148, v196, v197
v_max3_u32 v147, v147, v198, v199
v_max3_u32 v148, v148, v200, v201
v_max3_u32 v147, v147, v202, v203
v_max_u32 v147, v147, v148
v_cmp_eq_u32 vcc, -1, v147
s_nop 4
s_cbranch_vccz L_out_ready
s_sub_u32 s75, s75, 1
s_cmp_lg_u32 s75, 0
s_cbranch_scc1 L_out_poll
L_out_ready:
v_lshlrev_b32 v16, 16, v180
)"
R"(v_and_b32 v17, 0xffff0000, v180
v_pk_add_f32 v[0:1], v[0:1], v[16:17]
v_lshlrev_b32 v18, 16, v181
v_and_b32 v19, 0xffff0000, v181
v_pk_add_f32 v[2:3], v[2:3], v[18:19]
v_lshlrev_b32 v20, 16, v182
v_and_b32 v21, 0xffff0000, v182
v_pk_add_f32 v[4:5], v[4:5], v[20:21]
v_lshlrev_b32 v22, 16, v183
v_and_b32 v23, 0xffff0000, v183
v_pk_add_f32 v[6:7], v[6:7], v[22:23]
v_lshlrev_b32 v16, 16, v184
v_and_b32 v17, 0xffff0000, v184
v_pk_add_f32 v[8:9], v[8:9], v[16:17]
v_lshlrev_b32 v18, 16, v185
v_and_b32 v19, 0xffff0000, v185
)"
R"(v_pk_add_f32 v[10:11], v[10:11], v[18:19]
v_lshlrev_b32 v20, 16, v186
v_and_b32 v21, 0xffff0000, v186
v_pk_add_f32 v[12:13], v[12:13], v[20:21]
v_lshlrev_b32 v22, 16, v187
v_and_b32 v23, 0xffff0000, v187
v_pk_add_f32 v[14:15], v[14:15], v[22:23]
v_lshlrev_b32 v16, 16, v188
v_and_b32 v17, 0xffff0000, v188
v_pk_add_f32 v[0:1], v[0:1], v[16:17]
v_lshlrev_b32 v18, 16, v189
v_and_b32 v19, 0xffff0000, v189
v_pk_add_f32 v[2:3], v[2:3], v[18:19]
v_lshlrev_b32 v20, 16, v190
v_and_b32 v21, 0xffff0000, v190
v_pk_add_f32 v[4:5], v[4:5], v[20:21]
)"
R"(v_lshlrev_b32 v22, 16, v191
v_and_b32 v23, 0xffff0000, v191
v_pk_add_f32 v[6:7], v[6:7], v[22:23]
v_lshlrev_b32 v16, 16, v192
v_and_b32 v17, 0xffff0000, v192
v_pk_add_f32 v[8:9], v[8:9], v[16:17]
v_lshlrev_b32 v18, 16, v193
v_and_b32 v19, 0xffff0000, v193
v_pk_add_f32 v[10:11], v[10:11], v[18:19]
v_lshlrev_b32 v20, 16, v194
v_and_b32 v21, 0xffff0000, v194
v_pk_add_f32 v[12:13], v[12:13], v[20:21]
v_lshlrev_b32 v22, 16, v195
v_and_b32 v23, 0xffff0000, v195
v_pk_add_f32 v[14:15], v[14:15], v[22:23]
v_lshlrev_b32 v16, 16, v196
)"
R"(v_and_b32 v17, 0xffff0000, v196
v_pk_add_f32 v[0:1], v[0:1], v[16:17]
v_lshlrev_b32 v18, 16, v197
v_and_b32 v19, 0xffff0000, v197
v_pk_add_f32 v[2:3], v[2:3], v[18:19]
v_lshlrev_b32 v20, 16, v198
v_and_b32 v21, 0xffff0000, v198
v_pk_add_f32 v[4:5], v[4:5], v[20:21]
v_lshlrev_b32 v22, 16, v199
v_and_b32 v23, 0xffff0000, v199
v_pk_add_f32 v[6:7], v[6:7], v[22:23]
v_lshlrev_b32 v16, 16, v200
v_and_b32 v17, 0xffff0000, v200
v_pk_add_f32 v[8:9], v[8:9], v[16:17]
v_lshlrev_b32 v18, 16, v201
v_and_b32 v19, 0xffff0000, v201
)"
R"(v_pk_add_f32 v[10:11], v[10:11], v[18:19]
v_lshlrev_b32 v20, 16, v202
v_and_b32 v21, 0xffff0000, v202
v_pk_add_f32 v[12:13], v[12:13], v[20:21]
v_lshlrev_b32 v22, 16, v203
v_and_b32 v23, 0xffff0000, v203
v_pk_add_f32 v[14:15], v[14:15], v[22:23]
cv v0,v0,v1
cv v1,v2,v3
cv v2,v4,v5
cv v3,v6,v7
s_nop 1
v_permlane16_swap_b32 v0, v2
v_permlane16_swap_b32 v1, v3
buffer_store_dwordx4 v[0:3], v160, s[88:91], s79 offen offset:0 sc1
cv v8,v8,v9
)"
R"(cv v9,v10,v11
cv v10,v12,v13
cv v11,v14,v15
s_nop 1
v_permlane16_swap_b32 v8, v10
v_permlane16_swap_b32 v9, v11
buffer_store_dwordx4 v[8:11], v160, s[88:91], s79 offen offset:64 sc1
L_end:
s_branch L_exit
L_redo_seg0_16:
s_mov_b32 s66, 0
s_mov_b32 s67, s52
s_getpc_b64 s[64:65]
sa s64,s64,12
s_addc_u32 s65, s65, 0
s_branch L_slow
)"
R"(v_rcp_f32 v220, v192
v_rcp_f32 v222, v196
s_nop 0
v_pk_mul_f32 v[0:1], v[0:1], v[220:221] op_sel_hi:[1,0]
v_pk_mul_f32 v[2:3], v[2:3], v[220:221] op_sel_hi:[1,0]
v_pk_mul_f32 v[4:5], v[4:5], v[220:221] op_sel_hi:[1,0]
v_pk_mul_f32 v[6:7], v[6:7], v[220:221] op_sel_hi:[1,0]
cv v0,v0,v1
cv v1,v2,v3
cv v2,v4,v5
cv v3,v6,v7
v_pk_mul_f32 v[8:9], v[8:9], v[220:221] op_sel_hi:[1,0]
v_pk_mul_f32 v[10:11], v[10:11], v[220:221] op_sel_hi:[1,0]
v_pk_mul_f32 v[12:13], v[12:13], v[220:221] op_sel_hi:[1,0]
v_pk_mul_f32 v[14:15], v[14:15], v[220:221] op_sel_hi:[1,0]
cv v8,v8,v9
)"
R"(cv v9,v10,v11
cv v10,v12,v13
cv v11,v14,v15
v_pk_mul_f32 v[16:17], v[16:17], v[220:221] op_sel_hi:[1,0]
v_pk_mul_f32 v[18:19], v[18:19], v[220:221] op_sel_hi:[1,0]
v_pk_mul_f32 v[20:21], v[20:21], v[220:221] op_sel_hi:[1,0]
v_pk_mul_f32 v[22:23], v[22:23], v[220:221] op_sel_hi:[1,0]
cv v16,v16,v17
cv v17,v18,v19
cv v18,v20,v21
cv v19,v22,v23
v_pk_mul_f32 v[24:25], v[24:25], v[220:221] op_sel_hi:[1,0]
v_pk_mul_f32 v[26:27], v[26:27], v[220:221] op_sel_hi:[1,0]
v_pk_mul_f32 v[28:29], v[28:29], v[220:221] op_sel_hi:[1,0]
v_pk_mul_f32 v[30:31], v[30:31], v[220:221] op_sel_hi:[1,0]
cv v24,v24,v25
)"
R"(cv v25,v26,v27
cv v26,v28,v29
cv v27,v30,v31
v_pk_mul_f32 v[32:33], v[32:33], v[222:223] op_sel_hi:[1,0]
v_pk_mul_f32 v[34:35], v[34:35], v[222:223] op_sel_hi:[1,0]
v_pk_mul_f32 v[36:37], v[36:37], v[222:223] op_sel_hi:[1,0]
v_pk_mul_f32 v[38:39], v[38:39], v[222:223] op_sel_hi:[1,0]
cv v32,v32,v33
cv v33,v34,v35
cv v34,v36,v37
cv v35,v38,v39
v_pk_mul_f32 v[40:41], v[40:41], v[222:223] op_sel_hi:[1,0]
v_pk_mul_f32 v[42:43], v[42:43], v[222:223] op_sel_hi:[1,0]
v_pk_mul_f32 v[44:45], v[44:45], v[222:223] op_sel_hi:[1,0]
v_pk_mul_f32 v[46:47], v[46:47], v[222:223] op_sel_hi:[1,0]
cv v40,v40,v41
)"
R"(cv v41,v42,v43
cv v42,v44,v45
cv v43,v46,v47
v_pk_mul_f32 v[48:49], v[48:49], v[222:223] op_sel_hi:[1,0]
v_pk_mul_f32 v[50:51], v[50:51], v[222:223] op_sel_hi:[1,0]
v_pk_mul_f32 v[52:53], v[52:53], v[222:223] op_sel_hi:[1,0]
v_pk_mul_f32 v[54:55], v[54:55], v[222:223] op_sel_hi:[1,0]
cv v48,v48,v49
cv v49,v50,v51
cv v50,v52,v53
cv v51,v54,v55
v_pk_mul_f32 v[56:57], v[56:57], v[222:223] op_sel_hi:[1,0]
v_pk_mul_f32 v[58:59], v[58:59], v[222:223] op_sel_hi:[1,0]
v_pk_mul_f32 v[60:61], v[60:61], v[222:223] op_sel_hi:[1,0]
v_pk_mul_f32 v[62:63], v[62:63], v[222:223] op_sel_hi:[1,0]
cv v56,v56,v57
)"
R"(cv v57,v58,v59
cv v58,v60,v61
cv v59,v62,v63
s_lshl_b32 s56, s57, 5
sa s56,s56,s48
s_lshl_b32 s56, s56, 15
sa s56,s56,0x2100000
s_and_b32 s69, s50, 1
s_lshl_b32 s69, s69, 10
sa s68,s56,s69
s_sub_u32 s69, s56, s69
sa s56,s68,0x0
buffer_store_dwordx4 v[0:3], v214, s[44:47], s56 offen offset:0 sc1
sa s56,s68,0x2000
buffer_store_dwordx4 v[8:11], v214, s[44:47], s56 offen offset:0 sc1
sa s56,s68,0x4000
)"
R"(buffer_store_dwordx4 v[16:19], v214, s[44:47], s56 offen offset:0 sc1
sa s56,s68,0x6000
buffer_store_dwordx4 v[24:27], v214, s[44:47], s56 offen offset:0 sc1
sa s56,s69,0x0
buffer_store_dwordx4 v[32:35], v214, s[44:47], s56 offen offset:3072 sc1
sa s56,s69,0x2000
buffer_store_dwordx4 v[40:43], v214, s[44:47], s56 offen offset:3072 sc1
sa s56,s69,0x4000
buffer_store_dwordx4 v[48:51], v214, s[44:47], s56 offen offset:3072 sc1
sa s56,s69,0x6000
buffer_store_dwordx4 v[56:59], v214, s[44:47], s56 offen offset:3072 sc1
s_branch L_ok_seg0_16
L_redo_seg0_0:
s_mov_b32 s66, 0
s_mov_b32 s67, s52
s_getpc_b64 s[64:65]
)"
R"(sa s64,s64,12
s_addc_u32 s65, s65, 0
s_branch L_slow
v_rcp_f32 v220, v192
v_rcp_f32 v222, v196
s_nop 0
v_pk_mul_f32 v[0:1], v[0:1], v[220:221] op_sel_hi:[1,0]
v_pk_mul_f32 v[2:3], v[2:3], v[220:221] op_sel_hi:[1,0]
v_pk_mul_f32 v[4:5], v[4:5], v[220:221] op_sel_hi:[1,0]
v_pk_mul_f32 v[6:7], v[6:7], v[220:221] op_sel_hi:[1,0]
cv v0,v0,v1
cv v1,v2,v3
cv v2,v4,v5
cv v3,v6,v7
v_pk_mul_f32 v[8:9], v[8:9], v[220:221] op_sel_hi:[1,0]
v_pk_mul_f32 v[10:11], v[10:11], v[220:221] op_sel_hi:[1,0]
)"
R"(v_pk_mul_f32 v[12:13], v[12:13], v[220:221] op_sel_hi:[1,0]
v_pk_mul_f32 v[14:15], v[14:15], v[220:221] op_sel_hi:[1,0]
cv v8,v8,v9
cv v9,v10,v11
cv v10,v12,v13
cv v11,v14,v15
v_pk_mul_f32 v[16:17], v[16:17], v[220:221] op_sel_hi:[1,0]
v_pk_mul_f32 v[18:19], v[18:19], v[220:221] op_sel_hi:[1,0]
v_pk_mul_f32 v[20:21], v[20:21], v[220:221] op_sel_hi:[1,0]
v_pk_mul_f32 v[22:23], v[22:23], v[220:221] op_sel_hi:[1,0]
cv v16,v16,v17
cv v17,v18,v19
cv v18,v20,v21
cv v19,v22,v23
v_pk_mul_f32 v[24:25], v[24:25], v[220:221] op_sel_hi:[1,0]
v_pk_mul_f32 v[26:27], v[26:27], v[220:221] op_sel_hi:[1,0]
)"
R"(v_pk_mul_f32 v[28:29], v[28:29], v[220:221] op_sel_hi:[1,0]
v_pk_mul_f32 v[30:31], v[30:31], v[220:221] op_sel_hi:[1,0]
cv v24,v24,v25
cv v25,v26,v27
cv v26,v28,v29
cv v27,v30,v31
v_pk_mul_f32 v[32:33], v[32:33], v[222:223] op_sel_hi:[1,0]
v_pk_mul_f32 v[34:35], v[34:35], v[222:223] op_sel_hi:[1,0]
v_pk_mul_f32 v[36:37], v[36:37], v[222:223] op_sel_hi:[1,0]
v_pk_mul_f32 v[38:39], v[38:39], v[222:223] op_sel_hi:[1,0]
cv v32,v32,v33
cv v33,v34,v35
cv v34,v36,v37
cv v35,v38,v39
v_pk_mul_f32 v[40:41], v[40:41], v[222:223] op_sel_hi:[1,0]
v_pk_mul_f32 v[42:43], v[42:43], v[222:223] op_sel_hi:[1,0]
)"
R"(v_pk_mul_f32 v[44:45], v[44:45], v[222:223] op_sel_hi:[1,0]
v_pk_mul_f32 v[46:47], v[46:47], v[222:223] op_sel_hi:[1,0]
cv v40,v40,v41
cv v41,v42,v43
cv v42,v44,v45
cv v43,v46,v47
v_pk_mul_f32 v[48:49], v[48:49], v[222:223] op_sel_hi:[1,0]
v_pk_mul_f32 v[50:51], v[50:51], v[222:223] op_sel_hi:[1,0]
v_pk_mul_f32 v[52:53], v[52:53], v[222:223] op_sel_hi:[1,0]
v_pk_mul_f32 v[54:55], v[54:55], v[222:223] op_sel_hi:[1,0]
cv v48,v48,v49
cv v49,v50,v51
cv v50,v52,v53
cv v51,v54,v55
v_pk_mul_f32 v[56:57], v[56:57], v[222:223] op_sel_hi:[1,0]
v_pk_mul_f32 v[58:59], v[58:59], v[222:223] op_sel_hi:[1,0]
)"
R"(v_pk_mul_f32 v[60:61], v[60:61], v[222:223] op_sel_hi:[1,0]
v_pk_mul_f32 v[62:63], v[62:63], v[222:223] op_sel_hi:[1,0]
cv v56,v56,v57
cv v57,v58,v59
cv v58,v60,v61
cv v59,v62,v63
s_lshl_b32 s56, s57, 5
sa s56,s56,s48
s_lshl_b32 s56, s56, 15
sa s56,s56,0x2100000
s_and_b32 s69, s50, 1
s_lshl_b32 s69, s69, 10
sa s68,s56,s69
s_sub_u32 s69, s56, s69
sa s56,s68,0x0
buffer_store_dwordx4 v[0:3], v214, s[44:47], s56 offen offset:0 sc1
)"
R"(sa s56,s68,0x2000
buffer_store_dwordx4 v[8:11], v214, s[44:47], s56 offen offset:0 sc1
sa s56,s68,0x4000
buffer_store_dwordx4 v[16:19], v214, s[44:47], s56 offen offset:0 sc1
sa s56,s68,0x6000
buffer_store_dwordx4 v[24:27], v214, s[44:47], s56 offen offset:0 sc1
sa s56,s69,0x0
buffer_store_dwordx4 v[32:35], v214, s[44:47], s56 offen offset:3072 sc1
sa s56,s69,0x2000
buffer_store_dwordx4 v[40:43], v214, s[44:47], s56 offen offset:3072 sc1
sa s56,s69,0x4000
buffer_store_dwordx4 v[48:51], v214, s[44:47], s56 offen offset:3072 sc1
sa s56,s69,0x6000
buffer_store_dwordx4 v[56:59], v214, s[44:47], s56 offen offset:3072 sc1
s_branch L_ok_seg0_0
L_redo_seg1_16:
)"
R"(s_mov_b32 s66, 0
s_mov_b32 s67, s52
s_getpc_b64 s[64:65]
sa s64,s64,12
s_addc_u32 s65, s65, 0
s_branch L_slow
v_rcp_f32 v220, v192
v_rcp_f32 v222, v196
s_nop 0
v_pk_mul_f32 v[0:1], v[0:1], v[220:221] op_sel_hi:[1,0]
v_pk_mul_f32 v[2:3], v[2:3], v[220:221] op_sel_hi:[1,0]
v_pk_mul_f32 v[4:5], v[4:5], v[220:221] op_sel_hi:[1,0]
v_pk_mul_f32 v[6:7], v[6:7], v[220:221] op_sel_hi:[1,0]
cv v0,v0,v1
cv v1,v2,v3
cv v2,v4,v5
)"
R"(cv v3,v6,v7
v_pk_mul_f32 v[8:9], v[8:9], v[220:221] op_sel_hi:[1,0]
v_pk_mul_f32 v[10:11], v[10:11], v[220:221] op_sel_hi:[1,0]
v_pk_mul_f32 v[12:13], v[12:13], v[220:221] op_sel_hi:[1,0]
v_pk_mul_f32 v[14:15], v[14:15], v[220:221] op_sel_hi:[1,0]
cv v8,v8,v9
cv v9,v10,v11
cv v10,v12,v13
cv v11,v14,v15
v_pk_mul_f32 v[16:17], v[16:17], v[220:221] op_sel_hi:[1,0]
v_pk_mul_f32 v[18:19], v[18:19], v[220:221] op_sel_hi:[1,0]
v_pk_mul_f32 v[20:21], v[20:21], v[220:221] op_sel_hi:[1,0]
v_pk_mul_f32 v[22:23], v[22:23], v[220:221] op_sel_hi:[1,0]
cv v16,v16,v17
cv v17,v18,v19
cv v18,v20,v21
)"
R"(cv v19,v22,v23
v_pk_mul_f32 v[24:25], v[24:25], v[220:221] op_sel_hi:[1,0]
v_pk_mul_f32 v[26:27], v[26:27], v[220:221] op_sel_hi:[1,0]
v_pk_mul_f32 v[28:29], v[28:29], v[220:221] op_sel_hi:[1,0]
v_pk_mul_f32 v[30:31], v[30:31], v[220:221] op_sel_hi:[1,0]
cv v24,v24,v25
cv v25,v26,v27
cv v26,v28,v29
cv v27,v30,v31
v_pk_mul_f32 v[32:33], v[32:33], v[222:223] op_sel_hi:[1,0]
v_pk_mul_f32 v[34:35], v[34:35], v[222:223] op_sel_hi:[1,0]
v_pk_mul_f32 v[36:37], v[36:37], v[222:223] op_sel_hi:[1,0]
v_pk_mul_f32 v[38:39], v[38:39], v[222:223] op_sel_hi:[1,0]
cv v32,v32,v33
cv v33,v34,v35
cv v34,v36,v37
)"
R"(cv v35,v38,v39
v_pk_mul_f32 v[40:41], v[40:41], v[222:223] op_sel_hi:[1,0]
v_pk_mul_f32 v[42:43], v[42:43], v[222:223] op_sel_hi:[1,0]
v_pk_mul_f32 v[44:45], v[44:45], v[222:223] op_sel_hi:[1,0]
v_pk_mul_f32 v[46:47], v[46:47], v[222:223] op_sel_hi:[1,0]
cv v40,v40,v41
cv v41,v42,v43
cv v42,v44,v45
cv v43,v46,v47
v_pk_mul_f32 v[48:49], v[48:49], v[222:223] op_sel_hi:[1,0]
v_pk_mul_f32 v[50:51], v[50:51], v[222:223] op_sel_hi:[1,0]
v_pk_mul_f32 v[52:53], v[52:53], v[222:223] op_sel_hi:[1,0]
v_pk_mul_f32 v[54:55], v[54:55], v[222:223] op_sel_hi:[1,0]
cv v48,v48,v49
cv v49,v50,v51
cv v50,v52,v53
)"
R"(cv v51,v54,v55
v_pk_mul_f32 v[56:57], v[56:57], v[222:223] op_sel_hi:[1,0]
v_pk_mul_f32 v[58:59], v[58:59], v[222:223] op_sel_hi:[1,0]
v_pk_mul_f32 v[60:61], v[60:61], v[222:223] op_sel_hi:[1,0]
v_pk_mul_f32 v[62:63], v[62:63], v[222:223] op_sel_hi:[1,0]
cv v56,v56,v57
cv v57,v58,v59
cv v58,v60,v61
cv v59,v62,v63
s_lshl_b32 s56, s57, 5
sa s56,s56,s48
s_lshl_b32 s56, s56, 15
sa s56,s56,0x2100000
s_and_b32 s69, s50, 1
s_lshl_b32 s69, s69, 10
sa s68,s56,s69
)"
R"(s_sub_u32 s69, s56, s69
sa s56,s68,0x0
buffer_store_dwordx4 v[0:3], v214, s[44:47], s56 offen offset:0 sc1
sa s56,s68,0x2000
buffer_store_dwordx4 v[8:11], v214, s[44:47], s56 offen offset:0 sc1
sa s56,s68,0x4000
buffer_store_dwordx4 v[16:19], v214, s[44:47], s56 offen offset:0 sc1
sa s56,s68,0x6000
buffer_store_dwordx4 v[24:27], v214, s[44:47], s56 offen offset:0 sc1
sa s56,s69,0x0
buffer_store_dwordx4 v[32:35], v214, s[44:47], s56 offen offset:3072 sc1
sa s56,s69,0x2000
buffer_store_dwordx4 v[40:43], v214, s[44:47], s56 offen offset:3072 sc1
sa s56,s69,0x4000
buffer_store_dwordx4 v[48:51], v214, s[44:47], s56 offen offset:3072 sc1
sa s56,s69,0x6000
)"
R"(buffer_store_dwordx4 v[56:59], v214, s[44:47], s56 offen offset:3072 sc1
s_branch L_ok_seg1_16
L_redo_seg1_0:
s_mov_b32 s66, 0
s_mov_b32 s67, s52
s_getpc_b64 s[64:65]
sa s64,s64,12
s_addc_u32 s65, s65, 0
s_branch L_slow
v_rcp_f32 v220, v192
v_rcp_f32 v222, v196
s_nop 0
v_pk_mul_f32 v[0:1], v[0:1], v[220:221] op_sel_hi:[1,0]
v_pk_mul_f32 v[2:3], v[2:3], v[220:221] op_sel_hi:[1,0]
v_pk_mul_f32 v[4:5], v[4:5], v[220:221] op_sel_hi:[1,0]
v_pk_mul_f32 v[6:7], v[6:7], v[220:221] op_sel_hi:[1,0]
)"
R"(cv v0,v0,v1
cv v1,v2,v3
cv v2,v4,v5
cv v3,v6,v7
v_pk_mul_f32 v[8:9], v[8:9], v[220:221] op_sel_hi:[1,0]
v_pk_mul_f32 v[10:11], v[10:11], v[220:221] op_sel_hi:[1,0]
v_pk_mul_f32 v[12:13], v[12:13], v[220:221] op_sel_hi:[1,0]
v_pk_mul_f32 v[14:15], v[14:15], v[220:221] op_sel_hi:[1,0]
cv v8,v8,v9
cv v9,v10,v11
cv v10,v12,v13
cv v11,v14,v15
v_pk_mul_f32 v[16:17], v[16:17], v[220:221] op_sel_hi:[1,0]
v_pk_mul_f32 v[18:19], v[18:19], v[220:221] op_sel_hi:[1,0]
v_pk_mul_f32 v[20:21], v[20:21], v[220:221] op_sel_hi:[1,0]
v_pk_mul_f32 v[22:23], v[22:23], v[220:221] op_sel_hi:[1,0]
)"
R"(cv v16,v16,v17
cv v17,v18,v19
cv v18,v20,v21
cv v19,v22,v23
v_pk_mul_f32 v[24:25], v[24:25], v[220:221] op_sel_hi:[1,0]
v_pk_mul_f32 v[26:27], v[26:27], v[220:221] op_sel_hi:[1,0]
v_pk_mul_f32 v[28:29], v[28:29], v[220:221] op_sel_hi:[1,0]
v_pk_mul_f32 v[30:31], v[30:31], v[220:221] op_sel_hi:[1,0]
cv v24,v24,v25
cv v25,v26,v27
cv v26,v28,v29
cv v27,v30,v31
v_pk_mul_f32 v[32:33], v[32:33], v[222:223] op_sel_hi:[1,0]
v_pk_mul_f32 v[34:35], v[34:35], v[222:223] op_sel_hi:[1,0]
v_pk_mul_f32 v[36:37], v[36:37], v[222:223] op_sel_hi:[1,0]
v_pk_mul_f32 v[38:39], v[38:39], v[222:223] op_sel_hi:[1,0]
)"
R"(cv v32,v32,v33
cv v33,v34,v35
cv v34,v36,v37
cv v35,v38,v39
v_pk_mul_f32 v[40:41], v[40:41], v[222:223] op_sel_hi:[1,0]
v_pk_mul_f32 v[42:43], v[42:43], v[222:223] op_sel_hi:[1,0]
v_pk_mul_f32 v[44:45], v[44:45], v[222:223] op_sel_hi:[1,0]
v_pk_mul_f32 v[46:47], v[46:47], v[222:223] op_sel_hi:[1,0]
cv v40,v40,v41
cv v41,v42,v43
cv v42,v44,v45
cv v43,v46,v47
v_pk_mul_f32 v[48:49], v[48:49], v[222:223] op_sel_hi:[1,0]
v_pk_mul_f32 v[50:51], v[50:51], v[222:223] op_sel_hi:[1,0]
v_pk_mul_f32 v[52:53], v[52:53], v[222:223] op_sel_hi:[1,0]
v_pk_mul_f32 v[54:55], v[54:55], v[222:223] op_sel_hi:[1,0]
)"
R"(cv v48,v48,v49
cv v49,v50,v51
cv v50,v52,v53
cv v51,v54,v55
v_pk_mul_f32 v[56:57], v[56:57], v[222:223] op_sel_hi:[1,0]
v_pk_mul_f32 v[58:59], v[58:59], v[222:223] op_sel_hi:[1,0]
v_pk_mul_f32 v[60:61], v[60:61], v[222:223] op_sel_hi:[1,0]
v_pk_mul_f32 v[62:63], v[62:63], v[222:223] op_sel_hi:[1,0]
cv v56,v56,v57
cv v57,v58,v59
cv v58,v60,v61
cv v59,v62,v63
s_lshl_b32 s56, s57, 5
sa s56,s56,s48
s_lshl_b32 s56, s56, 15
sa s56,s56,0x2100000
)"
R"(s_and_b32 s69, s50, 1
s_lshl_b32 s69, s69, 10
sa s68,s56,s69
s_sub_u32 s69, s56, s69
sa s56,s68,0x0
buffer_store_dwordx4 v[0:3], v214, s[44:47], s56 offen offset:0 sc1
sa s56,s68,0x2000
buffer_store_dwordx4 v[8:11], v214, s[44:47], s56 offen offset:0 sc1
sa s56,s68,0x4000
buffer_store_dwordx4 v[16:19], v214, s[44:47], s56 offen offset:0 sc1
sa s56,s68,0x6000
buffer_store_dwordx4 v[24:27], v214, s[44:47], s56 offen offset:0 sc1
sa s56,s69,0x0
buffer_store_dwordx4 v[32:35], v214, s[44:47], s56 offen offset:3072 sc1
sa s56,s69,0x2000
buffer_store_dwordx4 v[40:43], v214, s[44:47], s56 offen offset:3072 sc1
)"
R"(sa s56,s69,0x4000
buffer_store_dwordx4 v[48:51], v214, s[44:47], s56 offen offset:3072 sc1
sa s56,s69,0x6000
buffer_store_dwordx4 v[56:59], v214, s[44:47], s56 offen offset:3072 sc1
s_branch L_ok_seg1_0
L_redo_finlo:
s_mov_b32 s66, 0
s_mov_b32 s67, 17
s_getpc_b64 s[64:65]
sa s64,s64,12
s_addc_u32 s65, s65, 0
s_branch L_slow
ds_write_b128 v230, v[0:3] offset:0
ds_write_b128 v230, v[4:7] offset:1024
ds_write_b128 v230, v[8:11] offset:2048
ds_write_b128 v230, v[12:15] offset:3072
)"
R"(ds_write_b128 v230, v[16:19] offset:4096
ds_write_b128 v230, v[20:23] offset:5120
ds_write_b128 v230, v[24:27] offset:6144
ds_write_b128 v230, v[28:31] offset:7168
ds_write_b128 v230, v[32:35] offset:8192
ds_write_b128 v230, v[36:39] offset:9216
ds_write_b128 v230, v[40:43] offset:10240
ds_write_b128 v230, v[44:47] offset:11264
ds_write_b128 v230, v[48:51] offset:12288
ds_write_b128 v230, v[52:55] offset:13312
ds_write_b128 v230, v[56:59] offset:14336
ds_write_b128 v230, v[60:63] offset:15360
s_branch L_ok_finlo
L_redo_finhi0:
s_lshl_b32 s66, s49, 1
sa s66,s66,2
)"
R"(s_mov_b32 s67, 17
s_getpc_b64 s[64:65]
sa s64,s64,12
s_addc_u32 s65, s65, 0
s_branch L_slow
s_branch L_ok_finhi0
L_redo_finhi1:
s_lshl_b32 s66, s49, 1
sa s66,s66,2
s_mov_b32 s67, 17
s_getpc_b64 s[64:65]
sa s64,s64,12
s_addc_u32 s65, s65, 0
s_branch L_slow
s_branch L_ok_finhi1
L_slow:
)"
R"(s_waitcnt lgkmcnt(0)
s_lshl_b32 s68, s49, 1
sa s68,s68,2
s_lshl_b32 s66, s66, 1
s_lshl_b32 s67, s67, 1
v_mov_b32 v0, 0
v_mov_b32 v1, 0
v_mov_b32 v2, 0
v_mov_b32 v3, 0
v_mov_b32 v4, 0
v_mov_b32 v5, 0
v_mov_b32 v6, 0
v_mov_b32 v7, 0
v_mov_b32 v8, 0
v_mov_b32 v9, 0
v_mov_b32 v10, 0
)"
R"(v_mov_b32 v11, 0
v_mov_b32 v12, 0
v_mov_b32 v13, 0
v_mov_b32 v14, 0
v_mov_b32 v15, 0
v_mov_b32 v16, 0
v_mov_b32 v17, 0
v_mov_b32 v18, 0
v_mov_b32 v19, 0
v_mov_b32 v20, 0
v_mov_b32 v21, 0
v_mov_b32 v22, 0
v_mov_b32 v23, 0
v_mov_b32 v24, 0
v_mov_b32 v25, 0
v_mov_b32 v26, 0
)"
R"(v_mov_b32 v27, 0
v_mov_b32 v28, 0
v_mov_b32 v29, 0
v_mov_b32 v30, 0
v_mov_b32 v31, 0
v_mov_b32 v32, 0
v_mov_b32 v33, 0
v_mov_b32 v34, 0
v_mov_b32 v35, 0
v_mov_b32 v36, 0
v_mov_b32 v37, 0
v_mov_b32 v38, 0
v_mov_b32 v39, 0
v_mov_b32 v40, 0
v_mov_b32 v41, 0
v_mov_b32 v42, 0
)"
R"(v_mov_b32 v43, 0
v_mov_b32 v44, 0
v_mov_b32 v45, 0
v_mov_b32 v46, 0
v_mov_b32 v47, 0
v_mov_b32 v48, 0
v_mov_b32 v49, 0
v_mov_b32 v50, 0
v_mov_b32 v51, 0
v_mov_b32 v52, 0
v_mov_b32 v53, 0
v_mov_b32 v54, 0
v_mov_b32 v55, 0
v_mov_b32 v56, 0
v_mov_b32 v57, 0
v_mov_b32 v58, 0
)"
R"(v_mov_b32 v59, 0
v_mov_b32 v60, 0
v_mov_b32 v61, 0
v_mov_b32 v62, 0
v_mov_b32 v63, 0
v_mov_b32 v64, 0
v_mov_b32 v65, 0
v_mov_b32 v66, 0
v_mov_b32 v67, 0
v_mov_b32 v68, 0
v_mov_b32 v69, 0
v_mov_b32 v70, 0
v_mov_b32 v71, 0
v_mov_b32 v72, 0
v_mov_b32 v73, 0
v_mov_b32 v74, 0
)"
R"(v_mov_b32 v75, 0
v_mov_b32 v76, 0
v_mov_b32 v77, 0
v_mov_b32 v78, 0
v_mov_b32 v79, 0
v_mov_b32 v80, 0
v_mov_b32 v81, 0
v_mov_b32 v82, 0
v_mov_b32 v83, 0
v_mov_b32 v84, 0
v_mov_b32 v85, 0
v_mov_b32 v86, 0
v_mov_b32 v87, 0
v_mov_b32 v88, 0
v_mov_b32 v89, 0
v_mov_b32 v90, 0
)"
R"(v_mov_b32 v91, 0
v_mov_b32 v92, 0
v_mov_b32 v93, 0
v_mov_b32 v94, 0
v_mov_b32 v95, 0
v_mov_b32 v96, 0
v_mov_b32 v97, 0
v_mov_b32 v98, 0
v_mov_b32 v99, 0
v_mov_b32 v100, 0
v_mov_b32 v101, 0
v_mov_b32 v102, 0
v_mov_b32 v103, 0
v_mov_b32 v104, 0
v_mov_b32 v105, 0
v_mov_b32 v106, 0
)"
R"(v_mov_b32 v107, 0
v_mov_b32 v108, 0
v_mov_b32 v109, 0
v_mov_b32 v110, 0
v_mov_b32 v111, 0
v_mov_b32 v112, 0
v_mov_b32 v113, 0
v_mov_b32 v114, 0
v_mov_b32 v115, 0
v_mov_b32 v116, 0
v_mov_b32 v117, 0
v_mov_b32 v118, 0
v_mov_b32 v119, 0
v_mov_b32 v120, 0
v_mov_b32 v121, 0
v_mov_b32 v122, 0
)"
R"(v_mov_b32 v123, 0
v_mov_b32 v124, 0
v_mov_b32 v125, 0
v_mov_b32 v126, 0
v_mov_b32 v127, 0
v_mov_b32 v192, 0
v_mov_b32 v193, 0
v_mov_b32 v194, 0
v_mov_b32 v195, 0
v_mov_b32 v196, 0
v_mov_b32 v197, 0
v_mov_b32 v198, 0
v_mov_b32 v199, 0
v_mov_b32 v200, 0
v_mov_b32 v201, 0
v_mov_b32 v202, 0
)"
R"(v_mov_b32 v203, 0
v_mov_b32 v204, 0
v_mov_b32 v205, 0
v_mov_b32 v206, 0
v_mov_b32 v207, 0
v_mov_b32 v176, 0x7f000000
v_mov_b32 v180, 0x7f000000
v_mov_b32 v184, 0x7f000000
v_mov_b32 v188, 0x7f000000
L_slow_unit:
s_lshr_b32 s69, s66, 1
s_lshl_b32 s72, s69, 1
s_sub_u32 s72, s72, s68
s_lshr_b32 s73, s50, 1
sa s72,s72,s73
s_cmp_lt_u32 s69, s68
)"
R"(s_cselect_b32 s69, s69, s72
s_lshl_b32 s72, s48, 5
sa s72,s72,s69
s_lshl_b32 s72, s72, 15
s_and_b32 s73, s66, 1
s_lshl_b32 s74, s73, 13
sa s72,s72,s74
sa s72,s72,0x100000
sa s74,s72,0x0
bl a[96:99],v214,s[44:47],s74 offen offset:0 sc1
bl a[100:103],v214,s[44:47],s74 offen offset:1024 sc1
bl a[104:107],v214,s[44:47],s74 offen offset:2048 sc1
bl a[108:111],v214,s[44:47],s74 offen offset:3072 sc1
sa s74,s72,0x1000
bl a[112:115],v214,s[44:47],s74 offen offset:0 sc1
bl a[116:119],v214,s[44:47],s74 offen offset:1024 sc1
)"
R"(bl a[120:123],v214,s[44:47],s74 offen offset:2048 sc1
bl a[124:127],v214,s[44:47],s74 offen offset:3072 sc1
sa s74,s72,0x4000
bl a[160:163],v214,s[44:47],s74 offen offset:0 sc1
bl a[164:167],v214,s[44:47],s74 offen offset:1024 sc1
bl a[168:171],v214,s[44:47],s74 offen offset:2048 sc1
bl a[172:175],v214,s[44:47],s74 offen offset:3072 sc1
sa s74,s72,0x5000
bl a[176:179],v214,s[44:47],s74 offen offset:0 sc1
bl a[180:183],v214,s[44:47],s74 offen offset:1024 sc1
bl a[184:187],v214,s[44:47],s74 offen offset:2048 sc1
bl a[188:191],v214,s[44:47],s74 offen offset:3072 sc1
s_lshl_b32 s69, s69, 1
sa s69,s69,s73
s_lshl_b32 s69, s69, 5
v_lshrrev_b32 v221, 8, v214
)"
R"(v_lshl_add_u32 v221, v221, 2, s69
s_waitcnt vmcnt(0)
s_and_b32 s73, s50, 1
s_xor_b32 s73, s73, 0
s_lshl_b32 s74, s57, 3
sa s73,s73,s74
s_lshl_b32 s73, s73, 4
v_bfe_u32 v220, v214, 4, 4
v_add_u32 v220, s73, v220
v_sub_u32 v220, v220, v221
mm v[128:131],a[96:99],a[0:3],0
mm v[128:131],a[100:103],a[4:7],v[128:131]
mm v[128:131],a[104:107],a[8:11],v[128:131]
mm v[128:131],a[108:111],a[12:15],v[128:131]
mm v[132:135],a[112:115],a[0:3],0
mm v[132:135],a[116:119],a[4:7],v[132:135]
)"
R"(mm v[132:135],a[120:123],a[8:11],v[132:135]
mm v[132:135],a[124:127],a[12:15],v[132:135]
s_nop 7
v_cmp_le_i32 vcc, 0, v220
s_nop 1
v_cndmask_b32 v128, v213, v128, vcc
v_cmp_le_i32 vcc, 1, v220
s_nop 1
v_cndmask_b32 v129, v213, v129, vcc
v_cmp_le_i32 vcc, 2, v220
s_nop 1
v_cndmask_b32 v130, v213, v130, vcc
v_cmp_le_i32 vcc, 3, v220
s_nop 1
v_cndmask_b32 v131, v213, v131, vcc
v_cmp_le_i32 vcc, 16, v220
)"
R"(s_nop 1
v_cndmask_b32 v132, v213, v132, vcc
v_cmp_le_i32 vcc, 17, v220
s_nop 1
v_cndmask_b32 v133, v213, v133, vcc
v_cmp_le_i32 vcc, 18, v220
s_nop 1
v_cndmask_b32 v134, v213, v134, vcc
v_cmp_le_i32 vcc, 19, v220
s_nop 1
v_cndmask_b32 v135, v213, v135, vcc
v_max3_f32 v224, v128, v129, v130
v_max3_f32 v224, v224, v131, v132
v_max3_f32 v224, v224, v133, v134
v_max_f32 v224, v224, v135
v_mov_b32 v225, v224
)"
R"(s_nop 1
v_permlane32_swap_b32 v224, v225
s_nop 1
v_max_f32 v224, v224, v225
v_mov_b32 v225, v224
s_nop 1
v_permlane16_swap_b32 v224, v225
s_nop 1
v_max_f32 v224, v224, v225
v_sub_f32 v224, 0xc2c80000, v224
v_min_f32 v224, v224, v176
v_sub_f32 v222, v224, v176
ex v222,v222
v_mov_b32 v176, v224
v_add_f32 v128, v128, v224
v_add_f32 v129, v129, v224
)"
R"(v_add_f32 v130, v130, v224
v_add_f32 v131, v131, v224
v_add_f32 v132, v132, v224
v_add_f32 v133, v133, v224
v_add_f32 v134, v134, v224
v_add_f32 v135, v135, v224
ex v128,v128
ex v129,v129
ex v130,v130
ex v131,v131
ex v132,v132
ex v133,v133
ex v134,v134
ex v135,v135
v_mul_f32 v0, v0, v222
v_mul_f32 v1, v1, v222
)"
R"(v_mul_f32 v2, v2, v222
v_mul_f32 v3, v3, v222
v_mul_f32 v4, v4, v222
v_mul_f32 v5, v5, v222
v_mul_f32 v6, v6, v222
v_mul_f32 v7, v7, v222
v_mul_f32 v8, v8, v222
v_mul_f32 v9, v9, v222
v_mul_f32 v10, v10, v222
v_mul_f32 v11, v11, v222
v_mul_f32 v12, v12, v222
v_mul_f32 v13, v13, v222
v_mul_f32 v14, v14, v222
v_mul_f32 v15, v15, v222
v_mul_f32 v16, v16, v222
v_mul_f32 v17, v17, v222
)"
R"(v_mul_f32 v18, v18, v222
v_mul_f32 v19, v19, v222
v_mul_f32 v20, v20, v222
v_mul_f32 v21, v21, v222
v_mul_f32 v22, v22, v222
v_mul_f32 v23, v23, v222
v_mul_f32 v24, v24, v222
v_mul_f32 v25, v25, v222
v_mul_f32 v26, v26, v222
v_mul_f32 v27, v27, v222
v_mul_f32 v28, v28, v222
v_mul_f32 v29, v29, v222
v_mul_f32 v30, v30, v222
v_mul_f32 v31, v31, v222
v_mul_f32 v192, v192, v222
v_mul_f32 v193, v193, v222
)"
R"(v_mul_f32 v194, v194, v222
v_mul_f32 v195, v195, v222
cv v160,v128,v129
cv v161,v130,v131
cv v162,v132,v133
cv v163,v134,v135
s_nop 1
mm v[192:195],v[208:211],v[160:163],v[192:195]
mm v[0:3],a[160:163],v[160:163],v[0:3]
mm v[4:7],a[164:167],v[160:163],v[4:7]
mm v[8:11],a[168:171],v[160:163],v[8:11]
mm v[12:15],a[172:175],v[160:163],v[12:15]
mm v[16:19],a[176:179],v[160:163],v[16:19]
mm v[20:23],a[180:183],v[160:163],v[20:23]
mm v[24:27],a[184:187],v[160:163],v[24:27]
mm v[28:31],a[188:191],v[160:163],v[28:31]
)"
R"(s_and_b32 s73, s50, 1
s_xor_b32 s73, s73, 3
s_lshl_b32 s74, s57, 3
sa s73,s73,s74
s_lshl_b32 s73, s73, 4
v_bfe_u32 v220, v214, 4, 4
v_add_u32 v220, s73, v220
v_sub_u32 v220, v220, v221
mm v[136:139],a[96:99],a[16:19],0
mm v[136:139],a[100:103],a[20:23],v[136:139]
mm v[136:139],a[104:107],a[24:27],v[136:139]
mm v[136:139],a[108:111],a[28:31],v[136:139]
mm v[140:143],a[112:115],a[16:19],0
mm v[140:143],a[116:119],a[20:23],v[140:143]
mm v[140:143],a[120:123],a[24:27],v[140:143]
mm v[140:143],a[124:127],a[28:31],v[140:143]
)"
R"(s_nop 7
v_cmp_le_i32 vcc, 0, v220
s_nop 1
v_cndmask_b32 v136, v213, v136, vcc
v_cmp_le_i32 vcc, 1, v220
s_nop 1
v_cndmask_b32 v137, v213, v137, vcc
v_cmp_le_i32 vcc, 2, v220
s_nop 1
v_cndmask_b32 v138, v213, v138, vcc
v_cmp_le_i32 vcc, 3, v220
s_nop 1
v_cndmask_b32 v139, v213, v139, vcc
v_cmp_le_i32 vcc, 16, v220
s_nop 1
v_cndmask_b32 v140, v213, v140, vcc
)"
R"(v_cmp_le_i32 vcc, 17, v220
s_nop 1
v_cndmask_b32 v141, v213, v141, vcc
v_cmp_le_i32 vcc, 18, v220
s_nop 1
v_cndmask_b32 v142, v213, v142, vcc
v_cmp_le_i32 vcc, 19, v220
s_nop 1
v_cndmask_b32 v143, v213, v143, vcc
v_max3_f32 v224, v136, v137, v138
v_max3_f32 v224, v224, v139, v140
v_max3_f32 v224, v224, v141, v142
v_max_f32 v224, v224, v143
v_mov_b32 v225, v224
s_nop 1
v_permlane32_swap_b32 v224, v225
)"
R"(s_nop 1
v_max_f32 v224, v224, v225
v_mov_b32 v225, v224
s_nop 1
v_permlane16_swap_b32 v224, v225
s_nop 1
v_max_f32 v224, v224, v225
v_sub_f32 v224, 0xc2c80000, v224
v_min_f32 v224, v224, v180
v_sub_f32 v222, v224, v180
ex v222,v222
v_mov_b32 v180, v224
v_add_f32 v136, v136, v224
v_add_f32 v137, v137, v224
v_add_f32 v138, v138, v224
v_add_f32 v139, v139, v224
)"
R"(v_add_f32 v140, v140, v224
v_add_f32 v141, v141, v224
v_add_f32 v142, v142, v224
v_add_f32 v143, v143, v224
ex v136,v136
ex v137,v137
ex v138,v138
ex v139,v139
ex v140,v140
ex v141,v141
ex v142,v142
ex v143,v143
v_mul_f32 v32, v32, v222
v_mul_f32 v33, v33, v222
v_mul_f32 v34, v34, v222
v_mul_f32 v35, v35, v222
)"
R"(v_mul_f32 v36, v36, v222
v_mul_f32 v37, v37, v222
v_mul_f32 v38, v38, v222
v_mul_f32 v39, v39, v222
v_mul_f32 v40, v40, v222
v_mul_f32 v41, v41, v222
v_mul_f32 v42, v42, v222
v_mul_f32 v43, v43, v222
v_mul_f32 v44, v44, v222
v_mul_f32 v45, v45, v222
v_mul_f32 v46, v46, v222
v_mul_f32 v47, v47, v222
v_mul_f32 v48, v48, v222
v_mul_f32 v49, v49, v222
v_mul_f32 v50, v50, v222
v_mul_f32 v51, v51, v222
)"
R"(v_mul_f32 v52, v52, v222
v_mul_f32 v53, v53, v222
v_mul_f32 v54, v54, v222
v_mul_f32 v55, v55, v222
v_mul_f32 v56, v56, v222
v_mul_f32 v57, v57, v222
v_mul_f32 v58, v58, v222
v_mul_f32 v59, v59, v222
v_mul_f32 v60, v60, v222
v_mul_f32 v61, v61, v222
v_mul_f32 v62, v62, v222
v_mul_f32 v63, v63, v222
v_mul_f32 v196, v196, v222
v_mul_f32 v197, v197, v222
v_mul_f32 v198, v198, v222
v_mul_f32 v199, v199, v222
)"
R"(cv v164,v136,v137
cv v165,v138,v139
cv v166,v140,v141
cv v167,v142,v143
s_nop 1
mm v[196:199],v[208:211],v[164:167],v[196:199]
mm v[32:35],a[160:163],v[164:167],v[32:35]
mm v[36:39],a[164:167],v[164:167],v[36:39]
mm v[40:43],a[168:171],v[164:167],v[40:43]
mm v[44:47],a[172:175],v[164:167],v[44:47]
mm v[48:51],a[176:179],v[164:167],v[48:51]
mm v[52:55],a[180:183],v[164:167],v[52:55]
mm v[56:59],a[184:187],v[164:167],v[56:59]
mm v[60:63],a[188:191],v[164:167],v[60:63]
s_and_b32 s73, s50, 1
s_xor_b32 s73, s73, 4
)"
R"(s_lshl_b32 s74, s57, 3
sa s73,s73,s74
s_lshl_b32 s73, s73, 4
v_bfe_u32 v220, v214, 4, 4
v_add_u32 v220, s73, v220
v_sub_u32 v220, v220, v221
mm v[144:147],a[96:99],a[32:35],0
mm v[144:147],a[100:103],a[36:39],v[144:147]
mm v[144:147],a[104:107],a[40:43],v[144:147]
mm v[144:147],a[108:111],a[44:47],v[144:147]
mm v[148:151],a[112:115],a[32:35],0
mm v[148:151],a[116:119],a[36:39],v[148:151]
mm v[148:151],a[120:123],a[40:43],v[148:151]
mm v[148:151],a[124:127],a[44:47],v[148:151]
s_nop 7
v_cmp_le_i32 vcc, 0, v220
)"
R"(s_nop 1
v_cndmask_b32 v144, v213, v144, vcc
v_cmp_le_i32 vcc, 1, v220
s_nop 1
v_cndmask_b32 v145, v213, v145, vcc
v_cmp_le_i32 vcc, 2, v220
s_nop 1
v_cndmask_b32 v146, v213, v146, vcc
v_cmp_le_i32 vcc, 3, v220
s_nop 1
v_cndmask_b32 v147, v213, v147, vcc
v_cmp_le_i32 vcc, 16, v220
s_nop 1
v_cndmask_b32 v148, v213, v148, vcc
v_cmp_le_i32 vcc, 17, v220
s_nop 1
)"
R"(v_cndmask_b32 v149, v213, v149, vcc
v_cmp_le_i32 vcc, 18, v220
s_nop 1
v_cndmask_b32 v150, v213, v150, vcc
v_cmp_le_i32 vcc, 19, v220
s_nop 1
v_cndmask_b32 v151, v213, v151, vcc
v_max3_f32 v224, v144, v145, v146
v_max3_f32 v224, v224, v147, v148
v_max3_f32 v224, v224, v149, v150
v_max_f32 v224, v224, v151
v_mov_b32 v225, v224
s_nop 1
v_permlane32_swap_b32 v224, v225
s_nop 1
v_max_f32 v224, v224, v225
)"
R"(v_mov_b32 v225, v224
s_nop 1
v_permlane16_swap_b32 v224, v225
s_nop 1
v_max_f32 v224, v224, v225
v_sub_f32 v224, 0xc2c80000, v224
v_min_f32 v224, v224, v184
v_sub_f32 v222, v224, v184
ex v222,v222
v_mov_b32 v184, v224
v_add_f32 v144, v144, v224
v_add_f32 v145, v145, v224
v_add_f32 v146, v146, v224
v_add_f32 v147, v147, v224
v_add_f32 v148, v148, v224
v_add_f32 v149, v149, v224
)"
R"(v_add_f32 v150, v150, v224
v_add_f32 v151, v151, v224
ex v144,v144
ex v145,v145
ex v146,v146
ex v147,v147
ex v148,v148
ex v149,v149
ex v150,v150
ex v151,v151
v_mul_f32 v64, v64, v222
v_mul_f32 v65, v65, v222
v_mul_f32 v66, v66, v222
v_mul_f32 v67, v67, v222
v_mul_f32 v68, v68, v222
v_mul_f32 v69, v69, v222
)"
R"(v_mul_f32 v70, v70, v222
v_mul_f32 v71, v71, v222
v_mul_f32 v72, v72, v222
v_mul_f32 v73, v73, v222
v_mul_f32 v74, v74, v222
v_mul_f32 v75, v75, v222
v_mul_f32 v76, v76, v222
v_mul_f32 v77, v77, v222
v_mul_f32 v78, v78, v222
v_mul_f32 v79, v79, v222
v_mul_f32 v80, v80, v222
v_mul_f32 v81, v81, v222
v_mul_f32 v82, v82, v222
v_mul_f32 v83, v83, v222
v_mul_f32 v84, v84, v222
v_mul_f32 v85, v85, v222
)"
R"(v_mul_f32 v86, v86, v222
v_mul_f32 v87, v87, v222
v_mul_f32 v88, v88, v222
v_mul_f32 v89, v89, v222
v_mul_f32 v90, v90, v222
v_mul_f32 v91, v91, v222
v_mul_f32 v92, v92, v222
v_mul_f32 v93, v93, v222
v_mul_f32 v94, v94, v222
v_mul_f32 v95, v95, v222
v_mul_f32 v200, v200, v222
v_mul_f32 v201, v201, v222
v_mul_f32 v202, v202, v222
v_mul_f32 v203, v203, v222
cv v168,v144,v145
cv v169,v146,v147
)"
R"(cv v170,v148,v149
cv v171,v150,v151
s_nop 1
mm v[200:203],v[208:211],v[168:171],v[200:203]
mm v[64:67],a[160:163],v[168:171],v[64:67]
mm v[68:71],a[164:167],v[168:171],v[68:71]
mm v[72:75],a[168:171],v[168:171],v[72:75]
mm v[76:79],a[172:175],v[168:171],v[76:79]
mm v[80:83],a[176:179],v[168:171],v[80:83]
mm v[84:87],a[180:183],v[168:171],v[84:87]
mm v[88:91],a[184:187],v[168:171],v[88:91]
mm v[92:95],a[188:191],v[168:171],v[92:95]
s_and_b32 s73, s50, 1
s_xor_b32 s73, s73, 7
s_lshl_b32 s74, s57, 3
sa s73,s73,s74
)"
R"(s_lshl_b32 s73, s73, 4
v_bfe_u32 v220, v214, 4, 4
v_add_u32 v220, s73, v220
v_sub_u32 v220, v220, v221
mm v[152:155],a[96:99],a[48:51],0
mm v[152:155],a[100:103],a[52:55],v[152:155]
mm v[152:155],a[104:107],a[56:59],v[152:155]
mm v[152:155],a[108:111],a[60:63],v[152:155]
mm v[156:159],a[112:115],a[48:51],0
mm v[156:159],a[116:119],a[52:55],v[156:159]
mm v[156:159],a[120:123],a[56:59],v[156:159]
mm v[156:159],a[124:127],a[60:63],v[156:159]
s_nop 7
v_cmp_le_i32 vcc, 0, v220
s_nop 1
v_cndmask_b32 v152, v213, v152, vcc
)"
R"(v_cmp_le_i32 vcc, 1, v220
s_nop 1
v_cndmask_b32 v153, v213, v153, vcc
v_cmp_le_i32 vcc, 2, v220
s_nop 1
v_cndmask_b32 v154, v213, v154, vcc
v_cmp_le_i32 vcc, 3, v220
s_nop 1
v_cndmask_b32 v155, v213, v155, vcc
v_cmp_le_i32 vcc, 16, v220
s_nop 1
v_cndmask_b32 v156, v213, v156, vcc
v_cmp_le_i32 vcc, 17, v220
s_nop 1
v_cndmask_b32 v157, v213, v157, vcc
v_cmp_le_i32 vcc, 18, v220
)"
R"(s_nop 1
v_cndmask_b32 v158, v213, v158, vcc
v_cmp_le_i32 vcc, 19, v220
s_nop 1
v_cndmask_b32 v159, v213, v159, vcc
v_max3_f32 v224, v152, v153, v154
v_max3_f32 v224, v224, v155, v156
v_max3_f32 v224, v224, v157, v158
v_max_f32 v224, v224, v159
v_mov_b32 v225, v224
s_nop 1
v_permlane32_swap_b32 v224, v225
s_nop 1
v_max_f32 v224, v224, v225
v_mov_b32 v225, v224
s_nop 1
)"
R"(v_permlane16_swap_b32 v224, v225
s_nop 1
v_max_f32 v224, v224, v225
v_sub_f32 v224, 0xc2c80000, v224
v_min_f32 v224, v224, v188
v_sub_f32 v222, v224, v188
ex v222,v222
v_mov_b32 v188, v224
v_add_f32 v152, v152, v224
v_add_f32 v153, v153, v224
v_add_f32 v154, v154, v224
v_add_f32 v155, v155, v224
v_add_f32 v156, v156, v224
v_add_f32 v157, v157, v224
v_add_f32 v158, v158, v224
v_add_f32 v159, v159, v224
)"
R"(ex v152,v152
ex v153,v153
ex v154,v154
ex v155,v155
ex v156,v156
ex v157,v157
ex v158,v158
ex v159,v159
v_mul_f32 v96, v96, v222
v_mul_f32 v97, v97, v222
v_mul_f32 v98, v98, v222
v_mul_f32 v99, v99, v222
v_mul_f32 v100, v100, v222
v_mul_f32 v101, v101, v222
v_mul_f32 v102, v102, v222
v_mul_f32 v103, v103, v222
)"
R"(v_mul_f32 v104, v104, v222
v_mul_f32 v105, v105, v222
v_mul_f32 v106, v106, v222
v_mul_f32 v107, v107, v222
v_mul_f32 v108, v108, v222
v_mul_f32 v109, v109, v222
v_mul_f32 v110, v110, v222
v_mul_f32 v111, v111, v222
v_mul_f32 v112, v112, v222
v_mul_f32 v113, v113, v222
v_mul_f32 v114, v114, v222
v_mul_f32 v115, v115, v222
v_mul_f32 v116, v116, v222
v_mul_f32 v117, v117, v222
v_mul_f32 v118, v118, v222
v_mul_f32 v119, v119, v222
)"
R"(v_mul_f32 v120, v120, v222
v_mul_f32 v121, v121, v222
v_mul_f32 v122, v122, v222
v_mul_f32 v123, v123, v222
v_mul_f32 v124, v124, v222
v_mul_f32 v125, v125, v222
v_mul_f32 v126, v126, v222
v_mul_f32 v127, v127, v222
v_mul_f32 v204, v204, v222
v_mul_f32 v205, v205, v222
v_mul_f32 v206, v206, v222
v_mul_f32 v207, v207, v222
cv v172,v152,v153
cv v173,v154,v155
cv v174,v156,v157
cv v175,v158,v159
)"
R"(s_nop 1
mm v[204:207],v[208:211],v[172:175],v[204:207]
mm v[96:99],a[160:163],v[172:175],v[96:99]
mm v[100:103],a[164:167],v[172:175],v[100:103]
mm v[104:107],a[168:171],v[172:175],v[104:107]
mm v[108:111],a[172:175],v[172:175],v[108:111]
mm v[112:115],a[176:179],v[172:175],v[112:115]
mm v[116:119],a[180:183],v[172:175],v[116:119]
mm v[120:123],a[184:187],v[172:175],v[120:123]
mm v[124:127],a[188:191],v[172:175],v[124:127]
sa s66,s66,1
s_cmp_lt_u32 s66, s67
s_cbranch_scc1 L_slow_unit
s_nop 7
s_setpc_b64 s[64:65]
L_f3:
)"
R"(s_mov_b32 s80, 0
s_mov_b32 s62, 0x8000
L_pollf3:
buffer_load_dword v245, v249, s[44:47], s80 offen sc1
s_waitcnt vmcnt(0)
v_cmp_eq_u32 vcc, 1, v245
s_nop 1
s_cmp_eq_u64 vcc, exec
s_cbranch_scc1 L_pollf3_ok
s_sleep 1
s_sub_u32 s62, s62, 1
s_cmp_lg_u32 s62, 0
s_cbranch_scc1 L_pollf3
L_pollf3_ok:
s_branch L_f3d
L_f2o:
)"
R"(s_waitcnt vmcnt(0)
s_mov_b64 exec, 1
buffer_store_dword v246, v247, s[44:47], s55 offen sc1
s_mov_b64 exec, -1
s_mov_b32 s62, 0x8000
L_pollf2o:
buffer_load_dword v251, v250, s[44:47], s81 offen sc1
s_waitcnt vmcnt(0)
v_cmp_eq_u32 vcc, 1, v251
s_nop 1
s_cmp_eq_u64 vcc, exec
s_cbranch_scc1 L_pollf2o_ok
s_sleep 1
s_sub_u32 s62, s62, 1
s_cmp_lg_u32 s62, 0
s_cbranch_scc1 L_pollf2o
)"
R"(L_pollf2o_ok:
s_branch L_f2od
L_f2e:
s_mov_b32 s62, 0x8000
L_pollf2e:
buffer_load_dword v251, v250, s[44:47], s81 offen sc1
s_waitcnt vmcnt(0)
v_cmp_eq_u32 vcc, 1, v251
s_nop 1
s_cmp_eq_u64 vcc, exec
s_cbranch_scc1 L_pollf2e_ok
s_sleep 1
s_sub_u32 s62, s62, 1
s_cmp_lg_u32 s62, 0
s_cbranch_scc1 L_pollf2e
L_pollf2e_ok:
)"
R"(s_branch L_f2ed
L_f2b0:
s_mov_b32 s62, 0x8000
L_pollf2b0:
buffer_load_dword v251, v250, s[44:47], s53 offen sc1
s_waitcnt vmcnt(0)
v_cmp_eq_u32 vcc, 1, v251
s_nop 1
s_cmp_eq_u64 vcc, exec
s_cbranch_scc1 L_pollf2b0_ok
s_sleep 1
s_sub_u32 s62, s62, 1
s_cmp_lg_u32 s62, 0
s_cbranch_scc1 L_pollf2b0
L_pollf2b0_ok:
s_branch L_f2b0d
)"
R"(L_f2b1:
s_mov_b32 s62, 0x8000
L_pollf2b1:
buffer_load_dword v251, v250, s[44:47], s53 offen sc1
s_waitcnt vmcnt(0)
v_cmp_eq_u32 vcc, 1, v251
s_nop 1
s_cmp_eq_u64 vcc, exec
s_cbranch_scc1 L_pollf2b1_ok
s_sleep 1
s_sub_u32 s62, s62, 1
s_cmp_lg_u32 s62, 0
s_cbranch_scc1 L_pollf2b1
L_pollf2b1_ok:
s_branch L_f2b1d
L_fp0:
)"
R"(s_mov_b32 s57, 0
s_mov_b32 s62, 0x8000
L_pollfp0:
buffer_load_dword v251, v244, s[44:47], s57 offen sc1
s_waitcnt vmcnt(0)
v_cmp_eq_u32 vcc, 1, v251
s_nop 1
s_cmp_eq_u64 vcc, exec
s_cbranch_scc1 L_pollfp0_ok
s_sleep 1
s_sub_u32 s62, s62, 1
s_cmp_lg_u32 s62, 0
s_cbranch_scc1 L_pollfp0
L_pollfp0_ok:
s_branch L_fp0d
L_fp1:
)"
R"(s_mov_b32 s57, 0
s_mov_b32 s62, 0x8000
L_pollfp1:
buffer_load_dword v251, v244, s[44:47], s57 offen sc1
s_waitcnt vmcnt(0)
v_cmp_eq_u32 vcc, 1, v251
s_nop 1
s_cmp_eq_u64 vcc, exec
s_cbranch_scc1 L_pollfp1_ok
s_sleep 1
s_sub_u32 s62, s62, 1
s_cmp_lg_u32 s62, 0
s_cbranch_scc1 L_pollfp1
L_pollfp1_ok:
s_branch L_fp1d
L_exit:
)"
R"(.purgem mm
.purgem dr
.purgem dt
.purgem cv
.purgem ar
.purgem ex
.purgem bl
.purgem sa
)"
  :: "s"(__builtin_amdgcn_kernarg_segment_ptr()), "s"(bid), "s"(wave), "s"((unsigned)(size_t)lds) : "v0", "v1", "v2", "v3", "v4", "v5", "v6", "v7", "v8", "v9", "v10", "v11", "v12", "v13", "v14", "v15", "v16", "v17", "v18", "v19", "v20", "v21", "v22", "v23", "v24", "v25", "v26", "v27", "v28", "v29", "v30", "v31", "v32", "v33", "v34", "v35", "v36", "v37", "v38", "v39", "v40", "v41", "v42", "v43", "v44", "v45", "v46", "v47", "v48", "v49", "v50", "v51", "v52", "v53", "v54", "v55", "v56", "v57", "v58", "v59", "v60", "v61", "v62", "v63", "v64", "v65", "v66", "v67", "v68", "v69", "v70", "v71", "v72", "v73", "v74", "v75", "v76", "v77", "v78", "v79", "v80", "v81", "v82", "v83", "v84", "v85", "v86", "v87", "v88", "v89", "v90", "v91", "v92", "v93", "v94", "v95", "v96", "v97", "v98", "v99", "v100", "v101", "v102", "v103", "v104", "v105", "v106", "v107", "v108", "v109", "v110", "v111", "v112", "v113", "v114", "v115", "v116", "v117", "v118", "v119", "v120", "v121", "v122", "v123", "v124", "v125", "v126", "v127", "v128", "v129", "v130", "v131", "v132", "v133", "v134", "v135", "v136", "v137", "v138", "v139", "v140", "v141", "v142", "v143", "v144", "v145", "v146", "v147", "v148", "v149", "v150", "v151", "v152", "v153", "v154", "v155", "v156", "v157", "v158", "v159", "v160", "v161", "v162", "v163", "v164", "v165", "v166", "v167", "v168", "v169", "v170", "v171", "v172", "v173", "v174", "v175", "v176", "v177", "v178", "v179", "v180", "v181", "v182", "v183", "v184", "v185", "v186", "v187", "v188", "v189", "v190", "v191", "v192", "v193", "v194", "v195", "v196", "v197", "v198", "v199", "v200", "v201", "v202", "v203", "v204", "v205", "v206", "v207", "v208", "v209", "v210", "v211", "v212", "v213", "v214", "v215", "v216", "v217", "v218", "v219", "v220", "v221", "v222", "v223", "v224", "v225", "v226", "v227", "v228", "v229", "v230", "v231", "v232", "v233", "v234", "v235", "v236", "v237", "v238", "v239", "v240", "v241", "v242", "v243", "v244", "v245", "v246", "v247", "v248", "v249", "v250", "v251", "v252", "v253", "v254", "v255", "a0", "a1", "a2", "a3", "a4", "a5", "a6", "a7", "a8", "a9", "a10", "a11", "a12", "a13", "a14", "a15", "a16", "a17", "a18", "a19", "a20", "a21", "a22", "a23", "a24", "a25", "a26", "a27", "a28", "a29", "a30", "a31", "a32", "a33", "a34", "a35", "a36", "a37", "a38", "a39", "a40", "a41", "a42", "a43", "a44", "a45", "a46", "a47", "a48", "a49", "a50", "a51", "a52", "a53", "a54", "a55", "a56", "a57", "a58", "a59", "a60", "a61", "a62", "a63", "a64", "a65", "a66", "a67", "a68", "a69", "a70", "a71", "a72", "a73", "a74", "a75", "a76", "a77", "a78", "a79", "a80", "a81", "a82", "a83", "a84", "a85", "a86", "a87", "a88", "a89", "a90", "a91", "a92", "a93", "a94", "a95", "a96", "a97", "a98", "a99", "a100", "a101", "a102", "a103", "a104", "a105", "a106", "a107", "a108", "a109", "a110", "a111", "a112", "a113", "a114", "a115", "a116", "a117", "a118", "a119", "a120", "a121", "a122", "a123", "a124", "a125", "a126", "a127", "a128", "a129", "a130", "a131", "a132", "a133", "a134", "a135", "a136", "a137", "a138", "a139", "a140", "a141", "a142", "a143", "a144", "a145", "a146", "a147", "a148", "a149", "a150", "a151", "a152", "a153", "a154", "a155", "a156", "a157", "a158", "a159", "a160", "a161", "a162", "a163", "a164", "a165", "a166", "a167", "a168", "a169", "a170", "a171", "a172", "a173", "a174", "a175", "a176", "a177", "a178", "a179", "a180", "a181", "a182", "a183", "a184", "a185", "a186", "a187", "a188", "a189", "a190", "a191", "a192", "a193", "a194", "a195", "a196", "a197", "a198", "a199", "a200", "a201", "a202", "a203", "a204", "a205", "a206", "a207", "a208", "a209", "a210", "a211", "a212", "a213", "a214", "a215", "a216", "a217", "a218", "a219", "a220", "a221", "a222", "a223", "a224", "a225", "a226", "a227", "a228", "a229", "a230", "a231", "a232", "a233", "a234", "a235", "a236", "a237", "a238", "a239", "a240", "a241", "a242", "a243", "a244", "a245", "a246", "a247", "a248", "a249", "a250", "a251", "a252", "a253", "a254", "a255", "s36", "s37", "s38", "s39", "s40", "s41", "s42", "s43", "s44", "s45", "s46", "s47", "s48", "s49", "s50", "s51", "s52", "s53", "s54", "s55", "s56", "s57", "s58", "s59", "s60", "s61", "s62", "s63", "s64", "s65", "s66", "s67", "s68", "s69", "s70", "s71", "s72", "s73", "s74", "s75", "s76", "s77", "s78", "s79", "s80", "s81", "s82", "s83", "s84", "s85", "s86", "s87", "s88", "s89", "s90", "s91", "s92", "s93", "s94", "s95", "s96", "s97", "s98", "s99", "vcc", "memory");
}

extern "C" void kernel_launch(void* const* d_in, const int* in_sizes, int n_in, void* d_out, int out_size, void* d_ws, size_t ws_size,
                              hipStream_t stream) {
  fused_fwd<<<256, 256, 0, stream>>>(d_in[0], d_in[1], d_in[2], d_in[3], d_in[4], d_in[5], d_in[6], d_in[7], d_in[8], d_in[9], d_out, d_ws);
}
```
